```python
import torch

def _softmax(x, axis=-1):
    return torch.softmax(x, dim=axis)
def _gelu(x):
    return torch.nn.functional.gelu(x, approximate="tanh")
def _silu(x):
    return torch.nn.functional.silu(x)
def _sigmoid(x):
    return torch.sigmoid(x)
def _layernorm(x, axis=-1, eps=1e-5):
    m = x.mean(dim=axis, keepdim=True)
    v = x.var(dim=axis, keepdim=True, unbiased=False)
    return (x - m) / torch.sqrt(v + eps)
def _scatter_add(tgt, idx, src, axis=0):
    return tgt.clone().index_add_(axis, idx, src)
def _segment_sum(src, seg, n):
    out = torch.zeros((n,) + src.shape[1:], dtype=src.dtype)
    return out.index_add_(0, seg, src)
def _pad(x, pw):
    flat = tuple(v for pair in reversed(pw) for v in pair)
    return torch.nn.functional.pad(x, flat)

kernel_name = 'concat_heads__mha_block_cached__gqa_block_cached__near_ridge'

def setup_inputs():

    _w = (lambda a: (lambda t: t.to(torch.bfloat16) if t.is_floating_point() else t)(torch.as_tensor(a)))
    def _A_setup():
        B, Sq, D = 8, 224, 512
        H, Sk, Dh = 32, 512, 128
        return {
            "x": _w((lambda *s: torch.randn(*s, dtype=torch.bfloat16) * 0.8)(B, Sq, D)),
            "k_cache": _w((lambda *s: torch.randn(*s, dtype=torch.bfloat16) * 0.8)(B, H, Sk, Dh)),
            "v_cache": _w((lambda *s: torch.randn(*s, dtype=torch.bfloat16) * 0.05)(B, H, Sk, Dh)),
            "wq": _w((lambda *s: torch.randn(*s, dtype=torch.bfloat16) * 0.8)(D, H * Dh) * (D ** -0.5)),
            "wo": _w((lambda *s: torch.randn(*s, dtype=torch.bfloat16) * 0.05)(H * Dh, D) * ((H * Dh) ** -0.5)),
        }
    _A_in = _A_setup()

    def _B_setup():
        B, Sq, D = 8, 224, 512
        Hq, Hkv, Sk, Dh = 32, 32, 512, 128
        assert Hq % Hkv == 0, (Hq, Hkv)
        return {
            "x": _w((lambda *s: torch.randn(*s, dtype=torch.bfloat16) * 0.8)(B, Sq, D)),
            "k_cache": _w((lambda *s: torch.randn(*s, dtype=torch.bfloat16) * 0.8)(B, Hkv, Sk, Dh)),
            "v_cache": _w((lambda *s: torch.randn(*s, dtype=torch.bfloat16) * 0.05)(B, Hkv, Sk, Dh)),
            "wq": _w((lambda *s: torch.randn(*s, dtype=torch.bfloat16) * 0.8)(D, Hq * Dh) * (D ** -0.5)),
            "wo": _w((lambda *s: torch.randn(*s, dtype=torch.bfloat16) * 0.05)(Hq * Dh, D) * ((Hq * Dh) ** -0.5)),
        }
    _B_in = _B_setup()

    return {
        "A_x": _A_in["x"],
        "A_k_cache": _A_in["k_cache"],
        "A_v_cache": _A_in["v_cache"],
        "A_wq": _A_in["wq"],
        "A_wo": _A_in["wo"],
        "B_x": _B_in["x"],
        "B_k_cache": _B_in["k_cache"],
        "B_v_cache": _B_in["v_cache"],
        "B_wq": _B_in["wq"],
        "B_wo": _B_in["wo"],
    }

def reference(A_x, A_k_cache, A_v_cache, A_wq, A_wo, B_x, B_k_cache, B_v_cache, B_wq, B_wo):
    def _A_ref(x, k_cache, v_cache, wq, wo):
        B, Sq, D = x.shape
        H, Dh = 32, 128
        q = (lambda x, *d: x.permute(*d))((x @ wq).reshape(B, Sq, H, Dh), 0, 2, 1, 3)
        scores = torch.einsum("bhqd,bhkd->bhqk", q, k_cache) * (Dh ** -0.5)
        p = _softmax(scores, axis=-1)
        o = torch.einsum("bhqk,bhkd->bhqd", p, v_cache)
        return (lambda x, *d: x.permute(*d))(o, 0, 2, 1, 3).reshape(B, Sq, H * Dh) @ wo

    def _B_ref(x, k_cache, v_cache, wq, wo):
        B, Sq, D = x.shape
        Hq, Dh = 32, 128
        Hkv = k_cache.shape[1]
        rep = Hq // Hkv
        q = (lambda x, *d: x.permute(*d))((x @ wq).reshape(B, Sq, Hq, Dh), 0, 2, 1, 3)
        k = (lambda x, n, axis: torch.repeat_interleave(x, n, dim=axis))(k_cache, rep, axis=1)
        v = (lambda x, n, axis: torch.repeat_interleave(x, n, dim=axis))(v_cache, rep, axis=1)
        scores = torch.einsum("bhqd,bhkd->bhqk", q, k) * (Dh ** -0.5)
        p = _softmax(scores, axis=-1)
        o = torch.einsum("bhqk,bhkd->bhqd", p, v)
        return (lambda x, *d: x.permute(*d))(o, 0, 2, 1, 3).reshape(B, Sq, Hq * Dh) @ wo

    a = _A_ref(A_x, A_k_cache, A_v_cache, A_wq, A_wo)
    b = _B_ref(B_x, B_k_cache, B_v_cache, B_wq, B_wo)
    return torch.concatenate([a, b], axis=-1)
```

```cpp
#include <hip/hip_runtime.h>
#include <hip/hip_bf16.h>
#include <stdint.h>

typedef __attribute__((ext_vector_type(8))) short bf16x8;
typedef __attribute__((ext_vector_type(4))) short s16x4;
typedef __attribute__((ext_vector_type(4))) float f32x4;
typedef __attribute__((ext_vector_type(16))) float f32x16;
typedef __attribute__((ext_vector_type(4))) unsigned u32x4;
typedef unsigned short u16;

constexpr int NB = 8, SQ = 224, DM = 512, NH = 32, SK = 512, DH = 128;

__device__ __forceinline__ unsigned cvtpk(float lo, float hi) {
  unsigned r; asm volatile("v_cvt_pk_bf16_f32 %0, %1, %2" : "=v"(r) : "v"(lo), "v"(hi)); return r;
}
__device__ __forceinline__ float bf2f(u16 x) { return __uint_as_float(((unsigned)x) << 16); }

__global__ __launch_bounds__(256) void gemm_simple(const u16* __restrict__ A0, const u16* __restrict__ A1, int lda,
                                                   const u16* __restrict__ B0, const u16* __restrict__ B1, int ldb,
                                                   u16* __restrict__ C0, u16* __restrict__ C1, int ldc, int K) {
  __shared__ __attribute__((aligned(16))) u16 As[64][40];
  __shared__ __attribute__((aligned(16))) u16 Bs[64][40];
  const u16* A = blockIdx.z ? A1 : A0; const u16* B = blockIdx.z ? B1 : B0; u16* C = blockIdx.z ? C1 : C0;
  int m0 = blockIdx.y * 64, n0 = blockIdx.x * 64;
  int tid = threadIdx.x, wid = tid >> 6, lane = tid & 63;
  int wr = wid >> 1, wc = wid & 1;
  f32x4 acc[2][2] = {};
  for (int k0 = 0; k0 < K; k0 += 32) {
    { int r = tid >> 2, c = (tid & 3) * 8;
      *(bf16x8*)&As[r][c] = *(const bf16x8*)&A[(long)(m0 + r) * lda + k0 + c]; }
    { int r = tid >> 3, c = (tid & 7) * 8;
      bf16x8 v = *(const bf16x8*)&B[(long)(k0 + r) * ldb + n0 + c];
      for (int j = 0; j < 8; ++j) Bs[c + j][r] = (u16)v[j]; }
    __syncthreads();
    bf16x8 a[2], b[2];
    for (int i = 0; i < 2; ++i) a[i] = *(const bf16x8*)&As[wr * 32 + i * 16 + (lane & 15)][(lane >> 4) * 8];
    for (int i = 0; i < 2; ++i) b[i] = *(const bf16x8*)&Bs[wc * 32 + i * 16 + (lane & 15)][(lane >> 4) * 8];
    for (int i = 0; i < 2; ++i) for (int j = 0; j < 2; ++j)
      acc[i][j] = __builtin_amdgcn_mfma_f32_16x16x32_bf16(a[i], b[j], acc[i][j], 0, 0, 0);
    __syncthreads();
  }
  for (int i = 0; i < 2; ++i) for (int j = 0; j < 2; ++j) for (int r = 0; r < 4; ++r) {
    int row = m0 + wr * 32 + i * 16 + (lane >> 4) * 4 + r, col = n0 + wc * 32 + j * 16 + (lane & 15);
    C[(long)row * ldc + col] = (u16)(cvtpk(acc[i][j][r], 0.f) & 0xffff);
  }
}

__global__ __launch_bounds__(448) void attn_simple(const u16* __restrict__ Q, const u16* __restrict__ K0, const u16* __restrict__ V0,
                                                   const u16* __restrict__ K1, const u16* __restrict__ V1, u16* __restrict__ O) {
  __shared__ __attribute__((aligned(16))) u16 Ks[32][136];
  __shared__ __attribute__((aligned(16))) u16 Vt[128][40];
  int item = blockIdx.x, blk = item >> 8, b = (item >> 5) & 7, h = item & 31;
  const u16* Kh = (blk ? K1 : K0) + (long)(b * NH + h) * SK * DH;
  const u16* Vh = (blk ? V1 : V0) + (long)(b * NH + h) * SK * DH;
  const u16* Qh = Q + ((long)blk * NB * SQ + b * SQ) * (NH * DH) + h * DH;
  u16* Oh = O + ((long)blk * NB * SQ + b * SQ) * (NH * DH) + h * DH;
  int tid = threadIdx.x, wid = tid >> 6, lane = tid & 63, g = lane >> 4, c = lane & 15;
  bf16x8 qf[2][4];
  for (int t = 0; t < 2; ++t) for (int s = 0; s < 4; ++s)
    qf[t][s] = *(const bf16x8*)&Qh[(long)(wid * 32 + t * 16 + c) * (NH * DH) + s * 32 + g * 8];
  f32x4 o[2][8] = {};
  float m[2] = {-1e30f, -1e30f}, l[2] = {0.f, 0.f};
  const float C = 0.088388347648318440f * 1.4426950408889634f;
  for (int k0 = 0; k0 < SK; k0 += 32) {
    __syncthreads();
    for (int i = tid; i < 512; i += 448) {
      int r = i >> 4, cc = (i & 15) * 8;
      *(bf16x8*)&Ks[r][cc] = *(const bf16x8*)&Kh[(long)(k0 + r) * DH + cc];
      bf16x8 v = *(const bf16x8*)&Vh[(long)(k0 + r) * DH + cc];
      for (int j = 0; j < 8; ++j) Vt[cc + j][r] = (u16)v[j];
    }
    __syncthreads();
    for (int t = 0; t < 2; ++t) {
      f32x4 s[2] = {};
      for (int kt = 0; kt < 2; ++kt) for (int st = 0; st < 4; ++st) {
        bf16x8 a = *(const bf16x8*)&Ks[kt * 16 + c][st * 32 + g * 8];
        s[kt] = __builtin_amdgcn_mfma_f32_16x16x32_bf16(a, qf[t][st], s[kt], 0, 0, 0);
      }
      float mx = -1e30f;
      for (int kt = 0; kt < 2; ++kt) for (int r = 0; r < 4; ++r) mx = fmaxf(mx, s[kt][r]);
      mx = fmaxf(mx, __shfl_xor(mx, 16)); mx = fmaxf(mx, __shfl_xor(mx, 32));
      float mn = fmaxf(m[t], mx), al = exp2f((m[t] - mn) * C); m[t] = mn;
      float p[8], ps = 0.f;
      for (int kt = 0; kt < 2; ++kt) for (int r = 0; r < 4; ++r) { p[kt * 4 + r] = exp2f((s[kt][r] - mn) * C); ps += p[kt * 4 + r]; }
      ps += __shfl_xor(ps, 16); ps += __shfl_xor(ps, 32);
      l[t] = l[t] * al + ps;
      u32x4 w = {cvtpk(p[0], p[1]), cvtpk(p[2], p[3]), cvtpk(p[4], p[5]), cvtpk(p[6], p[7])};
      bf16x8 pf = *(bf16x8*)&w;
      for (int dt = 0; dt < 8; ++dt) {
        s16x4 lo = *(const s16x4*)&Vt[dt * 16 + c][g * 4], hi = *(const s16x4*)&Vt[dt * 16 + c][16 + g * 4];
        bf16x8 a = {lo[0], lo[1], lo[2], lo[3], hi[0], hi[1], hi[2], hi[3]};
        for (int r = 0; r < 4; ++r) o[t][dt][r] *= al;
        o[t][dt] = __builtin_amdgcn_mfma_f32_16x16x32_bf16(a, pf, o[t][dt], 0, 0, 0);
      }
    }
  }
  for (int t = 0; t < 2; ++t) { float il = 1.f / l[t];
    for (int dt = 0; dt < 8; ++dt) {
      uint2 w = {cvtpk(o[t][dt][0] * il, o[t][dt][1] * il), cvtpk(o[t][dt][2] * il, o[t][dt][3] * il)};
      *(uint2*)&Oh[(long)(wid * 32 + t * 16 + c) * (NH * DH) + dt * 16 + g * 4] = w;
    } }
}

extern "C" void kernel_launch(void* const* d_in, const int* in_sizes, int n_in, void* d_out, int out_size, void* d_ws, size_t ws_size, hipStream_t stream) {
  const u16* xA = (const u16*)d_in[0]; const u16* kA = (const u16*)d_in[1]; const u16* vA = (const u16*)d_in[2];
  const u16* wqA = (const u16*)d_in[3]; const u16* woA = (const u16*)d_in[4];
  const u16* xB = (const u16*)d_in[5]; const u16* kB = (const u16*)d_in[6]; const u16* vB = (const u16*)d_in[7];
  const u16* wqB = (const u16*)d_in[8]; const u16* woB = (const u16*)d_in[9];
  u16* out = (u16*)d_out;
  u16* Q = (u16*)d_ws; u16* O = Q + (size_t)2 * NB * SQ * NH * DH;
  const size_t half = (size_t)NB * SQ * NH * DH;
  gemm_simple<<<dim3(4096 / 64, 1792 / 64, 2), 256, 0, stream>>>(xA, xB, DM, wqA, wqB, NH * DH, Q, Q + half, NH * DH, DM);
  attn_simple<<<512, 448, 0, stream>>>(Q, kA, vA, kB, vB, O);
  gemm_simple<<<dim3(512 / 64, 1792 / 64, 2), 256, 0, stream>>>(O, O + half, NH * DH, woA, woB, DM, out, out + 512, 1024, NH * DH);
}
```

```cpp
#include <hip/hip_runtime.h>
#include <hip/hip_bf16.h>
#include <stdint.h>

typedef __attribute__((ext_vector_type(8))) short bf16x8;
typedef __attribute__((ext_vector_type(4))) short s16x4;
typedef __attribute__((ext_vector_type(4))) float f32x4;
typedef __attribute__((ext_vector_type(16))) float f32x16;
typedef __attribute__((ext_vector_type(4))) unsigned u32x4;
typedef unsigned short u16;

constexpr int NB = 8, SQ = 224, DM = 512, NH = 32, SK = 512, DH = 128;

__device__ __forceinline__ unsigned cvtpk(float lo, float hi) {
  unsigned r; asm volatile("v_cvt_pk_bf16_f32 %0, %1, %2" : "=v"(r) : "v"(lo), "v"(hi)); return r;
}
__device__ __forceinline__ float bf2f(u16 x) { return __uint_as_float(((unsigned)x) << 16); }

__global__ __launch_bounds__(256) void gemm_simple(const u16* __restrict__ A0, const u16* __restrict__ A1, int lda,
                                                   const u16* __restrict__ B0, const u16* __restrict__ B1, int ldb,
                                                   u16* __restrict__ C0, u16* __restrict__ C1, int ldc, int K) {
  __shared__ __attribute__((aligned(16))) u16 As[64][40];
  __shared__ __attribute__((aligned(16))) u16 Bs[64][40];
  const u16* A = blockIdx.z ? A1 : A0; const u16* B = blockIdx.z ? B1 : B0; u16* C = blockIdx.z ? C1 : C0;
  int m0 = blockIdx.y * 64, n0 = blockIdx.x * 64;
  int tid = threadIdx.x, wid = tid >> 6, lane = tid & 63;
  int wr = wid >> 1, wc = wid & 1;
  f32x4 acc[2][2] = {};
  for (int k0 = 0; k0 < K; k0 += 32) {
    { int r = tid >> 2, c = (tid & 3) * 8;
      *(bf16x8*)&As[r][c] = *(const bf16x8*)&A[(long)(m0 + r) * lda + k0 + c]; }
    { int r = tid >> 3, c = (tid & 7) * 8;
      bf16x8 v = *(const bf16x8*)&B[(long)(k0 + r) * ldb + n0 + c];
      for (int j = 0; j < 8; ++j) Bs[c + j][r] = (u16)v[j]; }
    __syncthreads();
    bf16x8 a[2], b[2];
    for (int i = 0; i < 2; ++i) a[i] = *(const bf16x8*)&As[wr * 32 + i * 16 + (lane & 15)][(lane >> 4) * 8];
    for (int i = 0; i < 2; ++i) b[i] = *(const bf16x8*)&Bs[wc * 32 + i * 16 + (lane & 15)][(lane >> 4) * 8];
    for (int i = 0; i < 2; ++i) for (int j = 0; j < 2; ++j)
      acc[i][j] = __builtin_amdgcn_mfma_f32_16x16x32_bf16(a[i], b[j], acc[i][j], 0, 0, 0);
    __syncthreads();
  }
  for (int i = 0; i < 2; ++i) for (int j = 0; j < 2; ++j) for (int r = 0; r < 4; ++r) {
    int row = m0 + wr * 32 + i * 16 + (lane >> 4) * 4 + r, col = n0 + wc * 32 + j * 16 + (lane & 15);
    C[(long)row * ldc + col] = (u16)(cvtpk(acc[i][j][r], 0.f) & 0xffff);
  }
}

__global__ __launch_bounds__(448) void attn_simple(const u16* __restrict__ Q, const u16* __restrict__ K0, const u16* __restrict__ V0,
                                                   const u16* __restrict__ K1, const u16* __restrict__ V1, u16* __restrict__ O) {
  __shared__ __attribute__((aligned(16))) u16 Ks[32][136];
  __shared__ __attribute__((aligned(16))) u16 Vt[128][40];
  int item = blockIdx.x, blk = item >> 8, b = (item >> 5) & 7, h = item & 31;
  const u16* Kh = (blk ? K1 : K0) + (long)(b * NH + h) * SK * DH;
  const u16* Vh = (blk ? V1 : V0) + (long)(b * NH + h) * SK * DH;
  const u16* Qh = Q + ((long)blk * NB * SQ + b * SQ) * (NH * DH) + h * DH;
  u16* Oh = O + ((long)blk * NB * SQ + b * SQ) * (NH * DH) + h * DH;
  int tid = threadIdx.x, wid = tid >> 6, lane = tid & 63, g = lane >> 4, c = lane & 15;
  bf16x8 qf[2][4];
  for (int t = 0; t < 2; ++t) for (int s = 0; s < 4; ++s)
    qf[t][s] = *(const bf16x8*)&Qh[(long)(wid * 32 + t * 16 + c) * (NH * DH) + s * 32 + g * 8];
  f32x4 o[2][8] = {};
  float m[2] = {-1e30f, -1e30f}, l[2] = {0.f, 0.f};
  const float C = 0.088388347648318440f * 1.4426950408889634f;
  for (int k0 = 0; k0 < SK; k0 += 32) {
    __syncthreads();
    for (int i = tid; i < 512; i += 448) {
      int r = i >> 4, cc = (i & 15) * 8;
      *(bf16x8*)&Ks[r][cc] = *(const bf16x8*)&Kh[(long)(k0 + r) * DH + cc];
      bf16x8 v = *(const bf16x8*)&Vh[(long)(k0 + r) * DH + cc];
      for (int j = 0; j < 8; ++j) Vt[cc + j][r] = (u16)v[j];
    }
    __syncthreads();
    for (int t = 0; t < 2; ++t) {
      f32x4 s[2] = {};
      for (int kt = 0; kt < 2; ++kt) for (int st = 0; st < 4; ++st) {
        bf16x8 a = *(const bf16x8*)&Ks[kt * 16 + c][st * 32 + g * 8];
        s[kt] = __builtin_amdgcn_mfma_f32_16x16x32_bf16(a, qf[t][st], s[kt], 0, 0, 0);
      }
      float mx = -1e30f;
      for (int kt = 0; kt < 2; ++kt) for (int r = 0; r < 4; ++r) mx = fmaxf(mx, s[kt][r]);
      mx = fmaxf(mx, __shfl_xor(mx, 16)); mx = fmaxf(mx, __shfl_xor(mx, 32));
      float mn = fmaxf(m[t], mx), al = exp2f((m[t] - mn) * C); m[t] = mn;
      float p[8], ps = 0.f;
      for (int kt = 0; kt < 2; ++kt) for (int r = 0; r < 4; ++r) { p[kt * 4 + r] = exp2f((s[kt][r] - mn) * C); ps += p[kt * 4 + r]; }
      ps += __shfl_xor(ps, 16); ps += __shfl_xor(ps, 32);
      l[t] = l[t] * al + ps;
      u32x4 w = {cvtpk(p[0], p[1]), cvtpk(p[2], p[3]), cvtpk(p[4], p[5]), cvtpk(p[6], p[7])};
      bf16x8 pf = *(bf16x8*)&w;
      for (int dt = 0; dt < 8; ++dt) {
        s16x4 lo = *(const s16x4*)&Vt[dt * 16 + c][g * 4], hi = *(const s16x4*)&Vt[dt * 16 + c][16 + g * 4];
        bf16x8 a = {lo[0], lo[1], lo[2], lo[3], hi[0], hi[1], hi[2], hi[3]};
        for (int r = 0; r < 4; ++r) o[t][dt][r] *= al;
        o[t][dt] = __builtin_amdgcn_mfma_f32_16x16x32_bf16(a, pf, o[t][dt], 0, 0, 0);
      }
    }
  }
  for (int t = 0; t < 2; ++t) { float il = 1.f / l[t];
    for (int dt = 0; dt < 8; ++dt) {
      uint2 w = {cvtpk(o[t][dt][0] * il, o[t][dt][1] * il), cvtpk(o[t][dt][2] * il, o[t][dt][3] * il)};
      *(uint2*)&Oh[(long)(wid * 32 + t * 16 + c) * (NH * DH) + dt * 16 + g * 4] = w;
    } }
}


typedef __attribute__((ext_vector_type(4))) unsigned pu4;
__global__ __launch_bounds__(1024) void probe(const pu4* __restrict__ p0, const pu4* __restrict__ p1, const pu4* __restrict__ p2, const pu4* __restrict__ p3, unsigned* sink, int amp) {
  unsigned long long t0 = __builtin_amdgcn_s_memrealtime();
  const pu4* ps[4] = {p0, p1, p2, p3};
  unsigned acc = 0;
  #pragma unroll
  for (int a = 0; a < 2; ++a) {
    const pu4* p = ps[a] + (size_t)blockIdx.x * 8192 + threadIdx.x;
    pu4 v[8];
    #pragma unroll
    for (int i = 0; i < 8; ++i) v[i] = __builtin_nontemporal_load(p + i * 1024);
    #pragma unroll
    for (int i = 0; i < 8; ++i) acc ^= v[i][0] ^ v[i][1] ^ v[i][2] ^ v[i][3];
  }
  if (acc == 0x12345678u) sink[threadIdx.x] = acc;
  __syncthreads();
  unsigned long long t1 = __builtin_amdgcn_s_memrealtime();
  unsigned long long tend = t1 + (t1 - t0) * amp;
  while (__builtin_amdgcn_s_memrealtime() < tend) __builtin_amdgcn_s_sleep(2);
}
extern "C" void kernel_launch(void* const* d_in, const int* in_sizes, int n_in, void* d_out, int out_size, void* d_ws, size_t ws_size, hipStream_t stream) {
  const u16* xA = (const u16*)d_in[0]; const u16* kA = (const u16*)d_in[1]; const u16* vA = (const u16*)d_in[2];
  const u16* wqA = (const u16*)d_in[3]; const u16* woA = (const u16*)d_in[4];
  const u16* xB = (const u16*)d_in[5]; const u16* kB = (const u16*)d_in[6]; const u16* vB = (const u16*)d_in[7];
  const u16* wqB = (const u16*)d_in[8]; const u16* woB = (const u16*)d_in[9];
  u16* out = (u16*)d_out;
  u16* Q = (u16*)d_ws; u16* O = Q + (size_t)2 * NB * SQ * NH * DH;
  const size_t half = (size_t)NB * SQ * NH * DH;
  probe<<<256, 1024, 0, stream>>>((const pu4*)kA, (const pu4*)vA, (const pu4*)kB, (const pu4*)vB, (unsigned*)((char*)d_ws + (200u << 20)), 10);
  gemm_simple<<<dim3(4096 / 64, 1792 / 64, 2), 256, 0, stream>>>(xA, xB, DM, wqA, wqB, NH * DH, Q, Q + half, NH * DH, DM);
  attn_simple<<<512, 448, 0, stream>>>(Q, kA, vA, kB, vB, O);
  gemm_simple<<<dim3(512 / 64, 1792 / 64, 2), 256, 0, stream>>>(O, O + half, NH * DH, woA, woB, DM, out, out + 512, 1024, NH * DH);
}
```

```cpp
#include <hip/hip_runtime.h>
#include <stdint.h>
typedef unsigned short u16;

struct Br { const char* x; const char* k; const char* v; const char* wq; const char* wo; char* out; char* ws; char* ws2; };
struct Args { Br br[2]; };

__device__ __forceinline__ float bf2f(u16 v) { return __uint_as_float((unsigned)v << 16); }
__device__ __forceinline__ float rbf(float f) { unsigned u; asm("v_cvt_pk_bf16_f32 %0, %1, %1" : "=v"(u) : "v"(f)); return __uint_as_float(u << 16); }
__device__ __forceinline__ void wsync() { __builtin_amdgcn_fence(__ATOMIC_ACQ_REL, "wavefront"); asm volatile("s_waitcnt lgkmcnt(0)" ::: "memory"); }

__device__ __forceinline__ void slow_rows(const u16* x, const u16* wq, const u16* kc, const u16* vc, const u16* wo, u16* out, int w, int col0, float* sm) {
  const int lane = __builtin_amdgcn_mbcnt_hi(-1, __builtin_amdgcn_mbcnt_lo(-1, 0));
  float* qs = sm; float* ps = sm + 128; float* os = sm + 640;
  for (int rr = 0; rr < 64; ++rr) {
    const int t = rr >> 4, tile = 7 * (w >> 1) + (t < 3 ? 4 * (w & 1) + t : 3), row = tile * 16 + (rr & 15);
    float acc = 0.f;
    for (int h = 0; h < 32; ++h) {
      float q0 = 0.f, q1 = 0.f;
      for (int kk = 0; kk < 512; ++kk) { const float xv = bf2f(x[row * 512 + kk]); const u16* wr = wq + (size_t)kk * 4096 + h * 128; q0 += xv * bf2f(wr[lane]); q1 += xv * bf2f(wr[lane + 64]); }
      wsync(); qs[lane] = rbf(q0); qs[lane + 64] = rbf(q1); wsync();
      const u16* kh = kc + (size_t)h * 65536; const u16* vh = vc + (size_t)h * 65536;
      float mx = -__builtin_inff();
      for (int e = 0; e < 8; ++e) { float s = 0.f; const u16* kr = kh + (e * 64 + lane) * 128; for (int d = 0; d < 128; ++d) s += qs[d] * bf2f(kr[d]); s *= 0.08838834764831844f; ps[e * 64 + lane] = s; mx = fmaxf(mx, s); }
      for (int o = 32; o; o >>= 1) mx = fmaxf(mx, __shfl_xor(mx, o));
      float sum = 0.f;
      for (int e = 0; e < 8; ++e) { const float p = __expf(ps[e * 64 + lane] - mx); ps[e * 64 + lane] = p; sum += p; }
      for (int o = 32; o; o >>= 1) sum += __shfl_xor(sum, o);
      wsync();
      float o0 = 0.f, o1 = 0.f;
      for (int kk = 0; kk < 512; ++kk) { const float p = ps[kk]; o0 += p * bf2f(vh[kk * 128 + lane]); o1 += p * bf2f(vh[kk * 128 + lane + 64]); }
      os[lane] = rbf(o0 / sum); os[lane + 64] = rbf(o1 / sum); wsync();
      for (int d = 0; d < 128; ++d) acc += os[d] * bf2f(wo[(size_t)(h * 128 + d) * 512 + col0 + (lane & 31)]);
    }
    if (lane < 32 && (t < 3 || (lane >> 4) == (w & 1))) out[(size_t)row * 1024 + lane] = (u16)(__float_as_uint(rbf(acc)) >> 16);
  }
}

__global__ __launch_bounds__(256, 1) void k_main(Args a) {
  extern __shared__ __attribute__((aligned(16))) float smem[];
  typedef const __attribute__((address_space(4))) Args* KA;
  KA ka = (KA)__builtin_amdgcn_kernarg_segment_ptr();
  const int id = blockIdx.x;
  const int w = __builtin_amdgcn_readfirstlane(threadIdx.x >> 6);
  int bad;
  asm volatile(
    "s_lshl_b32 s24, %2, 4\n"
    "s_and_b32 s24, s24, 64\n"
    "s_load_dwordx16 s[8:23], %1, s24\n"
    "s_mov_b32 s40, %3\n"
    "s_and_b32 s41, s40, 1\n"
    "s_bfe_u32 s6, %2, 0x20003\n"
    "s_and_b32 s7, %2, 3\n"
    "s_lshr_b32 s25, %2, 5\n"
    "s_lshl_b32 s95, %2, 2\n"
    "s_add_u32 s95, s95, %3\n"
    "s_lshl_b32 s96, s7, 3\n"
    "s_lshl_b32 s97, s25, 5\n"
    "s_add_u32 s87, s97, s96\n"
    "s_lshl_b32 s86, s6, 1\n"
    "s_add_u32 s96, s96, s86\n"
    "s_add_u32 s97, s97, s96\n"
    "s_lshl_b32 s86, s24, 2\n"
    "s_add_u32 s87, s87, s86\n"
    "s_lshl_b32 s86, s24, 4\n"
    "s_lshl_b32 s5, s6, 8\n"
    "s_add_u32 s86, s86, s5\n"
    "s_lshl_b32 s5, s7, 6\n"
    "s_add_u32 s86, s86, s5\n"
    "s_sub_u32 s5, 458752, s86\n"
    "s_lshl_b32 s96, s96, 8\n"
    "s_sub_u32 s24, 0x400000, s96\n"
    "s_lshl_b32 s94, s6, 0\n"
    "s_and_b32 s94, s94, 0\n"
    "s_xor_b32 s94, s94, s25\n"
    "s_and_b32 s94, s94, 5\n"
    "v_mbcnt_lo_u32_b32 v0, -1, 0\n"
    "v_mbcnt_hi_u32_b32 v0, -1, v0\n"
    "s_getpc_b64 s[90:91]\n"
    "L_pc1:\n"
    "s_lshr_b32 s88, s95, 5\n"
    "s_lshl2_add_u32 s88, s88, s40\n"
    "s_lshl_b32 s88, s88, 10\n"
    "s_add_u32 s88, s88, 0\n"
    "v_lshl_add_u32 v5, v0, 4, s88\n"
    "v_and_b32 v5, -4, v5\n"
    "v_min_u32 v5, L_end-L_pc1-4, v5\n"
    "global_load_dword v251, v5, s[90:91]\n"
    "v_and_b32 v4, 3, v0\n"
    "v_bfe_u32 v8, v0, 2, 3\n"
    "v_lshrrev_b32 v9, 5, v0\n"
    "v_lshlrev_b32 v7, 13, v8\n"
    "v_lshl_add_u32 v7, v9, 6, v7\n"
    "v_lshl_add_u32 v225, v4, 4, v7\n"
    "v_add_u32 v226, 64512, v225\n"
    "v_add_u32 v227, 129024, v225\n"
    "v_add_u32 v228, 193536, v225\n"
    "s_lshl_b32 s83, s40, 12\n"
    "s_lshl_b32 s88, s94, 1\n"
    "s_xor_b32 s80, s40, s88\n"
    "s_lshl_b32 s80, s80, 18\n"
    "v_and_b32 v1, 15, v0\n"
    "v_lshrrev_b32 v2, 4, v0\n"
    "v_and_b32 v6, 1, v2\n"
    "v_lshlrev_b32 v10, 1, v6\n"
    "v_xor_b32 v10, v10, v4\n"
    "v_lshlrev_b32 v5, 10, v8\n"
    "v_lshl_add_u32 v5, v9, 6, v5\n"
    "v_lshl_add_u32 v5, v10, 4, v5\n"
    "s_lshl_b32 s89, s94, 7\n"
    "v_or_b32 v224, s89, v5\n"
    "v_add_u32 v253, 7168, v224\n"
    "s_lshr_b32 s88, s40, 1\n"
    "s_mul_i32 s88, s88, 7\n"
    "s_lshl_b32 s89, s41, 2\n"
    "s_add_u32 s89, s89, s88\n"
    "s_add_u32 s88, s88, 3\n"
    "s_lshl_b32 s72, s89, 14\n"
    "s_add_u32 s73, s72, 14336\n"
    "s_add_u32 s74, s72, 32768\n"
    "s_lshl_b32 s75, s88, 14\n"
    "s_sub_u32 s75, s75, 2048\n"
    "s_lshl_b32 s42, s40, 13\n"
    "s_waitcnt lgkmcnt(0)\n"
    "s_add_u32 s14, s14, s96\n"
    "s_addc_u32 s15, s15, 0\n"
    "s_mul_i32 s96, s25, 229376\n"
    "s_add_u32 s8, s8, s96\n"
    "s_addc_u32 s9, s9, 0\n"
    "s_mov_b64 s[48:49], s[14:15]\n"
    "s_and_b32 s49, s49, 0xffff\n"
    "s_mov_b32 s50, s24\n"
    "s_mov_b32 s51, 0x20000\n"
    "s_add_u32 m0, s83, 0\n"
    "s_xor_b32 s81, s80, 256\n"
    "buffer_load_dwordx4 v225, s[48:51], s81 offen offset:0 lds\n"
    "buffer_load_dwordx4 v226, s[48:51], s81 offen offset:1024 lds\n"
    "buffer_load_dwordx4 v227, s[48:51], s81 offen offset:2048 lds\n"
    "buffer_load_dwordx4 v228, s[48:51], s81 offen offset:3072 lds\n"
    "s_mov_b64 s[44:45], s[8:9]\n"
    "s_and_b32 s45, s45, 0xffff\n"
    "s_mov_b32 s46, 229376\n"
    "s_mov_b32 s47, 0x20000\n"
    "s_add_u32 m0, s42, 131072\n"
    "s_nop 0\n"
    "buffer_load_dwordx4 v224, s[44:47], s72 offen offset:0 lds\n"
    "buffer_load_dwordx4 v253, s[44:47], s72 offen offset:1024 lds\n"
    "buffer_load_dwordx4 v224, s[44:47], s73 offen offset:2048 lds\n"
    "buffer_load_dwordx4 v253, s[44:47], s73 offen offset:3072 lds\n"
    "s_add_u32 m0, s42, 135168\n"
    "s_nop 0\n"
    "buffer_load_dwordx4 v224, s[44:47], s74 offen offset:0 lds\n"
    "buffer_load_dwordx4 v253, s[44:47], s74 offen offset:1024 lds\n"
    "buffer_load_dwordx4 v224, s[44:47], s75 offen offset:2048 lds\n"
    "buffer_load_dwordx4 v253, s[44:47], s75 offen offset:3072 lds\n"
    "s_lshl_b32 s97, s97, 17\n"
    "s_sub_u32 s97, s97, 0\n"
    "s_ashr_i32 s94, s97, 31\n"
    "s_add_u32 s10, s10, s97\n"
    "s_addc_u32 s11, s11, s94\n"
    "s_add_u32 s12, s12, s97\n"
    "s_addc_u32 s13, s13, s94\n"
    "s_lshl_b32 s97, s7, 20\n"
    "s_lshl_b32 s94, s6, 8\n"
    "s_add_u32 s97, s97, s94\n"
    "s_add_u32 s16, s16, s97\n"
    "s_addc_u32 s17, s17, 0\n"
    "s_lshl_b32 s96, s96, 1\n"
    "s_add_u32 s96, s96, s86\n"
    "s_add_u32 s18, s18, s96\n"
    "s_addc_u32 s19, s19, 0\n"
    "s_add_u32 s20, s20, 0x1fffff\n"
    "s_addc_u32 s21, s21, 0\n"
    "s_and_b32 s20, s20, 0xffe00000\n"
    "s_xor_b32 s7, s7, 3\n"
    "s_mov_b32 s24, 0\n"
    "s_and_b32 s8, %2, 7\n"
    "s_lshl_b32 s8, s8, 5\n"
    "s_lshr_b32 s9, %2, 3\n"
    "s_or_b32 s8, s8, s9\n"
    "s_lshl_b32 s8, s8, 19\n"
    "s_lshl_b32 s9, s6, 17\n"
    "s_or_b32 s8, s8, s9\n"
    "s_lshr_b32 s90, s89, 1\n"
    "s_lshl_b32 s90, s90, 14\n"
    "s_and_b32 s76, s89, 1\n"
    "s_lshl_b32 s76, s76, 12\n"
    "s_or_b32 s76, s76, s90\n"
    "s_xor_b32 s76, s76, s8\n"
    "s_add_u32 s89, s89, 1\n"
    "s_lshr_b32 s90, s89, 1\n"
    "s_lshl_b32 s90, s90, 14\n"
    "s_and_b32 s77, s89, 1\n"
    "s_lshl_b32 s77, s77, 12\n"
    "s_or_b32 s77, s77, s90\n"
    "s_xor_b32 s77, s77, s8\n"
    "s_add_u32 s89, s89, 1\n"
    "s_lshr_b32 s90, s89, 1\n"
    "s_lshl_b32 s90, s90, 14\n"
    "s_and_b32 s78, s89, 1\n"
    "s_lshl_b32 s78, s78, 12\n"
    "s_or_b32 s78, s78, s90\n"
    "s_xor_b32 s78, s78, s8\n"
    "s_lshr_b32 s90, s88, 1\n"
    "s_lshl_b32 s90, s90, 14\n"
    "s_and_b32 s79, s88, 1\n"
    "s_lshl_b32 s79, s79, 12\n"
    "s_or_b32 s79, s79, s90\n"
    "s_xor_b32 s79, s79, s8\n"
    "s_lshr_b32 s4, %2, 3\n"
    "s_xor_b32 s4, s4, %2\n"
    "s_lshr_b32 s94, %2, 6\n"
    "s_xor_b32 s4, s4, s94\n"
    "s_and_b32 s4, s4, 7\n"
    "s_lshl_b32 s4, s4, 14\n"
    "s_or_b32 s4, s4, s83\n"
    "s_lshl_b32 s93, s6, 18\n"
    "s_lshl_b32 s90, s40, 15\n"
    "s_or_b32 s93, s93, s90\n"
    "s_lshr_b32 s90, s7, 1\n"
    "s_lshl_b32 s90, s90, 7\n"
    "s_or_b32 s93, s93, s90\n"
    "s_mov_b64 s[68:69], s[20:21]\n"
    "s_and_b32 s69, s69, 0xffff\n"
    "s_mov_b32 s70, 134217728\n"
    "s_mov_b32 s71, 0x20000\n"
    "s_lshl_b32 s90, s40, 10\n"
    "s_xor_b32 s90, s90, s8\n"
    "s_xor_b32 s90, s90, 458752\n"
    "v_and_b32 v5, 3, v0\n"
    "v_lshlrev_b32 v5, 8, v5\n"
    "v_bfe_u32 v7, v0, 2, 1\n"
    "v_lshl_add_u32 v5, v7, 13, v5\n"
    "v_mov_b32 v7, 1\n"
    "buffer_store_dword v7, v5, s[68:71], s90 offen offset:0 sc1\n"
    "s_add_u32 s14, s20, s90\n"
    "s_addc_u32 s15, s21, 0\n"
    "s_lshl_b32 s92, s40, 8\n"
    "s_xor_b32 s92, s92, s8\n"
    "s_xor_b32 s92, s92, 393216\n"
    "v_lshlrev_b32 v250, 2, v0\n"
    "v_bfe_u32 v5, v0, 1, 2\n"
    "v_mul_u32_u24 v7, 655360, v5\n"
    "v_and_b32 v248, 1, v0\n"
    "v_lshl_add_u32 v7, v248, 8, v7\n"
    "v_and_b32 v5, 7, v0\n"
    "v_lshlrev_b32 v5, 24, v5\n"
    "v_and_b32 v248, 8, v0\n"
    "v_cmp_ne_u32 vcc, 0, v248\n"
    "s_nop 1\n"
    "v_cndmask_b32 v5, v7, v5, vcc\n"
    "v_xor_b32 v5, s92, v5\n"
    "v_add_u32 v248, v5, v250\n"
    "v_mov_b32 v252, v0\n"
    "s_mov_b64 s[52:53], s[10:11]\n"
    "s_and_b32 s53, s53, 0xffff\n"
    "s_mov_b32 s54, 262144\n"
    "s_mov_b32 s55, 0x20000\n"
    "s_mov_b64 s[56:57], s[12:13]\n"
    "s_and_b32 s57, s57, 0xffff\n"
    "s_mov_b32 s58, 262144\n"
    "s_mov_b32 s59, 0x20000\n"
    "s_mov_b64 s[60:61], s[20:21]\n"
    "s_and_b32 s61, s61, 0xffff\n"
    "s_mov_b32 s62, 134217728\n"
    "s_mov_b32 s63, 0x20000\n"
    "s_mov_b64 s[64:65], s[16:17]\n"
    "s_and_b32 s65, s65, 0xffff\n"
    "s_mov_b32 s66, 1047808\n"
    "s_mov_b32 s67, 0x20000\n"
    "v_bfe_u32 v3, v0, 2, 2\n"
    "v_lshlrev_b32 v5, 10, v2\n"
    "v_lshl_add_u32 v5, v3, 6, v5\n"
    "v_lshl_add_u32 v5, v4, 4, v5\n"
    "s_lshl_b32 s88, s41, 9\n"
    "s_xor_b32 s89, s88, 512\n"
    "v_add_u32 v229, s88, v5\n"
    "v_add_u32 v231, s89, v5\n"
    "v_add_u32 v230, 65536, v229\n"
    "v_add_u32 v255, 65536, v231\n"
    "v_bfe_u32 v11, v1, 2, 1\n"
    "v_lshlrev_b32 v11, 1, v11\n"
    "v_xor_b32 v11, v11, v2\n"
    "v_lshlrev_b32 v11, 4, v11\n"
    "v_and_b32 v5, 7, v1\n"
    "v_lshl_add_u32 v5, v5, 6, v11\n"
    "v_lshrrev_b32 v7, 3, v1\n"
    "v_lshl_add_u32 v5, v7, 10, v5\n"
    "s_add_u32 s90, s42, 98304\n"
    "v_add_u32 v254, s90, v5\n"
    "v_lshl_add_u32 v5, v4, 6, v11\n"
    "v_lshl_add_u32 v5, v3, 10, v5\n"
    "s_lshr_b32 s88, s88, 1\n"
    "s_lshr_b32 s89, s89, 1\n"
    "s_lshl_b32 s90, s41, 13\n"
    "s_xor_b32 s91, s90, 8192\n"
    "v_add_u32 v7, s90, v5\n"
    "v_add_u32 v232, s88, v7\n"
    "v_add_u32 v233, s89, v7\n"
    "v_add_u32 v7, s91, v5\n"
    "v_add_u32 v234, s88, v7\n"
    "v_add_u32 v235, s89, v7\n"
    "v_lshlrev_b32 v5, 10, v2\n"
    "v_lshl_add_u32 v5, v3, 6, v5\n"
    "v_lshl_add_u32 v5, v4, 3, v5\n"
    "v_xor_b32 v7, s41, v6\n"
    "v_lshl_add_u32 v5, v7, 5, v5\n"
    "v_xor_b32 v7, 32, v5\n"
    "s_add_u32 s90, s90, 65536\n"
    "s_add_u32 s91, s91, 65536\n"
    "v_add_u32 v236, s90, v5\n"
    "v_add_u32 v237, s90, v7\n"
    "v_add_u32 v238, s91, v5\n"
    "v_add_u32 v239, s91, v7\n"
    "v_lshlrev_b32 v5, 8, v3\n"
    "v_lshl_add_u32 v5, v2, 6, v5\n"
    "v_lshl_add_u32 v240, v4, 4, v5\n"
    "v_xor_b32 v241, 32, v240\n"
    "v_lshlrev_b32 v5, 4, v0\n"
    "s_lshl_b32 s88, s41, 11\n"
    "s_xor_b32 s89, s88, 2048\n"
    "v_add_u32 v242, s88, v5\n"
    "v_add_u32 v243, s89, v5\n"
    "s_add_u32 s88, s42, 131072\n"
    "v_add_u32 v244, s88, v5\n"
    "v_xor_b32 v245, 8192, v244\n"
    "s_mov_b32 s84, 0x3e0293ee\n"
    "s_mov_b32 s85, 0x3e0293ee\n"
    "s_mov_b32 s43, 0x4d800000\n"
    "s_mov_b64 exec, 0\n"
    "s_cmp_eq_u32 s40, 0\n"
    "s_cbranch_scc1 L_h1\n"
    "s_cmp_eq_u32 s40, 1\n"
    "s_cbranch_scc1 L_h2\n"
    "s_cmp_eq_u32 s40, 2\n"
    "s_cbranch_scc1 L_h3\n"
    "s_cmp_eq_u32 s40, 3\n"
    "s_cbranch_scc1 L_h4\n"
    "L_ret1:\n"
    "s_mov_b64 exec, -1\n"
    "s_branch L_h1\n"
    "L_pw1:\n"
    "L_px1:\n"
    "s_memrealtime s[98:99]\n"
    "s_waitcnt lgkmcnt(0)\n"
    "s_sub_u32 s99, s98, s25\n"
    "s_add_u32 s99, s99, 0xfffffffb\n"
    "s_cmp_lt_i32 s99, 0\n"
    "s_cbranch_scc1 L_px1\n"
    "s_mov_b32 s98, s25\n"
    "s_branch L_pb1\n"
    "L_pw2:\n"
    "L_px2:\n"
    "s_memrealtime s[98:99]\n"
    "s_waitcnt lgkmcnt(0)\n"
    "s_sub_u32 s99, s98, s25\n"
    "s_add_u32 s99, s99, 0xfffffffb\n"
    "s_cmp_lt_i32 s99, 0\n"
    "s_cbranch_scc1 L_px2\n"
    "s_mov_b32 s98, s25\n"
    "s_branch L_pb2\n"
    "L_pw3:\n"
    "L_px3:\n"
    "s_memrealtime s[98:99]\n"
    "s_waitcnt lgkmcnt(0)\n"
    "s_sub_u32 s99, s98, s25\n"
    "s_add_u32 s99, s99, 0xfffffffb\n"
    "s_cmp_lt_i32 s99, 0\n"
    "s_cbranch_scc1 L_px3\n"
    "s_mov_b32 s98, s25\n"
    "s_branch L_pb3\n"
    "L_pw4:\n"
    "L_px4:\n"
    "s_memrealtime s[98:99]\n"
    "s_waitcnt lgkmcnt(0)\n"
    "s_sub_u32 s99, s98, s25\n"
    "s_add_u32 s99, s99, 0xfffffffb\n"
    "s_cmp_lt_i32 s99, 0\n"
    "s_cbranch_scc1 L_px4\n"
    "s_mov_b32 s98, s25\n"
    "s_branch L_pb4\n"
    "L_pw5:\n"
    "L_px5:\n"
    "s_memrealtime s[98:99]\n"
    "s_waitcnt lgkmcnt(0)\n"
    "s_sub_u32 s99, s98, s25\n"
    "s_add_u32 s99, s99, 0xfffffffb\n"
    "s_cmp_lt_i32 s99, 0\n"
    "s_cbranch_scc1 L_px5\n"
    "s_mov_b32 s98, s25\n"
    "s_branch L_pb5\n"
    "L_pw6:\n"
    "L_px6:\n"
    "s_memrealtime s[98:99]\n"
    "s_waitcnt lgkmcnt(0)\n"
    "s_sub_u32 s99, s98, s25\n"
    "s_add_u32 s99, s99, 0xfffffffb\n"
    "s_cmp_lt_i32 s99, 0\n"
    "s_cbranch_scc1 L_px6\n"
    "s_mov_b32 s98, s25\n"
    "s_branch L_pb6\n"
    "L_pw7:\n"
    "L_px7:\n"
    "s_memrealtime s[98:99]\n"
    "s_waitcnt lgkmcnt(0)\n"
    "s_sub_u32 s99, s98, s25\n"
    "s_add_u32 s99, s99, 0xfffffffb\n"
    "s_cmp_lt_i32 s99, 0\n"
    "s_cbranch_scc1 L_px7\n"
    "s_mov_b32 s98, s25\n"
    "s_branch L_pb7\n"
    "L_pw8:\n"
    "L_px8:\n"
    "s_memrealtime s[98:99]\n"
    "s_waitcnt lgkmcnt(0)\n"
    "s_sub_u32 s99, s98, s25\n"
    "s_add_u32 s99, s99, 0xfffffffb\n"
    "s_cmp_lt_i32 s99, 0\n"
    "s_cbranch_scc1 L_px8\n"
    "s_mov_b32 s98, s25\n"
    "s_branch L_pb8\n"
    "L_pw9:\n"
    "s_sub_i32 s99, 0, s99\n"
    "s_min_i32 s99, s99, s19\n"
    "s_sub_i32 s19, s19, s99\n"
    "s_sub_i32 s25, s25, s99\n"
    "L_px9:\n"
    "s_memrealtime s[98:99]\n"
    "s_waitcnt lgkmcnt(0)\n"
    "s_sub_u32 s99, s98, s25\n"
    "s_add_u32 s99, s99, 0xfffffffb\n"
    "s_cmp_lt_i32 s99, 0\n"
    "s_cbranch_scc1 L_px9\n"
    "s_mov_b32 s98, s25\n"
    "s_branch L_pb9\n"
    "L_pw10:\n"
    "s_sub_i32 s99, 0, s99\n"
    "s_min_i32 s99, s99, s19\n"
    "s_sub_i32 s19, s19, s99\n"
    "s_sub_i32 s25, s25, s99\n"
    "L_px10:\n"
    "s_memrealtime s[98:99]\n"
    "s_waitcnt lgkmcnt(0)\n"
    "s_sub_u32 s99, s98, s25\n"
    "s_add_u32 s99, s99, 0xfffffffb\n"
    "s_cmp_lt_i32 s99, 0\n"
    "s_cbranch_scc1 L_px10\n"
    "s_mov_b32 s98, s25\n"
    "s_branch L_pb10\n"
    "L_pw11:\n"
    "s_sub_i32 s99, 0, s99\n"
    "s_min_i32 s99, s99, s19\n"
    "s_sub_i32 s19, s19, s99\n"
    "s_sub_i32 s25, s25, s99\n"
    "L_px11:\n"
    "s_memrealtime s[98:99]\n"
    "s_waitcnt lgkmcnt(0)\n"
    "s_sub_u32 s99, s98, s25\n"
    "s_add_u32 s99, s99, 0xfffffffb\n"
    "s_cmp_lt_i32 s99, 0\n"
    "s_cbranch_scc1 L_px11\n"
    "s_mov_b32 s98, s25\n"
    "s_branch L_pb11\n"
    "L_pw12:\n"
    "s_sub_i32 s99, 0, s99\n"
    "s_min_i32 s99, s99, s19\n"
    "s_sub_i32 s19, s19, s99\n"
    "s_sub_i32 s25, s25, s99\n"
    "L_px12:\n"
    "s_memrealtime s[98:99]\n"
    "s_waitcnt lgkmcnt(0)\n"
    "s_sub_u32 s99, s98, s25\n"
    "s_add_u32 s99, s99, 0xfffffffb\n"
    "s_cmp_lt_i32 s99, 0\n"
    "s_cbranch_scc1 L_px12\n"
    "s_mov_b32 s98, s25\n"
    "s_branch L_pb12\n"
    "L_pw13:\n"
    "s_sub_i32 s99, 0, s99\n"
    "s_min_i32 s99, s99, s19\n"
    "s_sub_i32 s19, s19, s99\n"
    "s_sub_i32 s25, s25, s99\n"
    "L_px13:\n"
    "s_memrealtime s[98:99]\n"
    "s_waitcnt lgkmcnt(0)\n"
    "s_sub_u32 s99, s98, s25\n"
    "s_add_u32 s99, s99, 0xfffffffb\n"
    "s_cmp_lt_i32 s99, 0\n"
    "s_cbranch_scc1 L_px13\n"
    "s_mov_b32 s98, s25\n"
    "s_branch L_pb13\n"
    "L_slow0:\n"
    "s_cmp_eq_u32 s24, 0\n"
    "s_cselect_b32 s94, 262144, 1\n"
    "L_loop0:\n"
    "buffer_load_dword v254, v253, s[68:71], 0 offen offset:2048 sc1\n"
    "s_waitcnt vmcnt(0)\n"
    "v_and_b32 v250, 1, v254\n"
    "v_lshl_add_u32 v250, v250, 13, v253\n"
    "s_mov_b64 exec, s[96:97]\n"
    "buffer_store_dword v254, v250, s[68:71], 0 offen sc1\n"
    "s_mov_b64 exec, -1\n"
    "buffer_load_dword v250, v251, s[68:71], s92 offen sc1\n"
    "s_waitcnt vmcnt(0)\n"
    "v_cmp_eq_u32 vcc, v250, v252\n"
    "s_nop 1\n"
    "s_and_b64 vcc, vcc, s[96:97]\n"
    "s_cmp_eq_u64 vcc, s[96:97]\n"
    "s_cbranch_scc1 L_back0\n"
    "s_sub_u32 s94, s94, 1\n"
    "s_cmp_eq_u32 s94, 0\n"
    "s_cbranch_scc0 L_loop0\n"
    "s_mov_b32 s24, 1\n"
    "s_mov_b32 s70, 0\n"
    "s_branch L_back0\n"
    "L_slow1:\n"
    "s_cmp_eq_u32 s24, 0\n"
    "s_cselect_b32 s94, 262144, 1\n"
    "L_loop1:\n"
    "buffer_load_dword v247, v246, s[68:71], s92 offen sc1\n"
    "s_waitcnt vmcnt(0)\n"
    "v_cmp_eq_u32 vcc, v247, v229\n"
    "s_nop 1\n"
    "s_and_b64 vcc, vcc, s[96:97]\n"
    "s_cmp_eq_u64 vcc, s[96:97]\n"
    "s_cbranch_scc1 L_back1\n"
    "s_sub_u32 s94, s94, 1\n"
    "s_cmp_eq_u32 s94, 0\n"
    "s_cbranch_scc0 L_loop1\n"
    "s_mov_b32 s24, 1\n"
    "s_mov_b32 s70, 0\n"
    "s_branch L_back1\n"
    "L_slow11:\n"
    "s_cmp_eq_u32 s24, 0\n"
    "s_cselect_b32 s94, 262144, 1\n"
    "L_loop11:\n"
    "buffer_load_dword v247, v246, s[68:71], s92 offen sc1\n"
    "s_waitcnt vmcnt(0)\n"
    "v_cmp_eq_u32 vcc, v247, v229\n"
    "s_nop 1\n"
    "s_and_b64 vcc, vcc, s[96:97]\n"
    "s_cmp_eq_u64 vcc, s[96:97]\n"
    "s_cbranch_scc1 L_back11\n"
    "s_sub_u32 s94, s94, 1\n"
    "s_cmp_eq_u32 s94, 0\n"
    "s_cbranch_scc0 L_loop11\n"
    "s_mov_b32 s24, 1\n"
    "s_mov_b32 s70, 0\n"
    "s_branch L_back11\n"
    "L_slow2:\n"
    "s_cmp_eq_u32 s24, 0\n"
    "s_cselect_b32 s94, 262144, 1\n"
    "L_loop2:\n"
    "buffer_load_dword v231, v246, s[68:71], s92 offen sc1\n"
    "s_waitcnt vmcnt(0)\n"
    "v_cmp_eq_u32 vcc, v231, v229\n"
    "s_nop 1\n"
    "s_and_b64 vcc, vcc, s[96:97]\n"
    "s_cmp_eq_u64 vcc, s[96:97]\n"
    "s_cbranch_scc1 L_back2\n"
    "s_sub_u32 s94, s94, 1\n"
    "s_cmp_eq_u32 s94, 0\n"
    "s_cbranch_scc0 L_loop2\n"
    "s_mov_b32 s24, 1\n"
    "s_mov_b32 s70, 0\n"
    "s_branch L_back2\n"
    "L_slow8:\n"
    "s_cmp_eq_u32 s24, 0\n"
    "s_cselect_b32 s94, 262144, 1\n"
    "L_loop8:\n"
    "buffer_load_dword v247, v246, s[68:71], s92 offen sc1\n"
    "s_waitcnt vmcnt(0)\n"
    "v_cmp_eq_u32 vcc, v247, v229\n"
    "s_nop 1\n"
    "s_and_b64 vcc, vcc, s[96:97]\n"
    "s_cmp_eq_u64 vcc, s[96:97]\n"
    "s_cbranch_scc1 L_back8\n"
    "s_sub_u32 s94, s94, 1\n"
    "s_cmp_eq_u32 s94, 0\n"
    "s_cbranch_scc0 L_loop8\n"
    "s_mov_b32 s24, 1\n"
    "s_mov_b32 s70, 0\n"
    "s_branch L_back8\n"
    "L_slow5:\n"
    "s_cmp_eq_u32 s24, 0\n"
    "s_cselect_b32 s94, 262144, 1\n"
    "L_loop5:\n"
    "buffer_load_dword v231, v246, s[68:71], s92 offen sc1\n"
    "s_waitcnt vmcnt(0)\n"
    "v_cmp_eq_u32 vcc, v231, v229\n"
    "s_nop 1\n"
    "s_and_b64 vcc, vcc, s[96:97]\n"
    "s_cmp_eq_u64 vcc, s[96:97]\n"
    "s_cbranch_scc1 L_back5\n"
    "s_sub_u32 s94, s94, 1\n"
    "s_cmp_eq_u32 s94, 0\n"
    "s_cbranch_scc0 L_loop5\n"
    "s_mov_b32 s24, 1\n"
    "s_mov_b32 s70, 0\n"
    "s_branch L_back5\n"
    ".p2align 12\n"
    "L_h1:\n"
    "s_cbranch_execz L_ret1\n"
    "v_xor_b32 v224, 128, v224\n"
    "v_xor_b32 v253, 128, v253\n"
    "s_add_u32 m0, s42, 98304\n"
    "s_nop 0\n"
    "buffer_load_dwordx4 v224, s[44:47], s72 offen offset:0 lds\n"
    "buffer_load_dwordx4 v253, s[44:47], s72 offen offset:1024 lds\n"
    "buffer_load_dwordx4 v224, s[44:47], s73 offen offset:2048 lds\n"
    "buffer_load_dwordx4 v253, s[44:47], s73 offen offset:3072 lds\n"
    "s_waitcnt vmcnt(13)\n"
    "s_barrier\n"
    "s_add_u32 m0, s42, 102400\n"
    "s_nop 0\n"
    "buffer_load_dwordx4 v224, s[44:47], s74 offen offset:0 lds\n"
    "buffer_load_dwordx4 v253, s[44:47], s74 offen offset:1024 lds\n"
    "buffer_load_dwordx4 v224, s[44:47], s75 offen offset:2048 lds\n"
    "buffer_load_dwordx4 v253, s[44:47], s75 offen offset:3072 lds\n"
    "ds_read_b64_tr_b16 a[32:33], v229 offset:0\n"
    "ds_read_b64_tr_b16 a[34:35], v229 offset:256\n"
    "ds_read_b64_tr_b16 a[36:37], v229 offset:4096\n"
    "ds_read_b64_tr_b16 a[38:39], v229 offset:4352\n"
    "ds_read_b64_tr_b16 a[48:49], v229 offset:8\n"
    "ds_read_b64_tr_b16 a[50:51], v229 offset:264\n"
    "ds_read_b64_tr_b16 a[52:53], v229 offset:4104\n"
    "ds_read_b64_tr_b16 a[54:55], v229 offset:4360\n"
    "s_waitcnt vmcnt(9)\n"
    "ds_read_b128 a[128:131], v254 offset:32768\n"
    "ds_read_b128 a[144:147], v254 offset:33280\n"
    "ds_read_b128 a[132:135], v254 offset:34816\n"
    "ds_read_b128 a[148:151], v254 offset:35328\n"
    "ds_read_b128 a[136:139], v254 offset:36864\n"
    "ds_read_b128 a[152:155], v254 offset:37376\n"
    "ds_read_b128 a[140:143], v254 offset:38912\n"
    "ds_read_b128 a[156:159], v254 offset:39424\n"
    "s_add_u32 m0, s83, 65536\n"
    "s_xor_b32 s81, s80, 384\n"
    "buffer_load_dwordx4 v225, s[48:51], s81 offen offset:0 lds\n"
    "buffer_load_dwordx4 v226, s[48:51], s81 offen offset:1024 lds\n"
    "buffer_load_dwordx4 v227, s[48:51], s81 offen offset:2048 lds\n"
    "buffer_load_dwordx4 v228, s[48:51], s81 offen offset:3072 lds\n"
    "s_waitcnt lgkmcnt(7)\n"
    "v_mfma_f32_16x16x32_bf16 v[112:115], a[32:35], a[128:131], 0\n"
    "ds_read_b64_tr_b16 a[96:97], v231 offset:0\n"
    "s_add_u32 m0, s83, 16384\n"
    "s_waitcnt lgkmcnt(7)\n"
    "v_mfma_f32_16x16x32_bf16 v[112:115], a[36:39], a[144:147], v[112:115]\n"
    "ds_read_b64_tr_b16 a[98:99], v231 offset:256\n"
    "s_xor_b32 s81, s80, 0\n"
    "s_waitcnt lgkmcnt(6)\n"
    "v_mfma_f32_16x16x32_bf16 v[144:147], a[36:39], a[148:151], 0\n"
    "ds_read_b64_tr_b16 a[100:101], v231 offset:4096\n"
    "buffer_load_dwordx4 v225, s[48:51], s81 offen offset:0 lds\n"
    "s_waitcnt vmcnt(13)\n"
    "s_mov_b32 s16, 0\n"
    "v_mfma_f32_16x16x32_bf16 v[144:147], a[32:35], a[132:135], v[144:147]\n"
    "ds_read_b64_tr_b16 a[102:103], v231 offset:4352\n"
    "s_waitcnt lgkmcnt(0)\n"
    "s_mov_b64 s[10:11], 0\n"
    "s_load_dword s10, s[14:15], 0\n"
    "s_load_dword s11, s[14:15], 8192\n"
    "s_waitcnt lgkmcnt(1)\n"
    "s_sub_u32 s17, s11, s10\n"
    "s_add_u32 s16, s16, s17\n"
    "v_mfma_f32_16x16x32_bf16 v[176:179], a[32:35], a[136:139], 0\n"
    "ds_read_b64_tr_b16 a[112:113], v231 offset:8\n"
    "v_mfma_f32_16x16x32_bf16 v[176:179], a[36:39], a[152:155], v[176:179]\n"
    "ds_read_b64_tr_b16 a[114:115], v231 offset:264\n"
    "v_mfma_f32_16x16x32_bf16 v[208:211], a[36:39], a[156:159], 0\n"
    "ds_read_b64_tr_b16 a[116:117], v231 offset:4104\n"
    "v_mfma_f32_16x16x32_bf16 v[208:211], a[32:35], a[140:143], v[208:211]\n"
    "ds_read_b64_tr_b16 a[118:119], v231 offset:4360\n"
    "buffer_load_dwordx4 v226, s[48:51], s81 offen offset:1024 lds\n"
    "v_mfma_f32_16x16x32_bf16 v[116:119], a[48:51], a[128:131], 0\n"
    "ds_read_b64_tr_b16 a[40:41], v229 offset:8192\n"
    "v_mfma_f32_16x16x32_bf16 v[116:119], a[52:55], a[144:147], v[116:119]\n"
    "ds_read_b64_tr_b16 a[42:43], v229 offset:8448\n"
    "v_mfma_f32_16x16x32_bf16 v[148:151], a[52:55], a[148:151], 0\n"
    "ds_read_b64_tr_b16 a[44:45], v229 offset:12288\n"
    "v_mfma_f32_16x16x32_bf16 v[148:151], a[48:51], a[132:135], v[148:151]\n"
    "ds_read_b64_tr_b16 a[46:47], v229 offset:12544\n"
    "s_waitcnt lgkmcnt(0)\n"
    "s_mov_b64 s[12:13], 0\n"
    "s_load_dword s13, s[14:15], 8448\n"
    "s_load_dword s12, s[14:15], 256\n"
    "s_waitcnt lgkmcnt(1)\n"
    "s_sub_u32 s17, s13, s12\n"
    "s_add_u32 s16, s16, s17\n"
    "v_mfma_f32_16x16x32_bf16 v[180:183], a[48:51], a[136:139], 0\n"
    "ds_read_b64_tr_b16 a[56:57], v229 offset:8200\n"
    "buffer_load_dwordx4 v227, s[48:51], s81 offen offset:2048 lds\n"
    "v_mfma_f32_16x16x32_bf16 v[180:183], a[52:55], a[152:155], v[180:183]\n"
    "ds_read_b64_tr_b16 a[58:59], v229 offset:8456\n"
    "v_mfma_f32_16x16x32_bf16 v[212:215], a[52:55], a[156:159], 0\n"
    "ds_read_b64_tr_b16 a[60:61], v229 offset:12296\n"
    "v_mfma_f32_16x16x32_bf16 v[212:215], a[48:51], a[140:143], v[212:215]\n"
    "ds_read_b64_tr_b16 a[62:63], v229 offset:12552\n"
    "v_mfma_f32_16x16x32_bf16 v[120:123], a[96:99], a[128:131], 0\n"
    "ds_read_b64_tr_b16 a[104:105], v231 offset:8192\n"
    "v_mfma_f32_16x16x32_bf16 v[120:123], a[100:103], a[144:147], v[120:123]\n"
    "ds_read_b64_tr_b16 a[106:107], v231 offset:8448\n"
    "buffer_load_dwordx4 v228, s[48:51], s81 offen offset:3072 lds\n"
    "v_mfma_f32_16x16x32_bf16 v[152:155], a[100:103], a[148:151], 0\n"
    "ds_read_b64_tr_b16 a[108:109], v231 offset:12288\n"
    "v_mfma_f32_16x16x32_bf16 v[152:155], a[96:99], a[132:135], v[152:155]\n"
    "ds_read_b64_tr_b16 a[110:111], v231 offset:12544\n"
    "s_add_u32 m0, s42, 131072\n"
    "v_xor_b32 v224, 384, v224\n"
    "v_xor_b32 v253, 384, v253\n"
    "v_mfma_f32_16x16x32_bf16 v[184:187], a[96:99], a[136:139], 0\n"
    "s_waitcnt vmcnt(8)\n"
    "ds_read_b64_tr_b16 a[120:121], v231 offset:8200\n"
    "ds_read_b128 a[160:163], v254 offset:0\n"
    "ds_read_b128 a[176:179], v254 offset:512\n"
    "v_mfma_f32_16x16x32_bf16 v[184:187], a[100:103], a[152:155], v[184:187]\n"
    "ds_read_b64_tr_b16 a[122:123], v231 offset:8456\n"
    "ds_read_b128 a[164:167], v254 offset:2048\n"
    "ds_read_b128 a[180:183], v254 offset:2560\n"
    "v_mfma_f32_16x16x32_bf16 v[124:127], a[112:115], a[128:131], 0\n"
    "ds_read_b64_tr_b16 a[124:125], v231 offset:12296\n"
    "ds_read_b128 a[168:171], v254 offset:4096\n"
    "ds_read_b128 a[184:187], v254 offset:4608\n"
    "v_mfma_f32_16x16x32_bf16 v[124:127], a[116:119], a[144:147], v[124:127]\n"
    "ds_read_b64_tr_b16 a[126:127], v231 offset:12552\n"
    "ds_read_b128 a[172:175], v254 offset:6144\n"
    "ds_read_b128 a[188:191], v254 offset:6656\n"
    "v_mfma_f32_16x16x32_bf16 v[156:159], a[116:119], a[148:151], 0\n"
    "buffer_load_dwordx4 v224, s[44:47], s72 offen offset:0 lds\n"
    "v_mfma_f32_16x16x32_bf16 v[156:159], a[112:115], a[132:135], v[156:159]\n"
    "v_mfma_f32_16x16x32_bf16 v[188:191], a[112:115], a[136:139], 0\n"
    "v_mfma_f32_16x16x32_bf16 v[188:191], a[116:119], a[152:155], v[188:191]\n"
    "s_waitcnt lgkmcnt(10)\n"
    "v_mfma_f32_16x16x32_bf16 v[112:115], a[40:43], a[160:163], v[112:115]\n"
    "s_waitcnt lgkmcnt(0)\n"
    "v_mov_b32 v246, s16\n"
    "s_lshl_b32 s88, s40, 2\n"
    "s_add_u32 s88, s88, 49152\n"
    "v_mov_b32 v249, s88\n"
    "ds_write_b32 v249, v246 offset:0\n"
    "v_mfma_f32_16x16x32_bf16 v[112:115], a[44:47], a[176:179], v[112:115]\n"
    "buffer_load_dwordx4 v253, s[44:47], s72 offen offset:1024 lds\n"
    "v_mfma_f32_16x16x32_bf16 v[144:147], a[44:47], a[180:183], v[144:147]\n"
    "s_lshr_b32 s88, s95, 5\n"
    "s_lshl_b32 s88, s88, 15\n"
    "s_add_u32 s88, s88, s42\n"
    "s_lshl_b32 s89, s6, 8\n"
    "s_sub_u32 s86, s88, s89\n"
    "s_add_u32 s87, s86, 4096\n"
    "v_lshlrev_b32 v247, 4, v252\n"
    "v_mfma_f32_16x16x32_bf16 v[144:147], a[40:43], a[164:167], v[144:147]\n"
    "v_mfma_f32_16x16x32_bf16 v[176:179], a[40:43], a[168:171], v[176:179]\n"
    "v_mfma_f32_16x16x32_bf16 v[176:179], a[44:47], a[184:187], v[176:179]\n"
    "v_mfma_f32_16x16x32_bf16 v[208:211], a[44:47], a[188:191], v[208:211]\n"
    "buffer_load_dwordx4 v224, s[44:47], s73 offen offset:2048 lds\n"
    "v_mfma_f32_16x16x32_bf16 v[208:211], a[40:43], a[172:175], v[208:211]\n"
    "s_waitcnt vmcnt(7) lgkmcnt(0)\n"
    "s_barrier\n"
    "v_mfma_f32_16x16x32_bf16 v[116:119], a[56:59], a[160:163], v[116:119]\n"
    "v_and_b32 v249, 12, v250\n"
    "ds_read_b32 v246, v249 offset:49152\n"
    "v_mfma_f32_16x16x32_bf16 v[116:119], a[60:63], a[176:179], v[116:119]\n"
    "ds_read_b64_tr_b16 a[32:33], v230 offset:0\n"
    "v_mfma_f32_16x16x32_bf16 v[148:151], a[60:63], a[180:183], v[148:151]\n"
    "ds_read_b64_tr_b16 a[34:35], v230 offset:256\n"
    "v_mfma_f32_16x16x32_bf16 v[148:151], a[56:59], a[164:167], v[148:151]\n"
    "ds_read_b64_tr_b16 a[36:37], v230 offset:4096\n"
    "buffer_load_dwordx4 v253, s[44:47], s73 offen offset:3072 lds\n"
    "v_mfma_f32_16x16x32_bf16 v[180:183], a[56:59], a[168:171], v[180:183]\n"
    "ds_read_b64_tr_b16 a[38:39], v230 offset:4352\n"
    "s_add_u32 m0, s42, 135168\n"
    "v_mfma_f32_16x16x32_bf16 v[180:183], a[60:63], a[184:187], v[180:183]\n"
    "ds_read_b64_tr_b16 a[40:41], v230 offset:8192\n"
    "v_mfma_f32_16x16x32_bf16 v[212:215], a[60:63], a[188:191], v[212:215]\n"
    "ds_read_b64_tr_b16 a[42:43], v230 offset:8448\n"
    "v_mfma_f32_16x16x32_bf16 v[212:215], a[56:59], a[172:175], v[212:215]\n"
    "ds_read_b64_tr_b16 a[44:45], v230 offset:12288\n"
    "v_mfma_f32_16x16x32_bf16 v[120:123], a[104:107], a[160:163], v[120:123]\n"
    "ds_read_b64_tr_b16 a[46:47], v230 offset:12544\n"
    "buffer_load_dwordx4 v224, s[44:47], s74 offen offset:0 lds\n"
    "v_mfma_f32_16x16x32_bf16 v[120:123], a[108:111], a[176:179], v[120:123]\n"
    "ds_read_b64_tr_b16 a[48:49], v230 offset:8\n"
    "s_waitcnt lgkmcnt(8)\n"
    "v_readlane_b32 s88, v246, 0\n"
    "v_readlane_b32 s89, v246, 1\n"
    "v_readlane_b32 s90, v246, 2\n"
    "v_readlane_b32 s91, v246, 3\n"
    "s_add_u32 s88, s88, s89\n"
    "s_add_u32 s90, s90, s91\n"
    "s_add_u32 s88, s88, s90\n"
    "s_cmp_gt_i32 s88, 0\n"
    "s_cselect_b32 s9, 0x2000, 0\n"
    "s_or_b32 s76, s76, s9\n"
    "s_or_b32 s77, s77, s9\n"
    "s_or_b32 s78, s78, s9\n"
    "s_or_b32 s79, s79, s9\n"
    "s_mov_b32 s11, s92\n"
    "s_or_b32 s92, s92, s9\n"
    "buffer_load_dword v249, v250, s[68:71], s92 offen offset:0 sc1\n"
    "v_mfma_f32_16x16x32_bf16 v[152:155], a[108:111], a[180:183], v[152:155]\n"
    "ds_read_b64_tr_b16 a[50:51], v230 offset:264\n"
    "v_mfma_f32_16x16x32_bf16 v[152:155], a[104:107], a[164:167], v[152:155]\n"
    "ds_read_b64_tr_b16 a[52:53], v230 offset:4104\n"
    "v_mfma_f32_16x16x32_bf16 v[184:187], a[104:107], a[168:171], v[184:187]\n"
    "ds_read_b64_tr_b16 a[54:55], v230 offset:4360\n"
    "v_mfma_f32_16x16x32_bf16 v[184:187], a[108:111], a[184:187], v[184:187]\n"
    "ds_read_b64_tr_b16 a[56:57], v230 offset:8200\n"
    "buffer_load_dwordx4 v253, s[44:47], s74 offen offset:1024 lds\n"
    "v_mfma_f32_16x16x32_bf16 v[124:127], a[120:123], a[160:163], v[124:127]\n"
    "ds_read_b64_tr_b16 a[58:59], v230 offset:8456\n"
    "v_mfma_f32_16x16x32_bf16 v[124:127], a[124:127], a[176:179], v[124:127]\n"
    "ds_read_b64_tr_b16 a[60:61], v230 offset:12296\n"
    "v_mfma_f32_16x16x32_bf16 v[156:159], a[124:127], a[180:183], v[156:159]\n"
    "ds_read_b64_tr_b16 a[62:63], v230 offset:12552\n"
    "v_mfma_f32_16x16x32_bf16 v[156:159], a[120:123], a[164:167], v[156:159]\n"
    "v_mfma_f32_16x16x32_bf16 v[188:191], a[120:123], a[168:171], v[188:191]\n"
    "buffer_load_dwordx4 v224, s[44:47], s75 offen offset:2048 lds\n"
    "v_mfma_f32_16x16x32_bf16 v[188:191], a[124:127], a[184:187], v[188:191]\n"
    "s_waitcnt lgkmcnt(5)\n"
    "v_mfma_f32_16x16x32_bf16 v[128:131], a[32:35], a[128:131], 0\n"
    "ds_read_b64_tr_b16 a[96:97], v255 offset:0\n"
    "v_mfma_f32_16x16x32_bf16 v[128:131], a[36:39], a[144:147], v[128:131]\n"
    "ds_read_b64_tr_b16 a[98:99], v255 offset:256\n"
    "v_mfma_f32_16x16x32_bf16 v[128:131], a[40:43], a[160:163], v[128:131]\n"
    "ds_read_b64_tr_b16 a[100:101], v255 offset:4096\n"
    "v_mfma_f32_16x16x32_bf16 v[128:131], a[44:47], a[176:179], v[128:131]\n"
    "ds_read_b64_tr_b16 a[102:103], v255 offset:4352\n"
    "buffer_load_dwordx4 v253, s[44:47], s75 offen offset:3072 lds\n"
    "v_mfma_f32_16x16x32_bf16 v[160:163], a[44:47], a[180:183], 0\n"
    "ds_read_b64_tr_b16 a[104:105], v255 offset:8192\n"
    "s_mov_b64 exec, 0\n"
    "s_cmp_eq_u32 s40, 0\n"
    "s_cbranch_scc1 L_h5\n"
    "s_cmp_eq_u32 s40, 1\n"
    "s_cbranch_scc1 L_h6\n"
    "s_cmp_eq_u32 s40, 2\n"
    "s_cbranch_scc1 L_h7\n"
    "s_cmp_eq_u32 s40, 3\n"
    "s_cbranch_scc1 L_h8\n"
    "L_ret3:\n"
    "s_mov_b64 exec, -1\n"
    "v_mfma_f32_16x16x32_bf16 v[160:163], a[40:43], a[164:167], v[160:163]\n"
    "ds_read_b64_tr_b16 a[106:107], v255 offset:8448\n"
    "s_add_u32 m0, s83, 81920\n"
    "v_mfma_f32_16x16x32_bf16 v[160:163], a[36:39], a[148:151], v[160:163]\n"
    "ds_read_b64_tr_b16 a[108:109], v255 offset:12288\n"
    "s_xor_b32 s81, s80, 128\n"
    "v_mfma_f32_16x16x32_bf16 v[160:163], a[32:35], a[132:135], v[160:163]\n"
    "ds_read_b64_tr_b16 a[110:111], v255 offset:12544\n"
    "buffer_load_dwordx4 v225, s[48:51], s81 offen offset:0 lds\n"
    "v_mfma_f32_16x16x32_bf16 v[192:195], a[32:35], a[136:139], 0\n"
    "ds_read_b64_tr_b16 a[112:113], v255 offset:8\n"
    "v_mfma_f32_16x16x32_bf16 v[192:195], a[36:39], a[152:155], v[192:195]\n"
    "ds_read_b64_tr_b16 a[114:115], v255 offset:264\n"
    "v_mfma_f32_16x16x32_bf16 v[192:195], a[40:43], a[168:171], v[192:195]\n"
    "ds_read_b64_tr_b16 a[116:117], v255 offset:4104\n"
    "v_mfma_f32_16x16x32_bf16 v[192:195], a[44:47], a[184:187], v[192:195]\n"
    "ds_read_b64_tr_b16 a[118:119], v255 offset:4360\n"
    "v_mfma_f32_16x16x32_bf16 v[216:219], a[44:47], a[188:191], 0\n"
    "ds_read_b64_tr_b16 a[120:121], v255 offset:8200\n"
    "buffer_load_dwordx4 v226, s[48:51], s81 offen offset:1024 lds\n"
    "v_mfma_f32_16x16x32_bf16 v[216:219], a[40:43], a[172:175], v[216:219]\n"
    "ds_read_b64_tr_b16 a[122:123], v255 offset:8456\n"
    "v_mfma_f32_16x16x32_bf16 v[216:219], a[36:39], a[156:159], v[216:219]\n"
    "ds_read_b64_tr_b16 a[124:125], v255 offset:12296\n"
    "v_mfma_f32_16x16x32_bf16 v[216:219], a[32:35], a[140:143], v[216:219]\n"
    "ds_read_b64_tr_b16 a[126:127], v255 offset:12552\n"
    "v_mfma_f32_16x16x32_bf16 v[132:135], a[48:51], a[128:131], 0\n"
    "s_waitcnt lgkmcnt(7)\n"
    "v_mfma_f32_16x16x32_bf16 v[132:135], a[52:55], a[144:147], v[132:135]\n"
    "buffer_load_dwordx4 v227, s[48:51], s81 offen offset:2048 lds\n"
    "v_mfma_f32_16x16x32_bf16 v[132:135], a[56:59], a[160:163], v[132:135]\n"
    "v_mfma_f32_16x16x32_bf16 v[132:135], a[60:63], a[176:179], v[132:135]\n"
    "s_nop 9\n"
    "v_mfma_f32_16x16x32_bf16 v[164:167], a[60:63], a[180:183], 0\n"
    "v_mfma_f32_16x16x32_bf16 v[164:167], a[56:59], a[164:167], v[164:167]\n"
    "v_mfma_f32_16x16x32_bf16 v[164:167], a[52:55], a[148:151], v[164:167]\n"
    "buffer_load_dwordx4 v228, s[48:51], s81 offen offset:3072 lds\n"
    "v_mfma_f32_16x16x32_bf16 v[164:167], a[48:51], a[132:135], v[164:167]\n"
    "s_waitcnt vmcnt(13) lgkmcnt(0)\n"
    "s_barrier\n"
    "s_nop 9\n"
    "v_mfma_f32_16x16x32_bf16 v[196:199], a[48:51], a[136:139], 0\n"
    "ds_read_b64_tr_b16 a[32:33], v229 offset:16384\n"
    "s_add_u32 m0, s83, 0\n"
    "v_mfma_f32_16x16x32_bf16 v[196:199], a[52:55], a[152:155], v[196:199]\n"
    "ds_read_b64_tr_b16 a[34:35], v229 offset:16640\n"
    "s_xor_b32 s81, s80, 1048832\n"
    "v_mfma_f32_16x16x32_bf16 v[196:199], a[56:59], a[168:171], v[196:199]\n"
    "ds_read_b64_tr_b16 a[36:37], v229 offset:20480\n"
    "buffer_load_dwordx4 v225, s[48:51], s81 offen offset:0 lds\n"
    "v_mfma_f32_16x16x32_bf16 v[196:199], a[60:63], a[184:187], v[196:199]\n"
    "ds_read_b64_tr_b16 a[38:39], v229 offset:20736\n"
    "s_nop 9\n"
    "v_mfma_f32_16x16x32_bf16 v[220:223], a[60:63], a[188:191], 0\n"
    "ds_read_b64_tr_b16 a[40:41], v229 offset:24576\n"
    "v_mfma_f32_16x16x32_bf16 v[220:223], a[56:59], a[172:175], v[220:223]\n"
    "ds_read_b64_tr_b16 a[42:43], v229 offset:24832\n"
    "v_mfma_f32_16x16x32_bf16 v[220:223], a[52:55], a[156:159], v[220:223]\n"
    "ds_read_b64_tr_b16 a[44:45], v229 offset:28672\n"
    "v_mfma_f32_16x16x32_bf16 v[220:223], a[48:51], a[140:143], v[220:223]\n"
    "ds_read_b64_tr_b16 a[46:47], v229 offset:28928\n"
    "buffer_load_dwordx4 v226, s[48:51], s81 offen offset:1024 lds\n"
    "s_nop 9\n"
    "v_mfma_f32_16x16x32_bf16 v[136:139], a[96:99], a[128:131], 0\n"
    "v_mfma_f32_16x16x32_bf16 v[136:139], a[100:103], a[144:147], v[136:139]\n"
    "v_mfma_f32_16x16x32_bf16 v[136:139], a[104:107], a[160:163], v[136:139]\n"
    "v_mfma_f32_16x16x32_bf16 v[136:139], a[108:111], a[176:179], v[136:139]\n"
    "s_nop 8\n"
    "v_mfma_f32_16x16x32_bf16 v[168:171], a[108:111], a[180:183], 0\n"
    "buffer_load_dwordx4 v227, s[48:51], s81 offen offset:2048 lds\n"
    "v_mfma_f32_16x16x32_bf16 v[168:171], a[104:107], a[164:167], v[168:171]\n"
    "v_mfma_f32_16x16x32_bf16 v[168:171], a[100:103], a[148:151], v[168:171]\n"
    "v_mfma_f32_16x16x32_bf16 v[168:171], a[96:99], a[132:135], v[168:171]\n"
    "s_nop 8\n"
    "v_mfma_f32_16x16x32_bf16 v[200:203], a[96:99], a[136:139], 0\n"
    "ds_read_b64_tr_b16 a[48:49], v229 offset:16392\n"
    "v_mfma_f32_16x16x32_bf16 v[200:203], a[100:103], a[152:155], v[200:203]\n"
    "ds_read_b64_tr_b16 a[50:51], v229 offset:16648\n"
    "buffer_load_dwordx4 v228, s[48:51], s81 offen offset:3072 lds\n"
    "v_mfma_f32_16x16x32_bf16 v[200:203], a[104:107], a[168:171], v[200:203]\n"
    "ds_read_b64_tr_b16 a[52:53], v229 offset:20488\n"
    "v_mfma_f32_16x16x32_bf16 v[200:203], a[108:111], a[184:187], v[200:203]\n"
    "ds_read_b64_tr_b16 a[54:55], v229 offset:20744\n"
    "s_nop 8\n"
    "v_mfma_f32_16x16x32_bf16 v[140:143], a[112:115], a[128:131], 0\n"
    "ds_read_b64_tr_b16 a[56:57], v229 offset:24584\n"
    "v_xor_b32 v224, 128, v224\n"
    "v_mfma_f32_16x16x32_bf16 v[140:143], a[116:119], a[144:147], v[140:143]\n"
    "ds_read_b64_tr_b16 a[58:59], v229 offset:24840\n"
    "v_xor_b32 v253, 128, v253\n"
    "v_mfma_f32_16x16x32_bf16 v[140:143], a[120:123], a[160:163], v[140:143]\n"
    "ds_read_b64_tr_b16 a[60:61], v229 offset:28680\n"
    "s_add_u32 m0, s42, 98304\n"
    "v_mfma_f32_16x16x32_bf16 v[140:143], a[124:127], a[176:179], v[140:143]\n"
    "ds_read_b64_tr_b16 a[62:63], v229 offset:28936\n"
    "buffer_load_dwordx4 v224, s[44:47], s72 offen offset:0 lds\n"
    "s_nop 8\n"
    "v_mfma_f32_16x16x32_bf16 v[172:175], a[124:127], a[180:183], 0\n"
    "v_mfma_f32_16x16x32_bf16 v[172:175], a[120:123], a[164:167], v[172:175]\n"
    "v_mfma_f32_16x16x32_bf16 v[172:175], a[116:119], a[148:151], v[172:175]\n"
    "v_mfma_f32_16x16x32_bf16 v[172:175], a[112:115], a[132:135], v[172:175]\n"
    "s_nop 8\n"
    "v_mfma_f32_16x16x32_bf16 v[204:207], a[112:115], a[136:139], 0\n"
    "buffer_load_dwordx4 v253, s[44:47], s72 offen offset:1024 lds\n"
    "v_mfma_f32_16x16x32_bf16 v[204:207], a[116:119], a[152:155], v[204:207]\n"
    "v_mfma_f32_16x16x32_bf16 v[204:207], a[120:123], a[168:171], v[204:207]\n"
    "v_mfma_f32_16x16x32_bf16 v[204:207], a[124:127], a[184:187], v[204:207]\n"
    "s_nop 8\n"
    "s_waitcnt lgkmcnt(0)\n"
    "v_mfma_f32_16x16x32_bf16 v[0:3], a[32:35], a[128:131], 0\n"
    "ds_read_b64_tr_b16 a[96:97], v231 offset:16384\n"
    "v_mfma_f32_16x16x32_bf16 v[0:3], a[36:39], a[144:147], v[0:3]\n"
    "ds_read_b64_tr_b16 a[98:99], v231 offset:16640\n"
    "buffer_load_dwordx4 v224, s[44:47], s73 offen offset:2048 lds\n"
    "v_mfma_f32_16x16x32_bf16 v[0:3], a[40:43], a[160:163], v[0:3]\n"
    "ds_read_b64_tr_b16 a[100:101], v231 offset:20480\n"
    "v_mfma_f32_16x16x32_bf16 v[0:3], a[44:47], a[176:179], v[0:3]\n"
    "ds_read_b64_tr_b16 a[102:103], v231 offset:20736\n"
    "s_nop 7\n"
    "v_mfma_f32_16x16x32_bf16 v[32:35], a[44:47], a[180:183], 0\n"
    "ds_read_b64_tr_b16 a[104:105], v231 offset:24576\n"
    "v_mfma_f32_16x16x32_bf16 v[32:35], a[40:43], a[164:167], v[32:35]\n"
    "ds_read_b64_tr_b16 a[106:107], v231 offset:24832\n"
    "v_mfma_f32_16x16x32_bf16 v[32:35], a[36:39], a[148:151], v[32:35]\n"
    "ds_read_b64_tr_b16 a[108:109], v231 offset:28672\n"
    "buffer_load_dwordx4 v253, s[44:47], s73 offen offset:3072 lds\n"
    "v_mfma_f32_16x16x32_bf16 v[32:35], a[32:35], a[132:135], v[32:35]\n"
    "ds_read_b64_tr_b16 a[110:111], v231 offset:28928\n"
    "s_add_u32 m0, s42, 102400\n"
    "s_nop 7\n"
    "v_mfma_f32_16x16x32_bf16 v[64:67], a[32:35], a[136:139], 0\n"
    "ds_read_b64_tr_b16 a[112:113], v231 offset:16392\n"
    "v_mfma_f32_16x16x32_bf16 v[64:67], a[36:39], a[152:155], v[64:67]\n"
    "ds_read_b64_tr_b16 a[114:115], v231 offset:16648\n"
    "v_mfma_f32_16x16x32_bf16 v[64:67], a[40:43], a[168:171], v[64:67]\n"
    "ds_read_b64_tr_b16 a[116:117], v231 offset:20488\n"
    "v_mfma_f32_16x16x32_bf16 v[64:67], a[44:47], a[184:187], v[64:67]\n"
    "ds_read_b64_tr_b16 a[118:119], v231 offset:20744\n"
    "buffer_load_dwordx4 v224, s[44:47], s74 offen offset:0 lds\n"
    "s_nop 7\n"
    "v_mfma_f32_16x16x32_bf16 v[96:99], a[44:47], a[188:191], 0\n"
    "ds_read_b64_tr_b16 a[120:121], v231 offset:24584\n"
    "v_mfma_f32_16x16x32_bf16 v[96:99], a[40:43], a[172:175], v[96:99]\n"
    "ds_read_b64_tr_b16 a[122:123], v231 offset:24840\n"
    "v_mfma_f32_16x16x32_bf16 v[96:99], a[36:39], a[156:159], v[96:99]\n"
    "ds_read_b64_tr_b16 a[124:125], v231 offset:28680\n"
    "v_mfma_f32_16x16x32_bf16 v[96:99], a[32:35], a[140:143], v[96:99]\n"
    "ds_read_b64_tr_b16 a[126:127], v231 offset:28936\n"
    "s_nop 7\n"
    "v_mfma_f32_16x16x32_bf16 v[4:7], a[48:51], a[128:131], 0\n"
    "buffer_load_dwordx4 v253, s[44:47], s74 offen offset:1024 lds\n"
    "v_mfma_f32_16x16x32_bf16 v[4:7], a[52:55], a[144:147], v[4:7]\n"
    "v_mfma_f32_16x16x32_bf16 v[4:7], a[56:59], a[160:163], v[4:7]\n"
    "v_mfma_f32_16x16x32_bf16 v[4:7], a[60:63], a[176:179], v[4:7]\n"
    "s_nop 7\n"
    "v_mfma_f32_16x16x32_bf16 v[36:39], a[60:63], a[180:183], 0\n"
    "v_mfma_f32_16x16x32_bf16 v[36:39], a[56:59], a[164:167], v[36:39]\n"
    "buffer_load_dwordx4 v224, s[44:47], s75 offen offset:2048 lds\n"
    "v_mfma_f32_16x16x32_bf16 v[36:39], a[52:55], a[148:151], v[36:39]\n"
    "v_mfma_f32_16x16x32_bf16 v[36:39], a[48:51], a[132:135], v[36:39]\n"
    "s_waitcnt vmcnt(11) lgkmcnt(0)\n"
    "s_barrier\n"
    "s_nop 7\n"
    "v_mfma_f32_16x16x32_bf16 v[68:71], a[48:51], a[136:139], 0\n"
    "ds_read_b64_tr_b16 a[32:33], v230 offset:16384\n"
    "v_mfma_f32_16x16x32_bf16 v[68:71], a[52:55], a[152:155], v[68:71]\n"
    "ds_read_b64_tr_b16 a[34:35], v230 offset:16640\n"
    "v_mfma_f32_16x16x32_bf16 v[68:71], a[56:59], a[168:171], v[68:71]\n"
    "ds_read_b64_tr_b16 a[36:37], v230 offset:20480\n"
    "buffer_load_dwordx4 v253, s[44:47], s75 offen offset:3072 lds\n"
    "v_mfma_f32_16x16x32_bf16 v[68:71], a[60:63], a[184:187], v[68:71]\n"
    "ds_read_b64_tr_b16 a[38:39], v230 offset:20736\n"
    "s_nop 7\n"
    "v_mfma_f32_16x16x32_bf16 v[100:103], a[60:63], a[188:191], 0\n"
    "ds_read_b64_tr_b16 a[40:41], v230 offset:24576\n"
    "s_add_u32 m0, s83, 65536\n"
    "v_mfma_f32_16x16x32_bf16 v[100:103], a[56:59], a[172:175], v[100:103]\n"
    "ds_read_b64_tr_b16 a[42:43], v230 offset:24832\n"
    "s_xor_b32 s81, s80, 1048960\n"
    "v_mfma_f32_16x16x32_bf16 v[100:103], a[52:55], a[156:159], v[100:103]\n"
    "ds_read_b64_tr_b16 a[44:45], v230 offset:28672\n"
    "buffer_load_dwordx4 v225, s[48:51], s81 offen offset:0 lds\n"
    "v_mfma_f32_16x16x32_bf16 v[100:103], a[48:51], a[140:143], v[100:103]\n"
    "ds_read_b64_tr_b16 a[46:47], v230 offset:28928\n"
    "s_nop 6\n"
    "v_mfma_f32_16x16x32_bf16 v[8:11], a[96:99], a[128:131], 0\n"
    "v_mfma_f32_16x16x32_bf16 v[8:11], a[100:103], a[144:147], v[8:11]\n"
    "v_mfma_f32_16x16x32_bf16 v[8:11], a[104:107], a[160:163], v[8:11]\n"
    "v_mfma_f32_16x16x32_bf16 v[8:11], a[108:111], a[176:179], v[8:11]\n"
    "buffer_load_dwordx4 v226, s[48:51], s81 offen offset:1024 lds\n"
    "s_nop 6\n"
    "v_mfma_f32_16x16x32_bf16 v[40:43], a[108:111], a[180:183], 0\n"
    "v_mfma_f32_16x16x32_bf16 v[40:43], a[104:107], a[164:167], v[40:43]\n"
    "v_mfma_f32_16x16x32_bf16 v[40:43], a[100:103], a[148:151], v[40:43]\n"
    "ds_read_b128 a[192:195], v254 offset:32768\n"
    "v_mfma_f32_16x16x32_bf16 v[40:43], a[96:99], a[132:135], v[40:43]\n"
    "ds_read_b128 a[208:211], v254 offset:33280\n"
    "s_nop 6\n"
    "v_mfma_f32_16x16x32_bf16 v[72:75], a[96:99], a[136:139], 0\n"
    "ds_read_b64_tr_b16 a[48:49], v230 offset:16392\n"
    "buffer_load_dwordx4 v227, s[48:51], s81 offen offset:2048 lds\n"
    "v_mfma_f32_16x16x32_bf16 v[72:75], a[100:103], a[152:155], v[72:75]\n"
    "ds_read_b64_tr_b16 a[50:51], v230 offset:16648\n"
    "v_mfma_f32_16x16x32_bf16 v[72:75], a[104:107], a[168:171], v[72:75]\n"
    "ds_read_b64_tr_b16 a[52:53], v230 offset:20488\n"
    "v_mfma_f32_16x16x32_bf16 v[72:75], a[108:111], a[184:187], v[72:75]\n"
    "ds_read_b64_tr_b16 a[54:55], v230 offset:20744\n"
    "s_nop 6\n"
    "v_mfma_f32_16x16x32_bf16 v[12:15], a[112:115], a[128:131], 0\n"
    "ds_read_b64_tr_b16 a[56:57], v230 offset:24584\n"
    "v_mfma_f32_16x16x32_bf16 v[12:15], a[116:119], a[144:147], v[12:15]\n"
    "ds_read_b64_tr_b16 a[58:59], v230 offset:24840\n"
    "buffer_load_dwordx4 v228, s[48:51], s81 offen offset:3072 lds\n"
    "v_mfma_f32_16x16x32_bf16 v[12:15], a[120:123], a[160:163], v[12:15]\n"
    "ds_read_b64_tr_b16 a[60:61], v230 offset:28680\n"
    "v_mfma_f32_16x16x32_bf16 v[12:15], a[124:127], a[176:179], v[12:15]\n"
    "ds_read_b64_tr_b16 a[62:63], v230 offset:28936\n"
    "s_add_u32 m0, s83, 16384\n"
    "s_nop 6\n"
    "v_mfma_f32_16x16x32_bf16 v[44:47], a[124:127], a[180:183], 0\n"
    "ds_read_b128 a[196:199], v254 offset:34816\n"
    "s_xor_b32 s81, s80, 1048576\n"
    "v_mfma_f32_16x16x32_bf16 v[44:47], a[120:123], a[164:167], v[44:47]\n"
    "ds_read_b128 a[212:215], v254 offset:35328\n"
    "buffer_load_dwordx4 v225, s[48:51], s81 offen offset:0 lds\n"
    "v_mfma_f32_16x16x32_bf16 v[44:47], a[116:119], a[148:151], v[44:47]\n"
    "ds_read_b128 a[200:203], v254 offset:36864\n"
    "v_mfma_f32_16x16x32_bf16 v[44:47], a[112:115], a[132:135], v[44:47]\n"
    "ds_read_b128 a[216:219], v254 offset:37376\n"
    "s_nop 6\n"
    "v_mfma_f32_16x16x32_bf16 v[76:79], a[112:115], a[136:139], 0\n"
    "ds_read_b128 a[204:207], v254 offset:38912\n"
    "v_mfma_f32_16x16x32_bf16 v[76:79], a[116:119], a[152:155], v[76:79]\n"
    "ds_read_b128 a[220:223], v254 offset:39424\n"
    "v_mfma_f32_16x16x32_bf16 v[76:79], a[120:123], a[168:171], v[76:79]\n"
    "buffer_load_dwordx4 v226, s[48:51], s81 offen offset:1024 lds\n"
    "v_mfma_f32_16x16x32_bf16 v[76:79], a[124:127], a[184:187], v[76:79]\n"
    "s_nop 5\n"
    "s_waitcnt lgkmcnt(6)\n"
    "v_mfma_f32_16x16x32_bf16 v[16:19], a[32:35], a[128:131], 0\n"
    "ds_read_b64_tr_b16 a[96:97], v255 offset:16384\n"
    "v_mfma_f32_16x16x32_bf16 v[16:19], a[36:39], a[144:147], v[16:19]\n"
    "ds_read_b64_tr_b16 a[98:99], v255 offset:16640\n"
    "v_mfma_f32_16x16x32_bf16 v[16:19], a[40:43], a[160:163], v[16:19]\n"
    "ds_read_b64_tr_b16 a[100:101], v255 offset:20480\n"
    "v_mfma_f32_16x16x32_bf16 v[16:19], a[44:47], a[176:179], v[16:19]\n"
    "ds_read_b64_tr_b16 a[102:103], v255 offset:20736\n"
    "buffer_load_dwordx4 v227, s[48:51], s81 offen offset:2048 lds\n"
    "s_nop 5\n"
    "v_mfma_f32_16x16x32_bf16 v[48:51], a[44:47], a[180:183], 0\n"
    "ds_read_b64_tr_b16 a[104:105], v255 offset:24576\n"
    "v_mfma_f32_16x16x32_bf16 v[48:51], a[40:43], a[164:167], v[48:51]\n"
    "ds_read_b64_tr_b16 a[106:107], v255 offset:24832\n"
    "v_mfma_f32_16x16x32_bf16 v[48:51], a[36:39], a[148:151], v[48:51]\n"
    "ds_read_b64_tr_b16 a[108:109], v255 offset:28672\n"
    "v_mfma_f32_16x16x32_bf16 v[48:51], a[32:35], a[132:135], v[48:51]\n"
    "ds_read_b64_tr_b16 a[110:111], v255 offset:28928\n"
    "s_nop 5\n"
    "v_mfma_f32_16x16x32_bf16 v[80:83], a[32:35], a[136:139], 0\n"
    "ds_read_b64_tr_b16 a[112:113], v255 offset:16392\n"
    "buffer_load_dwordx4 v228, s[48:51], s81 offen offset:3072 lds\n"
    "v_mfma_f32_16x16x32_bf16 v[80:83], a[36:39], a[152:155], v[80:83]\n"
    "ds_read_b64_tr_b16 a[114:115], v255 offset:16648\n"
    "v_mfma_f32_16x16x32_bf16 v[80:83], a[40:43], a[168:171], v[80:83]\n"
    "ds_read_b64_tr_b16 a[116:117], v255 offset:20488\n"
    "v_mfma_f32_16x16x32_bf16 v[80:83], a[44:47], a[184:187], v[80:83]\n"
    "ds_read_b64_tr_b16 a[118:119], v255 offset:20744\n"
    "s_nop 5\n"
    "v_mfma_f32_16x16x32_bf16 v[104:107], a[44:47], a[188:191], 0\n"
    "ds_read_b64_tr_b16 a[120:121], v255 offset:24584\n"
    "v_mfma_f32_16x16x32_bf16 v[104:107], a[40:43], a[172:175], v[104:107]\n"
    "ds_read_b64_tr_b16 a[122:123], v255 offset:24840\n"
    "v_mfma_f32_16x16x32_bf16 v[104:107], a[36:39], a[156:159], v[104:107]\n"
    "ds_read_b64_tr_b16 a[124:125], v255 offset:28680\n"
    "v_mfma_f32_16x16x32_bf16 v[104:107], a[32:35], a[140:143], v[104:107]\n"
    "ds_read_b64_tr_b16 a[126:127], v255 offset:28936\n"
    "s_nop 5\n"
    "v_mfma_f32_16x16x32_bf16 v[20:23], a[48:51], a[128:131], 0\n"
    "v_mfma_f32_16x16x32_bf16 v[20:23], a[52:55], a[144:147], v[20:23]\n"
    "v_mfma_f32_16x16x32_bf16 v[20:23], a[56:59], a[160:163], v[20:23]\n"
    "v_mfma_f32_16x16x32_bf16 v[20:23], a[60:63], a[176:179], v[20:23]\n"
    "s_nop 5\n"
    "v_mfma_f32_16x16x32_bf16 v[52:55], a[60:63], a[180:183], 0\n"
    "v_mfma_f32_16x16x32_bf16 v[52:55], a[56:59], a[164:167], v[52:55]\n"
    "v_mfma_f32_16x16x32_bf16 v[52:55], a[52:55], a[148:151], v[52:55]\n"
    "s_waitcnt lgkmcnt(1)\n"
    "v_xor_b32 v224, 896, v224\n"
    "v_xor_b32 v253, 896, v253\n"
    "v_mfma_f32_16x16x32_bf16 v[52:55], a[48:51], a[132:135], v[52:55]\n"
    "s_waitcnt vmcnt(16) lgkmcnt(0)\n"
    "s_barrier\n"
    "s_add_u32 m0, s42, 131072\n"
    "s_nop 4\n"
    "v_add_u32 v249, 2, v249\n"
    "v_and_b32 v249, -2, v249\n"
    "s_lshr_b32 s88, s9, 13\n"
    "v_or_b32 v249, s88, v249\n"
    "s_mov_b32 s96, 0x0\n"
    "s_mov_b32 s97, 0xff00\n"
    "s_mov_b64 exec, s[96:97]\n"
    "v_mov_b32 v249, 0\n"
    "s_mov_b64 exec, -1\n"
    "buffer_store_dword v249, v250, s[68:71], s11 offen offset:2048 sc1\n"
    "v_mfma_f32_16x16x32_bf16 v[84:87], a[48:51], a[136:139], 0\n"
    "ds_read_b64_tr_b16 a[32:33], v229 offset:0\n"
    "buffer_load_dwordx4 v224, s[44:47], s72 offen offset:0 lds\n"
    "v_mfma_f32_16x16x32_bf16 v[84:87], a[52:55], a[152:155], v[84:87]\n"
    "ds_read_b64_tr_b16 a[34:35], v229 offset:256\n"
    "v_mfma_f32_16x16x32_bf16 v[84:87], a[56:59], a[168:171], v[84:87]\n"
    "ds_read_b64_tr_b16 a[36:37], v229 offset:4096\n"
    "v_mfma_f32_16x16x32_bf16 v[84:87], a[60:63], a[184:187], v[84:87]\n"
    "s_waitcnt vmcnt(10)\n"
    "ds_read_b64_tr_b16 a[38:39], v229 offset:4352\n"
    "s_nop 4\n"
    "v_mfma_f32_16x16x32_bf16 v[108:111], a[60:63], a[188:191], 0\n"
    "ds_read_b64_tr_b16 a[40:41], v229 offset:8192\n"
    "v_mfma_f32_16x16x32_bf16 v[108:111], a[56:59], a[172:175], v[108:111]\n"
    "ds_read_b64_tr_b16 a[42:43], v229 offset:8448\n"
    "buffer_load_dwordx4 v253, s[44:47], s72 offen offset:1024 lds\n"
    "v_mfma_f32_16x16x32_bf16 v[108:111], a[52:55], a[156:159], v[108:111]\n"
    "ds_read_b64_tr_b16 a[44:45], v229 offset:12288\n"
    "v_mfma_f32_16x16x32_bf16 v[108:111], a[48:51], a[140:143], v[108:111]\n"
    "ds_read_b64_tr_b16 a[46:47], v229 offset:12544\n"
    "s_nop 4\n"
    "v_mfma_f32_16x16x32_bf16 v[24:27], a[96:99], a[128:131], 0\n"
    "ds_read_b128 a[224:227], v254 offset:0\n"
    "v_mfma_f32_16x16x32_bf16 v[24:27], a[100:103], a[144:147], v[24:27]\n"
    "ds_read_b128 a[240:243], v254 offset:512\n"
    "v_mfma_f32_16x16x32_bf16 v[24:27], a[104:107], a[160:163], v[24:27]\n"
    "ds_read_b128 a[228:231], v254 offset:2048\n"
    "L_h2:\n"
    "s_cbranch_execz L_ret1\n"
    "buffer_load_dwordx4 v224, s[44:47], s73 offen offset:2048 lds\n"
    "v_mfma_f32_16x16x32_bf16 v[24:27], a[108:111], a[176:179], v[24:27]\n"
    "ds_read_b128 a[244:247], v254 offset:2560\n"
    "s_nop 4\n"
    "v_mfma_f32_16x16x32_bf16 v[56:59], a[108:111], a[180:183], 0\n"
    "ds_read_b128 a[232:235], v254 offset:4096\n"
    "v_mfma_f32_16x16x32_bf16 v[56:59], a[104:107], a[164:167], v[56:59]\n"
    "ds_read_b128 a[248:251], v254 offset:4608\n"
    "v_mfma_f32_16x16x32_bf16 v[56:59], a[100:103], a[148:151], v[56:59]\n"
    "ds_read_b128 a[236:239], v254 offset:6144\n"
    "v_mfma_f32_16x16x32_bf16 v[56:59], a[96:99], a[132:135], v[56:59]\n"
    "ds_read_b128 a[252:255], v254 offset:6656\n"
    "buffer_load_dwordx4 v253, s[44:47], s73 offen offset:3072 lds\n"
    "s_nop 4\n"
    "v_mfma_f32_16x16x32_bf16 v[88:91], a[96:99], a[136:139], 0\n"
    "ds_read_b64_tr_b16 a[48:49], v229 offset:8\n"
    "s_add_u32 m0, s42, 135168\n"
    "v_mfma_f32_16x16x32_bf16 v[88:91], a[100:103], a[152:155], v[88:91]\n"
    "ds_read_b64_tr_b16 a[50:51], v229 offset:264\n"
    "v_mfma_f32_16x16x32_bf16 v[88:91], a[104:107], a[168:171], v[88:91]\n"
    "ds_read_b64_tr_b16 a[52:53], v229 offset:4104\n"
    "v_mfma_f32_16x16x32_bf16 v[88:91], a[108:111], a[184:187], v[88:91]\n"
    "ds_read_b64_tr_b16 a[54:55], v229 offset:4360\n"
    "s_nop 4\n"
    "v_mfma_f32_16x16x32_bf16 v[28:31], a[112:115], a[128:131], 0\n"
    "ds_read_b64_tr_b16 a[56:57], v229 offset:8200\n"
    "buffer_load_dwordx4 v224, s[44:47], s74 offen offset:0 lds\n"
    "v_mfma_f32_16x16x32_bf16 v[28:31], a[116:119], a[144:147], v[28:31]\n"
    "ds_read_b64_tr_b16 a[58:59], v229 offset:8456\n"
    "v_mfma_f32_16x16x32_bf16 v[28:31], a[120:123], a[160:163], v[28:31]\n"
    "ds_read_b64_tr_b16 a[60:61], v229 offset:12296\n"
    "v_mfma_f32_16x16x32_bf16 v[28:31], a[124:127], a[176:179], v[28:31]\n"
    "ds_read_b64_tr_b16 a[62:63], v229 offset:12552\n"
    "s_nop 4\n"
    "v_mfma_f32_16x16x32_bf16 v[60:63], a[124:127], a[180:183], 0\n"
    "v_mfma_f32_16x16x32_bf16 v[60:63], a[120:123], a[164:167], v[60:63]\n"
    "buffer_load_dwordx4 v253, s[44:47], s74 offen offset:1024 lds\n"
    "v_mfma_f32_16x16x32_bf16 v[60:63], a[116:119], a[148:151], v[60:63]\n"
    "v_mfma_f32_16x16x32_bf16 v[60:63], a[112:115], a[132:135], v[60:63]\n"
    "s_nop 3\n"
    "v_mfma_f32_16x16x32_bf16 v[92:95], a[112:115], a[136:139], 0\n"
    "v_mfma_f32_16x16x32_bf16 v[92:95], a[116:119], a[152:155], v[92:95]\n"
    "v_mfma_f32_16x16x32_bf16 v[92:95], a[120:123], a[168:171], v[92:95]\n"
    "buffer_load_dwordx4 v224, s[44:47], s75 offen offset:2048 lds\n"
    "s_waitcnt lgkmcnt(1)\n"
    "v_mfma_f32_16x16x32_bf16 v[92:95], a[124:127], a[184:187], v[92:95]\n"
    "s_nop 3\n"
    "v_mfma_f32_16x16x32_bf16 v[112:115], a[32:35], a[192:195], v[112:115]\n"
    "ds_read_b64_tr_b16 a[96:97], v231 offset:0\n"
    "v_mfma_f32_16x16x32_bf16 v[112:115], a[36:39], a[208:211], v[112:115]\n"
    "ds_read_b64_tr_b16 a[98:99], v231 offset:256\n"
    "v_mfma_f32_16x16x32_bf16 v[112:115], a[40:43], a[224:227], v[112:115]\n"
    "ds_read_b64_tr_b16 a[100:101], v231 offset:4096\n"
    "v_mfma_f32_16x16x32_bf16 v[112:115], a[44:47], a[240:243], v[112:115]\n"
    "ds_read_b64_tr_b16 a[102:103], v231 offset:4352\n"
    "buffer_load_dwordx4 v253, s[44:47], s75 offen offset:3072 lds\n"
    "s_nop 3\n"
    "v_mfma_f32_16x16x32_bf16 v[144:147], a[44:47], a[244:247], v[144:147]\n"
    "ds_read_b64_tr_b16 a[104:105], v231 offset:8192\n"
    "v_mfma_f32_16x16x32_bf16 v[144:147], a[40:43], a[228:231], v[144:147]\n"
    "ds_read_b64_tr_b16 a[106:107], v231 offset:8448\n"
    "s_add_u32 m0, s83, 81920\n"
    "v_mfma_f32_16x16x32_bf16 v[144:147], a[36:39], a[212:215], v[144:147]\n"
    "ds_read_b64_tr_b16 a[108:109], v231 offset:12288\n"
    "s_xor_b32 s81, s80, 1048704\n"
    "v_mfma_f32_16x16x32_bf16 v[144:147], a[32:35], a[196:199], v[144:147]\n"
    "ds_read_b64_tr_b16 a[110:111], v231 offset:12544\n"
    "buffer_load_dwordx4 v225, s[48:51], s81 offen offset:0 lds\n"
    "s_nop 3\n"
    "v_mfma_f32_16x16x32_bf16 v[176:179], a[32:35], a[200:203], v[176:179]\n"
    "ds_read_b64_tr_b16 a[112:113], v231 offset:8\n"
    "v_mfma_f32_16x16x32_bf16 v[176:179], a[36:39], a[216:219], v[176:179]\n"
    "ds_read_b64_tr_b16 a[114:115], v231 offset:264\n"
    "v_mfma_f32_16x16x32_bf16 v[176:179], a[40:43], a[232:235], v[176:179]\n"
    "ds_read_b64_tr_b16 a[116:117], v231 offset:4104\n"
    "v_mfma_f32_16x16x32_bf16 v[176:179], a[44:47], a[248:251], v[176:179]\n"
    "ds_read_b64_tr_b16 a[118:119], v231 offset:4360\n"
    "s_nop 3\n"
    "v_mfma_f32_16x16x32_bf16 v[208:211], a[44:47], a[252:255], v[208:211]\n"
    "ds_read_b64_tr_b16 a[120:121], v231 offset:8200\n"
    "buffer_load_dwordx4 v226, s[48:51], s81 offen offset:1024 lds\n"
    "v_mfma_f32_16x16x32_bf16 v[208:211], a[40:43], a[236:239], v[208:211]\n"
    "ds_read_b64_tr_b16 a[122:123], v231 offset:8456\n"
    "v_mfma_f32_16x16x32_bf16 v[208:211], a[36:39], a[220:223], v[208:211]\n"
    "ds_read_b64_tr_b16 a[124:125], v231 offset:12296\n"
    "v_mfma_f32_16x16x32_bf16 v[208:211], a[32:35], a[204:207], v[208:211]\n"
    "ds_read_b64_tr_b16 a[126:127], v231 offset:12552\n"
    "s_nop 3\n"
    "v_mfma_f32_16x16x32_bf16 v[116:119], a[48:51], a[192:195], v[116:119]\n"
    "v_mfma_f32_16x16x32_bf16 v[116:119], a[52:55], a[208:211], v[116:119]\n"
    "buffer_load_dwordx4 v227, s[48:51], s81 offen offset:2048 lds\n"
    "v_mfma_f32_16x16x32_bf16 v[116:119], a[56:59], a[224:227], v[116:119]\n"
    "s_waitcnt lgkmcnt(5)\n"
    "v_mfma_f32_16x16x32_bf16 v[116:119], a[60:63], a[240:243], v[116:119]\n"
    "s_nop 2\n"
    "v_mfma_f32_16x16x32_bf16 v[148:151], a[60:63], a[244:247], v[148:151]\n"
    "v_mfma_f32_16x16x32_bf16 v[148:151], a[56:59], a[228:231], v[148:151]\n"
    "v_mfma_f32_16x16x32_bf16 v[148:151], a[52:55], a[212:215], v[148:151]\n"
    "buffer_load_dwordx4 v228, s[48:51], s81 offen offset:3072 lds\n"
    "v_mfma_f32_16x16x32_bf16 v[148:151], a[48:51], a[196:199], v[148:151]\n"
    "s_waitcnt vmcnt(17) lgkmcnt(0)\n"
    "s_barrier\n"
    "s_nop 2\n"
    "v_mfma_f32_16x16x32_bf16 v[180:183], a[48:51], a[200:203], v[180:183]\n"
    "ds_read_b64_tr_b16 a[32:33], v230 offset:0\n"
    "v_mfma_f32_16x16x32_bf16 v[180:183], a[52:55], a[216:219], v[180:183]\n"
    "ds_read_b64_tr_b16 a[34:35], v230 offset:256\n"
    "v_mfma_f32_16x16x32_bf16 v[180:183], a[56:59], a[232:235], v[180:183]\n"
    "ds_read_b64_tr_b16 a[36:37], v230 offset:4096\n"
    "buffer_load_dword v251, v247, s[64:67], s86 offen offset:0\n"
    "v_mfma_f32_16x16x32_bf16 v[180:183], a[60:63], a[248:251], v[180:183]\n"
    "ds_read_b64_tr_b16 a[38:39], v230 offset:4352\n"
    "s_nop 2\n"
    "v_mfma_f32_16x16x32_bf16 v[212:215], a[60:63], a[252:255], v[212:215]\n"
    "ds_read_b64_tr_b16 a[40:41], v230 offset:8192\n"
    "s_add_u32 m0, s83, 0\n"
    "v_mfma_f32_16x16x32_bf16 v[212:215], a[56:59], a[236:239], v[212:215]\n"
    "ds_read_b64_tr_b16 a[42:43], v230 offset:8448\n"
    "s_xor_b32 s81, s80, 2097408\n"
    "v_mfma_f32_16x16x32_bf16 v[212:215], a[52:55], a[220:223], v[212:215]\n"
    "ds_read_b64_tr_b16 a[44:45], v230 offset:12288\n"
    "buffer_load_dwordx4 v225, s[48:51], s81 offen offset:0 lds\n"
    "v_mfma_f32_16x16x32_bf16 v[212:215], a[48:51], a[204:207], v[212:215]\n"
    "ds_read_b64_tr_b16 a[46:47], v230 offset:12544\n"
    "s_nop 2\n"
    "v_mfma_f32_16x16x32_bf16 v[120:123], a[96:99], a[192:195], v[120:123]\n"
    "v_mfma_f32_16x16x32_bf16 v[120:123], a[100:103], a[208:211], v[120:123]\n"
    "v_mfma_f32_16x16x32_bf16 v[120:123], a[104:107], a[224:227], v[120:123]\n"
    "v_mfma_f32_16x16x32_bf16 v[120:123], a[108:111], a[240:243], v[120:123]\n"
    "buffer_load_dwordx4 v226, s[48:51], s81 offen offset:1024 lds\n"
    "s_nop 2\n"
    "v_mfma_f32_16x16x32_bf16 v[152:155], a[108:111], a[244:247], v[152:155]\n"
    "v_mfma_f32_16x16x32_bf16 v[152:155], a[104:107], a[228:231], v[152:155]\n"
    "v_mfma_f32_16x16x32_bf16 v[152:155], a[100:103], a[212:215], v[152:155]\n"
    "v_mfma_f32_16x16x32_bf16 v[152:155], a[96:99], a[196:199], v[152:155]\n"
    "s_nop 2\n"
    "v_mfma_f32_16x16x32_bf16 v[184:187], a[96:99], a[200:203], v[184:187]\n"
    "ds_read_b64_tr_b16 a[48:49], v230 offset:8\n"
    "buffer_load_dwordx4 v227, s[48:51], s81 offen offset:2048 lds\n"
    "v_mfma_f32_16x16x32_bf16 v[184:187], a[100:103], a[216:219], v[184:187]\n"
    "ds_read_b64_tr_b16 a[50:51], v230 offset:264\n"
    "v_mfma_f32_16x16x32_bf16 v[184:187], a[104:107], a[232:235], v[184:187]\n"
    "ds_read_b64_tr_b16 a[52:53], v230 offset:4104\n"
    "v_mfma_f32_16x16x32_bf16 v[184:187], a[108:111], a[248:251], v[184:187]\n"
    "ds_read_b64_tr_b16 a[54:55], v230 offset:4360\n"
    "s_nop 2\n"
    "v_mfma_f32_16x16x32_bf16 v[124:127], a[112:115], a[192:195], v[124:127]\n"
    "ds_read_b64_tr_b16 a[56:57], v230 offset:8200\n"
    "v_mfma_f32_16x16x32_bf16 v[124:127], a[116:119], a[208:211], v[124:127]\n"
    "ds_read_b64_tr_b16 a[58:59], v230 offset:8456\n"
    "buffer_load_dwordx4 v228, s[48:51], s81 offen offset:3072 lds\n"
    "v_mfma_f32_16x16x32_bf16 v[124:127], a[120:123], a[224:227], v[124:127]\n"
    "ds_read_b64_tr_b16 a[60:61], v230 offset:12296\n"
    "v_mfma_f32_16x16x32_bf16 v[124:127], a[124:127], a[240:243], v[124:127]\n"
    "ds_read_b64_tr_b16 a[62:63], v230 offset:12552\n"
    "s_nop 1\n"
    "v_mfma_f32_16x16x32_bf16 v[156:159], a[124:127], a[244:247], v[156:159]\n"
    "v_xor_b32 v224, 128, v224\n"
    "v_xor_b32 v253, 128, v253\n"
    "v_mfma_f32_16x16x32_bf16 v[156:159], a[120:123], a[228:231], v[156:159]\n"
    "s_add_u32 m0, s42, 98304\n"
    "v_mfma_f32_16x16x32_bf16 v[156:159], a[116:119], a[212:215], v[156:159]\n"
    "buffer_load_dwordx4 v224, s[44:47], s72 offen offset:0 lds\n"
    "v_mfma_f32_16x16x32_bf16 v[156:159], a[112:115], a[196:199], v[156:159]\n"
    "s_nop 1\n"
    "v_mfma_f32_16x16x32_bf16 v[188:191], a[112:115], a[200:203], v[188:191]\n"
    "v_mfma_f32_16x16x32_bf16 v[188:191], a[116:119], a[216:219], v[188:191]\n"
    "v_mfma_f32_16x16x32_bf16 v[188:191], a[120:123], a[232:235], v[188:191]\n"
    "v_mfma_f32_16x16x32_bf16 v[188:191], a[124:127], a[248:251], v[188:191]\n"
    "buffer_load_dwordx4 v253, s[44:47], s72 offen offset:1024 lds\n"
    "s_nop 1\n"
    "s_waitcnt lgkmcnt(0)\n"
    "v_mfma_f32_16x16x32_bf16 v[128:131], a[32:35], a[192:195], v[128:131]\n"
    "ds_read_b64_tr_b16 a[96:97], v255 offset:0\n"
    "v_mfma_f32_16x16x32_bf16 v[128:131], a[36:39], a[208:211], v[128:131]\n"
    "ds_read_b64_tr_b16 a[98:99], v255 offset:256\n"
    "v_mfma_f32_16x16x32_bf16 v[128:131], a[40:43], a[224:227], v[128:131]\n"
    "ds_read_b64_tr_b16 a[100:101], v255 offset:4096\n"
    "v_mfma_f32_16x16x32_bf16 v[128:131], a[44:47], a[240:243], v[128:131]\n"
    "ds_read_b64_tr_b16 a[102:103], v255 offset:4352\n"
    "v_mfma_f32_16x16x32_bf16 v[160:163], a[44:47], a[244:247], v[160:163]\n"
    "ds_read_b64_tr_b16 a[104:105], v255 offset:8192\n"
    "buffer_load_dwordx4 v224, s[44:47], s73 offen offset:2048 lds\n"
    "v_mfma_f32_16x16x32_bf16 v[160:163], a[40:43], a[228:231], v[160:163]\n"
    "ds_read_b64_tr_b16 a[106:107], v255 offset:8448\n"
    "v_mfma_f32_16x16x32_bf16 v[160:163], a[36:39], a[212:215], v[160:163]\n"
    "ds_read_b64_tr_b16 a[108:109], v255 offset:12288\n"
    "v_mfma_f32_16x16x32_bf16 v[160:163], a[32:35], a[196:199], v[160:163]\n"
    "ds_read_b64_tr_b16 a[110:111], v255 offset:12544\n"
    "v_mfma_f32_16x16x32_bf16 v[192:195], a[32:35], a[200:203], v[192:195]\n"
    "ds_read_b64_tr_b16 a[112:113], v255 offset:8\n"
    "v_mfma_f32_16x16x32_bf16 v[192:195], a[36:39], a[216:219], v[192:195]\n"
    "ds_read_b64_tr_b16 a[114:115], v255 offset:264\n"
    "buffer_load_dwordx4 v253, s[44:47], s73 offen offset:3072 lds\n"
    "v_mfma_f32_16x16x32_bf16 v[192:195], a[40:43], a[232:235], v[192:195]\n"
    "ds_read_b64_tr_b16 a[116:117], v255 offset:4104\n"
    "s_add_u32 m0, s42, 102400\n"
    "v_mfma_f32_16x16x32_bf16 v[192:195], a[44:47], a[248:251], v[192:195]\n"
    "ds_read_b64_tr_b16 a[118:119], v255 offset:4360\n"
    "v_mfma_f32_16x16x32_bf16 v[216:219], a[44:47], a[252:255], v[216:219]\n"
    "ds_read_b64_tr_b16 a[120:121], v255 offset:8200\n"
    "v_mfma_f32_16x16x32_bf16 v[216:219], a[40:43], a[236:239], v[216:219]\n"
    "ds_read_b64_tr_b16 a[122:123], v255 offset:8456\n"
    "v_mfma_f32_16x16x32_bf16 v[216:219], a[36:39], a[220:223], v[216:219]\n"
    "ds_read_b64_tr_b16 a[124:125], v255 offset:12296\n"
    "buffer_load_dwordx4 v224, s[44:47], s74 offen offset:0 lds\n"
    "v_mfma_f32_16x16x32_bf16 v[216:219], a[32:35], a[204:207], v[216:219]\n"
    "ds_read_b64_tr_b16 a[126:127], v255 offset:12552\n"
    "v_mfma_f32_16x16x32_bf16 v[132:135], a[48:51], a[192:195], v[132:135]\n"
    "v_mfma_f32_16x16x32_bf16 v[132:135], a[52:55], a[208:211], v[132:135]\n"
    "v_mfma_f32_16x16x32_bf16 v[132:135], a[56:59], a[224:227], v[132:135]\n"
    "v_mfma_f32_16x16x32_bf16 v[132:135], a[60:63], a[240:243], v[132:135]\n"
    "buffer_load_dwordx4 v253, s[44:47], s74 offen offset:1024 lds\n"
    "v_mfma_f32_16x16x32_bf16 v[164:167], a[60:63], a[244:247], v[164:167]\n"
    "v_mfma_f32_16x16x32_bf16 v[164:167], a[56:59], a[228:231], v[164:167]\n"
    "v_mfma_f32_16x16x32_bf16 v[164:167], a[52:55], a[212:215], v[164:167]\n"
    "v_mfma_f32_16x16x32_bf16 v[164:167], a[48:51], a[196:199], v[164:167]\n"
    "s_waitcnt vmcnt(24) lgkmcnt(0)\n"
    "s_barrier\n"
    "v_mfma_f32_16x16x32_bf16 v[196:199], a[48:51], a[200:203], v[196:199]\n"
    "ds_read_b64_tr_b16 a[32:33], v229 offset:16384\n"
    "buffer_load_dwordx4 v224, s[44:47], s75 offen offset:2048 lds\n"
    "v_mfma_f32_16x16x32_bf16 v[196:199], a[52:55], a[216:219], v[196:199]\n"
    "ds_read_b64_tr_b16 a[34:35], v229 offset:16640\n"
    "v_mfma_f32_16x16x32_bf16 v[196:199], a[56:59], a[232:235], v[196:199]\n"
    "ds_read_b64_tr_b16 a[36:37], v229 offset:20480\n"
    "v_mfma_f32_16x16x32_bf16 v[196:199], a[60:63], a[248:251], v[196:199]\n"
    "ds_read_b64_tr_b16 a[38:39], v229 offset:20736\n"
    "v_mfma_f32_16x16x32_bf16 v[220:223], a[60:63], a[252:255], v[220:223]\n"
    "ds_read_b64_tr_b16 a[40:41], v229 offset:24576\n"
    "v_mfma_f32_16x16x32_bf16 v[220:223], a[56:59], a[236:239], v[220:223]\n"
    "ds_read_b64_tr_b16 a[42:43], v229 offset:24832\n"
    "buffer_load_dwordx4 v253, s[44:47], s75 offen offset:3072 lds\n"
    "v_mfma_f32_16x16x32_bf16 v[220:223], a[52:55], a[220:223], v[220:223]\n"
    "ds_read_b64_tr_b16 a[44:45], v229 offset:28672\n"
    "v_mfma_f32_16x16x32_bf16 v[220:223], a[48:51], a[204:207], v[220:223]\n"
    "ds_read_b64_tr_b16 a[46:47], v229 offset:28928\n"
    "v_mfma_f32_16x16x32_bf16 v[136:139], a[96:99], a[192:195], v[136:139]\n"
    "v_mfma_f32_16x16x32_bf16 v[136:139], a[100:103], a[208:211], v[136:139]\n"
    "buffer_load_dword v251, v247, s[64:67], s86 offen offset:1024\n"
    "v_mfma_f32_16x16x32_bf16 v[136:139], a[104:107], a[224:227], v[136:139]\n"
    "v_mfma_f32_16x16x32_bf16 v[136:139], a[108:111], a[240:243], v[136:139]\n"
    "v_mfma_f32_16x16x32_bf16 v[168:171], a[108:111], a[244:247], v[168:171]\n"
    "v_mfma_f32_16x16x32_bf16 v[168:171], a[104:107], a[228:231], v[168:171]\n"
    "buffer_load_dword v251, v247, s[64:67], s86 offen offset:2048\n"
    "v_mfma_f32_16x16x32_bf16 v[168:171], a[100:103], a[212:215], v[168:171]\n"
    "v_mfma_f32_16x16x32_bf16 v[168:171], a[96:99], a[196:199], v[168:171]\n"
    "s_add_u32 m0, s83, 65536\n"
    "v_mfma_f32_16x16x32_bf16 v[200:203], a[96:99], a[200:203], v[200:203]\n"
    "ds_read_b64_tr_b16 a[48:49], v229 offset:16392\n"
    "s_xor_b32 s81, s80, 2097536\n"
    "v_mfma_f32_16x16x32_bf16 v[200:203], a[100:103], a[216:219], v[200:203]\n"
    "ds_read_b64_tr_b16 a[50:51], v229 offset:16648\n"
    "buffer_load_dwordx4 v225, s[48:51], s81 offen offset:0 lds\n"
    "v_mfma_f32_16x16x32_bf16 v[200:203], a[104:107], a[232:235], v[200:203]\n"
    "ds_read_b64_tr_b16 a[52:53], v229 offset:20488\n"
    "v_mfma_f32_16x16x32_bf16 v[200:203], a[108:111], a[248:251], v[200:203]\n"
    "ds_read_b64_tr_b16 a[54:55], v229 offset:20744\n"
    "v_mfma_f32_16x16x32_bf16 v[140:143], a[112:115], a[192:195], v[140:143]\n"
    "ds_read_b64_tr_b16 a[56:57], v229 offset:24584\n"
    "v_mfma_f32_16x16x32_bf16 v[140:143], a[116:119], a[208:211], v[140:143]\n"
    "ds_read_b64_tr_b16 a[58:59], v229 offset:24840\n"
    "v_mfma_f32_16x16x32_bf16 v[140:143], a[120:123], a[224:227], v[140:143]\n"
    "ds_read_b64_tr_b16 a[60:61], v229 offset:28680\n"
    "buffer_load_dwordx4 v226, s[48:51], s81 offen offset:1024 lds\n"
    "v_mfma_f32_16x16x32_bf16 v[140:143], a[124:127], a[240:243], v[140:143]\n"
    "ds_read_b64_tr_b16 a[62:63], v229 offset:28936\n"
    "v_mfma_f32_16x16x32_bf16 v[172:175], a[124:127], a[244:247], v[172:175]\n"
    "v_mfma_f32_16x16x32_bf16 v[172:175], a[120:123], a[228:231], v[172:175]\n"
    "v_mfma_f32_16x16x32_bf16 v[172:175], a[116:119], a[212:215], v[172:175]\n"
    "v_mfma_f32_16x16x32_bf16 v[172:175], a[112:115], a[196:199], v[172:175]\n"
    "buffer_load_dwordx4 v227, s[48:51], s81 offen offset:2048 lds\n"
    "v_mfma_f32_16x16x32_bf16 v[204:207], a[112:115], a[200:203], v[204:207]\n"
    "v_mfma_f32_16x16x32_bf16 v[204:207], a[116:119], a[216:219], v[204:207]\n"
    "v_mfma_f32_16x16x32_bf16 v[204:207], a[120:123], a[232:235], v[204:207]\n"
    "v_mfma_f32_16x16x32_bf16 v[204:207], a[124:127], a[248:251], v[204:207]\n"
    "s_waitcnt lgkmcnt(0)\n"
    "v_mfma_f32_16x16x32_bf16 v[0:3], a[32:35], a[192:195], v[0:3]\n"
    "ds_read_b64_tr_b16 a[96:97], v231 offset:16384\n"
    "buffer_load_dwordx4 v228, s[48:51], s81 offen offset:3072 lds\n"
    "v_mfma_f32_16x16x32_bf16 v[0:3], a[36:39], a[208:211], v[0:3]\n"
    "ds_read_b64_tr_b16 a[98:99], v231 offset:16640\n"
    "v_mfma_f32_16x16x32_bf16 v[0:3], a[40:43], a[224:227], v[0:3]\n"
    "ds_read_b64_tr_b16 a[100:101], v231 offset:20480\n"
    "v_mfma_f32_16x16x32_bf16 v[0:3], a[44:47], a[240:243], v[0:3]\n"
    "ds_read_b64_tr_b16 a[102:103], v231 offset:20736\n"
    "v_mfma_f32_16x16x32_bf16 v[32:35], a[44:47], a[244:247], v[32:35]\n"
    "ds_read_b64_tr_b16 a[104:105], v231 offset:24576\n"
    "buffer_load_dword v251, v247, s[64:67], s86 offen offset:3072\n"
    "v_mfma_f32_16x16x32_bf16 v[32:35], a[40:43], a[228:231], v[32:35]\n"
    "ds_read_b64_tr_b16 a[106:107], v231 offset:24832\n"
    "v_mfma_f32_16x16x32_bf16 v[32:35], a[36:39], a[212:215], v[32:35]\n"
    "ds_read_b64_tr_b16 a[108:109], v231 offset:28672\n"
    "v_mfma_f32_16x16x32_bf16 v[32:35], a[32:35], a[196:199], v[32:35]\n"
    "ds_read_b64_tr_b16 a[110:111], v231 offset:28928\n"
    "v_mfma_f32_16x16x32_bf16 v[64:67], a[32:35], a[200:203], v[64:67]\n"
    "ds_read_b64_tr_b16 a[112:113], v231 offset:16392\n"
    "v_mfma_f32_16x16x32_bf16 v[64:67], a[36:39], a[216:219], v[64:67]\n"
    "s_waitcnt vmcnt(24)\n"
    "ds_read_b64_tr_b16 a[114:115], v231 offset:16648\n"
    "v_mfma_f32_16x16x32_bf16 v[64:67], a[40:43], a[232:235], v[64:67]\n"
    "ds_read_b64_tr_b16 a[116:117], v231 offset:20488\n"
    "v_mfma_f32_16x16x32_bf16 v[64:67], a[44:47], a[248:251], v[64:67]\n"
    "ds_read_b64_tr_b16 a[118:119], v231 offset:20744\n"
    "v_mfma_f32_16x16x32_bf16 v[96:99], a[44:47], a[252:255], v[96:99]\n"
    "ds_read_b64_tr_b16 a[120:121], v231 offset:24584\n"
    "v_mfma_f32_16x16x32_bf16 v[96:99], a[40:43], a[236:239], v[96:99]\n"
    "ds_read_b64_tr_b16 a[122:123], v231 offset:24840\n"
    "v_mfma_f32_16x16x32_bf16 v[96:99], a[36:39], a[220:223], v[96:99]\n"
    "ds_read_b64_tr_b16 a[124:125], v231 offset:28680\n"
    "v_mfma_f32_16x16x32_bf16 v[96:99], a[32:35], a[204:207], v[96:99]\n"
    "ds_read_b64_tr_b16 a[126:127], v231 offset:28936\n"
    "v_mfma_f32_16x16x32_bf16 v[4:7], a[48:51], a[192:195], v[4:7]\n"
    "ds_read_b128 a[128:131], v254 offset:32768\n"
    "v_mfma_f32_16x16x32_bf16 v[4:7], a[52:55], a[208:211], v[4:7]\n"
    "ds_read_b128 a[144:147], v254 offset:33280\n"
    "v_mfma_f32_16x16x32_bf16 v[4:7], a[56:59], a[224:227], v[4:7]\n"
    "ds_read_b128 a[132:135], v254 offset:34816\n"
    "v_mfma_f32_16x16x32_bf16 v[4:7], a[60:63], a[240:243], v[4:7]\n"
    "ds_read_b128 a[148:151], v254 offset:35328\n"
    "v_mfma_f32_16x16x32_bf16 v[36:39], a[60:63], a[244:247], v[36:39]\n"
    "ds_read_b128 a[136:139], v254 offset:36864\n"
    "v_mfma_f32_16x16x32_bf16 v[36:39], a[56:59], a[228:231], v[36:39]\n"
    "ds_read_b128 a[152:155], v254 offset:37376\n"
    "v_mfma_f32_16x16x32_bf16 v[36:39], a[52:55], a[212:215], v[36:39]\n"
    "ds_read_b128 a[140:143], v254 offset:38912\n"
    "v_mfma_f32_16x16x32_bf16 v[36:39], a[48:51], a[196:199], v[36:39]\n"
    "s_waitcnt vmcnt(20) lgkmcnt(7)\n"
    "s_barrier\n"
    "v_mfma_f32_16x16x32_bf16 v[68:71], a[48:51], a[200:203], v[68:71]\n"
    "ds_read_b64_tr_b16 a[32:33], v230 offset:16384\n"
    "ds_read_b128 a[156:159], v254 offset:39424\n"
    "v_mfma_f32_16x16x32_bf16 v[68:71], a[52:55], a[216:219], v[68:71]\n"
    "ds_read_b64_tr_b16 a[34:35], v230 offset:16640\n"
    "s_add_u32 m0, s83, 16384\n"
    "v_mfma_f32_16x16x32_bf16 v[68:71], a[56:59], a[232:235], v[68:71]\n"
    "ds_read_b64_tr_b16 a[36:37], v230 offset:20480\n"
    "s_xor_b32 s81, s80, 2097152\n"
    "v_mfma_f32_16x16x32_bf16 v[68:71], a[60:63], a[248:251], v[68:71]\n"
    "ds_read_b64_tr_b16 a[38:39], v230 offset:20736\n"
    "buffer_load_dwordx4 v225, s[48:51], s81 offen offset:0 lds\n"
    "v_mfma_f32_16x16x32_bf16 v[100:103], a[60:63], a[252:255], v[100:103]\n"
    "ds_read_b64_tr_b16 a[40:41], v230 offset:24576\n"
    "v_mfma_f32_16x16x32_bf16 v[100:103], a[56:59], a[236:239], v[100:103]\n"
    "ds_read_b64_tr_b16 a[42:43], v230 offset:24832\n"
    "v_mfma_f32_16x16x32_bf16 v[100:103], a[52:55], a[220:223], v[100:103]\n"
    "ds_read_b64_tr_b16 a[44:45], v230 offset:28672\n"
    "v_mfma_f32_16x16x32_bf16 v[100:103], a[48:51], a[204:207], v[100:103]\n"
    "ds_read_b64_tr_b16 a[46:47], v230 offset:28928\n"
    "v_mfma_f32_16x16x32_bf16 v[8:11], a[96:99], a[192:195], v[8:11]\n"
    "buffer_load_dwordx4 v226, s[48:51], s81 offen offset:1024 lds\n"
    "v_mfma_f32_16x16x32_bf16 v[8:11], a[100:103], a[208:211], v[8:11]\n"
    "v_mfma_f32_16x16x32_bf16 v[8:11], a[104:107], a[224:227], v[8:11]\n"
    "v_mfma_f32_16x16x32_bf16 v[8:11], a[108:111], a[240:243], v[8:11]\n"
    "v_mfma_f32_16x16x32_bf16 v[40:43], a[108:111], a[244:247], v[40:43]\n"
    "v_mfma_f32_16x16x32_bf16 v[40:43], a[104:107], a[228:231], v[40:43]\n"
    "buffer_load_dwordx4 v227, s[48:51], s81 offen offset:2048 lds\n"
    "v_mfma_f32_16x16x32_bf16 v[40:43], a[100:103], a[212:215], v[40:43]\n"
    "v_mfma_f32_16x16x32_bf16 v[40:43], a[96:99], a[196:199], v[40:43]\n"
    "v_mfma_f32_16x16x32_bf16 v[72:75], a[96:99], a[200:203], v[72:75]\n"
    "ds_read_b64_tr_b16 a[48:49], v230 offset:16392\n"
    "v_mfma_f32_16x16x32_bf16 v[72:75], a[100:103], a[216:219], v[72:75]\n"
    "ds_read_b64_tr_b16 a[50:51], v230 offset:16648\n"
    "v_mfma_f32_16x16x32_bf16 v[72:75], a[104:107], a[232:235], v[72:75]\n"
    "ds_read_b64_tr_b16 a[52:53], v230 offset:20488\n"
    "buffer_load_dwordx4 v228, s[48:51], s81 offen offset:3072 lds\n"
    "v_mfma_f32_16x16x32_bf16 v[72:75], a[108:111], a[248:251], v[72:75]\n"
    "ds_read_b64_tr_b16 a[54:55], v230 offset:20744\n"
    "v_mfma_f32_16x16x32_bf16 v[12:15], a[112:115], a[192:195], v[12:15]\n"
    "ds_read_b64_tr_b16 a[56:57], v230 offset:24584\n"
    "v_mfma_f32_16x16x32_bf16 v[12:15], a[116:119], a[208:211], v[12:15]\n"
    "ds_read_b64_tr_b16 a[58:59], v230 offset:24840\n"
    "v_mfma_f32_16x16x32_bf16 v[12:15], a[120:123], a[224:227], v[12:15]\n"
    "ds_read_b64_tr_b16 a[60:61], v230 offset:28680\n"
    "buffer_load_dword v251, v247, s[64:67], s87 offen offset:0\n"
    "v_mfma_f32_16x16x32_bf16 v[12:15], a[124:127], a[240:243], v[12:15]\n"
    "ds_read_b64_tr_b16 a[62:63], v230 offset:28936\n"
    "v_mfma_f32_16x16x32_bf16 v[44:47], a[124:127], a[244:247], v[44:47]\n"
    "s_waitcnt lgkmcnt(7)\n"
    "v_xor_b32 v224, 384, v224\n"
    "v_mfma_f32_16x16x32_bf16 v[44:47], a[120:123], a[228:231], v[44:47]\n"
    "s_add_u32 m0, s42, 131072\n"
    "v_xor_b32 v253, 384, v253\n"
    "v_mfma_f32_16x16x32_bf16 v[44:47], a[116:119], a[212:215], v[44:47]\n"
    "buffer_load_dwordx4 v224, s[44:47], s72 offen offset:0 lds\n"
    "v_mfma_f32_16x16x32_bf16 v[44:47], a[112:115], a[196:199], v[44:47]\n"
    "v_mfma_f32_16x16x32_bf16 v[76:79], a[112:115], a[200:203], v[76:79]\n"
    "v_mfma_f32_16x16x32_bf16 v[76:79], a[116:119], a[216:219], v[76:79]\n"
    "v_mfma_f32_16x16x32_bf16 v[76:79], a[120:123], a[232:235], v[76:79]\n"
    "v_mfma_f32_16x16x32_bf16 v[76:79], a[124:127], a[248:251], v[76:79]\n"
    "buffer_load_dwordx4 v253, s[44:47], s72 offen offset:1024 lds\n"
    "v_mfma_f32_16x16x32_bf16 v[16:19], a[32:35], a[192:195], v[16:19]\n"
    "ds_read_b64_tr_b16 a[96:97], v255 offset:16384\n"
    "v_mfma_f32_16x16x32_bf16 v[16:19], a[36:39], a[208:211], v[16:19]\n"
    "ds_read_b64_tr_b16 a[98:99], v255 offset:16640\n"
    "v_mfma_f32_16x16x32_bf16 v[16:19], a[40:43], a[224:227], v[16:19]\n"
    "ds_read_b64_tr_b16 a[100:101], v255 offset:20480\n"
    "v_mfma_f32_16x16x32_bf16 v[16:19], a[44:47], a[240:243], v[16:19]\n"
    "ds_read_b64_tr_b16 a[102:103], v255 offset:20736\n"
    "v_mfma_f32_16x16x32_bf16 v[48:51], a[44:47], a[244:247], v[48:51]\n"
    "ds_read_b64_tr_b16 a[104:105], v255 offset:24576\n"
    "buffer_load_dwordx4 v224, s[44:47], s73 offen offset:2048 lds\n"
    "v_mfma_f32_16x16x32_bf16 v[48:51], a[40:43], a[228:231], v[48:51]\n"
    "ds_read_b64_tr_b16 a[106:107], v255 offset:24832\n"
    "v_mfma_f32_16x16x32_bf16 v[48:51], a[36:39], a[212:215], v[48:51]\n"
    "ds_read_b64_tr_b16 a[108:109], v255 offset:28672\n"
    "v_mfma_f32_16x16x32_bf16 v[48:51], a[32:35], a[196:199], v[48:51]\n"
    "ds_read_b64_tr_b16 a[110:111], v255 offset:28928\n"
    "v_mfma_f32_16x16x32_bf16 v[80:83], a[32:35], a[200:203], v[80:83]\n"
    "ds_read_b64_tr_b16 a[112:113], v255 offset:16392\n"
    "v_mfma_f32_16x16x32_bf16 v[80:83], a[36:39], a[216:219], v[80:83]\n"
    "ds_read_b64_tr_b16 a[114:115], v255 offset:16648\n"
    "buffer_load_dwordx4 v253, s[44:47], s73 offen offset:3072 lds\n"
    "v_mfma_f32_16x16x32_bf16 v[80:83], a[40:43], a[232:235], v[80:83]\n"
    "ds_read_b64_tr_b16 a[116:117], v255 offset:20488\n"
    "s_add_u32 m0, s42, 135168\n"
    "v_mfma_f32_16x16x32_bf16 v[80:83], a[44:47], a[248:251], v[80:83]\n"
    "ds_read_b64_tr_b16 a[118:119], v255 offset:20744\n"
    "v_mfma_f32_16x16x32_bf16 v[104:107], a[44:47], a[252:255], v[104:107]\n"
    "ds_read_b64_tr_b16 a[120:121], v255 offset:24584\n"
    "v_mfma_f32_16x16x32_bf16 v[104:107], a[40:43], a[236:239], v[104:107]\n"
    "ds_read_b64_tr_b16 a[122:123], v255 offset:24840\n"
    "v_mfma_f32_16x16x32_bf16 v[104:107], a[36:39], a[220:223], v[104:107]\n"
    "ds_read_b64_tr_b16 a[124:125], v255 offset:28680\n"
    "buffer_load_dwordx4 v224, s[44:47], s74 offen offset:0 lds\n"
    "v_mfma_f32_16x16x32_bf16 v[104:107], a[32:35], a[204:207], v[104:107]\n"
    "ds_read_b64_tr_b16 a[126:127], v255 offset:28936\n"
    "s_waitcnt lgkmcnt(8)\n"
    "v_mfma_f32_16x16x32_bf16 v[20:23], a[48:51], a[192:195], v[20:23]\n"
    "v_mfma_f32_16x16x32_bf16 v[20:23], a[52:55], a[208:211], v[20:23]\n"
    "v_mfma_f32_16x16x32_bf16 v[20:23], a[56:59], a[224:227], v[20:23]\n"
    "v_mfma_f32_16x16x32_bf16 v[20:23], a[60:63], a[240:243], v[20:23]\n"
    "buffer_load_dwordx4 v253, s[44:47], s74 offen offset:1024 lds\n"
    "v_mfma_f32_16x16x32_bf16 v[52:55], a[60:63], a[244:247], v[52:55]\n"
    "v_mfma_f32_16x16x32_bf16 v[52:55], a[56:59], a[228:231], v[52:55]\n"
    "v_mfma_f32_16x16x32_bf16 v[52:55], a[52:55], a[212:215], v[52:55]\n"
    "v_mfma_f32_16x16x32_bf16 v[52:55], a[48:51], a[196:199], v[52:55]\n"
    "s_waitcnt vmcnt(26) lgkmcnt(0)\n"
    "s_barrier\n"
    "v_mfma_f32_16x16x32_bf16 v[84:87], a[48:51], a[200:203], v[84:87]\n"
    "ds_read_b64_tr_b16 a[32:33], v229 offset:0\n"
    "buffer_load_dwordx4 v224, s[44:47], s75 offen offset:2048 lds\n"
    "v_mfma_f32_16x16x32_bf16 v[84:87], a[52:55], a[216:219], v[84:87]\n"
    "ds_read_b64_tr_b16 a[34:35], v229 offset:256\n"
    "v_mfma_f32_16x16x32_bf16 v[84:87], a[56:59], a[232:235], v[84:87]\n"
    "ds_read_b64_tr_b16 a[36:37], v229 offset:4096\n"
    "v_mfma_f32_16x16x32_bf16 v[84:87], a[60:63], a[248:251], v[84:87]\n"
    "ds_read_b64_tr_b16 a[38:39], v229 offset:4352\n"
    "v_mfma_f32_16x16x32_bf16 v[108:111], a[60:63], a[252:255], v[108:111]\n"
    "s_waitcnt vmcnt(19)\n"
    "ds_read_b64_tr_b16 a[40:41], v229 offset:8192\n"
    "v_mfma_f32_16x16x32_bf16 v[108:111], a[56:59], a[236:239], v[108:111]\n"
    "ds_read_b64_tr_b16 a[42:43], v229 offset:8448\n"
    "buffer_load_dwordx4 v253, s[44:47], s75 offen offset:3072 lds\n"
    "v_mfma_f32_16x16x32_bf16 v[108:111], a[52:55], a[220:223], v[108:111]\n"
    "ds_read_b64_tr_b16 a[44:45], v229 offset:12288\n"
    "v_mfma_f32_16x16x32_bf16 v[108:111], a[48:51], a[204:207], v[108:111]\n"
    "ds_read_b64_tr_b16 a[46:47], v229 offset:12544\n"
    "v_mfma_f32_16x16x32_bf16 v[24:27], a[96:99], a[192:195], v[24:27]\n"
    "ds_read_b128 a[160:163], v254 offset:0\n"
    "v_mfma_f32_16x16x32_bf16 v[24:27], a[100:103], a[208:211], v[24:27]\n"
    "ds_read_b128 a[176:179], v254 offset:512\n"
    "buffer_load_dword v251, v247, s[64:67], s87 offen offset:1024\n"
    "v_mfma_f32_16x16x32_bf16 v[24:27], a[104:107], a[224:227], v[24:27]\n"
    "ds_read_b128 a[164:167], v254 offset:2048\n"
    "v_mfma_f32_16x16x32_bf16 v[24:27], a[108:111], a[240:243], v[24:27]\n"
    "ds_read_b128 a[180:183], v254 offset:2560\n"
    "s_add_u32 m0, s83, 81920\n"
    "v_mfma_f32_16x16x32_bf16 v[56:59], a[108:111], a[244:247], v[56:59]\n"
    "ds_read_b128 a[168:171], v254 offset:4096\n"
    "s_xor_b32 s81, s80, 2097280\n"
    "v_mfma_f32_16x16x32_bf16 v[56:59], a[104:107], a[228:231], v[56:59]\n"
    "ds_read_b128 a[184:187], v254 offset:4608\n"
    "buffer_load_dwordx4 v225, s[48:51], s81 offen offset:0 lds\n"
    "v_mfma_f32_16x16x32_bf16 v[56:59], a[100:103], a[212:215], v[56:59]\n"
    "ds_read_b128 a[172:175], v254 offset:6144\n"
    "v_mfma_f32_16x16x32_bf16 v[56:59], a[96:99], a[196:199], v[56:59]\n"
    "ds_read_b128 a[188:191], v254 offset:6656\n"
    "v_mfma_f32_16x16x32_bf16 v[88:91], a[96:99], a[200:203], v[88:91]\n"
    "ds_read_b64_tr_b16 a[48:49], v229 offset:8\n"
    "v_mfma_f32_16x16x32_bf16 v[88:91], a[100:103], a[216:219], v[88:91]\n"
    "ds_read_b64_tr_b16 a[50:51], v229 offset:264\n"
    "v_mfma_f32_16x16x32_bf16 v[88:91], a[104:107], a[232:235], v[88:91]\n"
    "ds_read_b64_tr_b16 a[52:53], v229 offset:4104\n"
    "buffer_load_dwordx4 v226, s[48:51], s81 offen offset:1024 lds\n"
    "v_mfma_f32_16x16x32_bf16 v[88:91], a[108:111], a[248:251], v[88:91]\n"
    "ds_read_b64_tr_b16 a[54:55], v229 offset:4360\n"
    "v_mfma_f32_16x16x32_bf16 v[28:31], a[112:115], a[192:195], v[28:31]\n"
    "ds_read_b64_tr_b16 a[56:57], v229 offset:8200\n"
    "v_mfma_f32_16x16x32_bf16 v[28:31], a[116:119], a[208:211], v[28:31]\n"
    "ds_read_b64_tr_b16 a[58:59], v229 offset:8456\n"
    "v_mfma_f32_16x16x32_bf16 v[28:31], a[120:123], a[224:227], v[28:31]\n"
    "ds_read_b64_tr_b16 a[60:61], v229 offset:12296\n"
    "v_mfma_f32_16x16x32_bf16 v[28:31], a[124:127], a[240:243], v[28:31]\n"
    "ds_read_b64_tr_b16 a[62:63], v229 offset:12552\n"
    "buffer_load_dwordx4 v227, s[48:51], s81 offen offset:2048 lds\n"
    "v_mfma_f32_16x16x32_bf16 v[60:63], a[124:127], a[244:247], v[60:63]\n"
    "v_mfma_f32_16x16x32_bf16 v[60:63], a[120:123], a[228:231], v[60:63]\n"
    "v_mfma_f32_16x16x32_bf16 v[60:63], a[116:119], a[212:215], v[60:63]\n"
    "v_mfma_f32_16x16x32_bf16 v[60:63], a[112:115], a[196:199], v[60:63]\n"
    "v_mfma_f32_16x16x32_bf16 v[92:95], a[112:115], a[200:203], v[92:95]\n"
    "buffer_load_dwordx4 v228, s[48:51], s81 offen offset:3072 lds\n"
    "v_mfma_f32_16x16x32_bf16 v[92:95], a[116:119], a[216:219], v[92:95]\n"
    "v_mfma_f32_16x16x32_bf16 v[92:95], a[120:123], a[232:235], v[92:95]\n"
    "v_mfma_f32_16x16x32_bf16 v[92:95], a[124:127], a[248:251], v[92:95]\n"
    "s_waitcnt lgkmcnt(0)\n"
    "v_mfma_f32_16x16x32_bf16 v[112:115], a[32:35], a[128:131], v[112:115]\n"
    "ds_read_b64_tr_b16 a[96:97], v231 offset:0\n"
    "buffer_load_dword v251, v247, s[64:67], s87 offen offset:2048\n"
    "v_mfma_f32_16x16x32_bf16 v[112:115], a[36:39], a[144:147], v[112:115]\n"
    "ds_read_b64_tr_b16 a[98:99], v231 offset:256\n"
    "v_mfma_f32_16x16x32_bf16 v[112:115], a[40:43], a[160:163], v[112:115]\n"
    "ds_read_b64_tr_b16 a[100:101], v231 offset:4096\n"
    "v_mfma_f32_16x16x32_bf16 v[112:115], a[44:47], a[176:179], v[112:115]\n"
    "ds_read_b64_tr_b16 a[102:103], v231 offset:4352\n"
    "v_xor_b32 v224, 128, v224\n"
    "v_mfma_f32_16x16x32_bf16 v[144:147], a[44:47], a[180:183], v[144:147]\n"
    "ds_read_b64_tr_b16 a[104:105], v231 offset:8192\n"
    "v_xor_b32 v253, 128, v253\n"
    "v_mfma_f32_16x16x32_bf16 v[144:147], a[40:43], a[164:167], v[144:147]\n"
    "ds_read_b64_tr_b16 a[106:107], v231 offset:8448\n"
    "s_add_u32 m0, s42, 98304\n"
    "v_mfma_f32_16x16x32_bf16 v[144:147], a[36:39], a[148:151], v[144:147]\n"
    "ds_read_b64_tr_b16 a[108:109], v231 offset:12288\n"
    "buffer_load_dwordx4 v224, s[44:47], s72 offen offset:0 lds\n"
    "v_mfma_f32_16x16x32_bf16 v[144:147], a[32:35], a[132:135], v[144:147]\n"
    "ds_read_b64_tr_b16 a[110:111], v231 offset:12544\n"
    "v_mfma_f32_16x16x32_bf16 v[176:179], a[32:35], a[136:139], v[176:179]\n"
    "ds_read_b64_tr_b16 a[112:113], v231 offset:8\n"
    "v_mfma_f32_16x16x32_bf16 v[176:179], a[36:39], a[152:155], v[176:179]\n"
    "L_h3:\n"
    "s_cbranch_execz L_ret1\n"
    "ds_read_b64_tr_b16 a[114:115], v231 offset:264\n"
    "v_mfma_f32_16x16x32_bf16 v[176:179], a[40:43], a[168:171], v[176:179]\n"
    "ds_read_b64_tr_b16 a[116:117], v231 offset:4104\n"
    "v_mfma_f32_16x16x32_bf16 v[176:179], a[44:47], a[184:187], v[176:179]\n"
    "ds_read_b64_tr_b16 a[118:119], v231 offset:4360\n"
    "buffer_load_dwordx4 v253, s[44:47], s72 offen offset:1024 lds\n"
    "v_mfma_f32_16x16x32_bf16 v[208:211], a[44:47], a[188:191], v[208:211]\n"
    "ds_read_b64_tr_b16 a[120:121], v231 offset:8200\n"
    "v_mfma_f32_16x16x32_bf16 v[208:211], a[40:43], a[172:175], v[208:211]\n"
    "ds_read_b64_tr_b16 a[122:123], v231 offset:8456\n"
    "v_mfma_f32_16x16x32_bf16 v[208:211], a[36:39], a[156:159], v[208:211]\n"
    "ds_read_b64_tr_b16 a[124:125], v231 offset:12296\n"
    "v_mfma_f32_16x16x32_bf16 v[208:211], a[32:35], a[140:143], v[208:211]\n"
    "ds_read_b64_tr_b16 a[126:127], v231 offset:12552\n"
    "v_mfma_f32_16x16x32_bf16 v[116:119], a[48:51], a[128:131], v[116:119]\n"
    "buffer_load_dwordx4 v224, s[44:47], s73 offen offset:2048 lds\n"
    "v_mfma_f32_16x16x32_bf16 v[116:119], a[52:55], a[144:147], v[116:119]\n"
    "v_mfma_f32_16x16x32_bf16 v[116:119], a[56:59], a[160:163], v[116:119]\n"
    "v_mfma_f32_16x16x32_bf16 v[116:119], a[60:63], a[176:179], v[116:119]\n"
    "v_mfma_f32_16x16x32_bf16 v[148:151], a[60:63], a[180:183], v[148:151]\n"
    "v_mfma_f32_16x16x32_bf16 v[148:151], a[56:59], a[164:167], v[148:151]\n"
    "buffer_load_dwordx4 v253, s[44:47], s73 offen offset:3072 lds\n"
    "s_add_u32 m0, s42, 102400\n"
    "v_mfma_f32_16x16x32_bf16 v[148:151], a[52:55], a[148:151], v[148:151]\n"
    "v_mfma_f32_16x16x32_bf16 v[148:151], a[48:51], a[132:135], v[148:151]\n"
    "s_waitcnt vmcnt(24) lgkmcnt(0)\n"
    "s_barrier\n"
    "v_mfma_f32_16x16x32_bf16 v[180:183], a[48:51], a[136:139], v[180:183]\n"
    "ds_read_b64_tr_b16 a[32:33], v230 offset:0\n"
    "v_mfma_f32_16x16x32_bf16 v[180:183], a[52:55], a[152:155], v[180:183]\n"
    "ds_read_b64_tr_b16 a[34:35], v230 offset:256\n"
    "v_mfma_f32_16x16x32_bf16 v[180:183], a[56:59], a[168:171], v[180:183]\n"
    "ds_read_b64_tr_b16 a[36:37], v230 offset:4096\n"
    "buffer_load_dwordx4 v224, s[44:47], s74 offen offset:0 lds\n"
    "v_mfma_f32_16x16x32_bf16 v[180:183], a[60:63], a[184:187], v[180:183]\n"
    "ds_read_b64_tr_b16 a[38:39], v230 offset:4352\n"
    "v_mfma_f32_16x16x32_bf16 v[212:215], a[60:63], a[188:191], v[212:215]\n"
    "ds_read_b64_tr_b16 a[40:41], v230 offset:8192\n"
    "v_mfma_f32_16x16x32_bf16 v[212:215], a[56:59], a[172:175], v[212:215]\n"
    "ds_read_b64_tr_b16 a[42:43], v230 offset:8448\n"
    "v_mfma_f32_16x16x32_bf16 v[212:215], a[52:55], a[156:159], v[212:215]\n"
    "ds_read_b64_tr_b16 a[44:45], v230 offset:12288\n"
    "v_mfma_f32_16x16x32_bf16 v[212:215], a[48:51], a[140:143], v[212:215]\n"
    "ds_read_b64_tr_b16 a[46:47], v230 offset:12544\n"
    "buffer_load_dwordx4 v253, s[44:47], s74 offen offset:1024 lds\n"
    "v_mfma_f32_16x16x32_bf16 v[120:123], a[96:99], a[128:131], v[120:123]\n"
    "v_mfma_f32_16x16x32_bf16 v[120:123], a[100:103], a[144:147], v[120:123]\n"
    "v_mfma_f32_16x16x32_bf16 v[120:123], a[104:107], a[160:163], v[120:123]\n"
    "v_mfma_f32_16x16x32_bf16 v[120:123], a[108:111], a[176:179], v[120:123]\n"
    "v_mfma_f32_16x16x32_bf16 v[152:155], a[108:111], a[180:183], v[152:155]\n"
    "buffer_load_dwordx4 v224, s[44:47], s75 offen offset:2048 lds\n"
    "v_mfma_f32_16x16x32_bf16 v[152:155], a[104:107], a[164:167], v[152:155]\n"
    "v_mfma_f32_16x16x32_bf16 v[152:155], a[100:103], a[148:151], v[152:155]\n"
    "v_mfma_f32_16x16x32_bf16 v[152:155], a[96:99], a[132:135], v[152:155]\n"
    "v_mfma_f32_16x16x32_bf16 v[184:187], a[96:99], a[136:139], v[184:187]\n"
    "ds_read_b64_tr_b16 a[48:49], v230 offset:8\n"
    "v_mfma_f32_16x16x32_bf16 v[184:187], a[100:103], a[152:155], v[184:187]\n"
    "ds_read_b64_tr_b16 a[50:51], v230 offset:264\n"
    "buffer_load_dwordx4 v253, s[44:47], s75 offen offset:3072 lds\n"
    "v_mfma_f32_16x16x32_bf16 v[184:187], a[104:107], a[168:171], v[184:187]\n"
    "ds_read_b64_tr_b16 a[52:53], v230 offset:4104\n"
    "v_mfma_f32_16x16x32_bf16 v[184:187], a[108:111], a[184:187], v[184:187]\n"
    "ds_read_b64_tr_b16 a[54:55], v230 offset:4360\n"
    "s_add_u32 m0, s83, 0\n"
    "v_mfma_f32_16x16x32_bf16 v[124:127], a[112:115], a[128:131], v[124:127]\n"
    "ds_read_b64_tr_b16 a[56:57], v230 offset:8200\n"
    "s_xor_b32 s81, s80, 3145984\n"
    "v_mfma_f32_16x16x32_bf16 v[124:127], a[116:119], a[144:147], v[124:127]\n"
    "ds_read_b64_tr_b16 a[58:59], v230 offset:8456\n"
    "buffer_load_dwordx4 v225, s[48:51], s81 offen offset:0 lds\n"
    "v_mfma_f32_16x16x32_bf16 v[124:127], a[120:123], a[160:163], v[124:127]\n"
    "ds_read_b64_tr_b16 a[60:61], v230 offset:12296\n"
    "v_mfma_f32_16x16x32_bf16 v[124:127], a[124:127], a[176:179], v[124:127]\n"
    "ds_read_b64_tr_b16 a[62:63], v230 offset:12552\n"
    "v_mfma_f32_16x16x32_bf16 v[156:159], a[124:127], a[180:183], v[156:159]\n"
    "v_mfma_f32_16x16x32_bf16 v[156:159], a[120:123], a[164:167], v[156:159]\n"
    "v_mfma_f32_16x16x32_bf16 v[156:159], a[116:119], a[148:151], v[156:159]\n"
    "buffer_load_dwordx4 v226, s[48:51], s81 offen offset:1024 lds\n"
    "v_mfma_f32_16x16x32_bf16 v[156:159], a[112:115], a[132:135], v[156:159]\n"
    "v_mfma_f32_16x16x32_bf16 v[188:191], a[112:115], a[136:139], v[188:191]\n"
    "v_mfma_f32_16x16x32_bf16 v[188:191], a[116:119], a[152:155], v[188:191]\n"
    "v_mfma_f32_16x16x32_bf16 v[188:191], a[120:123], a[168:171], v[188:191]\n"
    "v_mfma_f32_16x16x32_bf16 v[188:191], a[124:127], a[184:187], v[188:191]\n"
    "buffer_load_dwordx4 v227, s[48:51], s81 offen offset:2048 lds\n"
    "s_waitcnt lgkmcnt(0)\n"
    "v_mfma_f32_16x16x32_bf16 v[128:131], a[32:35], a[128:131], v[128:131]\n"
    "ds_read_b64_tr_b16 a[96:97], v255 offset:0\n"
    "v_mfma_f32_16x16x32_bf16 v[128:131], a[36:39], a[144:147], v[128:131]\n"
    "ds_read_b64_tr_b16 a[98:99], v255 offset:256\n"
    "v_mfma_f32_16x16x32_bf16 v[128:131], a[40:43], a[160:163], v[128:131]\n"
    "ds_read_b64_tr_b16 a[100:101], v255 offset:4096\n"
    "v_mfma_f32_16x16x32_bf16 v[128:131], a[44:47], a[176:179], v[128:131]\n"
    "ds_read_b64_tr_b16 a[102:103], v255 offset:4352\n"
    "v_mfma_f32_16x16x32_bf16 v[160:163], a[44:47], a[180:183], v[160:163]\n"
    "ds_read_b64_tr_b16 a[104:105], v255 offset:8192\n"
    "buffer_load_dwordx4 v228, s[48:51], s81 offen offset:3072 lds\n"
    "v_mfma_f32_16x16x32_bf16 v[160:163], a[40:43], a[164:167], v[160:163]\n"
    "ds_read_b64_tr_b16 a[106:107], v255 offset:8448\n"
    "v_mfma_f32_16x16x32_bf16 v[160:163], a[36:39], a[148:151], v[160:163]\n"
    "ds_read_b64_tr_b16 a[108:109], v255 offset:12288\n"
    "v_mfma_f32_16x16x32_bf16 v[160:163], a[32:35], a[132:135], v[160:163]\n"
    "ds_read_b64_tr_b16 a[110:111], v255 offset:12544\n"
    "v_mfma_f32_16x16x32_bf16 v[192:195], a[32:35], a[136:139], v[192:195]\n"
    "ds_read_b64_tr_b16 a[112:113], v255 offset:8\n"
    "buffer_load_dword v251, v247, s[64:67], s87 offen offset:3072\n"
    "v_mfma_f32_16x16x32_bf16 v[192:195], a[36:39], a[152:155], v[192:195]\n"
    "ds_read_b64_tr_b16 a[114:115], v255 offset:264\n"
    "v_mfma_f32_16x16x32_bf16 v[192:195], a[40:43], a[168:171], v[192:195]\n"
    "ds_read_b64_tr_b16 a[116:117], v255 offset:4104\n"
    "v_mfma_f32_16x16x32_bf16 v[192:195], a[44:47], a[184:187], v[192:195]\n"
    "ds_read_b64_tr_b16 a[118:119], v255 offset:4360\n"
    "v_mfma_f32_16x16x32_bf16 v[216:219], a[44:47], a[188:191], v[216:219]\n"
    "ds_read_b64_tr_b16 a[120:121], v255 offset:8200\n"
    "v_mfma_f32_16x16x32_bf16 v[216:219], a[40:43], a[172:175], v[216:219]\n"
    "ds_read_b64_tr_b16 a[122:123], v255 offset:8456\n"
    "v_mfma_f32_16x16x32_bf16 v[216:219], a[36:39], a[156:159], v[216:219]\n"
    "ds_read_b64_tr_b16 a[124:125], v255 offset:12296\n"
    "v_mfma_f32_16x16x32_bf16 v[216:219], a[32:35], a[140:143], v[216:219]\n"
    "ds_read_b64_tr_b16 a[126:127], v255 offset:12552\n"
    "v_mfma_f32_16x16x32_bf16 v[132:135], a[48:51], a[128:131], v[132:135]\n"
    "v_mfma_f32_16x16x32_bf16 v[132:135], a[52:55], a[144:147], v[132:135]\n"
    "v_mfma_f32_16x16x32_bf16 v[132:135], a[56:59], a[160:163], v[132:135]\n"
    "v_mfma_f32_16x16x32_bf16 v[132:135], a[60:63], a[176:179], v[132:135]\n"
    "v_mfma_f32_16x16x32_bf16 v[164:167], a[60:63], a[180:183], v[164:167]\n"
    "v_mfma_f32_16x16x32_bf16 v[164:167], a[56:59], a[164:167], v[164:167]\n"
    "s_add_u32 m0, s83, 32768\n"
    "v_mfma_f32_16x16x32_bf16 v[164:167], a[52:55], a[148:151], v[164:167]\n"
    "s_xor_b32 s81, s4, 0\n"
    "buffer_load_dwordx4 v240, s[52:55], s81 offen offset:0 nt lds\n"
    "v_mfma_f32_16x16x32_bf16 v[164:167], a[48:51], a[132:135], v[164:167]\n"
    "s_waitcnt vmcnt(29) lgkmcnt(0)\n"
    "s_barrier\n"
    "v_mfma_f32_16x16x32_bf16 v[196:199], a[48:51], a[136:139], v[196:199]\n"
    "ds_read_b64_tr_b16 a[32:33], v229 offset:16384\n"
    "s_add_u32 m0, s83, 65536\n"
    "v_mfma_f32_16x16x32_bf16 v[196:199], a[52:55], a[152:155], v[196:199]\n"
    "ds_read_b64_tr_b16 a[34:35], v229 offset:16640\n"
    "s_xor_b32 s81, s80, 3146112\n"
    "v_mfma_f32_16x16x32_bf16 v[196:199], a[56:59], a[168:171], v[196:199]\n"
    "ds_read_b64_tr_b16 a[36:37], v229 offset:20480\n"
    "buffer_load_dwordx4 v225, s[48:51], s81 offen offset:0 lds\n"
    "v_mfma_f32_16x16x32_bf16 v[196:199], a[60:63], a[184:187], v[196:199]\n"
    "ds_read_b64_tr_b16 a[38:39], v229 offset:20736\n"
    "v_mfma_f32_16x16x32_bf16 v[220:223], a[60:63], a[188:191], v[220:223]\n"
    "ds_read_b64_tr_b16 a[40:41], v229 offset:24576\n"
    "v_mfma_f32_16x16x32_bf16 v[220:223], a[56:59], a[172:175], v[220:223]\n"
    "ds_read_b64_tr_b16 a[42:43], v229 offset:24832\n"
    "v_mfma_f32_16x16x32_bf16 v[220:223], a[52:55], a[156:159], v[220:223]\n"
    "ds_read_b64_tr_b16 a[44:45], v229 offset:28672\n"
    "v_mfma_f32_16x16x32_bf16 v[220:223], a[48:51], a[140:143], v[220:223]\n"
    "ds_read_b64_tr_b16 a[46:47], v229 offset:28928\n"
    "buffer_load_dwordx4 v226, s[48:51], s81 offen offset:1024 lds\n"
    "v_mfma_f32_16x16x32_bf16 v[136:139], a[96:99], a[128:131], v[136:139]\n"
    "v_mfma_f32_16x16x32_bf16 v[136:139], a[100:103], a[144:147], v[136:139]\n"
    "v_mfma_f32_16x16x32_bf16 v[136:139], a[104:107], a[160:163], v[136:139]\n"
    "v_mfma_f32_16x16x32_bf16 v[136:139], a[108:111], a[176:179], v[136:139]\n"
    "v_mfma_f32_16x16x32_bf16 v[168:171], a[108:111], a[180:183], v[168:171]\n"
    "buffer_load_dwordx4 v227, s[48:51], s81 offen offset:2048 lds\n"
    "v_mfma_f32_16x16x32_bf16 v[168:171], a[104:107], a[164:167], v[168:171]\n"
    "v_mfma_f32_16x16x32_bf16 v[168:171], a[100:103], a[148:151], v[168:171]\n"
    "v_mfma_f32_16x16x32_bf16 v[168:171], a[96:99], a[132:135], v[168:171]\n"
    "v_mfma_f32_16x16x32_bf16 v[200:203], a[96:99], a[136:139], v[200:203]\n"
    "ds_read_b64_tr_b16 a[48:49], v229 offset:16392\n"
    "v_mfma_f32_16x16x32_bf16 v[200:203], a[100:103], a[152:155], v[200:203]\n"
    "s_waitcnt vmcnt(23)\n"
    "ds_read_b64_tr_b16 a[50:51], v229 offset:16648\n"
    "v_mfma_f32_16x16x32_bf16 v[200:203], a[104:107], a[168:171], v[200:203]\n"
    "ds_read_b64_tr_b16 a[52:53], v229 offset:20488\n"
    "buffer_load_dwordx4 v228, s[48:51], s81 offen offset:3072 lds\n"
    "v_mfma_f32_16x16x32_bf16 v[200:203], a[108:111], a[184:187], v[200:203]\n"
    "ds_read_b64_tr_b16 a[54:55], v229 offset:20744\n"
    "v_mfma_f32_16x16x32_bf16 v[140:143], a[112:115], a[128:131], v[140:143]\n"
    "ds_read_b64_tr_b16 a[56:57], v229 offset:24584\n"
    "s_add_u32 m0, s83, 32768\n"
    "v_mfma_f32_16x16x32_bf16 v[140:143], a[116:119], a[144:147], v[140:143]\n"
    "ds_read_b64_tr_b16 a[58:59], v229 offset:24840\n"
    "s_xor_b32 s81, s4, 0\n"
    "v_mfma_f32_16x16x32_bf16 v[140:143], a[120:123], a[160:163], v[140:143]\n"
    "ds_read_b64_tr_b16 a[60:61], v229 offset:28680\n"
    "buffer_load_dwordx4 v241, s[52:55], s81 offen offset:1024 nt lds\n"
    "v_mfma_f32_16x16x32_bf16 v[140:143], a[124:127], a[176:179], v[140:143]\n"
    "ds_read_b64_tr_b16 a[62:63], v229 offset:28936\n"
    "v_mfma_f32_16x16x32_bf16 v[172:175], a[124:127], a[180:183], v[172:175]\n"
    "ds_read_b128 a[192:195], v254 offset:32768\n"
    "v_mfma_f32_16x16x32_bf16 v[172:175], a[120:123], a[164:167], v[172:175]\n"
    "ds_read_b128 a[208:211], v254 offset:33280\n"
    "v_mfma_f32_16x16x32_bf16 v[172:175], a[116:119], a[148:151], v[172:175]\n"
    "ds_read_b128 a[196:199], v254 offset:34816\n"
    "v_mfma_f32_16x16x32_bf16 v[172:175], a[112:115], a[132:135], v[172:175]\n"
    "ds_read_b128 a[212:215], v254 offset:35328\n"
    "v_mfma_f32_16x16x32_bf16 v[204:207], a[112:115], a[136:139], v[204:207]\n"
    "ds_read_b128 a[200:203], v254 offset:36864\n"
    "s_waitcnt lgkmcnt(8)\n"
    "v_mfma_f32_16x16x32_bf16 v[204:207], a[116:119], a[152:155], v[204:207]\n"
    "ds_read_b128 a[216:219], v254 offset:37376\n"
    "s_add_u32 m0, s83, 32768\n"
    "v_mfma_f32_16x16x32_bf16 v[204:207], a[120:123], a[168:171], v[204:207]\n"
    "ds_read_b128 a[204:207], v254 offset:38912\n"
    "s_xor_b32 s81, s4, 0\n"
    "v_mfma_f32_16x16x32_bf16 v[204:207], a[124:127], a[184:187], v[204:207]\n"
    "ds_read_b128 a[220:223], v254 offset:39424\n"
    "buffer_load_dwordx4 v240, s[52:55], s81 offen offset:2048 nt lds\n"
    "v_mfma_f32_16x16x32_bf16 v[0:3], a[32:35], a[128:131], v[0:3]\n"
    "ds_read_b64_tr_b16 a[96:97], v231 offset:16384\n"
    "v_mfma_f32_16x16x32_bf16 v[0:3], a[36:39], a[144:147], v[0:3]\n"
    "ds_read_b64_tr_b16 a[98:99], v231 offset:16640\n"
    "v_mfma_f32_16x16x32_bf16 v[0:3], a[40:43], a[160:163], v[0:3]\n"
    "ds_read_b64_tr_b16 a[100:101], v231 offset:20480\n"
    "v_mfma_f32_16x16x32_bf16 v[0:3], a[44:47], a[176:179], v[0:3]\n"
    "ds_read_b64_tr_b16 a[102:103], v231 offset:20736\n"
    "v_mfma_f32_16x16x32_bf16 v[32:35], a[44:47], a[180:183], v[32:35]\n"
    "ds_read_b64_tr_b16 a[104:105], v231 offset:24576\n"
    "v_mfma_f32_16x16x32_bf16 v[32:35], a[40:43], a[164:167], v[32:35]\n"
    "ds_read_b64_tr_b16 a[106:107], v231 offset:24832\n"
    "v_mfma_f32_16x16x32_bf16 v[32:35], a[36:39], a[148:151], v[32:35]\n"
    "ds_read_b64_tr_b16 a[108:109], v231 offset:28672\n"
    "v_mfma_f32_16x16x32_bf16 v[32:35], a[32:35], a[132:135], v[32:35]\n"
    "ds_read_b64_tr_b16 a[110:111], v231 offset:28928\n"
    "v_mfma_f32_16x16x32_bf16 v[64:67], a[32:35], a[136:139], v[64:67]\n"
    "ds_read_b64_tr_b16 a[112:113], v231 offset:16392\n"
    "v_mfma_f32_16x16x32_bf16 v[64:67], a[36:39], a[152:155], v[64:67]\n"
    "ds_read_b64_tr_b16 a[114:115], v231 offset:16648\n"
    "v_mfma_f32_16x16x32_bf16 v[64:67], a[40:43], a[168:171], v[64:67]\n"
    "ds_read_b64_tr_b16 a[116:117], v231 offset:20488\n"
    "v_mfma_f32_16x16x32_bf16 v[64:67], a[44:47], a[184:187], v[64:67]\n"
    "ds_read_b64_tr_b16 a[118:119], v231 offset:20744\n"
    "v_mfma_f32_16x16x32_bf16 v[96:99], a[44:47], a[188:191], v[96:99]\n"
    "ds_read_b64_tr_b16 a[120:121], v231 offset:24584\n"
    "v_mfma_f32_16x16x32_bf16 v[96:99], a[40:43], a[172:175], v[96:99]\n"
    "ds_read_b64_tr_b16 a[122:123], v231 offset:24840\n"
    "s_add_u32 m0, s83, 32768\n"
    "v_mfma_f32_16x16x32_bf16 v[96:99], a[36:39], a[156:159], v[96:99]\n"
    "ds_read_b64_tr_b16 a[124:125], v231 offset:28680\n"
    "s_xor_b32 s81, s4, 0\n"
    "v_mfma_f32_16x16x32_bf16 v[96:99], a[32:35], a[140:143], v[96:99]\n"
    "ds_read_b64_tr_b16 a[126:127], v231 offset:28936\n"
    "buffer_load_dwordx4 v241, s[52:55], s81 offen offset:3072 nt lds\n"
    "s_mov_b32 s96, 0xfefc\n"
    "s_mov_b32 s97, 0x0\n"
    "s_mov_b64 exec, s[96:97]\n"
    "buffer_load_dword v246, v248, s[68:71], 0 offen offset:2048 sc1\n"
    "s_mov_b64 exec, -1\n"
    "v_mfma_f32_16x16x32_bf16 v[4:7], a[48:51], a[128:131], v[4:7]\n"
    "v_mfma_f32_16x16x32_bf16 v[4:7], a[52:55], a[144:147], v[4:7]\n"
    "s_waitcnt lgkmcnt(5)\n"
    "v_mfma_f32_16x16x32_bf16 v[4:7], a[56:59], a[160:163], v[4:7]\n"
    "v_mfma_f32_16x16x32_bf16 v[4:7], a[60:63], a[176:179], v[4:7]\n"
    "v_mfma_f32_16x16x32_bf16 v[36:39], a[60:63], a[180:183], v[36:39]\n"
    "v_mfma_f32_16x16x32_bf16 v[36:39], a[56:59], a[164:167], v[36:39]\n"
    "v_mfma_f32_16x16x32_bf16 v[36:39], a[52:55], a[148:151], v[36:39]\n"
    "v_mfma_f32_16x16x32_bf16 v[36:39], a[48:51], a[132:135], v[36:39]\n"
    "s_waitcnt vmcnt(23) lgkmcnt(0)\n"
    "s_barrier\n"
    "v_mfma_f32_16x16x32_bf16 v[68:71], a[48:51], a[136:139], v[68:71]\n"
    "ds_read_b64_tr_b16 a[32:33], v230 offset:16384\n"
    "s_add_u32 m0, s83, 16384\n"
    "v_mfma_f32_16x16x32_bf16 v[68:71], a[52:55], a[152:155], v[68:71]\n"
    "ds_read_b64_tr_b16 a[34:35], v230 offset:16640\n"
    "s_xor_b32 s81, s80, 3145728\n"
    "v_mfma_f32_16x16x32_bf16 v[68:71], a[56:59], a[168:171], v[68:71]\n"
    "ds_read_b64_tr_b16 a[36:37], v230 offset:20480\n"
    "buffer_load_dwordx4 v225, s[48:51], s81 offen offset:0 lds\n"
    "v_mfma_f32_16x16x32_bf16 v[68:71], a[60:63], a[184:187], v[68:71]\n"
    "ds_read_b64_tr_b16 a[38:39], v230 offset:20736\n"
    "v_mfma_f32_16x16x32_bf16 v[100:103], a[60:63], a[188:191], v[100:103]\n"
    "ds_read_b64_tr_b16 a[40:41], v230 offset:24576\n"
    "v_mfma_f32_16x16x32_bf16 v[100:103], a[56:59], a[172:175], v[100:103]\n"
    "ds_read_b64_tr_b16 a[42:43], v230 offset:24832\n"
    "v_mfma_f32_16x16x32_bf16 v[100:103], a[52:55], a[156:159], v[100:103]\n"
    "ds_read_b64_tr_b16 a[44:45], v230 offset:28672\n"
    "v_mfma_f32_16x16x32_bf16 v[100:103], a[48:51], a[140:143], v[100:103]\n"
    "ds_read_b64_tr_b16 a[46:47], v230 offset:28928\n"
    "buffer_load_dwordx4 v226, s[48:51], s81 offen offset:1024 lds\n"
    "v_mfma_f32_16x16x32_bf16 v[8:11], a[96:99], a[128:131], v[8:11]\n"
    "v_mfma_f32_16x16x32_bf16 v[8:11], a[100:103], a[144:147], v[8:11]\n"
    "v_mfma_f32_16x16x32_bf16 v[8:11], a[104:107], a[160:163], v[8:11]\n"
    "v_mfma_f32_16x16x32_bf16 v[8:11], a[108:111], a[176:179], v[8:11]\n"
    "v_mfma_f32_16x16x32_bf16 v[40:43], a[108:111], a[180:183], v[40:43]\n"
    "buffer_load_dwordx4 v227, s[48:51], s81 offen offset:2048 lds\n"
    "v_mfma_f32_16x16x32_bf16 v[40:43], a[104:107], a[164:167], v[40:43]\n"
    "v_mfma_f32_16x16x32_bf16 v[40:43], a[100:103], a[148:151], v[40:43]\n"
    "v_mfma_f32_16x16x32_bf16 v[40:43], a[96:99], a[132:135], v[40:43]\n"
    "v_mfma_f32_16x16x32_bf16 v[72:75], a[96:99], a[136:139], v[72:75]\n"
    "ds_read_b64_tr_b16 a[48:49], v230 offset:16392\n"
    "v_mfma_f32_16x16x32_bf16 v[72:75], a[100:103], a[152:155], v[72:75]\n"
    "ds_read_b64_tr_b16 a[50:51], v230 offset:16648\n"
    "buffer_load_dwordx4 v228, s[48:51], s81 offen offset:3072 lds\n"
    "v_mfma_f32_16x16x32_bf16 v[72:75], a[104:107], a[168:171], v[72:75]\n"
    "ds_read_b64_tr_b16 a[52:53], v230 offset:20488\n"
    "v_mfma_f32_16x16x32_bf16 v[72:75], a[108:111], a[184:187], v[72:75]\n"
    "ds_read_b64_tr_b16 a[54:55], v230 offset:20744\n"
    "s_add_u32 m0, s83, 49152\n"
    "v_mfma_f32_16x16x32_bf16 v[12:15], a[112:115], a[128:131], v[12:15]\n"
    "ds_read_b64_tr_b16 a[56:57], v230 offset:24584\n"
    "s_xor_b32 s81, s4, 16384\n"
    "v_mfma_f32_16x16x32_bf16 v[12:15], a[116:119], a[144:147], v[12:15]\n"
    "ds_read_b64_tr_b16 a[58:59], v230 offset:24840\n"
    "buffer_load_dwordx4 v240, s[52:55], s81 offen offset:0 nt lds\n"
    "v_mfma_f32_16x16x32_bf16 v[12:15], a[120:123], a[160:163], v[12:15]\n"
    "ds_read_b64_tr_b16 a[60:61], v230 offset:28680\n"
    "v_mfma_f32_16x16x32_bf16 v[12:15], a[124:127], a[176:179], v[12:15]\n"
    "ds_read_b64_tr_b16 a[62:63], v230 offset:28936\n"
    "s_add_u32 m0, s83, 49152\n"
    "v_mfma_f32_16x16x32_bf16 v[44:47], a[124:127], a[180:183], v[44:47]\n"
    "s_xor_b32 s81, s4, 16384\n"
    "v_mfma_f32_16x16x32_bf16 v[44:47], a[120:123], a[164:167], v[44:47]\n"
    "buffer_load_dwordx4 v241, s[52:55], s81 offen offset:1024 nt lds\n"
    "v_mfma_f32_16x16x32_bf16 v[44:47], a[116:119], a[148:151], v[44:47]\n"
    "s_waitcnt vmcnt(20)\n"
    "ds_read_b128 a[224:227], v254 offset:0\n"
    "v_mfma_f32_16x16x32_bf16 v[44:47], a[112:115], a[132:135], v[44:47]\n"
    "ds_read_b128 a[240:243], v254 offset:512\n"
    "v_mfma_f32_16x16x32_bf16 v[76:79], a[112:115], a[136:139], v[76:79]\n"
    "ds_read_b128 a[228:231], v254 offset:2048\n"
    "v_mfma_f32_16x16x32_bf16 v[76:79], a[116:119], a[152:155], v[76:79]\n"
    "ds_read_b128 a[244:247], v254 offset:2560\n"
    "v_mfma_f32_16x16x32_bf16 v[76:79], a[120:123], a[168:171], v[76:79]\n"
    "ds_read_b128 a[232:235], v254 offset:4096\n"
    "v_mfma_f32_16x16x32_bf16 v[76:79], a[124:127], a[184:187], v[76:79]\n"
    "ds_read_b128 a[248:251], v254 offset:4608\n"
    "s_waitcnt lgkmcnt(6)\n"
    "v_mfma_f32_16x16x32_bf16 v[16:19], a[32:35], a[128:131], v[16:19]\n"
    "ds_read_b64_tr_b16 a[96:97], v255 offset:16384\n"
    "v_mfma_f32_16x16x32_bf16 v[16:19], a[36:39], a[144:147], v[16:19]\n"
    "ds_read_b64_tr_b16 a[98:99], v255 offset:16640\n"
    "v_mfma_f32_16x16x32_bf16 v[16:19], a[40:43], a[160:163], v[16:19]\n"
    "ds_read_b64_tr_b16 a[100:101], v255 offset:20480\n"
    "v_mfma_f32_16x16x32_bf16 v[16:19], a[44:47], a[176:179], v[16:19]\n"
    "ds_read_b64_tr_b16 a[102:103], v255 offset:20736\n"
    "v_mfma_f32_16x16x32_bf16 v[48:51], a[44:47], a[180:183], v[48:51]\n"
    "ds_read_b64_tr_b16 a[104:105], v255 offset:24576\n"
    "v_mfma_f32_16x16x32_bf16 v[48:51], a[40:43], a[164:167], v[48:51]\n"
    "ds_read_b64_tr_b16 a[106:107], v255 offset:24832\n"
    "s_add_u32 m0, s83, 49152\n"
    "v_mfma_f32_16x16x32_bf16 v[48:51], a[36:39], a[148:151], v[48:51]\n"
    "ds_read_b64_tr_b16 a[108:109], v255 offset:28672\n"
    "s_xor_b32 s81, s4, 16384\n"
    "v_mfma_f32_16x16x32_bf16 v[48:51], a[32:35], a[132:135], v[48:51]\n"
    "ds_read_b64_tr_b16 a[110:111], v255 offset:28928\n"
    "buffer_load_dwordx4 v240, s[52:55], s81 offen offset:2048 nt lds\n"
    "v_mfma_f32_16x16x32_bf16 v[80:83], a[32:35], a[136:139], v[80:83]\n"
    "ds_read_b64_tr_b16 a[112:113], v255 offset:16392\n"
    "v_mfma_f32_16x16x32_bf16 v[80:83], a[36:39], a[152:155], v[80:83]\n"
    "ds_read_b64_tr_b16 a[114:115], v255 offset:16648\n"
    "v_mfma_f32_16x16x32_bf16 v[80:83], a[40:43], a[168:171], v[80:83]\n"
    "ds_read_b64_tr_b16 a[116:117], v255 offset:20488\n"
    "v_mfma_f32_16x16x32_bf16 v[80:83], a[44:47], a[184:187], v[80:83]\n"
    "ds_read_b64_tr_b16 a[118:119], v255 offset:20744\n"
    "v_mfma_f32_16x16x32_bf16 v[104:107], a[44:47], a[188:191], v[104:107]\n"
    "ds_read_b64_tr_b16 a[120:121], v255 offset:24584\n"
    "v_mfma_f32_16x16x32_bf16 v[104:107], a[40:43], a[172:175], v[104:107]\n"
    "ds_read_b64_tr_b16 a[122:123], v255 offset:24840\n"
    "v_mfma_f32_16x16x32_bf16 v[104:107], a[36:39], a[156:159], v[104:107]\n"
    "ds_read_b64_tr_b16 a[124:125], v255 offset:28680\n"
    "v_mfma_f32_16x16x32_bf16 v[104:107], a[32:35], a[140:143], v[104:107]\n"
    "ds_read_b64_tr_b16 a[126:127], v255 offset:28936\n"
    "v_mfma_f32_16x16x32_bf16 v[20:23], a[48:51], a[128:131], v[20:23]\n"
    "ds_read_b128 a[236:239], v254 offset:6144\n"
    "ds_read_b128 a[252:255], v254 offset:6656\n"
    "v_mfma_f32_16x16x32_bf16 v[20:23], a[52:55], a[144:147], v[20:23]\n"
    "v_mfma_f32_16x16x32_bf16 v[20:23], a[56:59], a[160:163], v[20:23]\n"
    "v_mfma_f32_16x16x32_bf16 v[20:23], a[60:63], a[176:179], v[20:23]\n"
    "v_mfma_f32_16x16x32_bf16 v[52:55], a[60:63], a[180:183], v[52:55]\n"
    "v_mfma_f32_16x16x32_bf16 v[52:55], a[56:59], a[164:167], v[52:55]\n"
    "s_add_u32 m0, s83, 49152\n"
    "v_mfma_f32_16x16x32_bf16 v[52:55], a[52:55], a[148:151], v[52:55]\n"
    "s_xor_b32 s81, s4, 16384\n"
    "buffer_load_dwordx4 v241, s[52:55], s81 offen offset:3072 nt lds\n"
    "v_mfma_f32_16x16x32_bf16 v[52:55], a[48:51], a[132:135], v[52:55]\n"
    "s_waitcnt vmcnt(18) lgkmcnt(0)\n"
    "s_barrier\n"
    "v_mfma_f32_16x16x32_bf16 v[84:87], a[48:51], a[136:139], v[84:87]\n"
    "ds_read_b64_tr_b16 a[32:33], v229 offset:0\n"
    "s_add_u32 m0, s83, 81920\n"
    "v_mfma_f32_16x16x32_bf16 v[84:87], a[52:55], a[152:155], v[84:87]\n"
    "ds_read_b64_tr_b16 a[34:35], v229 offset:256\n"
    "s_xor_b32 s81, s80, 3145856\n"
    "v_mfma_f32_16x16x32_bf16 v[84:87], a[56:59], a[168:171], v[84:87]\n"
    "ds_read_b64_tr_b16 a[36:37], v229 offset:4096\n"
    "buffer_load_dwordx4 v225, s[48:51], s81 offen offset:0 lds\n"
    "v_mfma_f32_16x16x32_bf16 v[84:87], a[60:63], a[184:187], v[84:87]\n"
    "ds_read_b64_tr_b16 a[38:39], v229 offset:4352\n"
    "v_mfma_f32_16x16x32_bf16 v[108:111], a[60:63], a[188:191], v[108:111]\n"
    "ds_read_b64_tr_b16 a[40:41], v229 offset:8192\n"
    "v_mfma_f32_16x16x32_bf16 v[108:111], a[56:59], a[172:175], v[108:111]\n"
    "ds_read_b64_tr_b16 a[42:43], v229 offset:8448\n"
    "v_mfma_f32_16x16x32_bf16 v[108:111], a[52:55], a[156:159], v[108:111]\n"
    "ds_read_b64_tr_b16 a[44:45], v229 offset:12288\n"
    "v_mfma_f32_16x16x32_bf16 v[108:111], a[48:51], a[140:143], v[108:111]\n"
    "ds_read_b64_tr_b16 a[46:47], v229 offset:12544\n"
    "buffer_load_dwordx4 v226, s[48:51], s81 offen offset:1024 lds\n"
    "v_mfma_f32_16x16x32_bf16 v[24:27], a[96:99], a[128:131], v[24:27]\n"
    "v_mfma_f32_16x16x32_bf16 v[24:27], a[100:103], a[144:147], v[24:27]\n"
    "v_mfma_f32_16x16x32_bf16 v[24:27], a[104:107], a[160:163], v[24:27]\n"
    "v_mfma_f32_16x16x32_bf16 v[24:27], a[108:111], a[176:179], v[24:27]\n"
    "v_mfma_f32_16x16x32_bf16 v[56:59], a[108:111], a[180:183], v[56:59]\n"
    "buffer_load_dwordx4 v227, s[48:51], s81 offen offset:2048 lds\n"
    "v_mfma_f32_16x16x32_bf16 v[56:59], a[104:107], a[164:167], v[56:59]\n"
    "v_mfma_f32_16x16x32_bf16 v[56:59], a[100:103], a[148:151], v[56:59]\n"
    "v_mfma_f32_16x16x32_bf16 v[56:59], a[96:99], a[132:135], v[56:59]\n"
    "v_mfma_f32_16x16x32_bf16 v[88:91], a[96:99], a[136:139], v[88:91]\n"
    "ds_read_b64_tr_b16 a[48:49], v229 offset:8\n"
    "v_mfma_f32_16x16x32_bf16 v[88:91], a[100:103], a[152:155], v[88:91]\n"
    "ds_read_b64_tr_b16 a[50:51], v229 offset:264\n"
    "buffer_load_dwordx4 v228, s[48:51], s81 offen offset:3072 lds\n"
    "v_mfma_f32_16x16x32_bf16 v[88:91], a[104:107], a[168:171], v[88:91]\n"
    "ds_read_b64_tr_b16 a[52:53], v229 offset:4104\n"
    "v_mfma_f32_16x16x32_bf16 v[88:91], a[108:111], a[184:187], v[88:91]\n"
    "ds_read_b64_tr_b16 a[54:55], v229 offset:4360\n"
    "s_add_u32 m0, s83, 98304\n"
    "v_mfma_f32_16x16x32_bf16 v[28:31], a[112:115], a[128:131], v[28:31]\n"
    "ds_read_b64_tr_b16 a[56:57], v229 offset:8200\n"
    "s_xor_b32 s81, s4, 0\n"
    "v_mfma_f32_16x16x32_bf16 v[28:31], a[116:119], a[144:147], v[28:31]\n"
    "ds_read_b64_tr_b16 a[58:59], v229 offset:8456\n"
    "buffer_load_dwordx4 v240, s[56:59], s81 offen offset:0 nt lds\n"
    "v_mfma_f32_16x16x32_bf16 v[28:31], a[120:123], a[160:163], v[28:31]\n"
    "ds_read_b64_tr_b16 a[60:61], v229 offset:12296\n"
    "v_mfma_f32_16x16x32_bf16 v[28:31], a[124:127], a[176:179], v[28:31]\n"
    "ds_read_b64_tr_b16 a[62:63], v229 offset:12552\n"
    "v_mfma_f32_16x16x32_bf16 v[60:63], a[124:127], a[180:183], v[60:63]\n"
    "v_mfma_f32_16x16x32_bf16 v[60:63], a[120:123], a[164:167], v[60:63]\n"
    "v_mfma_f32_16x16x32_bf16 v[60:63], a[116:119], a[148:151], v[60:63]\n"
    "v_mfma_f32_16x16x32_bf16 v[60:63], a[112:115], a[132:135], v[60:63]\n"
    "v_mfma_f32_16x16x32_bf16 v[92:95], a[112:115], a[136:139], v[92:95]\n"
    "v_mfma_f32_16x16x32_bf16 v[92:95], a[116:119], a[152:155], v[92:95]\n"
    "s_add_u32 m0, s83, 98304\n"
    "v_mfma_f32_16x16x32_bf16 v[92:95], a[120:123], a[168:171], v[92:95]\n"
    "s_xor_b32 s81, s4, 0\n"
    "buffer_load_dwordx4 v241, s[56:59], s81 offen offset:1024 nt lds\n"
    "v_mfma_f32_16x16x32_bf16 v[92:95], a[124:127], a[184:187], v[92:95]\n"
    "s_waitcnt lgkmcnt(0)\n"
    "v_mfma_f32_16x16x32_bf16 v[112:115], a[32:35], a[192:195], v[112:115]\n"
    "ds_read_b64_tr_b16 a[96:97], v231 offset:0\n"
    "v_mfma_f32_16x16x32_bf16 v[112:115], a[36:39], a[208:211], v[112:115]\n"
    "ds_read_b64_tr_b16 a[98:99], v231 offset:256\n"
    "v_mfma_f32_16x16x32_bf16 v[112:115], a[40:43], a[224:227], v[112:115]\n"
    "ds_read_b64_tr_b16 a[100:101], v231 offset:4096\n"
    "v_mfma_f32_16x16x32_bf16 v[112:115], a[44:47], a[240:243], v[112:115]\n"
    "ds_read_b64_tr_b16 a[102:103], v231 offset:4352\n"
    "s_mov_b64 s[44:45], s[18:19]\n"
    "v_mfma_f32_16x16x32_bf16 v[144:147], a[44:47], a[244:247], v[144:147]\n"
    "ds_read_b64_tr_b16 a[104:105], v231 offset:8192\n"
    "s_and_b32 s45, s45, 0xffff\n"
    "v_mfma_f32_16x16x32_bf16 v[144:147], a[40:43], a[228:231], v[144:147]\n"
    "ds_read_b64_tr_b16 a[106:107], v231 offset:8448\n"
    "s_mov_b32 s46, s5\n"
    "v_mfma_f32_16x16x32_bf16 v[144:147], a[36:39], a[212:215], v[144:147]\n"
    "ds_read_b64_tr_b16 a[108:109], v231 offset:12288\n"
    "s_mov_b32 s47, 0x20000\n"
    "v_mfma_f32_16x16x32_bf16 v[144:147], a[32:35], a[196:199], v[144:147]\n"
    "ds_read_b64_tr_b16 a[110:111], v231 offset:12544\n"
    "s_mov_b32 s48, s68\n"
    "v_mfma_f32_16x16x32_bf16 v[176:179], a[32:35], a[200:203], v[176:179]\n"
    "ds_read_b64_tr_b16 a[112:113], v231 offset:8\n"
    "s_mov_b32 s49, s69\n"
    "v_mfma_f32_16x16x32_bf16 v[176:179], a[36:39], a[216:219], v[176:179]\n"
    "ds_read_b64_tr_b16 a[114:115], v231 offset:264\n"
    "s_mov_b32 s50, s70\n"
    "v_mfma_f32_16x16x32_bf16 v[176:179], a[40:43], a[232:235], v[176:179]\n"
    "ds_read_b64_tr_b16 a[116:117], v231 offset:4104\n"
    "v_mfma_f32_16x16x32_bf16 v[176:179], a[44:47], a[248:251], v[176:179]\n"
    "ds_read_b64_tr_b16 a[118:119], v231 offset:4360\n"
    "v_mfma_f32_16x16x32_bf16 v[208:211], a[44:47], a[252:255], v[208:211]\n"
    "ds_read_b64_tr_b16 a[120:121], v231 offset:8200\n"
    "v_mfma_f32_16x16x32_bf16 v[208:211], a[40:43], a[236:239], v[208:211]\n"
    "ds_read_b64_tr_b16 a[122:123], v231 offset:8456\n"
    "s_add_u32 m0, s83, 98304\n"
    "v_mfma_f32_16x16x32_bf16 v[208:211], a[36:39], a[220:223], v[208:211]\n"
    "ds_read_b64_tr_b16 a[124:125], v231 offset:12296\n"
    "s_xor_b32 s81, s4, 0\n"
    "v_mfma_f32_16x16x32_bf16 v[208:211], a[32:35], a[204:207], v[208:211]\n"
    "ds_read_b64_tr_b16 a[126:127], v231 offset:12552\n"
    "buffer_load_dwordx4 v240, s[56:59], s81 offen offset:2048 nt lds\n"
    "v_mfma_f32_16x16x32_bf16 v[116:119], a[48:51], a[192:195], v[116:119]\n"
    "v_mfma_f32_16x16x32_bf16 v[116:119], a[52:55], a[208:211], v[116:119]\n"
    "v_mfma_f32_16x16x32_bf16 v[116:119], a[56:59], a[224:227], v[116:119]\n"
    "v_mfma_f32_16x16x32_bf16 v[116:119], a[60:63], a[240:243], v[116:119]\n"
    "v_mfma_f32_16x16x32_bf16 v[148:151], a[60:63], a[244:247], v[148:151]\n"
    "v_mfma_f32_16x16x32_bf16 v[148:151], a[56:59], a[228:231], v[148:151]\n"
    "v_mfma_f32_16x16x32_bf16 v[148:151], a[52:55], a[212:215], v[148:151]\n"
    "v_mfma_f32_16x16x32_bf16 v[148:151], a[48:51], a[196:199], v[148:151]\n"
    "s_waitcnt vmcnt(19) lgkmcnt(0)\n"
    "s_barrier\n"
    "v_mfma_f32_16x16x32_bf16 v[180:183], a[48:51], a[200:203], v[180:183]\n"
    "ds_read_b64_tr_b16 a[32:33], v230 offset:0\n"
    "v_mfma_f32_16x16x32_bf16 v[180:183], a[52:55], a[216:219], v[180:183]\n"
    "ds_read_b64_tr_b16 a[34:35], v230 offset:256\n"
    "v_mfma_f32_16x16x32_bf16 v[180:183], a[56:59], a[232:235], v[180:183]\n"
    "ds_read_b64_tr_b16 a[36:37], v230 offset:4096\n"
    "v_mfma_f32_16x16x32_bf16 v[180:183], a[60:63], a[248:251], v[180:183]\n"
    "ds_read_b64_tr_b16 a[38:39], v230 offset:4352\n"
    "v_mfma_f32_16x16x32_bf16 v[212:215], a[60:63], a[252:255], v[212:215]\n"
    "ds_read_b64_tr_b16 a[40:41], v230 offset:8192\n"
    "v_mfma_f32_16x16x32_bf16 v[212:215], a[56:59], a[236:239], v[212:215]\n"
    "ds_read_b64_tr_b16 a[42:43], v230 offset:8448\n"
    "s_add_u32 m0, s83, 98304\n"
    "v_mfma_f32_16x16x32_bf16 v[212:215], a[52:55], a[220:223], v[212:215]\n"
    "ds_read_b64_tr_b16 a[44:45], v230 offset:12288\n"
    "s_xor_b32 s81, s4, 0\n"
    "v_mfma_f32_16x16x32_bf16 v[212:215], a[48:51], a[204:207], v[212:215]\n"
    "ds_read_b64_tr_b16 a[46:47], v230 offset:12544\n"
    "buffer_load_dwordx4 v241, s[56:59], s81 offen offset:3072 nt lds\n"
    "v_mfma_f32_16x16x32_bf16 v[120:123], a[96:99], a[192:195], v[120:123]\n"
    "v_mfma_f32_16x16x32_bf16 v[120:123], a[100:103], a[208:211], v[120:123]\n"
    "v_mfma_f32_16x16x32_bf16 v[120:123], a[104:107], a[224:227], v[120:123]\n"
    "v_mfma_f32_16x16x32_bf16 v[120:123], a[108:111], a[240:243], v[120:123]\n"
    "v_mul_f32 v208, s84, v208\n"
    "v_mul_f32 v209, s84, v209\n"
    "v_mfma_f32_16x16x32_bf16 v[152:155], a[108:111], a[244:247], v[152:155]\n"
    "v_mul_f32 v210, s84, v210\n"
    "v_mul_f32 v211, s84, v211\n"
    "v_mul_f32 v212, s84, v212\n"
    "v_mfma_f32_16x16x32_bf16 v[152:155], a[104:107], a[228:231], v[152:155]\n"
    "v_mul_f32 v213, s84, v213\n"
    "v_mul_f32 v214, s84, v214\n"
    "v_mfma_f32_16x16x32_bf16 v[152:155], a[100:103], a[212:215], v[152:155]\n"
    "v_mul_f32 v215, s84, v215\n"
    "v_cvt_pk_bf16_f32 v208, v208, v209\n"
    "v_cvt_pk_bf16_f32 v209, v210, v211\n"
    "v_mfma_f32_16x16x32_bf16 v[152:155], a[96:99], a[196:199], v[152:155]\n"
    "v_cvt_pk_bf16_f32 v210, v212, v213\n"
    "v_cvt_pk_bf16_f32 v211, v214, v215\n"
    "v_mfma_f32_16x16x32_bf16 v[184:187], a[96:99], a[200:203], v[184:187]\n"
    "ds_read_b64_tr_b16 a[48:49], v230 offset:8\n"
    "ds_write_b128 v244, v[208:211] offset:2048\n"
    "v_mfma_f32_16x16x32_bf16 v[184:187], a[100:103], a[216:219], v[184:187]\n"
    "ds_read_b64_tr_b16 a[50:51], v230 offset:264\n"
    "v_accvgpr_write_b32 a176, v208\n"
    "v_accvgpr_write_b32 a177, v209\n"
    "v_mfma_f32_16x16x32_bf16 v[184:187], a[104:107], a[232:235], v[184:187]\n"
    "ds_read_b64_tr_b16 a[52:53], v230 offset:4104\n"
    "v_accvgpr_write_b32 a178, v210\n"
    "L_h4:\n"
    "s_cbranch_execz L_ret1\n"
    "v_mfma_f32_16x16x32_bf16 v[184:187], a[108:111], a[248:251], v[184:187]\n"
    "ds_read_b64_tr_b16 a[54:55], v230 offset:4360\n"
    "v_accvgpr_write_b32 a179, v211\n"
    "v_mul_f32 v112, s84, v112\n"
    "v_mfma_f32_16x16x32_bf16 v[124:127], a[112:115], a[192:195], v[124:127]\n"
    "ds_read_b64_tr_b16 a[56:57], v230 offset:8200\n"
    "v_mul_f32 v113, s84, v113\n"
    "v_mul_f32 v114, s84, v114\n"
    "v_mfma_f32_16x16x32_bf16 v[124:127], a[116:119], a[208:211], v[124:127]\n"
    "ds_read_b64_tr_b16 a[58:59], v230 offset:8456\n"
    "s_add_u32 m0, s83, 114688\n"
    "v_mfma_f32_16x16x32_bf16 v[124:127], a[120:123], a[224:227], v[124:127]\n"
    "ds_read_b64_tr_b16 a[60:61], v230 offset:12296\n"
    "s_xor_b32 s81, s4, 16384\n"
    "v_mul_f32 v115, s84, v115\n"
    "v_mfma_f32_16x16x32_bf16 v[124:127], a[124:127], a[240:243], v[124:127]\n"
    "ds_read_b64_tr_b16 a[62:63], v230 offset:12552\n"
    "buffer_load_dwordx4 v240, s[56:59], s81 offen offset:0 nt lds\n"
    "v_mfma_f32_16x16x32_bf16 v[156:159], a[124:127], a[244:247], v[156:159]\n"
    "v_mul_f32 v116, s84, v116\n"
    "v_mul_f32 v117, s84, v117\n"
    "v_mul_f32 v118, s84, v118\n"
    "v_mfma_f32_16x16x32_bf16 v[156:159], a[120:123], a[228:231], v[156:159]\n"
    "v_mul_f32 v119, s84, v119\n"
    "v_cvt_pk_bf16_f32 v112, v112, v113\n"
    "v_cvt_pk_bf16_f32 v113, v114, v115\n"
    "v_mfma_f32_16x16x32_bf16 v[156:159], a[116:119], a[212:215], v[156:159]\n"
    "v_cvt_pk_bf16_f32 v114, v116, v117\n"
    "v_cvt_pk_bf16_f32 v115, v118, v119\n"
    "v_mfma_f32_16x16x32_bf16 v[156:159], a[112:115], a[196:199], v[156:159]\n"
    "v_accvgpr_write_b32 a128, v112\n"
    "v_accvgpr_write_b32 a129, v113\n"
    "v_accvgpr_write_b32 a130, v114\n"
    "v_mfma_f32_16x16x32_bf16 v[188:191], a[112:115], a[200:203], v[188:191]\n"
    "v_accvgpr_write_b32 a131, v115\n"
    "v_mul_f32 v144, s84, v144\n"
    "v_mfma_f32_16x16x32_bf16 v[188:191], a[116:119], a[216:219], v[188:191]\n"
    "v_mul_f32 v145, s84, v145\n"
    "v_mul_f32 v146, s84, v146\n"
    "v_mul_f32 v147, s84, v147\n"
    "v_mfma_f32_16x16x32_bf16 v[188:191], a[120:123], a[232:235], v[188:191]\n"
    "v_mul_f32 v148, s84, v148\n"
    "v_mul_f32 v149, s84, v149\n"
    "v_mul_f32 v150, s84, v150\n"
    "v_mfma_f32_16x16x32_bf16 v[188:191], a[124:127], a[248:251], v[188:191]\n"
    "v_mul_f32 v151, s84, v151\n"
    "v_cvt_pk_bf16_f32 v144, v144, v145\n"
    "s_waitcnt lgkmcnt(0)\n"
    "v_mfma_f32_16x16x32_bf16 v[128:131], a[32:35], a[192:195], v[128:131]\n"
    "ds_read_b64_tr_b16 a[96:97], v255 offset:0\n"
    "v_cvt_pk_bf16_f32 v145, v146, v147\n"
    "v_cvt_pk_bf16_f32 v146, v148, v149\n"
    "v_mfma_f32_16x16x32_bf16 v[128:131], a[36:39], a[208:211], v[128:131]\n"
    "ds_read_b64_tr_b16 a[98:99], v255 offset:256\n"
    "v_cvt_pk_bf16_f32 v147, v150, v151\n"
    "v_mfma_f32_16x16x32_bf16 v[128:131], a[40:43], a[224:227], v[128:131]\n"
    "ds_read_b64_tr_b16 a[100:101], v255 offset:4096\n"
    "v_accvgpr_write_b32 a144, v144\n"
    "v_accvgpr_write_b32 a145, v145\n"
    "v_mfma_f32_16x16x32_bf16 v[128:131], a[44:47], a[240:243], v[128:131]\n"
    "ds_read_b64_tr_b16 a[102:103], v255 offset:4352\n"
    "v_accvgpr_write_b32 a146, v146\n"
    "v_accvgpr_write_b32 a147, v147\n"
    "v_mfma_f32_16x16x32_bf16 v[160:163], a[44:47], a[244:247], v[160:163]\n"
    "ds_read_b64_tr_b16 a[104:105], v255 offset:8192\n"
    "v_mul_f32 v176, s84, v176\n"
    "v_mfma_f32_16x16x32_bf16 v[160:163], a[40:43], a[228:231], v[160:163]\n"
    "ds_read_b64_tr_b16 a[106:107], v255 offset:8448\n"
    "s_add_u32 m0, s83, 114688\n"
    "v_mul_f32 v177, s84, v177\n"
    "v_mfma_f32_16x16x32_bf16 v[160:163], a[36:39], a[212:215], v[160:163]\n"
    "ds_read_b64_tr_b16 a[108:109], v255 offset:12288\n"
    "s_xor_b32 s81, s4, 16384\n"
    "v_mfma_f32_16x16x32_bf16 v[160:163], a[32:35], a[196:199], v[160:163]\n"
    "ds_read_b64_tr_b16 a[110:111], v255 offset:12544\n"
    "buffer_load_dwordx4 v241, s[56:59], s81 offen offset:1024 nt lds\n"
    "v_mul_f32 v178, s84, v178\n"
    "v_mfma_f32_16x16x32_bf16 v[192:195], a[32:35], a[200:203], v[192:195]\n"
    "ds_read_b64_tr_b16 a[112:113], v255 offset:8\n"
    "v_mul_f32 v179, s84, v179\n"
    "v_mul_f32 v180, s84, v180\n"
    "v_mfma_f32_16x16x32_bf16 v[192:195], a[36:39], a[216:219], v[192:195]\n"
    "ds_read_b64_tr_b16 a[114:115], v255 offset:264\n"
    "v_mul_f32 v181, s84, v181\n"
    "v_mfma_f32_16x16x32_bf16 v[192:195], a[40:43], a[232:235], v[192:195]\n"
    "ds_read_b64_tr_b16 a[116:117], v255 offset:4104\n"
    "v_mul_f32 v182, s84, v182\n"
    "v_mul_f32 v183, s84, v183\n"
    "v_mfma_f32_16x16x32_bf16 v[192:195], a[44:47], a[248:251], v[192:195]\n"
    "ds_read_b64_tr_b16 a[118:119], v255 offset:4360\n"
    "v_cvt_pk_bf16_f32 v176, v176, v177\n"
    "v_mfma_f32_16x16x32_bf16 v[216:219], a[44:47], a[252:255], v[216:219]\n"
    "ds_read_b64_tr_b16 a[120:121], v255 offset:8200\n"
    "v_cvt_pk_bf16_f32 v177, v178, v179\n"
    "v_cvt_pk_bf16_f32 v178, v180, v181\n"
    "v_mfma_f32_16x16x32_bf16 v[216:219], a[40:43], a[236:239], v[216:219]\n"
    "ds_read_b64_tr_b16 a[122:123], v255 offset:8456\n"
    "v_cvt_pk_bf16_f32 v179, v182, v183\n"
    "v_accvgpr_write_b32 a160, v176\n"
    "v_mfma_f32_16x16x32_bf16 v[216:219], a[36:39], a[220:223], v[216:219]\n"
    "ds_read_b64_tr_b16 a[124:125], v255 offset:12296\n"
    "v_accvgpr_write_b32 a161, v177\n"
    "v_mfma_f32_16x16x32_bf16 v[216:219], a[32:35], a[204:207], v[216:219]\n"
    "ds_read_b64_tr_b16 a[126:127], v255 offset:12552\n"
    "v_accvgpr_write_b32 a162, v178\n"
    "v_accvgpr_write_b32 a163, v179\n"
    "v_mfma_f32_16x16x32_bf16 v[132:135], a[48:51], a[192:195], v[132:135]\n"
    "v_mul_f32 v120, s84, v120\n"
    "v_mul_f32 v121, s84, v121\n"
    "v_mfma_f32_16x16x32_bf16 v[132:135], a[52:55], a[208:211], v[132:135]\n"
    "v_mul_f32 v122, s84, v122\n"
    "v_mul_f32 v123, s84, v123\n"
    "v_mul_f32 v124, s84, v124\n"
    "v_mfma_f32_16x16x32_bf16 v[132:135], a[56:59], a[224:227], v[132:135]\n"
    "v_mul_f32 v125, s84, v125\n"
    "v_mul_f32 v126, s84, v126\n"
    "v_mul_f32 v127, s84, v127\n"
    "v_mfma_f32_16x16x32_bf16 v[132:135], a[60:63], a[240:243], v[132:135]\n"
    "v_cvt_pk_bf16_f32 v120, v120, v121\n"
    "v_cvt_pk_bf16_f32 v121, v122, v123\n"
    "v_mfma_f32_16x16x32_bf16 v[164:167], a[60:63], a[244:247], v[164:167]\n"
    "v_cvt_pk_bf16_f32 v122, v124, v125\n"
    "v_cvt_pk_bf16_f32 v123, v126, v127\n"
    "v_accvgpr_write_b32 a132, v120\n"
    "v_mfma_f32_16x16x32_bf16 v[164:167], a[56:59], a[228:231], v[164:167]\n"
    "s_add_u32 m0, s83, 114688\n"
    "v_accvgpr_write_b32 a133, v121\n"
    "v_mfma_f32_16x16x32_bf16 v[164:167], a[52:55], a[212:215], v[164:167]\n"
    "s_xor_b32 s81, s4, 16384\n"
    "buffer_load_dwordx4 v240, s[56:59], s81 offen offset:2048 nt lds\n"
    "v_accvgpr_write_b32 a134, v122\n"
    "v_mfma_f32_16x16x32_bf16 v[164:167], a[48:51], a[196:199], v[164:167]\n"
    "s_waitcnt vmcnt(15) lgkmcnt(0)\n"
    "s_barrier\n"
    "v_accvgpr_write_b32 a135, v123\n"
    "v_and_b32 v247, 1, v246\n"
    "v_lshl_add_u32 v247, v247, 13, v248\n"
    "s_mov_b32 s96, 0xfefc\n"
    "s_mov_b32 s97, 0x0\n"
    "s_mov_b64 exec, s[96:97]\n"
    "buffer_store_dword v246, v247, s[68:71], 0 offen offset:0 sc1\n"
    "s_mov_b64 exec, -1\n"
    "v_mfma_f32_16x16x32_bf16 v[196:199], a[48:51], a[200:203], v[196:199]\n"
    "ds_read_b64_tr_b16 a[32:33], v229 offset:16384\n"
    "v_mul_f32 v152, s84, v152\n"
    "v_mfma_f32_16x16x32_bf16 v[196:199], a[52:55], a[216:219], v[196:199]\n"
    "ds_read_b64_tr_b16 a[34:35], v229 offset:16640\n"
    "v_mul_f32 v153, s84, v153\n"
    "v_mul_f32 v154, s84, v154\n"
    "v_mfma_f32_16x16x32_bf16 v[196:199], a[56:59], a[232:235], v[196:199]\n"
    "ds_read_b64_tr_b16 a[36:37], v229 offset:20480\n"
    "v_mul_f32 v155, s84, v155\n"
    "v_mfma_f32_16x16x32_bf16 v[196:199], a[60:63], a[248:251], v[196:199]\n"
    "ds_read_b64_tr_b16 a[38:39], v229 offset:20736\n"
    "v_mul_f32 v156, s84, v156\n"
    "v_mul_f32 v157, s84, v157\n"
    "v_mfma_f32_16x16x32_bf16 v[220:223], a[60:63], a[252:255], v[220:223]\n"
    "ds_read_b64_tr_b16 a[40:41], v229 offset:24576\n"
    "v_mul_f32 v158, s84, v158\n"
    "v_mul_f32 v159, s84, v159\n"
    "v_mfma_f32_16x16x32_bf16 v[220:223], a[56:59], a[236:239], v[220:223]\n"
    "ds_read_b64_tr_b16 a[42:43], v229 offset:24832\n"
    "v_cvt_pk_bf16_f32 v152, v152, v153\n"
    "v_mfma_f32_16x16x32_bf16 v[220:223], a[52:55], a[220:223], v[220:223]\n"
    "ds_read_b64_tr_b16 a[44:45], v229 offset:28672\n"
    "v_cvt_pk_bf16_f32 v153, v154, v155\n"
    "v_cvt_pk_bf16_f32 v154, v156, v157\n"
    "v_mfma_f32_16x16x32_bf16 v[220:223], a[48:51], a[204:207], v[220:223]\n"
    "ds_read_b64_tr_b16 a[46:47], v229 offset:28928\n"
    "v_cvt_pk_bf16_f32 v155, v158, v159\n"
    "v_mfma_f32_16x16x32_bf16 v[136:139], a[96:99], a[192:195], v[136:139]\n"
    "v_accvgpr_write_b32 a148, v152\n"
    "v_accvgpr_write_b32 a149, v153\n"
    "v_accvgpr_write_b32 a150, v154\n"
    "v_mfma_f32_16x16x32_bf16 v[136:139], a[100:103], a[208:211], v[136:139]\n"
    "v_accvgpr_write_b32 a151, v155\n"
    "v_mul_f32 v184, s84, v184\n"
    "v_mul_f32 v185, s84, v185\n"
    "v_mfma_f32_16x16x32_bf16 v[136:139], a[104:107], a[224:227], v[136:139]\n"
    "v_mul_f32 v186, s84, v186\n"
    "v_mul_f32 v187, s84, v187\n"
    "v_mfma_f32_16x16x32_bf16 v[136:139], a[108:111], a[240:243], v[136:139]\n"
    "v_mul_f32 v188, s84, v188\n"
    "v_mul_f32 v189, s84, v189\n"
    "v_mul_f32 v190, s84, v190\n"
    "v_mfma_f32_16x16x32_bf16 v[168:171], a[108:111], a[244:247], v[168:171]\n"
    "v_mul_f32 v191, s84, v191\n"
    "v_cvt_pk_bf16_f32 v184, v184, v185\n"
    "v_mfma_f32_16x16x32_bf16 v[168:171], a[104:107], a[228:231], v[168:171]\n"
    "s_add_u32 m0, s83, 114688\n"
    "v_cvt_pk_bf16_f32 v185, v186, v187\n"
    "v_cvt_pk_bf16_f32 v186, v188, v189\n"
    "v_mfma_f32_16x16x32_bf16 v[168:171], a[100:103], a[212:215], v[168:171]\n"
    "s_xor_b32 s81, s4, 16384\n"
    "buffer_load_dwordx4 v241, s[56:59], s81 offen offset:3072 nt lds\n"
    "v_cvt_pk_bf16_f32 v187, v190, v191\n"
    "v_mfma_f32_16x16x32_bf16 v[168:171], a[96:99], a[196:199], v[168:171]\n"
    "v_accvgpr_write_b32 a164, v184\n"
    "v_accvgpr_write_b32 a165, v185\n"
    "v_mfma_f32_16x16x32_bf16 v[200:203], a[96:99], a[200:203], v[200:203]\n"
    "ds_read_b64_tr_b16 a[48:49], v229 offset:16392\n"
    "v_accvgpr_write_b32 a166, v186\n"
    "v_accvgpr_write_b32 a167, v187\n"
    "v_mfma_f32_16x16x32_bf16 v[200:203], a[100:103], a[216:219], v[200:203]\n"
    "ds_read_b64_tr_b16 a[50:51], v229 offset:16648\n"
    "v_mul_f32 v216, s84, v216\n"
    "v_mfma_f32_16x16x32_bf16 v[200:203], a[104:107], a[232:235], v[200:203]\n"
    "ds_read_b64_tr_b16 a[52:53], v229 offset:20488\n"
    "v_mul_f32 v217, s84, v217\n"
    "v_mul_f32 v218, s84, v218\n"
    "v_mfma_f32_16x16x32_bf16 v[200:203], a[108:111], a[248:251], v[200:203]\n"
    "ds_read_b64_tr_b16 a[54:55], v229 offset:20744\n"
    "v_mul_f32 v219, s84, v219\n"
    "v_mul_f32 v220, s84, v220\n"
    "v_mfma_f32_16x16x32_bf16 v[140:143], a[112:115], a[192:195], v[140:143]\n"
    "ds_read_b64_tr_b16 a[56:57], v229 offset:24584\n"
    "v_mul_f32 v221, s84, v221\n"
    "v_mfma_f32_16x16x32_bf16 v[140:143], a[116:119], a[208:211], v[140:143]\n"
    "ds_read_b64_tr_b16 a[58:59], v229 offset:24840\n"
    "v_mul_f32 v222, s84, v222\n"
    "v_mul_f32 v223, s84, v223\n"
    "v_mfma_f32_16x16x32_bf16 v[140:143], a[120:123], a[224:227], v[140:143]\n"
    "ds_read_b64_tr_b16 a[60:61], v229 offset:28680\n"
    "v_cvt_pk_bf16_f32 v216, v216, v217\n"
    "v_mfma_f32_16x16x32_bf16 v[140:143], a[124:127], a[240:243], v[140:143]\n"
    "ds_read_b64_tr_b16 a[62:63], v229 offset:28936\n"
    "v_cvt_pk_bf16_f32 v217, v218, v219\n"
    "v_cvt_pk_bf16_f32 v218, v220, v221\n"
    "v_mfma_f32_16x16x32_bf16 v[172:175], a[124:127], a[244:247], v[172:175]\n"
    "v_cvt_pk_bf16_f32 v219, v222, v223\n"
    "ds_write_b128 v244, v[216:219] offset:3072\n"
    "v_mfma_f32_16x16x32_bf16 v[172:175], a[120:123], a[228:231], v[172:175]\n"
    "v_accvgpr_write_b32 a184, v216\n"
    "v_accvgpr_write_b32 a185, v217\n"
    "v_mfma_f32_16x16x32_bf16 v[172:175], a[116:119], a[212:215], v[172:175]\n"
    "v_accvgpr_write_b32 a186, v218\n"
    "v_accvgpr_write_b32 a187, v219\n"
    "v_mul_f32 v128, s84, v128\n"
    "v_mfma_f32_16x16x32_bf16 v[172:175], a[112:115], a[196:199], v[172:175]\n"
    "v_mul_f32 v129, s84, v129\n"
    "v_mul_f32 v130, s84, v130\n"
    "v_mfma_f32_16x16x32_bf16 v[204:207], a[112:115], a[200:203], v[204:207]\n"
    "v_mul_f32 v131, s84, v131\n"
    "v_mul_f32 v132, s84, v132\n"
    "v_mul_f32 v133, s84, v133\n"
    "v_mfma_f32_16x16x32_bf16 v[204:207], a[116:119], a[216:219], v[204:207]\n"
    "s_add_u32 m0, s83, 0\n"
    "v_mul_f32 v134, s84, v134\n"
    "v_mul_f32 v135, s84, v135\n"
    "v_mfma_f32_16x16x32_bf16 v[204:207], a[120:123], a[232:235], v[204:207]\n"
    "s_xor_b32 s81, s4, 32768\n"
    "buffer_load_dwordx4 v240, s[52:55], s81 offen offset:0 nt lds\n"
    "v_mfma_f32_16x16x32_bf16 v[204:207], a[124:127], a[248:251], v[204:207]\n"
    "v_cvt_pk_bf16_f32 v128, v128, v129\n"
    "v_cvt_pk_bf16_f32 v129, v130, v131\n"
    "v_cvt_pk_bf16_f32 v130, v132, v133\n"
    "s_waitcnt lgkmcnt(0)\n"
    "v_mfma_f32_16x16x32_bf16 v[0:3], a[32:35], a[192:195], v[0:3]\n"
    "ds_read_b64_tr_b16 a[96:97], v231 offset:16384\n"
    "v_cvt_pk_bf16_f32 v131, v134, v135\n"
    "v_mfma_f32_16x16x32_bf16 v[0:3], a[36:39], a[208:211], v[0:3]\n"
    "ds_read_b64_tr_b16 a[98:99], v231 offset:16640\n"
    "v_accvgpr_write_b32 a136, v128\n"
    "v_accvgpr_write_b32 a137, v129\n"
    "v_mfma_f32_16x16x32_bf16 v[0:3], a[40:43], a[224:227], v[0:3]\n"
    "ds_read_b64_tr_b16 a[100:101], v231 offset:20480\n"
    "v_accvgpr_write_b32 a138, v130\n"
    "v_accvgpr_write_b32 a139, v131\n"
    "v_mfma_f32_16x16x32_bf16 v[0:3], a[44:47], a[240:243], v[0:3]\n"
    "ds_read_b64_tr_b16 a[102:103], v231 offset:20736\n"
    "v_mul_f32 v160, s84, v160\n"
    "v_mfma_f32_16x16x32_bf16 v[32:35], a[44:47], a[244:247], v[32:35]\n"
    "ds_read_b64_tr_b16 a[104:105], v231 offset:24576\n"
    "v_mul_f32 v161, s84, v161\n"
    "v_mul_f32 v162, s84, v162\n"
    "v_mfma_f32_16x16x32_bf16 v[32:35], a[40:43], a[228:231], v[32:35]\n"
    "ds_read_b64_tr_b16 a[106:107], v231 offset:24832\n"
    "v_mul_f32 v163, s84, v163\n"
    "v_mfma_f32_16x16x32_bf16 v[32:35], a[36:39], a[212:215], v[32:35]\n"
    "ds_read_b64_tr_b16 a[108:109], v231 offset:28672\n"
    "v_mul_f32 v164, s84, v164\n"
    "v_mul_f32 v165, s84, v165\n"
    "v_mfma_f32_16x16x32_bf16 v[32:35], a[32:35], a[196:199], v[32:35]\n"
    "ds_read_b64_tr_b16 a[110:111], v231 offset:28928\n"
    "v_mul_f32 v166, s84, v166\n"
    "v_mul_f32 v167, s84, v167\n"
    "v_mfma_f32_16x16x32_bf16 v[64:67], a[32:35], a[200:203], v[64:67]\n"
    "ds_read_b64_tr_b16 a[112:113], v231 offset:16392\n"
    "v_cvt_pk_bf16_f32 v160, v160, v161\n"
    "v_mfma_f32_16x16x32_bf16 v[64:67], a[36:39], a[216:219], v[64:67]\n"
    "ds_read_b64_tr_b16 a[114:115], v231 offset:16648\n"
    "v_cvt_pk_bf16_f32 v161, v162, v163\n"
    "v_cvt_pk_bf16_f32 v162, v164, v165\n"
    "v_mfma_f32_16x16x32_bf16 v[64:67], a[40:43], a[232:235], v[64:67]\n"
    "ds_read_b64_tr_b16 a[116:117], v231 offset:20488\n"
    "v_cvt_pk_bf16_f32 v163, v166, v167\n"
    "v_mfma_f32_16x16x32_bf16 v[64:67], a[44:47], a[248:251], v[64:67]\n"
    "ds_read_b64_tr_b16 a[118:119], v231 offset:20744\n"
    "v_accvgpr_write_b32 a152, v160\n"
    "v_accvgpr_write_b32 a153, v161\n"
    "v_mfma_f32_16x16x32_bf16 v[96:99], a[44:47], a[252:255], v[96:99]\n"
    "ds_read_b64_tr_b16 a[120:121], v231 offset:24584\n"
    "v_accvgpr_write_b32 a154, v162\n"
    "v_accvgpr_write_b32 a155, v163\n"
    "v_mfma_f32_16x16x32_bf16 v[96:99], a[40:43], a[236:239], v[96:99]\n"
    "ds_read_b64_tr_b16 a[122:123], v231 offset:24840\n"
    "s_add_u32 m0, s83, 0\n"
    "v_mfma_f32_16x16x32_bf16 v[96:99], a[36:39], a[220:223], v[96:99]\n"
    "ds_read_b64_tr_b16 a[124:125], v231 offset:28680\n"
    "s_xor_b32 s81, s4, 32768\n"
    "v_mul_f32 v192, s84, v192\n"
    "v_mfma_f32_16x16x32_bf16 v[96:99], a[32:35], a[204:207], v[96:99]\n"
    "ds_read_b64_tr_b16 a[126:127], v231 offset:28936\n"
    "buffer_load_dwordx4 v241, s[52:55], s81 offen offset:1024 nt lds\n"
    "v_mfma_f32_16x16x32_bf16 v[4:7], a[48:51], a[192:195], v[4:7]\n"
    "v_mul_f32 v193, s84, v193\n"
    "v_mul_f32 v194, s84, v194\n"
    "v_mul_f32 v195, s84, v195\n"
    "v_mfma_f32_16x16x32_bf16 v[4:7], a[52:55], a[208:211], v[4:7]\n"
    "v_mul_f32 v196, s84, v196\n"
    "v_mul_f32 v197, s84, v197\n"
    "v_mul_f32 v198, s84, v198\n"
    "v_mfma_f32_16x16x32_bf16 v[4:7], a[56:59], a[224:227], v[4:7]\n"
    "v_mul_f32 v199, s84, v199\n"
    "v_cvt_pk_bf16_f32 v192, v192, v193\n"
    "v_mfma_f32_16x16x32_bf16 v[4:7], a[60:63], a[240:243], v[4:7]\n"
    "v_cvt_pk_bf16_f32 v193, v194, v195\n"
    "v_cvt_pk_bf16_f32 v194, v196, v197\n"
    "v_cvt_pk_bf16_f32 v195, v198, v199\n"
    "v_mfma_f32_16x16x32_bf16 v[36:39], a[60:63], a[244:247], v[36:39]\n"
    "v_accvgpr_write_b32 a168, v192\n"
    "v_accvgpr_write_b32 a169, v193\n"
    "v_mfma_f32_16x16x32_bf16 v[36:39], a[56:59], a[228:231], v[36:39]\n"
    "v_accvgpr_write_b32 a170, v194\n"
    "v_accvgpr_write_b32 a171, v195\n"
    "v_mul_f32 v136, s84, v136\n"
    "v_mfma_f32_16x16x32_bf16 v[36:39], a[52:55], a[212:215], v[36:39]\n"
    "v_mul_f32 v137, s84, v137\n"
    "v_mul_f32 v138, s84, v138\n"
    "v_mul_f32 v139, s84, v139\n"
    "v_mfma_f32_16x16x32_bf16 v[36:39], a[48:51], a[196:199], v[36:39]\n"
    "s_waitcnt vmcnt(11) lgkmcnt(0)\n"
    "s_barrier\n"
    "v_mfma_f32_16x16x32_bf16 v[68:71], a[48:51], a[200:203], v[68:71]\n"
    "ds_read_b64_tr_b16 a[32:33], v230 offset:16384\n"
    "v_mul_f32 v140, s84, v140\n"
    "v_mul_f32 v141, s84, v141\n"
    "v_mfma_f32_16x16x32_bf16 v[68:71], a[52:55], a[216:219], v[68:71]\n"
    "ds_read_b64_tr_b16 a[34:35], v230 offset:16640\n"
    "v_mul_f32 v142, s84, v142\n"
    "v_mfma_f32_16x16x32_bf16 v[68:71], a[56:59], a[232:235], v[68:71]\n"
    "ds_read_b64_tr_b16 a[36:37], v230 offset:20480\n"
    "v_mul_f32 v143, s84, v143\n"
    "v_cvt_pk_bf16_f32 v136, v136, v137\n"
    "v_mfma_f32_16x16x32_bf16 v[68:71], a[60:63], a[248:251], v[68:71]\n"
    "ds_read_b64_tr_b16 a[38:39], v230 offset:20736\n"
    "v_cvt_pk_bf16_f32 v137, v138, v139\n"
    "v_cvt_pk_bf16_f32 v138, v140, v141\n"
    "v_mfma_f32_16x16x32_bf16 v[100:103], a[60:63], a[252:255], v[100:103]\n"
    "ds_read_b64_tr_b16 a[40:41], v230 offset:24576\n"
    "v_cvt_pk_bf16_f32 v139, v142, v143\n"
    "v_mfma_f32_16x16x32_bf16 v[100:103], a[56:59], a[236:239], v[100:103]\n"
    "ds_read_b64_tr_b16 a[42:43], v230 offset:24832\n"
    "s_add_u32 m0, s83, 0\n"
    "v_accvgpr_write_b32 a140, v136\n"
    "v_mfma_f32_16x16x32_bf16 v[100:103], a[52:55], a[220:223], v[100:103]\n"
    "ds_read_b64_tr_b16 a[44:45], v230 offset:28672\n"
    "s_xor_b32 s81, s4, 32768\n"
    "v_mfma_f32_16x16x32_bf16 v[100:103], a[48:51], a[204:207], v[100:103]\n"
    "ds_read_b64_tr_b16 a[46:47], v230 offset:28928\n"
    "buffer_load_dwordx4 v240, s[52:55], s81 offen offset:2048 nt lds\n"
    "v_accvgpr_write_b32 a141, v137\n"
    "v_mfma_f32_16x16x32_bf16 v[8:11], a[96:99], a[192:195], v[8:11]\n"
    "v_accvgpr_write_b32 a142, v138\n"
    "v_accvgpr_write_b32 a143, v139\n"
    "v_mul_f32 v168, s84, v168\n"
    "v_mfma_f32_16x16x32_bf16 v[8:11], a[100:103], a[208:211], v[8:11]\n"
    "v_mul_f32 v169, s84, v169\n"
    "v_mul_f32 v170, s84, v170\n"
    "v_mfma_f32_16x16x32_bf16 v[8:11], a[104:107], a[224:227], v[8:11]\n"
    "v_mul_f32 v171, s84, v171\n"
    "v_mul_f32 v172, s84, v172\n"
    "v_mul_f32 v173, s84, v173\n"
    "v_mfma_f32_16x16x32_bf16 v[8:11], a[108:111], a[240:243], v[8:11]\n"
    "v_mul_f32 v174, s84, v174\n"
    "v_mul_f32 v175, s84, v175\n"
    "v_mfma_f32_16x16x32_bf16 v[40:43], a[108:111], a[244:247], v[40:43]\n"
    "v_cvt_pk_bf16_f32 v168, v168, v169\n"
    "v_cvt_pk_bf16_f32 v169, v170, v171\n"
    "v_cvt_pk_bf16_f32 v170, v172, v173\n"
    "v_mfma_f32_16x16x32_bf16 v[40:43], a[104:107], a[228:231], v[40:43]\n"
    "v_cvt_pk_bf16_f32 v171, v174, v175\n"
    "v_accvgpr_write_b32 a156, v168\n"
    "v_accvgpr_write_b32 a157, v169\n"
    "v_mfma_f32_16x16x32_bf16 v[40:43], a[100:103], a[212:215], v[40:43]\n"
    "v_accvgpr_write_b32 a158, v170\n"
    "v_accvgpr_write_b32 a159, v171\n"
    "v_mfma_f32_16x16x32_bf16 v[40:43], a[96:99], a[196:199], v[40:43]\n"
    "v_mul_f32 v200, s84, v200\n"
    "v_mul_f32 v201, s84, v201\n"
    "v_mul_f32 v202, s84, v202\n"
    "v_mfma_f32_16x16x32_bf16 v[72:75], a[96:99], a[200:203], v[72:75]\n"
    "ds_read_b64_tr_b16 a[48:49], v230 offset:16392\n"
    "v_mul_f32 v203, s84, v203\n"
    "v_mfma_f32_16x16x32_bf16 v[72:75], a[100:103], a[216:219], v[72:75]\n"
    "ds_read_b64_tr_b16 a[50:51], v230 offset:16648\n"
    "v_mul_f32 v204, s84, v204\n"
    "v_mul_f32 v205, s84, v205\n"
    "v_mfma_f32_16x16x32_bf16 v[72:75], a[104:107], a[232:235], v[72:75]\n"
    "ds_read_b64_tr_b16 a[52:53], v230 offset:20488\n"
    "v_mul_f32 v206, s84, v206\n"
    "v_mul_f32 v207, s84, v207\n"
    "v_mfma_f32_16x16x32_bf16 v[72:75], a[108:111], a[248:251], v[72:75]\n"
    "ds_read_b64_tr_b16 a[54:55], v230 offset:20744\n"
    "v_cvt_pk_bf16_f32 v200, v200, v201\n"
    "v_mfma_f32_16x16x32_bf16 v[12:15], a[112:115], a[192:195], v[12:15]\n"
    "ds_read_b64_tr_b16 a[56:57], v230 offset:24584\n"
    "v_cvt_pk_bf16_f32 v201, v202, v203\n"
    "v_cvt_pk_bf16_f32 v202, v204, v205\n"
    "v_mfma_f32_16x16x32_bf16 v[12:15], a[116:119], a[208:211], v[12:15]\n"
    "ds_read_b64_tr_b16 a[58:59], v230 offset:24840\n"
    "s_add_u32 m0, s83, 0\n"
    "v_mfma_f32_16x16x32_bf16 v[12:15], a[120:123], a[224:227], v[12:15]\n"
    "ds_read_b64_tr_b16 a[60:61], v230 offset:28680\n"
    "s_xor_b32 s81, s4, 32768\n"
    "v_cvt_pk_bf16_f32 v203, v206, v207\n"
    "v_mfma_f32_16x16x32_bf16 v[12:15], a[124:127], a[240:243], v[12:15]\n"
    "ds_read_b64_tr_b16 a[62:63], v230 offset:28936\n"
    "buffer_load_dwordx4 v241, s[52:55], s81 offen offset:3072 nt lds\n"
    "v_accvgpr_write_b32 a172, v200\n"
    "v_mfma_f32_16x16x32_bf16 v[44:47], a[124:127], a[244:247], v[44:47]\n"
    "v_accvgpr_write_b32 a173, v201\n"
    "v_accvgpr_write_b32 a174, v202\n"
    "v_mfma_f32_16x16x32_bf16 v[44:47], a[120:123], a[228:231], v[44:47]\n"
    "v_accvgpr_write_b32 a175, v203\n"
    "v_mul_f32 v96, s84, v96\n"
    "v_mul_f32 v97, s84, v97\n"
    "v_mfma_f32_16x16x32_bf16 v[44:47], a[116:119], a[212:215], v[44:47]\n"
    "v_mul_f32 v98, s84, v98\n"
    "v_mul_f32 v99, s84, v99\n"
    "v_mfma_f32_16x16x32_bf16 v[44:47], a[112:115], a[196:199], v[44:47]\n"
    "v_mul_f32 v100, s84, v100\n"
    "v_mul_f32 v101, s84, v101\n"
    "v_mul_f32 v102, s84, v102\n"
    "v_mfma_f32_16x16x32_bf16 v[76:79], a[112:115], a[200:203], v[76:79]\n"
    "v_mul_f32 v103, s84, v103\n"
    "v_cvt_pk_bf16_f32 v176, v96, v97\n"
    "v_cvt_pk_bf16_f32 v177, v98, v99\n"
    "v_mfma_f32_16x16x32_bf16 v[76:79], a[116:119], a[216:219], v[76:79]\n"
    "v_cvt_pk_bf16_f32 v178, v100, v101\n"
    "v_cvt_pk_bf16_f32 v179, v102, v103\n"
    "v_mfma_f32_16x16x32_bf16 v[76:79], a[120:123], a[232:235], v[76:79]\n"
    "ds_write_b128 v244, v[176:179] offset:0\n"
    "v_mul_f32 v0, s84, v0\n"
    "v_mfma_f32_16x16x32_bf16 v[76:79], a[124:127], a[248:251], v[76:79]\n"
    "v_mul_f32 v1, s84, v1\n"
    "v_mul_f32 v2, s84, v2\n"
    "s_waitcnt lgkmcnt(1)\n"
    "v_mfma_f32_16x16x32_bf16 v[16:19], a[32:35], a[192:195], v[16:19]\n"
    "ds_read_b64_tr_b16 a[96:97], v255 offset:16384\n"
    "v_mul_f32 v3, s84, v3\n"
    "v_mul_f32 v4, s84, v4\n"
    "v_mfma_f32_16x16x32_bf16 v[16:19], a[36:39], a[208:211], v[16:19]\n"
    "ds_read_b64_tr_b16 a[98:99], v255 offset:16640\n"
    "v_mul_f32 v5, s84, v5\n"
    "v_mul_f32 v6, s84, v6\n"
    "v_mfma_f32_16x16x32_bf16 v[16:19], a[40:43], a[224:227], v[16:19]\n"
    "ds_read_b64_tr_b16 a[100:101], v255 offset:20480\n"
    "v_mul_f32 v7, s84, v7\n"
    "v_mfma_f32_16x16x32_bf16 v[16:19], a[44:47], a[240:243], v[16:19]\n"
    "ds_read_b64_tr_b16 a[102:103], v255 offset:20736\n"
    "v_cvt_pk_bf16_f32 v128, v0, v1\n"
    "v_cvt_pk_bf16_f32 v129, v2, v3\n"
    "v_mfma_f32_16x16x32_bf16 v[48:51], a[44:47], a[244:247], v[48:51]\n"
    "ds_read_b64_tr_b16 a[104:105], v255 offset:24576\n"
    "v_cvt_pk_bf16_f32 v130, v4, v5\n"
    "v_mfma_f32_16x16x32_bf16 v[48:51], a[40:43], a[228:231], v[48:51]\n"
    "ds_read_b64_tr_b16 a[106:107], v255 offset:24832\n"
    "s_add_u32 m0, s83, 65536\n"
    "v_cvt_pk_bf16_f32 v131, v6, v7\n"
    "v_mfma_f32_16x16x32_bf16 v[48:51], a[36:39], a[212:215], v[48:51]\n"
    "ds_read_b64_tr_b16 a[108:109], v255 offset:28672\n"
    "s_xor_b32 s81, s4, 32768\n"
    "v_mul_f32 v32, s84, v32\n"
    "v_mfma_f32_16x16x32_bf16 v[48:51], a[32:35], a[196:199], v[48:51]\n"
    "ds_read_b64_tr_b16 a[110:111], v255 offset:28928\n"
    "buffer_load_dwordx4 v240, s[56:59], s81 offen offset:0 nt lds\n"
    "v_mfma_f32_16x16x32_bf16 v[80:83], a[32:35], a[200:203], v[80:83]\n"
    "ds_read_b64_tr_b16 a[112:113], v255 offset:16392\n"
    "v_mul_f32 v33, s84, v33\n"
    "v_mul_f32 v34, s84, v34\n"
    "v_mfma_f32_16x16x32_bf16 v[80:83], a[36:39], a[216:219], v[80:83]\n"
    "ds_read_b64_tr_b16 a[114:115], v255 offset:16648\n"
    "v_mul_f32 v35, s84, v35\n"
    "v_mfma_f32_16x16x32_bf16 v[80:83], a[40:43], a[232:235], v[80:83]\n"
    "ds_read_b64_tr_b16 a[116:117], v255 offset:20488\n"
    "v_mul_f32 v36, s84, v36\n"
    "v_mul_f32 v37, s84, v37\n"
    "v_mfma_f32_16x16x32_bf16 v[80:83], a[44:47], a[248:251], v[80:83]\n"
    "ds_read_b64_tr_b16 a[118:119], v255 offset:20744\n"
    "v_mul_f32 v38, s84, v38\n"
    "v_mul_f32 v39, s84, v39\n"
    "v_mfma_f32_16x16x32_bf16 v[104:107], a[44:47], a[252:255], v[104:107]\n"
    "ds_read_b64_tr_b16 a[120:121], v255 offset:24584\n"
    "v_cvt_pk_bf16_f32 v144, v32, v33\n"
    "v_mfma_f32_16x16x32_bf16 v[104:107], a[40:43], a[236:239], v[104:107]\n"
    "ds_read_b64_tr_b16 a[122:123], v255 offset:24840\n"
    "v_cvt_pk_bf16_f32 v145, v34, v35\n"
    "v_cvt_pk_bf16_f32 v146, v36, v37\n"
    "v_mfma_f32_16x16x32_bf16 v[104:107], a[36:39], a[220:223], v[104:107]\n"
    "ds_read_b64_tr_b16 a[124:125], v255 offset:28680\n"
    "v_cvt_pk_bf16_f32 v147, v38, v39\n"
    "v_mfma_f32_16x16x32_bf16 v[104:107], a[32:35], a[204:207], v[104:107]\n"
    "ds_read_b64_tr_b16 a[126:127], v255 offset:28936\n"
    "v_mul_f32 v64, s84, v64\n"
    "v_mul_f32 v65, s84, v65\n"
    "v_mfma_f32_16x16x32_bf16 v[20:23], a[48:51], a[192:195], v[20:23]\n"
    "v_mul_f32 v66, s84, v66\n"
    "v_mul_f32 v67, s84, v67\n"
    "v_mul_f32 v68, s84, v68\n"
    "v_mfma_f32_16x16x32_bf16 v[20:23], a[52:55], a[208:211], v[20:23]\n"
    "v_mul_f32 v69, s84, v69\n"
    "v_mul_f32 v70, s84, v70\n"
    "v_mfma_f32_16x16x32_bf16 v[20:23], a[56:59], a[224:227], v[20:23]\n"
    "v_mul_f32 v71, s84, v71\n"
    "v_cvt_pk_bf16_f32 v160, v64, v65\n"
    "v_cvt_pk_bf16_f32 v161, v66, v67\n"
    "v_mfma_f32_16x16x32_bf16 v[20:23], a[60:63], a[240:243], v[20:23]\n"
    "v_cvt_pk_bf16_f32 v162, v68, v69\n"
    "v_cvt_pk_bf16_f32 v163, v70, v71\n"
    "v_mfma_f32_16x16x32_bf16 v[52:55], a[60:63], a[244:247], v[52:55]\n"
    "v_mul_f32 v8, s84, v8\n"
    "v_mul_f32 v9, s84, v9\n"
    "v_mul_f32 v10, s84, v10\n"
    "v_mfma_f32_16x16x32_bf16 v[52:55], a[56:59], a[228:231], v[52:55]\n"
    "s_add_u32 m0, s83, 65536\n"
    "v_mul_f32 v11, s84, v11\n"
    "v_mul_f32 v12, s84, v12\n"
    "v_mfma_f32_16x16x32_bf16 v[52:55], a[52:55], a[212:215], v[52:55]\n"
    "s_xor_b32 s81, s4, 32768\n"
    "buffer_load_dwordx4 v241, s[56:59], s81 offen offset:1024 nt lds\n"
    "v_mfma_f32_16x16x32_bf16 v[52:55], a[48:51], a[196:199], v[52:55]\n"
    "s_waitcnt vmcnt(11) lgkmcnt(0)\n"
    "s_barrier\n"
    "v_mul_f32 v13, s84, v13\n"
    "v_mfma_f32_16x16x32_bf16 v[84:87], a[48:51], a[200:203], v[84:87]\n"
    "ds_read_b128 a[0:3], v232 offset:32768\n"
    "v_mul_f32 v14, s84, v14\n"
    "v_mfma_f32_16x16x32_bf16 v[84:87], a[52:55], a[216:219], v[84:87]\n"
    "ds_read_b128 a[4:7], v233 offset:32768\n"
    "v_mul_f32 v15, s84, v15\n"
    "v_cvt_pk_bf16_f32 v132, v8, v9\n"
    "v_mfma_f32_16x16x32_bf16 v[84:87], a[56:59], a[232:235], v[84:87]\n"
    "ds_read_b128 a[8:11], v232 offset:33280\n"
    "v_cvt_pk_bf16_f32 v133, v10, v11\n"
    "v_cvt_pk_bf16_f32 v134, v12, v13\n"
    "v_mfma_f32_16x16x32_bf16 v[84:87], a[60:63], a[248:251], v[84:87]\n"
    "ds_read_b128 a[12:15], v233 offset:33280\n"
    "v_cvt_pk_bf16_f32 v135, v14, v15\n"
    "v_mfma_f32_16x16x32_bf16 v[108:111], a[60:63], a[252:255], v[108:111]\n"
    "ds_read_b128 a[16:19], v232 offset:36864\n"
    "v_mul_f32 v40, s84, v40\n"
    "v_mul_f32 v41, s84, v41\n"
    "v_mfma_f32_16x16x32_bf16 v[108:111], a[56:59], a[236:239], v[108:111]\n"
    "ds_read_b128 a[20:23], v233 offset:36864\n"
    "v_mul_f32 v42, s84, v42\n"
    "v_mfma_f32_16x16x32_bf16 v[108:111], a[52:55], a[220:223], v[108:111]\n"
    "ds_read_b128 a[24:27], v232 offset:37376\n"
    "v_mul_f32 v43, s84, v43\n"
    "v_mul_f32 v44, s84, v44\n"
    "v_mfma_f32_16x16x32_bf16 v[108:111], a[48:51], a[204:207], v[108:111]\n"
    "ds_read_b128 a[28:31], v233 offset:37376\n"
    "v_mul_f32 v45, s84, v45\n"
    "v_mul_f32 v46, s84, v46\n"
    "v_mfma_f32_16x16x32_bf16 v[24:27], a[96:99], a[192:195], v[24:27]\n"
    "ds_read_b64_tr_b16 a[64:65], v236 offset:32768\n"
    "v_mul_f32 v47, s84, v47\n"
    "v_mfma_f32_16x16x32_bf16 v[24:27], a[100:103], a[208:211], v[24:27]\n"
    "ds_read_b64_tr_b16 a[66:67], v236 offset:36864\n"
    "v_cvt_pk_bf16_f32 v148, v40, v41\n"
    "v_cvt_pk_bf16_f32 v149, v42, v43\n"
    "v_mfma_f32_16x16x32_bf16 v[24:27], a[104:107], a[224:227], v[24:27]\n"
    "ds_read_b64_tr_b16 a[68:69], v237 offset:32768\n"
    "v_cvt_pk_bf16_f32 v150, v44, v45\n"
    "v_mfma_f32_16x16x32_bf16 v[24:27], a[108:111], a[240:243], v[24:27]\n"
    "ds_read_b64_tr_b16 a[70:71], v237 offset:36864\n"
    "v_cvt_pk_bf16_f32 v151, v46, v47\n"
    "v_mul_f32 v104, s84, v104\n"
    "v_mfma_f32_16x16x32_bf16 v[56:59], a[108:111], a[244:247], v[56:59]\n"
    "ds_read_b64_tr_b16 a[72:73], v236 offset:33024\n"
    "v_mul_f32 v105, s84, v105\n"
    "v_mul_f32 v106, s84, v106\n"
    "v_mfma_f32_16x16x32_bf16 v[56:59], a[104:107], a[228:231], v[56:59]\n"
    "ds_read_b64_tr_b16 a[74:75], v236 offset:37120\n"
    "s_add_u32 m0, s83, 65536\n"
    "v_mfma_f32_16x16x32_bf16 v[56:59], a[100:103], a[212:215], v[56:59]\n"
    "s_xor_b32 s81, s4, 32768\n"
    "L_h5:\n"
    "s_cbranch_execz L_h9\n"
    "buffer_load_dwordx4 v240, s[56:59], s81 offen offset:2048 nt lds\n"
    "v_mul_f32 v107, s84, v107\n"
    "v_mfma_f32_16x16x32_bf16 v[56:59], a[96:99], a[196:199], v[56:59]\n"
    "ds_read_b64_tr_b16 a[76:77], v237 offset:33024\n"
    "v_mul_f32 v108, s84, v108\n"
    "v_mfma_f32_16x16x32_bf16 v[88:91], a[96:99], a[200:203], v[88:91]\n"
    "ds_read_b64_tr_b16 a[78:79], v237 offset:37120\n"
    "v_mul_f32 v109, s84, v109\n"
    "v_mul_f32 v110, s84, v110\n"
    "v_mfma_f32_16x16x32_bf16 v[88:91], a[100:103], a[216:219], v[88:91]\n"
    "ds_read_b64_tr_b16 a[80:81], v236 offset:33280\n"
    "v_mul_f32 v111, s84, v111\n"
    "v_cvt_pk_bf16_f32 v180, v104, v105\n"
    "v_mfma_f32_16x16x32_bf16 v[88:91], a[104:107], a[232:235], v[88:91]\n"
    "ds_read_b64_tr_b16 a[82:83], v236 offset:37376\n"
    "v_cvt_pk_bf16_f32 v181, v106, v107\n"
    "v_mfma_f32_16x16x32_bf16 v[88:91], a[108:111], a[248:251], v[88:91]\n"
    "ds_read_b64_tr_b16 a[84:85], v237 offset:33280\n"
    "v_cvt_pk_bf16_f32 v182, v108, v109\n"
    "v_cvt_pk_bf16_f32 v183, v110, v111\n"
    "v_mfma_f32_16x16x32_bf16 v[28:31], a[112:115], a[192:195], v[28:31]\n"
    "ds_read_b64_tr_b16 a[86:87], v237 offset:37376\n"
    "v_mfma_f32_16x16x32_bf16 v[28:31], a[116:119], a[208:211], v[28:31]\n"
    "ds_read_b64_tr_b16 a[88:89], v236 offset:33536\n"
    "ds_write_b128 v244, v[180:183] offset:1024\n"
    "v_mfma_f32_16x16x32_bf16 v[28:31], a[120:123], a[224:227], v[28:31]\n"
    "ds_read_b64_tr_b16 a[90:91], v236 offset:37632\n"
    "v_mfma_f32_16x16x32_bf16 v[28:31], a[124:127], a[240:243], v[28:31]\n"
    "ds_read_b64_tr_b16 a[92:93], v237 offset:33536\n"
    "v_mfma_f32_16x16x32_bf16 v[60:63], a[124:127], a[244:247], v[60:63]\n"
    "ds_read_b64_tr_b16 a[94:95], v237 offset:37632\n"
    "v_mfma_f32_16x16x32_bf16 v[60:63], a[120:123], a[228:231], v[60:63]\n"
    "v_mfma_f32_16x16x32_bf16 v[60:63], a[116:119], a[212:215], v[60:63]\n"
    "v_mfma_f32_16x16x32_bf16 v[60:63], a[112:115], a[196:199], v[60:63]\n"
    "v_mfma_f32_16x16x32_bf16 v[92:95], a[112:115], a[200:203], v[92:95]\n"
    "v_mfma_f32_16x16x32_bf16 v[92:95], a[116:119], a[216:219], v[92:95]\n"
    "v_mfma_f32_16x16x32_bf16 v[92:95], a[120:123], a[232:235], v[92:95]\n"
    "s_waitcnt lgkmcnt(2)\n"
    "v_mfma_f32_16x16x32_bf16 v[92:95], a[124:127], a[248:251], v[92:95]\n"
    "s_barrier\n"
    "ds_read_b128 a[248:251], v245 offset:0\n"
    "ds_read_b128 a[252:255], v245 offset:1024\n"
    "ds_read_b128 a[180:183], v245 offset:2048\n"
    "ds_read_b128 a[188:191], v245 offset:3072\n"
    "v_pk_mul_f32 v[72:73], v[72:73], s[84:85]\n"
    "v_pk_mul_f32 v[74:75], v[74:75], s[84:85]\n"
    "v_pk_mul_f32 v[76:77], v[76:77], s[84:85]\n"
    "v_pk_mul_f32 v[78:79], v[78:79], s[84:85]\n"
    "v_cvt_pk_bf16_f32 v164, v72, v73\n"
    "v_cvt_pk_bf16_f32 v165, v74, v75\n"
    "v_cvt_pk_bf16_f32 v166, v76, v77\n"
    "v_cvt_pk_bf16_f32 v167, v78, v79\n"
    "v_pk_mul_f32 v[16:17], v[16:17], s[84:85]\n"
    "v_pk_mul_f32 v[18:19], v[18:19], s[84:85]\n"
    "v_pk_mul_f32 v[20:21], v[20:21], s[84:85]\n"
    "v_pk_mul_f32 v[22:23], v[22:23], s[84:85]\n"
    "v_cvt_pk_bf16_f32 v136, v16, v17\n"
    "v_cvt_pk_bf16_f32 v137, v18, v19\n"
    "v_cvt_pk_bf16_f32 v138, v20, v21\n"
    "v_cvt_pk_bf16_f32 v139, v22, v23\n"
    "v_pk_mul_f32 v[48:49], v[48:49], s[84:85]\n"
    "v_pk_mul_f32 v[50:51], v[50:51], s[84:85]\n"
    "v_pk_mul_f32 v[52:53], v[52:53], s[84:85]\n"
    "v_pk_mul_f32 v[54:55], v[54:55], s[84:85]\n"
    "v_cvt_pk_bf16_f32 v152, v48, v49\n"
    "v_cvt_pk_bf16_f32 v153, v50, v51\n"
    "v_cvt_pk_bf16_f32 v154, v52, v53\n"
    "v_cvt_pk_bf16_f32 v155, v54, v55\n"
    "v_pk_mul_f32 v[80:81], v[80:81], s[84:85]\n"
    "v_pk_mul_f32 v[82:83], v[82:83], s[84:85]\n"
    "v_pk_mul_f32 v[84:85], v[84:85], s[84:85]\n"
    "v_pk_mul_f32 v[86:87], v[86:87], s[84:85]\n"
    "v_cvt_pk_bf16_f32 v168, v80, v81\n"
    "v_cvt_pk_bf16_f32 v169, v82, v83\n"
    "v_cvt_pk_bf16_f32 v170, v84, v85\n"
    "v_cvt_pk_bf16_f32 v171, v86, v87\n"
    "s_add_u32 m0, s83, 65536\n"
    "s_xor_b32 s81, s4, 32768\n"
    "buffer_load_dwordx4 v241, s[56:59], s81 offen offset:3072 nt lds\n"
    "v_pk_mul_f32 v[24:25], v[24:25], s[84:85]\n"
    "v_pk_mul_f32 v[26:27], v[26:27], s[84:85]\n"
    "v_pk_mul_f32 v[28:29], v[28:29], s[84:85]\n"
    "v_pk_mul_f32 v[30:31], v[30:31], s[84:85]\n"
    "v_cvt_pk_bf16_f32 v140, v24, v25\n"
    "v_cvt_pk_bf16_f32 v141, v26, v27\n"
    "v_cvt_pk_bf16_f32 v142, v28, v29\n"
    "v_cvt_pk_bf16_f32 v143, v30, v31\n"
    "v_pk_mul_f32 v[56:57], v[56:57], s[84:85]\n"
    "v_pk_mul_f32 v[58:59], v[58:59], s[84:85]\n"
    "v_pk_mul_f32 v[60:61], v[60:61], s[84:85]\n"
    "v_pk_mul_f32 v[62:63], v[62:63], s[84:85]\n"
    "v_cvt_pk_bf16_f32 v156, v56, v57\n"
    "v_cvt_pk_bf16_f32 v157, v58, v59\n"
    "v_cvt_pk_bf16_f32 v158, v60, v61\n"
    "v_cvt_pk_bf16_f32 v159, v62, v63\n"
    "v_pk_mul_f32 v[88:89], v[88:89], s[84:85]\n"
    "v_pk_mul_f32 v[90:91], v[90:91], s[84:85]\n"
    "v_pk_mul_f32 v[92:93], v[92:93], s[84:85]\n"
    "v_pk_mul_f32 v[94:95], v[94:95], s[84:85]\n"
    "v_cvt_pk_bf16_f32 v172, v88, v89\n"
    "v_cvt_pk_bf16_f32 v173, v90, v91\n"
    "v_cvt_pk_bf16_f32 v174, v92, v93\n"
    "v_cvt_pk_bf16_f32 v175, v94, v95\n"
    "s_add_u32 m0, s83, 16384\n"
    "s_xor_b32 s81, s4, 49152\n"
    "buffer_load_dwordx4 v240, s[52:55], s81 offen offset:0 nt lds\n"
    "v_mfma_f32_16x16x32_bf16 v[64:67], a[0:3], v[176:179], 0\n"
    "v_and_b32 v246, 12, v250\n"
    "v_min_u32 v246, 1, v246\n"
    "s_waitcnt lgkmcnt(0)\n"
    "v_mfma_f32_16x16x32_bf16 v[64:67], a[4:7], a[248:251], v[64:67]\n"
    "v_add_u32 v246, -1, v246\n"
    "v_and_b32 v246, 0x3f803f80, v246\n"
    "v_mfma_f32_16x16x32_bf16 v[64:67], a[8:11], v[180:183], v[64:67]\n"
    "s_add_u32 m0, s83, 16384\n"
    "v_accvgpr_write_b32 a212, v248\n"
    "v_accvgpr_write_b32 a213, v249\n"
    "v_mfma_f32_16x16x32_bf16 v[64:67], a[12:15], a[252:255], v[64:67]\n"
    "s_xor_b32 s81, s4, 49152\n"
    "v_accvgpr_write_b32 a208, v246\n"
    "v_accvgpr_write_b32 a209, v246\n"
    "v_mfma_f32_16x16x32_bf16 v[68:71], a[28:31], a[252:255], 0\n"
    "v_accvgpr_write_b32 a210, v246\n"
    "v_accvgpr_write_b32 a211, v246\n"
    "v_mfma_f32_16x16x32_bf16 v[68:71], a[24:27], v[180:183], v[68:71]\n"
    "buffer_load_dwordx4 v241, s[52:55], s81 offen offset:1024 nt lds\n"
    "v_mfma_f32_16x16x32_bf16 v[68:71], a[20:23], a[248:251], v[68:71]\n"
    "ds_read_b128 a[32:35], v234 offset:32768\n"
    "v_mfma_f32_16x16x32_bf16 v[68:71], a[16:19], v[176:179], v[68:71]\n"
    "ds_read_b128 a[36:39], v235 offset:32768\n"
    "v_mfma_f32_16x16x32_bf16 v[72:75], a[0:3], v[128:131], 0\n"
    "ds_read_b128 a[40:43], v234 offset:33280\n"
    "v_mfma_f32_16x16x32_bf16 v[72:75], a[4:7], v[132:135], v[72:75]\n"
    "ds_read_b128 a[44:47], v235 offset:33280\n"
    "v_mfma_f32_16x16x32_bf16 v[72:75], a[8:11], v[136:139], v[72:75]\n"
    "ds_read_b128 a[60:63], v235 offset:37376\n"
    "v_max3_f32 v246, v64, v65, v66\n"
    "v_max3_f32 v247, v67, v68, v69\n"
    "v_max3_f32 v246, v246, v70, v71\n"
    "v_mfma_f32_16x16x32_bf16 v[72:75], a[12:15], v[140:143], v[72:75]\n"
    "ds_read_b128 a[56:59], v234 offset:37376\n"
    "v_max_f32 v246, v246, v247\n"
    "v_mov_b32 v247, v246\n"
    "v_mfma_f32_16x16x32_bf16 v[76:79], a[28:31], v[140:143], 0\n"
    "ds_read_b128 a[52:55], v235 offset:36864\n"
    "v_permlane32_swap_b32 v246, v247\n"
    "v_max_f32 v246, v246, v247\n"
    "v_mov_b32 v247, v246\n"
    "v_mfma_f32_16x16x32_bf16 v[76:79], a[24:27], v[136:139], v[76:79]\n"
    "ds_read_b128 a[48:51], v234 offset:36864\n"
    "v_permlane16_swap_b32 v246, v247\n"
    "v_max_f32 v228, v246, v247\n"
    "v_sub_f32 v228, 0, v228\n"
    "v_mfma_f32_16x16x32_bf16 v[76:79], a[20:23], v[132:135], v[76:79]\n"
    "ds_read_b64_tr_b16 a[96:97], v238 offset:32768\n"
    "v_add_f32 v64, v64, v228\n"
    "v_add_f32 v65, v65, v228\n"
    "v_add_f32 v66, v66, v228\n"
    "v_add_f32 v67, v67, v228\n"
    "v_mfma_f32_16x16x32_bf16 v[76:79], a[16:19], v[128:131], v[76:79]\n"
    "ds_read_b64_tr_b16 a[98:99], v238 offset:36864\n"
    "v_add_f32 v68, v68, v228\n"
    "v_add_f32 v69, v69, v228\n"
    "v_add_f32 v70, v70, v228\n"
    "s_waitcnt lgkmcnt(5)\n"
    "v_mfma_f32_16x16x32_bf16 v[192:195], a[32:35], v[128:131], 0\n"
    "buffer_load_dwordx4 v240, s[52:55], s81 offen offset:2048 nt lds\n"
    "v_add_f32 v71, v71, v228\n"
    "v_exp_f32 v64, v64\n"
    "v_mfma_f32_16x16x32_bf16 v[192:195], a[36:39], v[132:135], v[192:195]\n"
    "ds_read_b64_tr_b16 a[100:101], v239 offset:32768\n"
    "v_exp_f32 v65, v65\n"
    "v_mov_b32 v229, v228\n"
    "v_mfma_f32_16x16x32_bf16 v[192:195], a[40:43], v[136:139], v[192:195]\n"
    "ds_read_b64_tr_b16 a[102:103], v239 offset:36864\n"
    "v_exp_f32 v66, v66\n"
    "v_cvt_pk_bf16_f32 v208, v64, v65\n"
    "v_mfma_f32_16x16x32_bf16 v[192:195], a[44:47], v[140:143], v[192:195]\n"
    "ds_read_b64_tr_b16 a[104:105], v238 offset:33024\n"
    "v_exp_f32 v67, v67\n"
    "v_mov_b32 v230, v228\n"
    "v_exp_f32 v68, v68\n"
    "v_mfma_f32_16x16x32_bf16 v[196:199], a[60:63], v[140:143], 0\n"
    "ds_read_b64_tr_b16 a[106:107], v238 offset:37120\n"
    "v_cvt_pk_bf16_f32 v209, v66, v67\n"
    "v_exp_f32 v69, v69\n"
    "s_waitcnt lgkmcnt(4)\n"
    "v_mfma_f32_16x16x32_bf16 v[196:199], a[56:59], v[136:139], v[196:199]\n"
    "ds_read_b64_tr_b16 a[108:109], v239 offset:33024\n"
    "v_mov_b32 v231, v228\n"
    "v_exp_f32 v70, v70\n"
    "v_mfma_f32_16x16x32_bf16 v[196:199], a[52:55], v[132:135], v[196:199]\n"
    "ds_read_b64_tr_b16 a[110:111], v239 offset:37120\n"
    "v_cvt_pk_bf16_f32 v210, v68, v69\n"
    "v_exp_f32 v71, v71\n"
    "s_nop 0\n"
    "v_cvt_pk_bf16_f32 v211, v70, v71\n"
    "v_mfma_f32_16x16x32_bf16 v[196:199], a[48:51], v[128:131], v[196:199]\n"
    "ds_read_b64_tr_b16 a[112:113], v238 offset:33280\n"
    "v_max3_f32 v246, v72, v73, v74\n"
    "v_max3_f32 v247, v75, v76, v77\n"
    "v_max3_f32 v246, v246, v78, v79\n"
    "v_max_f32 v246, v246, v247\n"
    "v_mfma_f32_16x16x32_bf16 v[96:99], a[64:67], v[208:211], 0\n"
    "ds_read_b64_tr_b16 a[114:115], v238 offset:37376\n"
    "v_mov_b32 v247, v246\n"
    "s_nop 1\n"
    "v_permlane32_swap_b32 v246, v247\n"
    "v_max_f32 v246, v246, v247\n"
    "v_mov_b32 v247, v246\n"
    "v_mfma_f32_16x16x32_bf16 v[100:103], a[68:71], v[208:211], 0\n"
    "ds_read_b64_tr_b16 a[116:117], v239 offset:33280\n"
    "v_permlane16_swap_b32 v246, v247\n"
    "v_max_f32 v216, v246, v247\n"
    "v_sub_f32 v216, 0, v216\n"
    "v_mfma_f32_16x16x32_bf16 v[104:107], a[72:75], v[208:211], 0\n"
    "ds_read_b64_tr_b16 a[118:119], v239 offset:37376\n"
    "v_add_f32 v72, v72, v216\n"
    "v_add_f32 v73, v73, v216\n"
    "v_add_f32 v74, v74, v216\n"
    "v_add_f32 v75, v75, v216\n"
    "v_add_f32 v76, v76, v216\n"
    "v_mfma_f32_16x16x32_bf16 v[108:111], a[76:79], v[208:211], 0\n"
    "ds_read_b64_tr_b16 a[120:121], v238 offset:33536\n"
    "v_add_f32 v77, v77, v216\n"
    "v_add_f32 v78, v78, v216\n"
    "v_add_f32 v79, v79, v216\n"
    "v_mfma_f32_16x16x32_bf16 v[112:115], a[80:83], v[208:211], 0\n"
    "ds_read_b64_tr_b16 a[122:123], v238 offset:37632\n"
    "v_exp_f32 v72, v72\n"
    "v_exp_f32 v73, v73\n"
    "v_mov_b32 v217, v216\n"
    "v_mfma_f32_16x16x32_bf16 v[116:119], a[84:87], v[208:211], 0\n"
    "ds_read_b64_tr_b16 a[124:125], v239 offset:33536\n"
    "v_exp_f32 v74, v74\n"
    "v_cvt_pk_bf16_f32 v212, v72, v73\n"
    "v_exp_f32 v75, v75\n"
    "v_mfma_f32_16x16x32_bf16 v[120:123], a[88:91], v[208:211], 0\n"
    "buffer_load_dwordx4 v241, s[52:55], s81 offen offset:3072 nt lds\n"
    "v_mov_b32 v218, v216\n"
    "v_exp_f32 v76, v76\n"
    "v_cvt_pk_bf16_f32 v213, v74, v75\n"
    "v_mfma_f32_16x16x32_bf16 v[124:127], a[92:95], v[208:211], 0\n"
    "ds_read_b64_tr_b16 a[126:127], v239 offset:37632\n"
    "s_add_u32 m0, s83, 81920\n"
    "v_exp_f32 v77, v77\n"
    "v_mov_b32 v219, v216\n"
    "v_mfma_f32_16x16x32_bf16 a[204:207], a[208:211], v[208:211], 0\n"
    "s_xor_b32 s81, s4, 49152\n"
    "v_exp_f32 v78, v78\n"
    "v_cvt_pk_bf16_f32 v214, v76, v77\n"
    "v_exp_f32 v79, v79\n"
    "s_nop 0\n"
    "v_cvt_pk_bf16_f32 v215, v78, v79\n"
    "v_mfma_f32_16x16x32_bf16 v[200:203], a[0:3], v[144:147], 0\n"
    "v_add_f32 v192, v192, v216\n"
    "v_add_f32 v193, v193, v216\n"
    "v_add_f32 v194, v194, v216\n"
    "v_add_f32 v195, v195, v216\n"
    "v_mfma_f32_16x16x32_bf16 v[200:203], a[4:7], v[148:151], v[200:203]\n"
    "v_add_f32 v196, v196, v216\n"
    "v_add_f32 v197, v197, v216\n"
    "v_add_f32 v198, v198, v216\n"
    "v_add_f32 v199, v199, v216\n"
    "v_mfma_f32_16x16x32_bf16 v[200:203], a[8:11], v[152:155], v[200:203]\n"
    "v_exp_f32 v192, v192\n"
    "v_exp_f32 v193, v193\n"
    "v_mfma_f32_16x16x32_bf16 v[200:203], a[12:15], v[156:159], v[200:203]\n"
    "v_exp_f32 v194, v194\n"
    "v_cvt_pk_bf16_f32 v184, v192, v193\n"
    "v_mfma_f32_16x16x32_bf16 v[204:207], a[28:31], v[156:159], 0\n"
    "v_exp_f32 v195, v195\n"
    "v_exp_f32 v196, v196\n"
    "v_cvt_pk_bf16_f32 v185, v194, v195\n"
    "v_mfma_f32_16x16x32_bf16 v[204:207], a[24:27], v[152:155], v[204:207]\n"
    "v_exp_f32 v197, v197\n"
    "v_exp_f32 v198, v198\n"
    "v_mfma_f32_16x16x32_bf16 v[204:207], a[20:23], v[148:151], v[204:207]\n"
    "v_cvt_pk_bf16_f32 v186, v196, v197\n"
    "v_exp_f32 v199, v199\n"
    "s_nop 0\n"
    "v_cvt_pk_bf16_f32 v187, v198, v199\n"
    "v_mfma_f32_16x16x32_bf16 v[204:207], a[16:19], v[144:147], v[204:207]\n"
    "buffer_load_dword a215, v242, s[64:67], s86 offen offset:0\n"
    "v_mfma_f32_16x16x32_bf16 v[0:3], a[64:67], v[212:215], 0\n"
    "v_mfma_f32_16x16x32_bf16 v[0:3], a[96:99], v[184:187], v[0:3]\n"
    "v_mfma_f32_16x16x32_bf16 v[4:7], a[68:71], v[212:215], 0\n"
    "v_max3_f32 v246, v200, v201, v202\n"
    "s_nop 2\n"
    "v_max3_f32 v247, v203, v204, v205\n"
    "s_waitcnt lgkmcnt(0)\n"
    "v_mfma_f32_16x16x32_bf16 v[4:7], a[100:103], v[184:187], v[4:7]\n"
    "v_max3_f32 v246, v246, v206, v207\n"
    "v_max_f32 v246, v246, v247\n"
    "v_mov_b32 v247, v246\n"
    "v_mfma_f32_16x16x32_bf16 v[8:11], a[72:75], v[212:215], 0\n"
    "buffer_load_dwordx4 v240, s[56:59], s81 offen offset:0 nt lds\n"
    "v_mfma_f32_16x16x32_bf16 v[8:11], a[104:107], v[184:187], v[8:11]\n"
    "v_permlane32_swap_b32 v246, v247\n"
    "v_max_f32 v246, v246, v247\n"
    "v_mfma_f32_16x16x32_bf16 v[12:15], a[76:79], v[212:215], 0\n"
    "v_mov_b32 v247, v246\n"
    "v_mfma_f32_16x16x32_bf16 v[12:15], a[108:111], v[184:187], v[12:15]\n"
    "s_nop 0\n"
    "v_permlane16_swap_b32 v246, v247\n"
    "v_max_f32 v220, v246, v247\n"
    "v_mfma_f32_16x16x32_bf16 a[192:195], a[208:211], v[212:215], 0\n"
    "v_sub_f32 v220, 0, v220\n"
    "v_add_f32 v200, v200, v220\n"
    "v_mfma_f32_16x16x32_bf16 a[192:195], a[208:211], v[184:187], a[192:195]\n"
    "v_add_f32 v201, v201, v220\n"
    "v_add_f32 v202, v202, v220\n"
    "v_add_f32 v203, v203, v220\n"
    "v_mfma_f32_16x16x32_bf16 v[192:195], a[32:35], v[144:147], 0\n"
    "v_add_f32 v204, v204, v220\n"
    "v_add_f32 v205, v205, v220\n"
    "v_mfma_f32_16x16x32_bf16 v[192:195], a[36:39], v[148:151], v[192:195]\n"
    "v_add_f32 v206, v206, v220\n"
    "v_add_f32 v207, v207, v220\n"
    "v_mfma_f32_16x16x32_bf16 v[192:195], a[40:43], v[152:155], v[192:195]\n"
    "s_memrealtime s[98:99]\n"
    "v_exp_f32 v200, v200\n"
    "v_mfma_f32_16x16x32_bf16 v[192:195], a[44:47], v[156:159], v[192:195]\n"
    "v_exp_f32 v201, v201\n"
    "v_mov_b32 v221, v220\n"
    "v_mfma_f32_16x16x32_bf16 v[196:199], a[60:63], v[156:159], 0\n"
    "v_exp_f32 v202, v202\n"
    "v_cvt_pk_bf16_f32 v188, v200, v201\n"
    "v_mfma_f32_16x16x32_bf16 v[196:199], a[56:59], v[152:155], v[196:199]\n"
    "v_exp_f32 v203, v203\n"
    "v_mfma_f32_16x16x32_bf16 v[196:199], a[52:55], v[148:151], v[196:199]\n"
    "s_waitcnt lgkmcnt(0)\n"
    "s_add_u32 s25, s98, 130\n"
    "v_mov_b32 v222, v220\n"
    "v_mfma_f32_16x16x32_bf16 v[196:199], a[48:51], v[144:147], v[196:199]\n"
    "v_exp_f32 v204, v204\n"
    "v_mfma_f32_16x16x32_bf16 v[16:19], a[80:83], v[212:215], 0\n"
    "buffer_load_dwordx4 v241, s[56:59], s81 offen offset:1024 nt lds\n"
    "v_cvt_pk_bf16_f32 v189, v202, v203\n"
    "v_mfma_f32_16x16x32_bf16 v[16:19], a[112:115], v[184:187], v[16:19]\n"
    "v_exp_f32 v205, v205\n"
    "v_mov_b32 v223, v220\n"
    "v_mfma_f32_16x16x32_bf16 v[20:23], a[84:87], v[212:215], 0\n"
    "v_exp_f32 v206, v206\n"
    "v_cvt_pk_bf16_f32 v190, v204, v205\n"
    "v_mfma_f32_16x16x32_bf16 v[20:23], a[116:119], v[184:187], v[20:23]\n"
    "v_exp_f32 v207, v207\n"
    "s_nop 0\n"
    "v_cvt_pk_bf16_f32 v191, v206, v207\n"
    "v_bfe_u32 v251, v242, 2, 8\n"
    "v_mfma_f32_16x16x32_bf16 v[24:27], a[88:91], v[212:215], 0\n"
    "s_waitcnt vmcnt(15)\n"
    "s_barrier\n"
    "v_add_f32 v192, v192, v220\n"
    "s_or_b32 s18, s25, 1\n"
    "v_mov_b32 v252, s18\n"
    "s_mov_b32 s96, 0x0\n"
    "s_mov_b32 s97, 0xff00\n"
    "s_mov_b64 exec, s[96:97]\n"
    "buffer_store_dword v252, v251, s[68:71], s11 offen offset:2048 sc1\n"
    "s_mov_b64 exec, -1\n"
    "v_mfma_f32_16x16x32_bf16 v[24:27], a[120:123], v[184:187], v[24:27]\n"
    "ds_read_b64_tr_b16 a[216:217], v236 offset:49152\n"
    "v_add_f32 v193, v193, v220\n"
    "v_add_f32 v194, v194, v220\n"
    "v_add_f32 v195, v195, v220\n"
    "v_mfma_f32_16x16x32_bf16 v[28:31], a[92:95], v[212:215], 0\n"
    "ds_read_b64_tr_b16 a[218:219], v236 offset:53248\n"
    "v_add_f32 v196, v196, v220\n"
    "v_add_f32 v197, v197, v220\n"
    "v_mfma_f32_16x16x32_bf16 v[28:31], a[124:127], v[184:187], v[28:31]\n"
    "ds_read_b64_tr_b16 a[220:221], v237 offset:49152\n"
    "v_add_f32 v198, v198, v220\n"
    "v_add_f32 v199, v199, v220\n"
    "v_mfma_f32_16x16x32_bf16 v[200:203], a[0:3], v[160:163], 0\n"
    "ds_read_b64_tr_b16 a[222:223], v237 offset:53248\n"
    "v_exp_f32 v192, v192\n"
    "v_exp_f32 v193, v193\n"
    "v_mfma_f32_16x16x32_bf16 v[200:203], a[4:7], v[164:167], v[200:203]\n"
    "ds_read_b64_tr_b16 a[224:225], v236 offset:49408\n"
    "v_exp_f32 v194, v194\n"
    "v_mfma_f32_16x16x32_bf16 v[200:203], a[8:11], v[168:171], v[200:203]\n"
    "ds_read_b64_tr_b16 a[226:227], v236 offset:53504\n"
    "v_cvt_pk_bf16_f32 v208, v192, v193\n"
    "v_exp_f32 v195, v195\n"
    "v_mfma_f32_16x16x32_bf16 v[200:203], a[12:15], v[172:175], v[200:203]\n"
    "ds_read_b128 a[0:3], v232 offset:49152\n"
    "v_exp_f32 v196, v196\n"
    "v_cvt_pk_bf16_f32 v209, v194, v195\n"
    "v_mfma_f32_16x16x32_bf16 v[204:207], a[28:31], v[172:175], 0\n"
    "ds_read_b128 a[4:7], v233 offset:49152\n"
    "v_exp_f32 v197, v197\n"
    "v_mfma_f32_16x16x32_bf16 v[204:207], a[24:27], v[168:171], v[204:207]\n"
    "ds_read_b128 a[8:11], v232 offset:49664\n"
    "v_exp_f32 v198, v198\n"
    "v_cvt_pk_bf16_f32 v210, v196, v197\n"
    "v_mfma_f32_16x16x32_bf16 v[204:207], a[20:23], v[164:167], v[204:207]\n"
    "ds_read_b128 a[12:15], v233 offset:49664\n"
    "v_exp_f32 v199, v199\n"
    "s_nop 0\n"
    "v_cvt_pk_bf16_f32 v211, v198, v199\n"
    "v_mfma_f32_16x16x32_bf16 v[204:207], a[16:19], v[160:163], v[204:207]\n"
    "buffer_load_dwordx4 v240, s[56:59], s81 offen offset:2048 nt lds\n"
    "v_mfma_f32_16x16x32_bf16 v[32:35], a[64:67], v[188:191], 0\n"
    "ds_read_b128 a[28:31], v233 offset:53760\n"
    "v_mfma_f32_16x16x32_bf16 v[32:35], a[96:99], v[208:211], v[32:35]\n"
    "ds_read_b128 a[24:27], v232 offset:53760\n"
    "v_mfma_f32_16x16x32_bf16 v[36:39], a[68:71], v[188:191], 0\n"
    "ds_read_b128 a[20:23], v233 offset:53248\n"
    "v_max3_f32 v246, v200, v201, v202\n"
    "v_max3_f32 v247, v203, v204, v205\n"
    "v_mfma_f32_16x16x32_bf16 v[36:39], a[100:103], v[208:211], v[36:39]\n"
    "ds_read_b128 a[16:19], v232 offset:53248\n"
    "v_max3_f32 v246, v246, v206, v207\n"
    "v_max_f32 v246, v246, v247\n"
    "v_mfma_f32_16x16x32_bf16 v[40:43], a[72:75], v[188:191], 0\n"
    "ds_read_b64_tr_b16 a[228:229], v237 offset:49408\n"
    "v_mov_b32 v247, v246\n"
    "v_mfma_f32_16x16x32_bf16 v[40:43], a[104:107], v[208:211], v[40:43]\n"
    "ds_read_b64_tr_b16 a[230:231], v237 offset:53504\n"
    "v_permlane32_swap_b32 v246, v247\n"
    "v_max_f32 v246, v246, v247\n"
    "v_mfma_f32_16x16x32_bf16 v[44:47], a[76:79], v[188:191], 0\n"
    "ds_read_b64_tr_b16 a[232:233], v236 offset:49664\n"
    "v_mov_b32 v247, v246\n"
    "v_mfma_f32_16x16x32_bf16 v[44:47], a[108:111], v[208:211], v[44:47]\n"
    "ds_read_b64_tr_b16 a[234:235], v236 offset:53760\n"
    "v_permlane16_swap_b32 v246, v247\n"
    "v_max_f32 v224, v246, v247\n"
    "v_mfma_f32_16x16x32_bf16 a[196:199], a[208:211], v[188:191], 0\n"
    "ds_read_b64_tr_b16 a[236:237], v237 offset:49664\n"
    "v_sub_f32 v224, 0, v224\n"
    "v_add_f32 v200, v200, v224\n"
    "v_mfma_f32_16x16x32_bf16 a[196:199], a[208:211], v[208:211], a[196:199]\n"
    "ds_read_b64_tr_b16 a[238:239], v237 offset:53760\n"
    "v_add_f32 v201, v201, v224\n"
    "v_add_f32 v202, v202, v224\n"
    "v_mfma_f32_16x16x32_bf16 v[192:195], a[32:35], v[160:163], 0\n"
    "ds_read_b64_tr_b16 a[240:241], v236 offset:49920\n"
    "v_add_f32 v203, v203, v224\n"
    "v_add_f32 v204, v204, v224\n"
    "v_mfma_f32_16x16x32_bf16 v[192:195], a[36:39], v[164:167], v[192:195]\n"
    "ds_read_b64_tr_b16 a[242:243], v236 offset:54016\n"
    "v_add_f32 v205, v205, v224\n"
    "v_add_f32 v206, v206, v224\n"
    "v_mfma_f32_16x16x32_bf16 v[192:195], a[40:43], v[168:171], v[192:195]\n"
    "ds_read_b64_tr_b16 a[244:245], v237 offset:49920\n"
    "v_add_f32 v207, v207, v224\n"
    "v_mfma_f32_16x16x32_bf16 v[192:195], a[44:47], v[172:175], v[192:195]\n"
    "buffer_load_dwordx4 v241, s[56:59], s81 offen offset:3072 nt lds\n"
    "v_exp_f32 v200, v200\n"
    "v_mfma_f32_16x16x32_bf16 v[196:199], a[60:63], v[172:175], 0\n"
    "ds_read_b64_tr_b16 a[246:247], v237 offset:54016\n"
    "s_add_u32 m0, s83, 32768\n"
    "v_exp_f32 v201, v201\n"
    "v_mfma_f32_16x16x32_bf16 v[196:199], a[56:59], v[168:171], v[196:199]\n"
    "s_xor_b32 s81, s4, 65536\n"
    "v_mov_b32 v225, v224\n"
    "v_mfma_f32_16x16x32_bf16 v[196:199], a[52:55], v[164:167], v[196:199]\n"
    "v_exp_f32 v202, v202\n"
    "v_cvt_pk_bf16_f32 v212, v200, v201\n"
    "v_mfma_f32_16x16x32_bf16 v[196:199], a[48:51], v[160:163], v[196:199]\n"
    "v_exp_f32 v203, v203\n"
    "v_mov_b32 v226, v224\n"
    "v_mfma_f32_16x16x32_bf16 v[48:51], a[80:83], v[188:191], 0\n"
    "ds_read_b128 a[32:35], v234 offset:49152\n"
    "v_exp_f32 v204, v204\n"
    "v_cvt_pk_bf16_f32 v213, v202, v203\n"
    "v_mfma_f32_16x16x32_bf16 v[48:51], a[112:115], v[208:211], v[48:51]\n"
    "ds_read_b128 a[36:39], v235 offset:49152\n"
    "v_exp_f32 v205, v205\n"
    "v_mfma_f32_16x16x32_bf16 v[52:55], a[84:87], v[188:191], 0\n"
    "ds_read_b128 a[40:43], v234 offset:49664\n"
    "v_mov_b32 v227, v224\n"
    "v_mfma_f32_16x16x32_bf16 v[52:55], a[116:119], v[208:211], v[52:55]\n"
    "ds_read_b128 a[44:47], v235 offset:49664\n"
    "v_exp_f32 v206, v206\n"
    "v_cvt_pk_bf16_f32 v214, v204, v205\n"
    "v_mfma_f32_16x16x32_bf16 v[56:59], a[88:91], v[188:191], 0\n"
    "ds_read_b128 a[60:63], v235 offset:53760\n"
    "v_exp_f32 v207, v207\n"
    "s_nop 0\n"
    "v_cvt_pk_bf16_f32 v215, v206, v207\n"
    "v_mfma_f32_16x16x32_bf16 v[56:59], a[120:123], v[208:211], v[56:59]\n"
    "ds_read_b128 a[56:59], v234 offset:53760\n"
    "v_add_f32 v192, v192, v224\n"
    "v_add_f32 v193, v193, v224\n"
    "v_mfma_f32_16x16x32_bf16 v[60:63], a[92:95], v[188:191], 0\n"
    "ds_read_b128 a[52:55], v235 offset:53248\n"
    "v_add_f32 v194, v194, v224\n"
    "v_add_f32 v195, v195, v224\n"
    "v_mfma_f32_16x16x32_bf16 v[60:63], a[124:127], v[208:211], v[60:63]\n"
    "ds_read_b128 a[48:51], v234 offset:53248\n"
    "v_add_f32 v196, v196, v224\n"
    "v_add_f32 v197, v197, v224\n"
    "s_waitcnt lgkmcnt(6)\n"
    "v_mfma_f32_16x16x32_bf16 v[200:203], a[0:3], v[176:179], v[228:231]\n"
    "v_add_f32 v198, v198, v224\n"
    "v_add_f32 v199, v199, v224\n"
    "v_mfma_f32_16x16x32_bf16 v[200:203], a[4:7], a[248:251], v[200:203]\n"
    "v_exp_f32 v192, v192\n"
    "v_exp_f32 v193, v193\n"
    "v_mfma_f32_16x16x32_bf16 v[200:203], a[8:11], v[180:183], v[200:203]\n"
    "buffer_load_dwordx4 v240, s[52:55], s81 offen offset:0 nt lds\n"
    "v_exp_f32 v194, v194\n"
    "v_cvt_pk_bf16_f32 v184, v192, v193\n"
    "v_mfma_f32_16x16x32_bf16 v[200:203], a[12:15], a[252:255], v[200:203]\n"
    "v_exp_f32 v195, v195\n"
    "v_mfma_f32_16x16x32_bf16 v[204:207], a[28:31], a[252:255], v[228:231]\n"
    "v_exp_f32 v196, v196\n"
    "v_cvt_pk_bf16_f32 v185, v194, v195\n"
    "v_mfma_f32_16x16x32_bf16 v[204:207], a[24:27], v[180:183], v[204:207]\n"
    "v_exp_f32 v197, v197\n"
    "v_exp_f32 v198, v198\n"
    "v_mfma_f32_16x16x32_bf16 v[204:207], a[20:23], a[248:251], v[204:207]\n"
    "v_cvt_pk_bf16_f32 v186, v196, v197\n"
    "v_exp_f32 v199, v199\n"
    "s_nop 0\n"
    "v_cvt_pk_bf16_f32 v187, v198, v199\n"
    "v_mfma_f32_16x16x32_bf16 v[204:207], a[16:19], v[176:179], v[204:207]\n"
    "v_exp_f32 v200, v200\n"
    "v_mfma_f32_16x16x32_bf16 v[64:67], a[64:67], v[212:215], 0\n"
    "v_mfma_f32_16x16x32_bf16 v[64:67], a[96:99], v[184:187], v[64:67]\n"
    "v_exp_f32 v201, v201\n"
    "v_mfma_f32_16x16x32_bf16 v[68:71], a[68:71], v[212:215], 0\n"
    "ds_read_b64_tr_b16 a[64:65], v238 offset:49152\n"
    "v_cvt_pk_bf16_f32 v188, v200, v201\n"
    "v_mfma_f32_16x16x32_bf16 v[68:71], a[100:103], v[184:187], v[68:71]\n"
    "v_exp_f32 v202, v202\n"
    "v_mfma_f32_16x16x32_bf16 v[72:75], a[72:75], v[212:215], 0\n"
    "ds_read_b64_tr_b16 a[66:67], v238 offset:53248\n"
    "v_mfma_f32_16x16x32_bf16 v[72:75], a[104:107], v[184:187], v[72:75]\n"
    "v_exp_f32 v203, v203\n"
    "v_mfma_f32_16x16x32_bf16 v[76:79], a[76:79], v[212:215], 0\n"
    "ds_read_b64_tr_b16 a[68:69], v239 offset:49152\n"
    "v_cvt_pk_bf16_f32 v189, v202, v203\n"
    "v_mfma_f32_16x16x32_bf16 v[76:79], a[108:111], v[184:187], v[76:79]\n"
    "v_exp_f32 v204, v204\n"
    "v_mfma_f32_16x16x32_bf16 a[200:203], a[208:211], v[212:215], 0\n"
    "buffer_load_dwordx4 v241, s[52:55], s81 offen offset:1024 nt lds\n"
    "v_mfma_f32_16x16x32_bf16 a[200:203], a[208:211], v[184:187], a[200:203]\n"
    "v_exp_f32 v205, v205\n"
    "v_mfma_f32_16x16x32_bf16 v[192:195], a[0:3], v[128:131], v[216:219]\n"
    "ds_read_b64_tr_b16 a[70:71], v239 offset:53248\n"
    "v_cvt_pk_bf16_f32 v190, v204, v205\n"
    "v_mfma_f32_16x16x32_bf16 v[192:195], a[4:7], v[132:135], v[192:195]\n"
    "v_exp_f32 v206, v206\n"
    "v_mfma_f32_16x16x32_bf16 v[192:195], a[8:11], v[136:139], v[192:195]\n"
    "ds_read_b64_tr_b16 a[72:73], v238 offset:49408\n"
    "v_mfma_f32_16x16x32_bf16 v[192:195], a[12:15], v[140:143], v[192:195]\n"
    "v_exp_f32 v207, v207\n"
    "v_mfma_f32_16x16x32_bf16 v[196:199], a[28:31], v[140:143], v[216:219]\n"
    "ds_read_b64_tr_b16 a[74:75], v238 offset:53504\n"
    "v_cvt_pk_bf16_f32 v191, v206, v207\n"
    "v_mfma_f32_16x16x32_bf16 v[196:199], a[24:27], v[136:139], v[196:199]\n"
    "ds_read_b64_tr_b16 a[76:77], v239 offset:49408\n"
    "v_mfma_f32_16x16x32_bf16 v[196:199], a[20:23], v[132:135], v[196:199]\n"
    "ds_read_b64_tr_b16 a[78:79], v239 offset:53504\n"
    "v_mfma_f32_16x16x32_bf16 v[196:199], a[16:19], v[128:131], v[196:199]\n"
    "v_exp_f32 v192, v192\n"
    "v_mfma_f32_16x16x32_bf16 v[80:83], a[80:83], v[212:215], 0\n"
    "v_mfma_f32_16x16x32_bf16 v[80:83], a[112:115], v[184:187], v[80:83]\n"
    "v_exp_f32 v193, v193\n"
    "v_mfma_f32_16x16x32_bf16 v[84:87], a[84:87], v[212:215], 0\n"
    "ds_read_b64_tr_b16 a[80:81], v238 offset:49664\n"
    "v_cvt_pk_bf16_f32 v208, v192, v193\n"
    "v_mfma_f32_16x16x32_bf16 v[84:87], a[116:119], v[184:187], v[84:87]\n"
    "v_exp_f32 v194, v194\n"
    "v_mfma_f32_16x16x32_bf16 v[88:91], a[88:91], v[212:215], 0\n"
    "ds_read_b64_tr_b16 a[82:83], v238 offset:53760\n"
    "v_mfma_f32_16x16x32_bf16 v[88:91], a[120:123], v[184:187], v[88:91]\n"
    "v_exp_f32 v195, v195\n"
    "v_mfma_f32_16x16x32_bf16 v[92:95], a[92:95], v[212:215], 0\n"
    "buffer_load_dwordx4 v240, s[52:55], s81 offen offset:2048 nt lds\n"
    "v_cvt_pk_bf16_f32 v209, v194, v195\n"
    "v_mfma_f32_16x16x32_bf16 v[92:95], a[124:127], v[184:187], v[92:95]\n"
    "v_exp_f32 v196, v196\n"
    "v_mfma_f32_16x16x32_bf16 v[200:203], a[32:35], v[128:131], v[216:219]\n"
    "ds_read_b64_tr_b16 a[84:85], v239 offset:49664\n"
    "v_mfma_f32_16x16x32_bf16 v[200:203], a[36:39], v[132:135], v[200:203]\n"
    "v_exp_f32 v197, v197\n"
    "s_waitcnt lgkmcnt(3)\n"
    "v_mfma_f32_16x16x32_bf16 v[200:203], a[40:43], v[136:139], v[200:203]\n"
    "ds_read_b64_tr_b16 a[86:87], v239 offset:53760\n"
    "v_cvt_pk_bf16_f32 v210, v196, v197\n"
    "v_mfma_f32_16x16x32_bf16 v[200:203], a[44:47], v[140:143], v[200:203]\n"
    "v_exp_f32 v198, v198\n"
    "v_mfma_f32_16x16x32_bf16 v[204:207], a[60:63], v[140:143], v[216:219]\n"
    "ds_read_b64_tr_b16 a[88:89], v238 offset:49920\n"
    "v_mfma_f32_16x16x32_bf16 v[204:207], a[56:59], v[136:139], v[204:207]\n"
    "v_exp_f32 v199, v199\n"
    "v_mfma_f32_16x16x32_bf16 v[204:207], a[52:55], v[132:135], v[204:207]\n"
    "ds_read_b64_tr_b16 a[90:91], v238 offset:54016\n"
    "v_cvt_pk_bf16_f32 v211, v198, v199\n"
    "v_mfma_f32_16x16x32_bf16 v[204:207], a[48:51], v[128:131], v[204:207]\n"
    "v_exp_f32 v200, v200\n"
    "v_mfma_f32_16x16x32_bf16 v[96:99], a[216:219], v[188:191], v[96:99]\n"
    "ds_read_b64_tr_b16 a[92:93], v239 offset:49920\n"
    "v_mfma_f32_16x16x32_bf16 v[100:103], a[220:223], v[188:191], v[100:103]\n"
    "v_exp_f32 v201, v201\n"
    "v_mfma_f32_16x16x32_bf16 v[104:107], a[224:227], v[188:191], v[104:107]\n"
    "ds_read_b64_tr_b16 a[94:95], v239 offset:54016\n"
    "v_cvt_pk_bf16_f32 v212, v200, v201\n"
    "v_mfma_f32_16x16x32_bf16 v[108:111], a[228:231], v[188:191], v[108:111]\n"
    "v_exp_f32 v202, v202\n"
    "v_mfma_f32_16x16x32_bf16 v[112:115], a[232:235], v[188:191], v[112:115]\n"
    "buffer_load_dwordx4 v241, s[52:55], s81 offen offset:3072 nt lds\n"
    "v_mfma_f32_16x16x32_bf16 v[116:119], a[236:239], v[188:191], v[116:119]\n"
    "v_exp_f32 v203, v203\n"
    "v_mfma_f32_16x16x32_bf16 v[120:123], a[240:243], v[188:191], v[120:123]\n"
    "s_add_u32 m0, s83, 98304\n"
    "v_cvt_pk_bf16_f32 v213, v202, v203\n"
    "v_mfma_f32_16x16x32_bf16 v[124:127], a[244:247], v[188:191], v[124:127]\n"
    "v_exp_f32 v204, v204\n"
    "v_mfma_f32_16x16x32_bf16 a[204:207], a[208:211], v[188:191], a[204:207]\n"
    "buffer_load_dword a215, v242, s[64:67], s86 offen offset:1024\n"
    "s_xor_b32 s81, s4, 65536\n"
    "L_h6:\n"
    "s_cbranch_execz L_h10\n"
    "v_mfma_f32_16x16x32_bf16 v[192:195], a[0:3], v[144:147], v[220:223]\n"
    "v_exp_f32 v205, v205\n"
    "v_mfma_f32_16x16x32_bf16 v[192:195], a[4:7], v[148:151], v[192:195]\n"
    "v_cvt_pk_bf16_f32 v214, v204, v205\n"
    "v_mfma_f32_16x16x32_bf16 v[192:195], a[8:11], v[152:155], v[192:195]\n"
    "v_exp_f32 v206, v206\n"
    "v_mfma_f32_16x16x32_bf16 v[192:195], a[12:15], v[156:159], v[192:195]\n"
    "v_mfma_f32_16x16x32_bf16 v[196:199], a[28:31], v[156:159], v[220:223]\n"
    "v_exp_f32 v207, v207\n"
    "v_mfma_f32_16x16x32_bf16 v[196:199], a[24:27], v[152:155], v[196:199]\n"
    "v_cvt_pk_bf16_f32 v215, v206, v207\n"
    "v_mfma_f32_16x16x32_bf16 v[196:199], a[20:23], v[148:151], v[196:199]\n"
    "v_mfma_f32_16x16x32_bf16 v[196:199], a[16:19], v[144:147], v[196:199]\n"
    "s_nop 1\n"
    "v_exp_f32 v192, v192\n"
    "v_mfma_f32_16x16x32_bf16 v[0:3], a[216:219], v[208:211], v[0:3]\n"
    "v_mfma_f32_16x16x32_bf16 v[0:3], a[64:67], v[212:215], v[0:3]\n"
    "v_exp_f32 v193, v193\n"
    "v_mfma_f32_16x16x32_bf16 v[4:7], a[220:223], v[208:211], v[4:7]\n"
    "buffer_load_dwordx4 v240, s[56:59], s81 offen offset:0 nt lds\n"
    "v_cvt_pk_bf16_f32 v184, v192, v193\n"
    "v_mfma_f32_16x16x32_bf16 v[4:7], a[68:71], v[212:215], v[4:7]\n"
    "v_exp_f32 v194, v194\n"
    "v_mfma_f32_16x16x32_bf16 v[8:11], a[224:227], v[208:211], v[8:11]\n"
    "v_mfma_f32_16x16x32_bf16 v[8:11], a[72:75], v[212:215], v[8:11]\n"
    "v_exp_f32 v195, v195\n"
    "v_mfma_f32_16x16x32_bf16 v[12:15], a[228:231], v[208:211], v[12:15]\n"
    "v_cvt_pk_bf16_f32 v185, v194, v195\n"
    "v_mfma_f32_16x16x32_bf16 v[12:15], a[76:79], v[212:215], v[12:15]\n"
    "v_exp_f32 v196, v196\n"
    "v_mfma_f32_16x16x32_bf16 a[192:195], a[208:211], v[208:211], a[192:195]\n"
    "v_mfma_f32_16x16x32_bf16 a[192:195], a[208:211], v[212:215], a[192:195]\n"
    "v_exp_f32 v197, v197\n"
    "v_mfma_f32_16x16x32_bf16 v[200:203], a[32:35], v[144:147], v[220:223]\n"
    "v_cvt_pk_bf16_f32 v186, v196, v197\n"
    "v_mfma_f32_16x16x32_bf16 v[200:203], a[36:39], v[148:151], v[200:203]\n"
    "v_exp_f32 v198, v198\n"
    "v_mfma_f32_16x16x32_bf16 v[200:203], a[40:43], v[152:155], v[200:203]\n"
    "s_memrealtime s[98:99]\n"
    "v_mfma_f32_16x16x32_bf16 v[200:203], a[44:47], v[156:159], v[200:203]\n"
    "v_exp_f32 v199, v199\n"
    "v_mfma_f32_16x16x32_bf16 v[204:207], a[60:63], v[156:159], v[220:223]\n"
    "v_cvt_pk_bf16_f32 v187, v198, v199\n"
    "v_mfma_f32_16x16x32_bf16 v[204:207], a[56:59], v[152:155], v[204:207]\n"
    "buffer_load_dwordx4 v241, s[56:59], s81 offen offset:1024 nt lds\n"
    "v_mfma_f32_16x16x32_bf16 v[204:207], a[52:55], v[148:151], v[204:207]\n"
    "s_waitcnt lgkmcnt(0)\n"
    "s_sub_u32 s99, s98, s25\n"
    "v_mfma_f32_16x16x32_bf16 v[204:207], a[48:51], v[144:147], v[204:207]\n"
    "v_exp_f32 v200, v200\n"
    "v_mfma_f32_16x16x32_bf16 v[16:19], a[232:235], v[208:211], v[16:19]\n"
    "s_cmp_lt_i32 s99, 0\n"
    "s_cbranch_scc1 L_pw1\n"
    "L_pb1:\n"
    "v_mfma_f32_16x16x32_bf16 v[16:19], a[80:83], v[212:215], v[16:19]\n"
    "v_exp_f32 v201, v201\n"
    "v_mfma_f32_16x16x32_bf16 v[20:23], a[236:239], v[208:211], v[20:23]\n"
    "s_add_u32 s25, s98, 130\n"
    "v_cvt_pk_bf16_f32 v188, v200, v201\n"
    "buffer_load_dword v250, v251, s[68:71], s92 offen offset:0 sc1\n"
    "v_mfma_f32_16x16x32_bf16 v[20:23], a[84:87], v[212:215], v[20:23]\n"
    "v_exp_f32 v202, v202\n"
    "v_mfma_f32_16x16x32_bf16 v[24:27], a[240:243], v[208:211], v[24:27]\n"
    "s_waitcnt vmcnt(18)\n"
    "s_barrier\n"
    "v_mfma_f32_16x16x32_bf16 v[24:27], a[88:91], v[212:215], v[24:27]\n"
    "v_exp_f32 v203, v203\n"
    "v_mfma_f32_16x16x32_bf16 v[28:31], a[244:247], v[208:211], v[28:31]\n"
    "ds_read_b64_tr_b16 a[96:97], v236 offset:0\n"
    "v_cvt_pk_bf16_f32 v189, v202, v203\n"
    "v_mfma_f32_16x16x32_bf16 v[28:31], a[92:95], v[212:215], v[28:31]\n"
    "v_exp_f32 v204, v204\n"
    "v_mfma_f32_16x16x32_bf16 v[192:195], a[0:3], v[160:163], v[224:227]\n"
    "ds_read_b64_tr_b16 a[98:99], v236 offset:4096\n"
    "v_mfma_f32_16x16x32_bf16 v[192:195], a[4:7], v[164:167], v[192:195]\n"
    "v_exp_f32 v205, v205\n"
    "v_mfma_f32_16x16x32_bf16 v[192:195], a[8:11], v[168:171], v[192:195]\n"
    "ds_read_b64_tr_b16 a[100:101], v237 offset:0\n"
    "v_cvt_pk_bf16_f32 v190, v204, v205\n"
    "v_mfma_f32_16x16x32_bf16 v[192:195], a[12:15], v[172:175], v[192:195]\n"
    "v_exp_f32 v206, v206\n"
    "v_mfma_f32_16x16x32_bf16 v[196:199], a[28:31], v[172:175], v[224:227]\n"
    "buffer_load_dwordx4 v240, s[56:59], s81 offen offset:2048 nt lds\n"
    "v_mfma_f32_16x16x32_bf16 v[196:199], a[24:27], v[168:171], v[196:199]\n"
    "v_exp_f32 v207, v207\n"
    "v_mfma_f32_16x16x32_bf16 v[196:199], a[20:23], v[164:167], v[196:199]\n"
    "ds_read_b64_tr_b16 a[102:103], v237 offset:4096\n"
    "v_cvt_pk_bf16_f32 v191, v206, v207\n"
    "v_mfma_f32_16x16x32_bf16 v[196:199], a[16:19], v[160:163], v[196:199]\n"
    "v_exp_f32 v192, v192\n"
    "v_mfma_f32_16x16x32_bf16 v[32:35], a[216:219], v[184:187], v[32:35]\n"
    "ds_read_b64_tr_b16 a[104:105], v236 offset:256\n"
    "v_mfma_f32_16x16x32_bf16 v[32:35], a[64:67], v[188:191], v[32:35]\n"
    "v_exp_f32 v193, v193\n"
    "v_mfma_f32_16x16x32_bf16 v[36:39], a[220:223], v[184:187], v[36:39]\n"
    "ds_read_b64_tr_b16 a[106:107], v236 offset:4352\n"
    "v_cvt_pk_bf16_f32 v208, v192, v193\n"
    "v_mfma_f32_16x16x32_bf16 v[36:39], a[68:71], v[188:191], v[36:39]\n"
    "v_exp_f32 v194, v194\n"
    "v_mfma_f32_16x16x32_bf16 v[40:43], a[224:227], v[184:187], v[40:43]\n"
    "ds_read_b128 a[0:3], v232 offset:0\n"
    "v_mfma_f32_16x16x32_bf16 v[40:43], a[72:75], v[188:191], v[40:43]\n"
    "v_exp_f32 v195, v195\n"
    "v_mfma_f32_16x16x32_bf16 v[44:47], a[228:231], v[184:187], v[44:47]\n"
    "ds_read_b128 a[4:7], v233 offset:0\n"
    "v_cvt_pk_bf16_f32 v209, v194, v195\n"
    "v_mfma_f32_16x16x32_bf16 v[44:47], a[76:79], v[188:191], v[44:47]\n"
    "v_exp_f32 v196, v196\n"
    "v_mfma_f32_16x16x32_bf16 a[196:199], a[208:211], v[184:187], a[196:199]\n"
    "ds_read_b128 a[8:11], v232 offset:512\n"
    "v_mfma_f32_16x16x32_bf16 a[196:199], a[208:211], v[188:191], a[196:199]\n"
    "v_exp_f32 v197, v197\n"
    "v_mfma_f32_16x16x32_bf16 v[200:203], a[32:35], v[160:163], v[224:227]\n"
    "buffer_load_dwordx4 v241, s[56:59], s81 offen offset:3072 nt lds\n"
    "v_cvt_pk_bf16_f32 v210, v196, v197\n"
    "v_mfma_f32_16x16x32_bf16 v[200:203], a[36:39], v[164:167], v[200:203]\n"
    "v_exp_f32 v198, v198\n"
    "v_mfma_f32_16x16x32_bf16 v[200:203], a[40:43], v[168:171], v[200:203]\n"
    "ds_read_b128 a[12:15], v233 offset:512\n"
    "s_add_u32 m0, s83, 49152\n"
    "v_mfma_f32_16x16x32_bf16 v[200:203], a[44:47], v[172:175], v[200:203]\n"
    "v_exp_f32 v199, v199\n"
    "v_mfma_f32_16x16x32_bf16 v[204:207], a[60:63], v[172:175], v[224:227]\n"
    "ds_read_b128 a[28:31], v233 offset:4608\n"
    "v_cvt_pk_bf16_f32 v211, v198, v199\n"
    "v_mfma_f32_16x16x32_bf16 v[204:207], a[56:59], v[168:171], v[204:207]\n"
    "ds_read_b128 a[24:27], v232 offset:4608\n"
    "s_xor_b32 s81, s4, 81920\n"
    "v_mfma_f32_16x16x32_bf16 v[204:207], a[52:55], v[164:167], v[204:207]\n"
    "ds_read_b128 a[20:23], v233 offset:4096\n"
    "v_mfma_f32_16x16x32_bf16 v[204:207], a[48:51], v[160:163], v[204:207]\n"
    "v_exp_f32 v200, v200\n"
    "v_mfma_f32_16x16x32_bf16 v[48:51], a[232:235], v[184:187], v[48:51]\n"
    "ds_read_b128 a[16:19], v232 offset:4096\n"
    "v_mfma_f32_16x16x32_bf16 v[48:51], a[80:83], v[188:191], v[48:51]\n"
    "v_exp_f32 v201, v201\n"
    "v_mfma_f32_16x16x32_bf16 v[52:55], a[236:239], v[184:187], v[52:55]\n"
    "ds_read_b64_tr_b16 a[108:109], v237 offset:256\n"
    "v_cvt_pk_bf16_f32 v212, v200, v201\n"
    "v_mfma_f32_16x16x32_bf16 v[52:55], a[84:87], v[188:191], v[52:55]\n"
    "v_exp_f32 v202, v202\n"
    "v_mfma_f32_16x16x32_bf16 v[56:59], a[240:243], v[184:187], v[56:59]\n"
    "ds_read_b64_tr_b16 a[110:111], v237 offset:4352\n"
    "s_waitcnt lgkmcnt(5)\n"
    "v_mfma_f32_16x16x32_bf16 v[56:59], a[88:91], v[188:191], v[56:59]\n"
    "v_exp_f32 v203, v203\n"
    "v_mfma_f32_16x16x32_bf16 v[60:63], a[244:247], v[184:187], v[60:63]\n"
    "ds_read_b64_tr_b16 a[112:113], v236 offset:512\n"
    "v_cvt_pk_bf16_f32 v213, v202, v203\n"
    "v_mfma_f32_16x16x32_bf16 v[60:63], a[92:95], v[188:191], v[60:63]\n"
    "v_exp_f32 v204, v204\n"
    "v_mfma_f32_16x16x32_bf16 v[192:195], a[0:3], v[176:179], v[228:231]\n"
    "buffer_load_dwordx4 v240, s[52:55], s81 offen offset:0 nt lds\n"
    "v_mfma_f32_16x16x32_bf16 v[192:195], a[4:7], a[248:251], v[192:195]\n"
    "v_exp_f32 v205, v205\n"
    "v_mfma_f32_16x16x32_bf16 v[192:195], a[8:11], v[180:183], v[192:195]\n"
    "ds_read_b64_tr_b16 a[114:115], v236 offset:4608\n"
    "v_cvt_pk_bf16_f32 v214, v204, v205\n"
    "v_mfma_f32_16x16x32_bf16 v[192:195], a[12:15], a[252:255], v[192:195]\n"
    "v_exp_f32 v206, v206\n"
    "v_mfma_f32_16x16x32_bf16 v[196:199], a[28:31], a[252:255], v[228:231]\n"
    "ds_read_b64_tr_b16 a[116:117], v237 offset:512\n"
    "s_waitcnt lgkmcnt(3)\n"
    "v_mfma_f32_16x16x32_bf16 v[196:199], a[24:27], v[180:183], v[196:199]\n"
    "v_exp_f32 v207, v207\n"
    "v_mfma_f32_16x16x32_bf16 v[196:199], a[20:23], a[248:251], v[196:199]\n"
    "ds_read_b64_tr_b16 a[118:119], v237 offset:4608\n"
    "v_cvt_pk_bf16_f32 v215, v206, v207\n"
    "v_mfma_f32_16x16x32_bf16 v[196:199], a[16:19], v[176:179], v[196:199]\n"
    "v_exp_f32 v192, v192\n"
    "v_mfma_f32_16x16x32_bf16 v[64:67], a[216:219], v[208:211], v[64:67]\n"
    "ds_read_b64_tr_b16 a[120:121], v236 offset:768\n"
    "v_mfma_f32_16x16x32_bf16 v[64:67], a[64:67], v[212:215], v[64:67]\n"
    "v_exp_f32 v193, v193\n"
    "v_mfma_f32_16x16x32_bf16 v[68:71], a[220:223], v[208:211], v[68:71]\n"
    "ds_read_b64_tr_b16 a[122:123], v236 offset:4864\n"
    "v_cvt_pk_bf16_f32 v184, v192, v193\n"
    "v_mfma_f32_16x16x32_bf16 v[68:71], a[68:71], v[212:215], v[68:71]\n"
    "v_exp_f32 v194, v194\n"
    "v_mfma_f32_16x16x32_bf16 v[72:75], a[224:227], v[208:211], v[72:75]\n"
    "ds_read_b64_tr_b16 a[124:125], v237 offset:768\n"
    "v_mfma_f32_16x16x32_bf16 v[72:75], a[72:75], v[212:215], v[72:75]\n"
    "v_exp_f32 v195, v195\n"
    "v_mfma_f32_16x16x32_bf16 v[76:79], a[228:231], v[208:211], v[76:79]\n"
    "buffer_load_dwordx4 v241, s[52:55], s81 offen offset:1024 nt lds\n"
    "v_cvt_pk_bf16_f32 v185, v194, v195\n"
    "v_mfma_f32_16x16x32_bf16 v[76:79], a[76:79], v[212:215], v[76:79]\n"
    "v_exp_f32 v196, v196\n"
    "v_mfma_f32_16x16x32_bf16 a[200:203], a[208:211], v[208:211], a[200:203]\n"
    "ds_read_b64_tr_b16 a[126:127], v237 offset:4864\n"
    "v_mfma_f32_16x16x32_bf16 a[200:203], a[208:211], v[212:215], a[200:203]\n"
    "v_exp_f32 v197, v197\n"
    "v_mfma_f32_16x16x32_bf16 v[200:203], a[0:3], v[128:131], v[216:219]\n"
    "ds_read_b128 a[32:35], v234 offset:0\n"
    "v_cvt_pk_bf16_f32 v186, v196, v197\n"
    "v_mfma_f32_16x16x32_bf16 v[200:203], a[4:7], v[132:135], v[200:203]\n"
    "v_exp_f32 v198, v198\n"
    "v_mfma_f32_16x16x32_bf16 v[200:203], a[8:11], v[136:139], v[200:203]\n"
    "ds_read_b128 a[36:39], v235 offset:0\n"
    "v_mfma_f32_16x16x32_bf16 v[200:203], a[12:15], v[140:143], v[200:203]\n"
    "v_exp_f32 v199, v199\n"
    "v_mfma_f32_16x16x32_bf16 v[204:207], a[28:31], v[140:143], v[216:219]\n"
    "ds_read_b128 a[40:43], v234 offset:512\n"
    "v_cvt_pk_bf16_f32 v187, v198, v199\n"
    "v_mfma_f32_16x16x32_bf16 v[204:207], a[24:27], v[136:139], v[204:207]\n"
    "ds_read_b128 a[44:47], v235 offset:512\n"
    "v_mfma_f32_16x16x32_bf16 v[204:207], a[20:23], v[132:135], v[204:207]\n"
    "ds_read_b128 a[60:63], v235 offset:4608\n"
    "v_mfma_f32_16x16x32_bf16 v[204:207], a[16:19], v[128:131], v[204:207]\n"
    "v_exp_f32 v200, v200\n"
    "v_mfma_f32_16x16x32_bf16 v[80:83], a[232:235], v[208:211], v[80:83]\n"
    "ds_read_b128 a[56:59], v234 offset:4608\n"
    "v_mfma_f32_16x16x32_bf16 v[80:83], a[80:83], v[212:215], v[80:83]\n"
    "v_exp_f32 v201, v201\n"
    "v_mfma_f32_16x16x32_bf16 v[84:87], a[236:239], v[208:211], v[84:87]\n"
    "buffer_load_dwordx4 v240, s[52:55], s81 offen offset:2048 nt lds\n"
    "v_cvt_pk_bf16_f32 v188, v200, v201\n"
    "v_mfma_f32_16x16x32_bf16 v[84:87], a[84:87], v[212:215], v[84:87]\n"
    "v_exp_f32 v202, v202\n"
    "v_mfma_f32_16x16x32_bf16 v[88:91], a[240:243], v[208:211], v[88:91]\n"
    "ds_read_b128 a[52:55], v235 offset:4096\n"
    "s_waitcnt lgkmcnt(4)\n"
    "v_mfma_f32_16x16x32_bf16 v[88:91], a[88:91], v[212:215], v[88:91]\n"
    "v_exp_f32 v203, v203\n"
    "v_mfma_f32_16x16x32_bf16 v[92:95], a[244:247], v[208:211], v[92:95]\n"
    "ds_read_b128 a[48:51], v234 offset:4096\n"
    "buffer_load_dword a215, v243, s[64:67], s86 offen offset:0\n"
    "v_cvt_pk_bf16_f32 v189, v202, v203\n"
    "v_mfma_f32_16x16x32_bf16 v[92:95], a[92:95], v[212:215], v[92:95]\n"
    "v_exp_f32 v204, v204\n"
    "v_mfma_f32_16x16x32_bf16 v[192:195], a[32:35], v[128:131], v[216:219]\n"
    "ds_read_b64_tr_b16 a[216:217], v238 offset:0\n"
    "s_waitcnt lgkmcnt(3)\n"
    "v_mfma_f32_16x16x32_bf16 v[192:195], a[36:39], v[132:135], v[192:195]\n"
    "v_exp_f32 v205, v205\n"
    "v_mfma_f32_16x16x32_bf16 v[192:195], a[40:43], v[136:139], v[192:195]\n"
    "ds_read_b64_tr_b16 a[218:219], v238 offset:4096\n"
    "v_cvt_pk_bf16_f32 v190, v204, v205\n"
    "v_mfma_f32_16x16x32_bf16 v[192:195], a[44:47], v[140:143], v[192:195]\n"
    "v_exp_f32 v206, v206\n"
    "v_mfma_f32_16x16x32_bf16 v[196:199], a[60:63], v[140:143], v[216:219]\n"
    "ds_read_b64_tr_b16 a[220:221], v239 offset:0\n"
    "s_waitcnt lgkmcnt(4)\n"
    "v_mfma_f32_16x16x32_bf16 v[196:199], a[56:59], v[136:139], v[196:199]\n"
    "v_exp_f32 v207, v207\n"
    "v_mfma_f32_16x16x32_bf16 v[196:199], a[52:55], v[132:135], v[196:199]\n"
    "ds_read_b64_tr_b16 a[222:223], v239 offset:4096\n"
    "v_cvt_pk_bf16_f32 v191, v206, v207\n"
    "s_waitcnt lgkmcnt(4)\n"
    "v_mfma_f32_16x16x32_bf16 v[196:199], a[48:51], v[128:131], v[196:199]\n"
    "v_exp_f32 v192, v192\n"
    "v_mfma_f32_16x16x32_bf16 v[96:99], a[96:99], v[184:187], v[96:99]\n"
    "buffer_load_dwordx4 v241, s[52:55], s81 offen offset:3072 nt lds\n"
    "v_mfma_f32_16x16x32_bf16 v[100:103], a[100:103], v[184:187], v[100:103]\n"
    "v_exp_f32 v193, v193\n"
    "v_mfma_f32_16x16x32_bf16 v[104:107], a[104:107], v[184:187], v[104:107]\n"
    "ds_read_b64_tr_b16 a[224:225], v238 offset:256\n"
    "v_cvt_pk_bf16_f32 v208, v192, v193\n"
    "v_mfma_f32_16x16x32_bf16 v[108:111], a[108:111], v[184:187], v[108:111]\n"
    "v_exp_f32 v194, v194\n"
    "v_mfma_f32_16x16x32_bf16 v[112:115], a[112:115], v[184:187], v[112:115]\n"
    "ds_read_b64_tr_b16 a[226:227], v238 offset:4352\n"
    "s_add_u32 m0, s83, 114688\n"
    "v_mfma_f32_16x16x32_bf16 v[116:119], a[116:119], v[184:187], v[116:119]\n"
    "v_exp_f32 v195, v195\n"
    "v_mfma_f32_16x16x32_bf16 v[120:123], a[120:123], v[184:187], v[120:123]\n"
    "ds_read_b64_tr_b16 a[228:229], v239 offset:256\n"
    "v_cvt_pk_bf16_f32 v209, v194, v195\n"
    "v_mfma_f32_16x16x32_bf16 v[124:127], a[124:127], v[184:187], v[124:127]\n"
    "v_exp_f32 v196, v196\n"
    "v_mfma_f32_16x16x32_bf16 a[204:207], a[208:211], v[184:187], a[204:207]\n"
    "ds_read_b64_tr_b16 a[230:231], v239 offset:4352\n"
    "s_xor_b32 s81, s4, 81920\n"
    "v_mfma_f32_16x16x32_bf16 v[200:203], a[0:3], v[144:147], v[220:223]\n"
    "v_exp_f32 v197, v197\n"
    "v_mfma_f32_16x16x32_bf16 v[200:203], a[4:7], v[148:151], v[200:203]\n"
    "ds_read_b64_tr_b16 a[232:233], v238 offset:512\n"
    "v_cvt_pk_bf16_f32 v210, v196, v197\n"
    "v_mfma_f32_16x16x32_bf16 v[200:203], a[8:11], v[152:155], v[200:203]\n"
    "v_exp_f32 v198, v198\n"
    "v_mfma_f32_16x16x32_bf16 v[200:203], a[12:15], v[156:159], v[200:203]\n"
    "ds_read_b64_tr_b16 a[234:235], v238 offset:4608\n"
    "v_mfma_f32_16x16x32_bf16 v[204:207], a[28:31], v[156:159], v[220:223]\n"
    "v_exp_f32 v199, v199\n"
    "v_mfma_f32_16x16x32_bf16 v[204:207], a[24:27], v[152:155], v[204:207]\n"
    "ds_read_b64_tr_b16 a[236:237], v239 offset:512\n"
    "v_cvt_pk_bf16_f32 v211, v198, v199\n"
    "v_mfma_f32_16x16x32_bf16 v[204:207], a[20:23], v[148:151], v[204:207]\n"
    "buffer_load_dwordx4 v240, s[56:59], s81 offen offset:0 nt lds\n"
    "v_mfma_f32_16x16x32_bf16 v[204:207], a[16:19], v[144:147], v[204:207]\n"
    "v_exp_f32 v200, v200\n"
    "v_mfma_f32_16x16x32_bf16 v[0:3], a[96:99], v[188:191], v[0:3]\n"
    "ds_read_b64_tr_b16 a[238:239], v239 offset:4608\n"
    "s_waitcnt lgkmcnt(4)\n"
    "v_mfma_f32_16x16x32_bf16 v[0:3], a[216:219], v[208:211], v[0:3]\n"
    "v_exp_f32 v201, v201\n"
    "v_mfma_f32_16x16x32_bf16 v[4:7], a[100:103], v[188:191], v[4:7]\n"
    "ds_read_b64_tr_b16 a[240:241], v238 offset:768\n"
    "v_cvt_pk_bf16_f32 v212, v200, v201\n"
    "v_mfma_f32_16x16x32_bf16 v[4:7], a[220:223], v[208:211], v[4:7]\n"
    "v_exp_f32 v202, v202\n"
    "v_mfma_f32_16x16x32_bf16 v[8:11], a[104:107], v[188:191], v[8:11]\n"
    "ds_read_b64_tr_b16 a[242:243], v238 offset:4864\n"
    "v_mfma_f32_16x16x32_bf16 v[8:11], a[224:227], v[208:211], v[8:11]\n"
    "v_exp_f32 v203, v203\n"
    "v_mfma_f32_16x16x32_bf16 v[12:15], a[108:111], v[188:191], v[12:15]\n"
    "ds_read_b64_tr_b16 a[244:245], v239 offset:768\n"
    "v_cvt_pk_bf16_f32 v213, v202, v203\n"
    "v_mfma_f32_16x16x32_bf16 v[12:15], a[228:231], v[208:211], v[12:15]\n"
    "v_exp_f32 v204, v204\n"
    "v_mfma_f32_16x16x32_bf16 a[192:195], a[208:211], v[188:191], a[192:195]\n"
    "ds_read_b64_tr_b16 a[246:247], v239 offset:4864\n"
    "v_mfma_f32_16x16x32_bf16 a[192:195], a[208:211], v[208:211], a[192:195]\n"
    "v_exp_f32 v205, v205\n"
    "v_mfma_f32_16x16x32_bf16 v[192:195], a[32:35], v[144:147], v[220:223]\n"
    "v_cvt_pk_bf16_f32 v214, v204, v205\n"
    "v_mfma_f32_16x16x32_bf16 v[192:195], a[36:39], v[148:151], v[192:195]\n"
    "v_exp_f32 v206, v206\n"
    "v_mfma_f32_16x16x32_bf16 v[192:195], a[40:43], v[152:155], v[192:195]\n"
    "buffer_load_dwordx4 v241, s[56:59], s81 offen offset:1024 nt lds\n"
    "s_memrealtime s[98:99]\n"
    "v_mfma_f32_16x16x32_bf16 v[192:195], a[44:47], v[156:159], v[192:195]\n"
    "v_exp_f32 v207, v207\n"
    "v_mfma_f32_16x16x32_bf16 v[196:199], a[60:63], v[156:159], v[220:223]\n"
    "v_cvt_pk_bf16_f32 v215, v206, v207\n"
    "v_mfma_f32_16x16x32_bf16 v[196:199], a[56:59], v[152:155], v[196:199]\n"
    "s_waitcnt lgkmcnt(1)\n"
    "s_waitcnt lgkmcnt(0)\n"
    "v_mfma_f32_16x16x32_bf16 v[196:199], a[52:55], v[148:151], v[196:199]\n"
    "s_waitcnt lgkmcnt(0)\n"
    "s_sub_u32 s99, s98, s25\n"
    "v_mfma_f32_16x16x32_bf16 v[196:199], a[48:51], v[144:147], v[196:199]\n"
    "v_exp_f32 v192, v192\n"
    "v_mfma_f32_16x16x32_bf16 v[16:19], a[112:115], v[188:191], v[16:19]\n"
    "s_cmp_lt_i32 s99, 0\n"
    "s_cbranch_scc1 L_pw2\n"
    "L_pb2:\n"
    "v_mfma_f32_16x16x32_bf16 v[16:19], a[232:235], v[208:211], v[16:19]\n"
    "v_exp_f32 v193, v193\n"
    "v_mfma_f32_16x16x32_bf16 v[20:23], a[116:119], v[188:191], v[20:23]\n"
    "s_add_u32 s25, s98, 130\n"
    "v_cvt_pk_bf16_f32 v184, v192, v193\n"
    "v_mfma_f32_16x16x32_bf16 v[20:23], a[236:239], v[208:211], v[20:23]\n"
    "v_exp_f32 v194, v194\n"
    "v_mfma_f32_16x16x32_bf16 v[24:27], a[120:123], v[188:191], v[24:27]\n"
    "s_waitcnt vmcnt(17)\n"
    "s_barrier\n"
    "v_mfma_f32_16x16x32_bf16 v[24:27], a[240:243], v[208:211], v[24:27]\n"
    "v_exp_f32 v195, v195\n"
    "v_mfma_f32_16x16x32_bf16 v[28:31], a[124:127], v[188:191], v[28:31]\n"
    "ds_read_b64_tr_b16 a[64:65], v236 offset:16384\n"
    "v_cvt_pk_bf16_f32 v185, v194, v195\n"
    "v_mfma_f32_16x16x32_bf16 v[28:31], a[244:247], v[208:211], v[28:31]\n"
    "v_exp_f32 v196, v196\n"
    "v_mfma_f32_16x16x32_bf16 v[200:203], a[0:3], v[160:163], v[224:227]\n"
    "ds_read_b64_tr_b16 a[66:67], v236 offset:20480\n"
    "v_mfma_f32_16x16x32_bf16 v[200:203], a[4:7], v[164:167], v[200:203]\n"
    "v_exp_f32 v197, v197\n"
    "v_mfma_f32_16x16x32_bf16 v[200:203], a[8:11], v[168:171], v[200:203]\n"
    "buffer_load_dwordx4 v240, s[56:59], s81 offen offset:2048 nt lds\n"
    "v_cvt_pk_bf16_f32 v186, v196, v197\n"
    "v_mfma_f32_16x16x32_bf16 v[200:203], a[12:15], v[172:175], v[200:203]\n"
    "v_exp_f32 v198, v198\n"
    "v_mfma_f32_16x16x32_bf16 v[204:207], a[28:31], v[172:175], v[224:227]\n"
    "ds_read_b64_tr_b16 a[68:69], v237 offset:16384\n"
    "v_mfma_f32_16x16x32_bf16 v[204:207], a[24:27], v[168:171], v[204:207]\n"
    "v_exp_f32 v199, v199\n"
    "v_mfma_f32_16x16x32_bf16 v[204:207], a[20:23], v[164:167], v[204:207]\n"
    "ds_read_b64_tr_b16 a[70:71], v237 offset:20480\n"
    "v_cvt_pk_bf16_f32 v187, v198, v199\n"
    "v_mfma_f32_16x16x32_bf16 v[204:207], a[16:19], v[160:163], v[204:207]\n"
    "v_exp_f32 v200, v200\n"
    "v_mfma_f32_16x16x32_bf16 v[32:35], a[96:99], v[212:215], v[32:35]\n"
    "ds_read_b64_tr_b16 a[72:73], v236 offset:16640\n"
    "v_mfma_f32_16x16x32_bf16 v[32:35], a[216:219], v[184:187], v[32:35]\n"
    "v_exp_f32 v201, v201\n"
    "v_mfma_f32_16x16x32_bf16 v[36:39], a[100:103], v[212:215], v[36:39]\n"
    "ds_read_b64_tr_b16 a[74:75], v236 offset:20736\n"
    "v_cvt_pk_bf16_f32 v188, v200, v201\n"
    "v_mfma_f32_16x16x32_bf16 v[36:39], a[220:223], v[184:187], v[36:39]\n"
    "v_exp_f32 v202, v202\n"
    "v_mfma_f32_16x16x32_bf16 v[40:43], a[104:107], v[212:215], v[40:43]\n"
    "ds_read_b128 a[0:3], v232 offset:16384\n"
    "v_mfma_f32_16x16x32_bf16 v[40:43], a[224:227], v[184:187], v[40:43]\n"
    "v_exp_f32 v203, v203\n"
    "v_mfma_f32_16x16x32_bf16 v[44:47], a[108:111], v[212:215], v[44:47]\n"
    "ds_read_b128 a[4:7], v233 offset:16384\n"
    "v_cvt_pk_bf16_f32 v189, v202, v203\n"
    "v_mfma_f32_16x16x32_bf16 v[44:47], a[228:231], v[184:187], v[44:47]\n"
    "v_exp_f32 v204, v204\n"
    "v_mfma_f32_16x16x32_bf16 a[196:199], a[208:211], v[212:215], a[196:199]\n"
    "buffer_load_dwordx4 v241, s[56:59], s81 offen offset:3072 nt lds\n"
    "v_mfma_f32_16x16x32_bf16 a[196:199], a[208:211], v[184:187], a[196:199]\n"
    "v_exp_f32 v205, v205\n"
    "v_mfma_f32_16x16x32_bf16 v[192:195], a[32:35], v[160:163], v[224:227]\n"
    "ds_read_b128 a[8:11], v232 offset:16896\n"
    "v_cvt_pk_bf16_f32 v190, v204, v205\n"
    "v_mfma_f32_16x16x32_bf16 v[192:195], a[36:39], v[164:167], v[192:195]\n"
    "v_exp_f32 v206, v206\n"
    "v_mfma_f32_16x16x32_bf16 v[192:195], a[40:43], v[168:171], v[192:195]\n"
    "ds_read_b128 a[12:15], v233 offset:16896\n"
    "s_add_u32 m0, s83, 0\n"
    "v_mfma_f32_16x16x32_bf16 v[192:195], a[44:47], v[172:175], v[192:195]\n"
    "v_exp_f32 v207, v207\n"
    "v_mfma_f32_16x16x32_bf16 v[196:199], a[60:63], v[172:175], v[224:227]\n"
    "ds_read_b128 a[28:31], v233 offset:20992\n"
    "v_cvt_pk_bf16_f32 v191, v206, v207\n"
    "v_mfma_f32_16x16x32_bf16 v[196:199], a[56:59], v[168:171], v[196:199]\n"
    "ds_read_b128 a[24:27], v232 offset:20992\n"
    "s_xor_b32 s81, s4, 98304\n"
    "v_mfma_f32_16x16x32_bf16 v[196:199], a[52:55], v[164:167], v[196:199]\n"
    "ds_read_b128 a[20:23], v233 offset:20480\n"
    "v_mfma_f32_16x16x32_bf16 v[196:199], a[48:51], v[160:163], v[196:199]\n"
    "v_exp_f32 v192, v192\n"
    "v_mfma_f32_16x16x32_bf16 v[48:51], a[112:115], v[212:215], v[48:51]\n"
    "ds_read_b128 a[16:19], v232 offset:20480\n"
    "v_mfma_f32_16x16x32_bf16 v[48:51], a[232:235], v[184:187], v[48:51]\n"
    "v_exp_f32 v193, v193\n"
    "v_mfma_f32_16x16x32_bf16 v[52:55], a[116:119], v[212:215], v[52:55]\n"
    "ds_read_b64_tr_b16 a[76:77], v237 offset:16640\n"
    "v_cvt_pk_bf16_f32 v208, v192, v193\n"
    "v_mfma_f32_16x16x32_bf16 v[52:55], a[236:239], v[184:187], v[52:55]\n"
    "v_exp_f32 v194, v194\n"
    "v_mfma_f32_16x16x32_bf16 v[56:59], a[120:123], v[212:215], v[56:59]\n"
    "buffer_load_dwordx4 v240, s[52:55], s81 offen offset:0 nt lds\n"
    "s_waitcnt lgkmcnt(4)\n"
    "v_mfma_f32_16x16x32_bf16 v[56:59], a[240:243], v[184:187], v[56:59]\n"
    "v_exp_f32 v195, v195\n"
    "v_mfma_f32_16x16x32_bf16 v[60:63], a[124:127], v[212:215], v[60:63]\n"
    "ds_read_b64_tr_b16 a[78:79], v237 offset:20736\n"
    "v_cvt_pk_bf16_f32 v209, v194, v195\n"
    "v_mfma_f32_16x16x32_bf16 v[60:63], a[244:247], v[184:187], v[60:63]\n"
    "v_exp_f32 v196, v196\n"
    "v_mfma_f32_16x16x32_bf16 v[200:203], a[0:3], v[176:179], v[228:231]\n"
    "ds_read_b64_tr_b16 a[80:81], v236 offset:16896\n"
    "v_mfma_f32_16x16x32_bf16 v[200:203], a[4:7], a[248:251], v[200:203]\n"
    "v_exp_f32 v197, v197\n"
    "v_mfma_f32_16x16x32_bf16 v[200:203], a[8:11], v[180:183], v[200:203]\n"
    "ds_read_b64_tr_b16 a[82:83], v236 offset:20992\n"
    "v_cvt_pk_bf16_f32 v210, v196, v197\n"
    "v_mfma_f32_16x16x32_bf16 v[200:203], a[12:15], a[252:255], v[200:203]\n"
    "v_exp_f32 v198, v198\n"
    "v_mfma_f32_16x16x32_bf16 v[204:207], a[28:31], a[252:255], v[228:231]\n"
    "ds_read_b64_tr_b16 a[84:85], v237 offset:16896\n"
    "s_waitcnt lgkmcnt(4)\n"
    "v_mfma_f32_16x16x32_bf16 v[204:207], a[24:27], v[180:183], v[204:207]\n"
    "v_exp_f32 v199, v199\n"
    "v_mfma_f32_16x16x32_bf16 v[204:207], a[20:23], a[248:251], v[204:207]\n"
    "ds_read_b64_tr_b16 a[86:87], v237 offset:20992\n"
    "v_cvt_pk_bf16_f32 v211, v198, v199\n"
    "v_mfma_f32_16x16x32_bf16 v[204:207], a[16:19], v[176:179], v[204:207]\n"
    "v_exp_f32 v200, v200\n"
    "v_mfma_f32_16x16x32_bf16 v[64:67], a[96:99], v[188:191], v[64:67]\n"
    "ds_read_b64_tr_b16 a[88:89], v236 offset:17152\n"
    "v_mfma_f32_16x16x32_bf16 v[64:67], a[216:219], v[208:211], v[64:67]\n"
    "v_exp_f32 v201, v201\n"
    "v_mfma_f32_16x16x32_bf16 v[68:71], a[100:103], v[188:191], v[68:71]\n"
    "buffer_load_dwordx4 v241, s[52:55], s81 offen offset:1024 nt lds\n"
    "v_cvt_pk_bf16_f32 v212, v200, v201\n"
    "v_mfma_f32_16x16x32_bf16 v[68:71], a[220:223], v[208:211], v[68:71]\n"
    "v_exp_f32 v202, v202\n"
    "v_mfma_f32_16x16x32_bf16 v[72:75], a[104:107], v[188:191], v[72:75]\n"
    "ds_read_b64_tr_b16 a[90:91], v236 offset:21248\n"
    "v_mfma_f32_16x16x32_bf16 v[72:75], a[224:227], v[208:211], v[72:75]\n"
    "v_exp_f32 v203, v203\n"
    "v_mfma_f32_16x16x32_bf16 v[76:79], a[108:111], v[188:191], v[76:79]\n"
    "ds_read_b64_tr_b16 a[92:93], v237 offset:17152\n"
    "v_cvt_pk_bf16_f32 v213, v202, v203\n"
    "v_mfma_f32_16x16x32_bf16 v[76:79], a[228:231], v[208:211], v[76:79]\n"
    "v_exp_f32 v204, v204\n"
    "v_mfma_f32_16x16x32_bf16 a[200:203], a[208:211], v[188:191], a[200:203]\n"
    "ds_read_b64_tr_b16 a[94:95], v237 offset:21248\n"
    "buffer_load_dword a215, v243, s[64:67], s86 offen offset:1024\n"
    "v_mfma_f32_16x16x32_bf16 a[200:203], a[208:211], v[208:211], a[200:203]\n"
    "v_exp_f32 v205, v205\n"
    "v_mfma_f32_16x16x32_bf16 v[192:195], a[0:3], v[128:131], v[216:219]\n"
    "ds_read_b128 a[32:35], v234 offset:16384\n"
    "v_cvt_pk_bf16_f32 v214, v204, v205\n"
    "v_mfma_f32_16x16x32_bf16 v[192:195], a[4:7], v[132:135], v[192:195]\n"
    "v_exp_f32 v206, v206\n"
    "v_mfma_f32_16x16x32_bf16 v[192:195], a[8:11], v[136:139], v[192:195]\n"
    "ds_read_b128 a[36:39], v235 offset:16384\n"
    "v_mfma_f32_16x16x32_bf16 v[192:195], a[12:15], v[140:143], v[192:195]\n"
    "v_exp_f32 v207, v207\n"
    "v_mfma_f32_16x16x32_bf16 v[196:199], a[28:31], v[140:143], v[216:219]\n"
    "ds_read_b128 a[40:43], v234 offset:16896\n"
    "v_cvt_pk_bf16_f32 v215, v206, v207\n"
    "v_mfma_f32_16x16x32_bf16 v[196:199], a[24:27], v[136:139], v[196:199]\n"
    "ds_read_b128 a[44:47], v235 offset:16896\n"
    "v_mfma_f32_16x16x32_bf16 v[196:199], a[20:23], v[132:135], v[196:199]\n"
    "buffer_load_dwordx4 v240, s[52:55], s81 offen offset:2048 nt lds\n"
    "v_mfma_f32_16x16x32_bf16 v[196:199], a[16:19], v[128:131], v[196:199]\n"
    "v_exp_f32 v192, v192\n"
    "v_mfma_f32_16x16x32_bf16 v[80:83], a[112:115], v[188:191], v[80:83]\n"
    "ds_read_b128 a[60:63], v235 offset:20992\n"
    "v_mfma_f32_16x16x32_bf16 v[80:83], a[232:235], v[208:211], v[80:83]\n"
    "v_exp_f32 v193, v193\n"
    "v_mfma_f32_16x16x32_bf16 v[84:87], a[116:119], v[188:191], v[84:87]\n"
    "ds_read_b128 a[56:59], v234 offset:20992\n"
    "v_cvt_pk_bf16_f32 v184, v192, v193\n"
    "v_mfma_f32_16x16x32_bf16 v[84:87], a[236:239], v[208:211], v[84:87]\n"
    "v_exp_f32 v194, v194\n"
    "v_mfma_f32_16x16x32_bf16 v[88:91], a[120:123], v[188:191], v[88:91]\n"
    "ds_read_b128 a[52:55], v235 offset:20480\n"
    "s_waitcnt lgkmcnt(4)\n"
    "v_mfma_f32_16x16x32_bf16 v[88:91], a[240:243], v[208:211], v[88:91]\n"
    "v_exp_f32 v195, v195\n"
    "v_mfma_f32_16x16x32_bf16 v[92:95], a[124:127], v[188:191], v[92:95]\n"
    "ds_read_b128 a[48:51], v234 offset:20480\n"
    "v_cvt_pk_bf16_f32 v185, v194, v195\n"
    "v_mfma_f32_16x16x32_bf16 v[92:95], a[244:247], v[208:211], v[92:95]\n"
    "v_exp_f32 v196, v196\n"
    "v_mfma_f32_16x16x32_bf16 v[200:203], a[32:35], v[128:131], v[216:219]\n"
    "ds_read_b64_tr_b16 a[96:97], v238 offset:16384\n"
    "s_waitcnt lgkmcnt(4)\n"
    "v_mfma_f32_16x16x32_bf16 v[200:203], a[36:39], v[132:135], v[200:203]\n"
    "v_exp_f32 v197, v197\n"
    "v_mfma_f32_16x16x32_bf16 v[200:203], a[40:43], v[136:139], v[200:203]\n"
    "ds_read_b64_tr_b16 a[98:99], v238 offset:20480\n"
    "v_cvt_pk_bf16_f32 v186, v196, v197\n"
    "v_mfma_f32_16x16x32_bf16 v[200:203], a[44:47], v[140:143], v[200:203]\n"
    "v_exp_f32 v198, v198\n"
    "v_mfma_f32_16x16x32_bf16 v[204:207], a[60:63], v[140:143], v[216:219]\n"
    "ds_read_b64_tr_b16 a[100:101], v239 offset:16384\n"
    "s_waitcnt lgkmcnt(4)\n"
    "v_mfma_f32_16x16x32_bf16 v[204:207], a[56:59], v[136:139], v[204:207]\n"
    "v_exp_f32 v199, v199\n"
    "v_mfma_f32_16x16x32_bf16 v[204:207], a[52:55], v[132:135], v[204:207]\n"
    "buffer_load_dwordx4 v241, s[52:55], s81 offen offset:3072 nt lds\n"
    "v_cvt_pk_bf16_f32 v187, v198, v199\n"
    "s_waitcnt lgkmcnt(3)\n"
    "v_mfma_f32_16x16x32_bf16 v[204:207], a[48:51], v[128:131], v[204:207]\n"
    "v_exp_f32 v200, v200\n"
    "v_mfma_f32_16x16x32_bf16 v[96:99], a[64:67], v[212:215], v[96:99]\n"
    "ds_read_b64_tr_b16 a[102:103], v239 offset:20480\n"
    "s_add_u32 m0, s83, 65536\n"
    "v_mfma_f32_16x16x32_bf16 v[100:103], a[68:71], v[212:215], v[100:103]\n"
    "v_exp_f32 v201, v201\n"
    "v_mfma_f32_16x16x32_bf16 v[104:107], a[72:75], v[212:215], v[104:107]\n"
    "ds_read_b64_tr_b16 a[104:105], v238 offset:16640\n"
    "v_cvt_pk_bf16_f32 v188, v200, v201\n"
    "v_mfma_f32_16x16x32_bf16 v[108:111], a[76:79], v[212:215], v[108:111]\n"
    "v_exp_f32 v202, v202\n"
    "v_mfma_f32_16x16x32_bf16 v[112:115], a[80:83], v[212:215], v[112:115]\n"
    "ds_read_b64_tr_b16 a[106:107], v238 offset:20736\n"
    "s_xor_b32 s81, s4, 98304\n"
    "v_mfma_f32_16x16x32_bf16 v[116:119], a[84:87], v[212:215], v[116:119]\n"
    "v_exp_f32 v203, v203\n"
    "v_mfma_f32_16x16x32_bf16 v[120:123], a[88:91], v[212:215], v[120:123]\n"
    "ds_read_b64_tr_b16 a[108:109], v239 offset:16640\n"
    "v_cvt_pk_bf16_f32 v189, v202, v203\n"
    "v_mfma_f32_16x16x32_bf16 v[124:127], a[92:95], v[212:215], v[124:127]\n"
    "v_exp_f32 v204, v204\n"
    "v_mfma_f32_16x16x32_bf16 a[204:207], a[208:211], v[212:215], a[204:207]\n"
    "ds_read_b64_tr_b16 a[110:111], v239 offset:20736\n"
    "v_mfma_f32_16x16x32_bf16 v[192:195], a[0:3], v[144:147], v[220:223]\n"
    "v_exp_f32 v205, v205\n"
    "v_mfma_f32_16x16x32_bf16 v[192:195], a[4:7], v[148:151], v[192:195]\n"
    "ds_read_b64_tr_b16 a[112:113], v238 offset:16896\n"
    "v_cvt_pk_bf16_f32 v190, v204, v205\n"
    "v_mfma_f32_16x16x32_bf16 v[192:195], a[8:11], v[152:155], v[192:195]\n"
    "v_exp_f32 v206, v206\n"
    "v_mfma_f32_16x16x32_bf16 v[192:195], a[12:15], v[156:159], v[192:195]\n"
    "buffer_load_dwordx4 v240, s[56:59], s81 offen offset:0 nt lds\n"
    "v_mfma_f32_16x16x32_bf16 v[196:199], a[28:31], v[156:159], v[220:223]\n"
    "v_exp_f32 v207, v207\n"
    "v_mfma_f32_16x16x32_bf16 v[196:199], a[24:27], v[152:155], v[196:199]\n"
    "ds_read_b64_tr_b16 a[114:115], v238 offset:20992\n"
    "v_cvt_pk_bf16_f32 v191, v206, v207\n"
    "v_mfma_f32_16x16x32_bf16 v[196:199], a[20:23], v[148:151], v[196:199]\n"
    "ds_read_b64_tr_b16 a[116:117], v239 offset:16896\n"
    "v_mfma_f32_16x16x32_bf16 v[196:199], a[16:19], v[144:147], v[196:199]\n"
    "v_exp_f32 v192, v192\n"
    "v_mfma_f32_16x16x32_bf16 v[0:3], a[64:67], v[184:187], v[0:3]\n"
    "ds_read_b64_tr_b16 a[118:119], v239 offset:20992\n"
    "s_waitcnt lgkmcnt(4)\n"
    "v_mfma_f32_16x16x32_bf16 v[0:3], a[96:99], v[188:191], v[0:3]\n"
    "v_exp_f32 v193, v193\n"
    "v_mfma_f32_16x16x32_bf16 v[4:7], a[68:71], v[184:187], v[4:7]\n"
    "ds_read_b64_tr_b16 a[120:121], v238 offset:17152\n"
    "v_cvt_pk_bf16_f32 v208, v192, v193\n"
    "v_mfma_f32_16x16x32_bf16 v[4:7], a[100:103], v[188:191], v[4:7]\n"
    "v_exp_f32 v194, v194\n"
    "v_mfma_f32_16x16x32_bf16 v[8:11], a[72:75], v[184:187], v[8:11]\n"
    "ds_read_b64_tr_b16 a[122:123], v238 offset:21248\n"
    "v_mfma_f32_16x16x32_bf16 v[8:11], a[104:107], v[188:191], v[8:11]\n"
    "v_exp_f32 v195, v195\n"
    "v_mfma_f32_16x16x32_bf16 v[12:15], a[76:79], v[184:187], v[12:15]\n"
    "L_h7:\n"
    "s_cbranch_execz L_h11\n"
    "ds_read_b64_tr_b16 a[124:125], v239 offset:17152\n"
    "v_cvt_pk_bf16_f32 v209, v194, v195\n"
    "v_mfma_f32_16x16x32_bf16 v[12:15], a[108:111], v[188:191], v[12:15]\n"
    "v_exp_f32 v196, v196\n"
    "v_mfma_f32_16x16x32_bf16 a[192:195], a[208:211], v[184:187], a[192:195]\n"
    "ds_read_b64_tr_b16 a[126:127], v239 offset:21248\n"
    "v_mfma_f32_16x16x32_bf16 a[192:195], a[208:211], v[188:191], a[192:195]\n"
    "v_exp_f32 v197, v197\n"
    "v_mfma_f32_16x16x32_bf16 v[200:203], a[32:35], v[144:147], v[220:223]\n"
    "buffer_load_dwordx4 v241, s[56:59], s81 offen offset:1024 nt lds\n"
    "v_cvt_pk_bf16_f32 v210, v196, v197\n"
    "v_mfma_f32_16x16x32_bf16 v[200:203], a[36:39], v[148:151], v[200:203]\n"
    "v_exp_f32 v198, v198\n"
    "v_mfma_f32_16x16x32_bf16 v[200:203], a[40:43], v[152:155], v[200:203]\n"
    "s_memrealtime s[98:99]\n"
    "v_mfma_f32_16x16x32_bf16 v[200:203], a[44:47], v[156:159], v[200:203]\n"
    "v_exp_f32 v199, v199\n"
    "v_mfma_f32_16x16x32_bf16 v[204:207], a[60:63], v[156:159], v[220:223]\n"
    "v_cvt_pk_bf16_f32 v211, v198, v199\n"
    "v_mfma_f32_16x16x32_bf16 v[204:207], a[56:59], v[152:155], v[204:207]\n"
    "s_waitcnt lgkmcnt(1)\n"
    "s_waitcnt lgkmcnt(0)\n"
    "v_mfma_f32_16x16x32_bf16 v[204:207], a[52:55], v[148:151], v[204:207]\n"
    "s_waitcnt lgkmcnt(0)\n"
    "s_sub_u32 s99, s98, s25\n"
    "v_mfma_f32_16x16x32_bf16 v[204:207], a[48:51], v[144:147], v[204:207]\n"
    "v_exp_f32 v200, v200\n"
    "v_mfma_f32_16x16x32_bf16 v[16:19], a[80:83], v[184:187], v[16:19]\n"
    "s_cmp_lt_i32 s99, 0\n"
    "s_cbranch_scc1 L_pw3\n"
    "L_pb3:\n"
    "v_mfma_f32_16x16x32_bf16 v[16:19], a[112:115], v[188:191], v[16:19]\n"
    "v_exp_f32 v201, v201\n"
    "v_mfma_f32_16x16x32_bf16 v[20:23], a[84:87], v[184:187], v[20:23]\n"
    "s_add_u32 s25, s98, 130\n"
    "v_cvt_pk_bf16_f32 v212, v200, v201\n"
    "v_mfma_f32_16x16x32_bf16 v[20:23], a[116:119], v[188:191], v[20:23]\n"
    "v_exp_f32 v202, v202\n"
    "v_mfma_f32_16x16x32_bf16 v[24:27], a[88:91], v[184:187], v[24:27]\n"
    "s_waitcnt vmcnt(16)\n"
    "s_barrier\n"
    "v_accvgpr_read_b32 v252, a213\n"
    "v_accvgpr_read_b32 v253, a212\n"
    "s_mov_b32 s96, 0xfefc\n"
    "s_mov_b32 s97, 0x0\n"
    "v_cmp_eq_u32 vcc, v250, v252\n"
    "s_nop 1\n"
    "s_and_b64 vcc, vcc, s[96:97]\n"
    "s_cmp_eq_u64 vcc, s[96:97]\n"
    "s_cbranch_scc0 L_slow0\n"
    "L_back0:\n"
    "s_mov_b32 s96, 0xfefcfefc\n"
    "s_mov_b32 s97, 0xefcfefc\n"
    "s_mov_b64 exec, s[96:97]\n"
    "buffer_load_dword a214, v253, s[68:71], 0 offen offset:2048 sc1\n"
    "s_mov_b64 exec, -1\n"
    "v_mfma_f32_16x16x32_bf16 v[24:27], a[120:123], v[188:191], v[24:27]\n"
    "v_exp_f32 v203, v203\n"
    "v_mfma_f32_16x16x32_bf16 v[28:31], a[92:95], v[184:187], v[28:31]\n"
    "buffer_load_dwordx4 v240, s[56:59], s81 offen offset:2048 nt lds\n"
    "v_cvt_pk_bf16_f32 v213, v202, v203\n"
    "v_mfma_f32_16x16x32_bf16 v[28:31], a[124:127], v[188:191], v[28:31]\n"
    "v_exp_f32 v204, v204\n"
    "v_mfma_f32_16x16x32_bf16 v[192:195], a[0:3], v[160:163], v[224:227]\n"
    "ds_read_b64_tr_b16 a[216:217], v236 offset:32768\n"
    "v_mfma_f32_16x16x32_bf16 v[192:195], a[4:7], v[164:167], v[192:195]\n"
    "v_exp_f32 v205, v205\n"
    "v_mfma_f32_16x16x32_bf16 v[192:195], a[8:11], v[168:171], v[192:195]\n"
    "ds_read_b64_tr_b16 a[218:219], v236 offset:36864\n"
    "v_cvt_pk_bf16_f32 v214, v204, v205\n"
    "v_mfma_f32_16x16x32_bf16 v[192:195], a[12:15], v[172:175], v[192:195]\n"
    "v_exp_f32 v206, v206\n"
    "v_mfma_f32_16x16x32_bf16 v[196:199], a[28:31], v[172:175], v[224:227]\n"
    "ds_read_b64_tr_b16 a[220:221], v237 offset:32768\n"
    "v_mfma_f32_16x16x32_bf16 v[196:199], a[24:27], v[168:171], v[196:199]\n"
    "v_exp_f32 v207, v207\n"
    "v_mfma_f32_16x16x32_bf16 v[196:199], a[20:23], v[164:167], v[196:199]\n"
    "ds_read_b64_tr_b16 a[222:223], v237 offset:36864\n"
    "v_cvt_pk_bf16_f32 v215, v206, v207\n"
    "v_mfma_f32_16x16x32_bf16 v[196:199], a[16:19], v[160:163], v[196:199]\n"
    "v_exp_f32 v192, v192\n"
    "v_mfma_f32_16x16x32_bf16 v[32:35], a[64:67], v[208:211], v[32:35]\n"
    "ds_read_b64_tr_b16 a[224:225], v236 offset:33024\n"
    "v_mfma_f32_16x16x32_bf16 v[32:35], a[96:99], v[212:215], v[32:35]\n"
    "v_exp_f32 v193, v193\n"
    "v_mfma_f32_16x16x32_bf16 v[36:39], a[68:71], v[208:211], v[36:39]\n"
    "ds_read_b64_tr_b16 a[226:227], v236 offset:37120\n"
    "v_cvt_pk_bf16_f32 v184, v192, v193\n"
    "v_mfma_f32_16x16x32_bf16 v[36:39], a[100:103], v[212:215], v[36:39]\n"
    "v_exp_f32 v194, v194\n"
    "v_mfma_f32_16x16x32_bf16 v[40:43], a[72:75], v[208:211], v[40:43]\n"
    "buffer_load_dwordx4 v241, s[56:59], s81 offen offset:3072 nt lds\n"
    "v_mfma_f32_16x16x32_bf16 v[40:43], a[104:107], v[212:215], v[40:43]\n"
    "v_exp_f32 v195, v195\n"
    "v_mfma_f32_16x16x32_bf16 v[44:47], a[76:79], v[208:211], v[44:47]\n"
    "ds_read_b128 a[0:3], v232 offset:32768\n"
    "v_cvt_pk_bf16_f32 v185, v194, v195\n"
    "v_mfma_f32_16x16x32_bf16 v[44:47], a[108:111], v[212:215], v[44:47]\n"
    "v_exp_f32 v196, v196\n"
    "v_mfma_f32_16x16x32_bf16 a[196:199], a[208:211], v[208:211], a[196:199]\n"
    "ds_read_b128 a[4:7], v233 offset:32768\n"
    "s_add_u32 m0, s83, 16384\n"
    "v_mfma_f32_16x16x32_bf16 a[196:199], a[208:211], v[212:215], a[196:199]\n"
    "v_exp_f32 v197, v197\n"
    "v_mfma_f32_16x16x32_bf16 v[200:203], a[32:35], v[160:163], v[224:227]\n"
    "ds_read_b128 a[8:11], v232 offset:33280\n"
    "v_cvt_pk_bf16_f32 v186, v196, v197\n"
    "v_mfma_f32_16x16x32_bf16 v[200:203], a[36:39], v[164:167], v[200:203]\n"
    "v_exp_f32 v198, v198\n"
    "v_mfma_f32_16x16x32_bf16 v[200:203], a[40:43], v[168:171], v[200:203]\n"
    "ds_read_b128 a[12:15], v233 offset:33280\n"
    "s_xor_b32 s81, s4, 114688\n"
    "v_mfma_f32_16x16x32_bf16 v[200:203], a[44:47], v[172:175], v[200:203]\n"
    "v_exp_f32 v199, v199\n"
    "v_mfma_f32_16x16x32_bf16 v[204:207], a[60:63], v[172:175], v[224:227]\n"
    "ds_read_b128 a[28:31], v233 offset:37376\n"
    "v_cvt_pk_bf16_f32 v187, v198, v199\n"
    "v_mfma_f32_16x16x32_bf16 v[204:207], a[56:59], v[168:171], v[204:207]\n"
    "ds_read_b128 a[24:27], v232 offset:37376\n"
    "v_mfma_f32_16x16x32_bf16 v[204:207], a[52:55], v[164:167], v[204:207]\n"
    "ds_read_b128 a[20:23], v233 offset:36864\n"
    "v_mfma_f32_16x16x32_bf16 v[204:207], a[48:51], v[160:163], v[204:207]\n"
    "v_exp_f32 v200, v200\n"
    "v_mfma_f32_16x16x32_bf16 v[48:51], a[80:83], v[208:211], v[48:51]\n"
    "buffer_load_dwordx4 v240, s[52:55], s81 offen offset:0 nt lds\n"
    "v_mfma_f32_16x16x32_bf16 v[48:51], a[112:115], v[212:215], v[48:51]\n"
    "v_exp_f32 v201, v201\n"
    "v_mfma_f32_16x16x32_bf16 v[52:55], a[84:87], v[208:211], v[52:55]\n"
    "ds_read_b128 a[16:19], v232 offset:36864\n"
    "v_cvt_pk_bf16_f32 v188, v200, v201\n"
    "v_mfma_f32_16x16x32_bf16 v[52:55], a[116:119], v[212:215], v[52:55]\n"
    "v_exp_f32 v202, v202\n"
    "v_mfma_f32_16x16x32_bf16 v[56:59], a[88:91], v[208:211], v[56:59]\n"
    "ds_read_b64_tr_b16 a[228:229], v237 offset:33024\n"
    "s_waitcnt lgkmcnt(4)\n"
    "v_mfma_f32_16x16x32_bf16 v[56:59], a[120:123], v[212:215], v[56:59]\n"
    "v_exp_f32 v203, v203\n"
    "v_mfma_f32_16x16x32_bf16 v[60:63], a[92:95], v[208:211], v[60:63]\n"
    "ds_read_b64_tr_b16 a[230:231], v237 offset:37120\n"
    "v_cvt_pk_bf16_f32 v189, v202, v203\n"
    "v_mfma_f32_16x16x32_bf16 v[60:63], a[124:127], v[212:215], v[60:63]\n"
    "v_exp_f32 v204, v204\n"
    "v_mfma_f32_16x16x32_bf16 v[192:195], a[0:3], v[176:179], v[228:231]\n"
    "ds_read_b64_tr_b16 a[232:233], v236 offset:33280\n"
    "s_waitcnt lgkmcnt(4)\n"
    "v_mfma_f32_16x16x32_bf16 v[192:195], a[4:7], a[248:251], v[192:195]\n"
    "v_exp_f32 v205, v205\n"
    "v_mfma_f32_16x16x32_bf16 v[192:195], a[8:11], v[180:183], v[192:195]\n"
    "ds_read_b64_tr_b16 a[234:235], v236 offset:37376\n"
    "v_cvt_pk_bf16_f32 v190, v204, v205\n"
    "v_mfma_f32_16x16x32_bf16 v[192:195], a[12:15], a[252:255], v[192:195]\n"
    "v_exp_f32 v206, v206\n"
    "v_mfma_f32_16x16x32_bf16 v[196:199], a[28:31], a[252:255], v[228:231]\n"
    "ds_read_b64_tr_b16 a[236:237], v237 offset:33280\n"
    "buffer_load_dword a215, v242, s[64:67], s87 offen offset:0\n"
    "v_mfma_f32_16x16x32_bf16 v[196:199], a[24:27], v[180:183], v[196:199]\n"
    "v_exp_f32 v207, v207\n"
    "v_mfma_f32_16x16x32_bf16 v[196:199], a[20:23], a[248:251], v[196:199]\n"
    "ds_read_b64_tr_b16 a[238:239], v237 offset:37376\n"
    "v_cvt_pk_bf16_f32 v191, v206, v207\n"
    "s_waitcnt lgkmcnt(4)\n"
    "v_mfma_f32_16x16x32_bf16 v[196:199], a[16:19], v[176:179], v[196:199]\n"
    "v_exp_f32 v192, v192\n"
    "v_mfma_f32_16x16x32_bf16 v[64:67], a[64:67], v[184:187], v[64:67]\n"
    "buffer_load_dwordx4 v241, s[52:55], s81 offen offset:1024 nt lds\n"
    "v_mfma_f32_16x16x32_bf16 v[64:67], a[96:99], v[188:191], v[64:67]\n"
    "v_exp_f32 v193, v193\n"
    "v_mfma_f32_16x16x32_bf16 v[68:71], a[68:71], v[184:187], v[68:71]\n"
    "ds_read_b64_tr_b16 a[240:241], v236 offset:33536\n"
    "v_cvt_pk_bf16_f32 v208, v192, v193\n"
    "v_mfma_f32_16x16x32_bf16 v[68:71], a[100:103], v[188:191], v[68:71]\n"
    "v_exp_f32 v194, v194\n"
    "v_mfma_f32_16x16x32_bf16 v[72:75], a[72:75], v[184:187], v[72:75]\n"
    "ds_read_b64_tr_b16 a[242:243], v236 offset:37632\n"
    "v_mfma_f32_16x16x32_bf16 v[72:75], a[104:107], v[188:191], v[72:75]\n"
    "v_exp_f32 v195, v195\n"
    "v_mfma_f32_16x16x32_bf16 v[76:79], a[76:79], v[184:187], v[76:79]\n"
    "ds_read_b64_tr_b16 a[244:245], v237 offset:33536\n"
    "v_cvt_pk_bf16_f32 v209, v194, v195\n"
    "v_mfma_f32_16x16x32_bf16 v[76:79], a[108:111], v[188:191], v[76:79]\n"
    "v_exp_f32 v196, v196\n"
    "v_mfma_f32_16x16x32_bf16 a[200:203], a[208:211], v[184:187], a[200:203]\n"
    "ds_read_b64_tr_b16 a[246:247], v237 offset:37632\n"
    "v_mfma_f32_16x16x32_bf16 a[200:203], a[208:211], v[188:191], a[200:203]\n"
    "v_exp_f32 v197, v197\n"
    "v_mfma_f32_16x16x32_bf16 v[200:203], a[0:3], v[128:131], v[216:219]\n"
    "ds_read_b128 a[32:35], v234 offset:32768\n"
    "v_cvt_pk_bf16_f32 v210, v196, v197\n"
    "v_mfma_f32_16x16x32_bf16 v[200:203], a[4:7], v[132:135], v[200:203]\n"
    "v_exp_f32 v198, v198\n"
    "v_mfma_f32_16x16x32_bf16 v[200:203], a[8:11], v[136:139], v[200:203]\n"
    "ds_read_b128 a[36:39], v235 offset:32768\n"
    "ds_read_b128 a[40:43], v234 offset:33280\n"
    "v_mfma_f32_16x16x32_bf16 v[200:203], a[12:15], v[140:143], v[200:203]\n"
    "v_exp_f32 v199, v199\n"
    "v_mfma_f32_16x16x32_bf16 v[204:207], a[28:31], v[140:143], v[216:219]\n"
    "buffer_load_dwordx4 v240, s[52:55], s81 offen offset:2048 nt lds\n"
    "v_cvt_pk_bf16_f32 v211, v198, v199\n"
    "v_mfma_f32_16x16x32_bf16 v[204:207], a[24:27], v[136:139], v[204:207]\n"
    "ds_read_b128 a[44:47], v235 offset:33280\n"
    "v_mfma_f32_16x16x32_bf16 v[204:207], a[20:23], v[132:135], v[204:207]\n"
    "ds_read_b128 a[60:63], v235 offset:37376\n"
    "v_mfma_f32_16x16x32_bf16 v[204:207], a[16:19], v[128:131], v[204:207]\n"
    "v_exp_f32 v200, v200\n"
    "v_mfma_f32_16x16x32_bf16 v[80:83], a[80:83], v[184:187], v[80:83]\n"
    "ds_read_b128 a[56:59], v234 offset:37376\n"
    "v_mfma_f32_16x16x32_bf16 v[80:83], a[112:115], v[188:191], v[80:83]\n"
    "v_exp_f32 v201, v201\n"
    "v_mfma_f32_16x16x32_bf16 v[84:87], a[84:87], v[184:187], v[84:87]\n"
    "ds_read_b128 a[52:55], v235 offset:36864\n"
    "v_cvt_pk_bf16_f32 v212, v200, v201\n"
    "v_mfma_f32_16x16x32_bf16 v[84:87], a[116:119], v[188:191], v[84:87]\n"
    "v_exp_f32 v202, v202\n"
    "v_mfma_f32_16x16x32_bf16 v[88:91], a[88:91], v[184:187], v[88:91]\n"
    "ds_read_b128 a[48:51], v234 offset:36864\n"
    "s_waitcnt lgkmcnt(5)\n"
    "v_mfma_f32_16x16x32_bf16 v[88:91], a[120:123], v[188:191], v[88:91]\n"
    "v_exp_f32 v203, v203\n"
    "v_mfma_f32_16x16x32_bf16 v[92:95], a[92:95], v[184:187], v[92:95]\n"
    "ds_read_b64_tr_b16 a[64:65], v238 offset:32768\n"
    "v_cvt_pk_bf16_f32 v213, v202, v203\n"
    "v_mfma_f32_16x16x32_bf16 v[92:95], a[124:127], v[188:191], v[92:95]\n"
    "v_exp_f32 v204, v204\n"
    "v_mfma_f32_16x16x32_bf16 v[192:195], a[32:35], v[128:131], v[216:219]\n"
    "ds_read_b64_tr_b16 a[66:67], v238 offset:36864\n"
    "s_waitcnt lgkmcnt(4)\n"
    "v_mfma_f32_16x16x32_bf16 v[192:195], a[36:39], v[132:135], v[192:195]\n"
    "v_exp_f32 v205, v205\n"
    "v_mfma_f32_16x16x32_bf16 v[192:195], a[40:43], v[136:139], v[192:195]\n"
    "buffer_load_dwordx4 v241, s[52:55], s81 offen offset:3072 nt lds\n"
    "v_cvt_pk_bf16_f32 v214, v204, v205\n"
    "v_mfma_f32_16x16x32_bf16 v[192:195], a[44:47], v[140:143], v[192:195]\n"
    "v_exp_f32 v206, v206\n"
    "v_mfma_f32_16x16x32_bf16 v[196:199], a[60:63], v[140:143], v[216:219]\n"
    "ds_read_b64_tr_b16 a[68:69], v239 offset:32768\n"
    "s_add_u32 m0, s83, 81920\n"
    "v_mfma_f32_16x16x32_bf16 v[196:199], a[56:59], v[136:139], v[196:199]\n"
    "v_exp_f32 v207, v207\n"
    "s_waitcnt lgkmcnt(3)\n"
    "v_mfma_f32_16x16x32_bf16 v[196:199], a[52:55], v[132:135], v[196:199]\n"
    "ds_read_b64_tr_b16 a[70:71], v239 offset:36864\n"
    "v_cvt_pk_bf16_f32 v215, v206, v207\n"
    "v_mfma_f32_16x16x32_bf16 v[196:199], a[48:51], v[128:131], v[196:199]\n"
    "v_exp_f32 v192, v192\n"
    "v_mfma_f32_16x16x32_bf16 v[96:99], a[216:219], v[208:211], v[96:99]\n"
    "ds_read_b64_tr_b16 a[72:73], v238 offset:33024\n"
    "s_xor_b32 s81, s4, 114688\n"
    "v_mfma_f32_16x16x32_bf16 v[100:103], a[220:223], v[208:211], v[100:103]\n"
    "v_exp_f32 v193, v193\n"
    "v_mfma_f32_16x16x32_bf16 v[104:107], a[224:227], v[208:211], v[104:107]\n"
    "ds_read_b64_tr_b16 a[74:75], v238 offset:37120\n"
    "v_cvt_pk_bf16_f32 v184, v192, v193\n"
    "v_mfma_f32_16x16x32_bf16 v[108:111], a[228:231], v[208:211], v[108:111]\n"
    "v_exp_f32 v194, v194\n"
    "v_mfma_f32_16x16x32_bf16 v[112:115], a[232:235], v[208:211], v[112:115]\n"
    "ds_read_b64_tr_b16 a[76:77], v239 offset:33024\n"
    "v_mfma_f32_16x16x32_bf16 v[116:119], a[236:239], v[208:211], v[116:119]\n"
    "v_exp_f32 v195, v195\n"
    "v_mfma_f32_16x16x32_bf16 v[120:123], a[240:243], v[208:211], v[120:123]\n"
    "ds_read_b64_tr_b16 a[78:79], v239 offset:37120\n"
    "v_cvt_pk_bf16_f32 v185, v194, v195\n"
    "v_mfma_f32_16x16x32_bf16 v[124:127], a[244:247], v[208:211], v[124:127]\n"
    "v_exp_f32 v196, v196\n"
    "v_mfma_f32_16x16x32_bf16 a[204:207], a[208:211], v[208:211], a[204:207]\n"
    "buffer_load_dwordx4 v240, s[56:59], s81 offen offset:0 nt lds\n"
    "v_mfma_f32_16x16x32_bf16 v[200:203], a[0:3], v[144:147], v[220:223]\n"
    "v_exp_f32 v197, v197\n"
    "v_mfma_f32_16x16x32_bf16 v[200:203], a[4:7], v[148:151], v[200:203]\n"
    "ds_read_b64_tr_b16 a[80:81], v238 offset:33280\n"
    "v_cvt_pk_bf16_f32 v186, v196, v197\n"
    "v_mfma_f32_16x16x32_bf16 v[200:203], a[8:11], v[152:155], v[200:203]\n"
    "v_exp_f32 v198, v198\n"
    "v_mfma_f32_16x16x32_bf16 v[200:203], a[12:15], v[156:159], v[200:203]\n"
    "ds_read_b64_tr_b16 a[82:83], v238 offset:37376\n"
    "v_mfma_f32_16x16x32_bf16 v[204:207], a[28:31], v[156:159], v[220:223]\n"
    "v_exp_f32 v199, v199\n"
    "v_mfma_f32_16x16x32_bf16 v[204:207], a[24:27], v[152:155], v[204:207]\n"
    "ds_read_b64_tr_b16 a[84:85], v239 offset:33280\n"
    "v_cvt_pk_bf16_f32 v187, v198, v199\n"
    "v_mfma_f32_16x16x32_bf16 v[204:207], a[20:23], v[148:151], v[204:207]\n"
    "ds_read_b64_tr_b16 a[86:87], v239 offset:37376\n"
    "v_mfma_f32_16x16x32_bf16 v[204:207], a[16:19], v[144:147], v[204:207]\n"
    "v_exp_f32 v200, v200\n"
    "v_mfma_f32_16x16x32_bf16 v[0:3], a[216:219], v[212:215], v[0:3]\n"
    "ds_read_b64_tr_b16 a[88:89], v238 offset:33536\n"
    "s_waitcnt lgkmcnt(5)\n"
    "v_mfma_f32_16x16x32_bf16 v[0:3], a[64:67], v[184:187], v[0:3]\n"
    "v_exp_f32 v201, v201\n"
    "v_mfma_f32_16x16x32_bf16 v[4:7], a[220:223], v[212:215], v[4:7]\n"
    "ds_read_b64_tr_b16 a[90:91], v238 offset:37632\n"
    "v_cvt_pk_bf16_f32 v188, v200, v201\n"
    "v_mfma_f32_16x16x32_bf16 v[4:7], a[68:71], v[184:187], v[4:7]\n"
    "v_exp_f32 v202, v202\n"
    "v_mfma_f32_16x16x32_bf16 v[8:11], a[224:227], v[212:215], v[8:11]\n"
    "ds_read_b64_tr_b16 a[92:93], v239 offset:33536\n"
    "v_mfma_f32_16x16x32_bf16 v[8:11], a[72:75], v[184:187], v[8:11]\n"
    "v_exp_f32 v203, v203\n"
    "v_mfma_f32_16x16x32_bf16 v[12:15], a[228:231], v[212:215], v[12:15]\n"
    "buffer_load_dwordx4 v241, s[56:59], s81 offen offset:1024 nt lds\n"
    "v_cvt_pk_bf16_f32 v189, v202, v203\n"
    "v_mfma_f32_16x16x32_bf16 v[12:15], a[76:79], v[184:187], v[12:15]\n"
    "v_exp_f32 v204, v204\n"
    "v_mfma_f32_16x16x32_bf16 a[192:195], a[208:211], v[212:215], a[192:195]\n"
    "ds_read_b64_tr_b16 a[94:95], v239 offset:37632\n"
    "v_mfma_f32_16x16x32_bf16 a[192:195], a[208:211], v[184:187], a[192:195]\n"
    "v_exp_f32 v205, v205\n"
    "v_mfma_f32_16x16x32_bf16 v[192:195], a[32:35], v[144:147], v[220:223]\n"
    "v_cvt_pk_bf16_f32 v190, v204, v205\n"
    "v_mfma_f32_16x16x32_bf16 v[192:195], a[36:39], v[148:151], v[192:195]\n"
    "v_exp_f32 v206, v206\n"
    "v_mfma_f32_16x16x32_bf16 v[192:195], a[40:43], v[152:155], v[192:195]\n"
    "s_memrealtime s[98:99]\n"
    "v_mfma_f32_16x16x32_bf16 v[192:195], a[44:47], v[156:159], v[192:195]\n"
    "v_exp_f32 v207, v207\n"
    "v_mfma_f32_16x16x32_bf16 v[196:199], a[60:63], v[156:159], v[220:223]\n"
    "v_cvt_pk_bf16_f32 v191, v206, v207\n"
    "v_mfma_f32_16x16x32_bf16 v[196:199], a[56:59], v[152:155], v[196:199]\n"
    "s_waitcnt lgkmcnt(1)\n"
    "s_waitcnt lgkmcnt(0)\n"
    "v_mfma_f32_16x16x32_bf16 v[196:199], a[52:55], v[148:151], v[196:199]\n"
    "s_waitcnt lgkmcnt(0)\n"
    "s_sub_u32 s99, s98, s25\n"
    "v_mfma_f32_16x16x32_bf16 v[196:199], a[48:51], v[144:147], v[196:199]\n"
    "v_exp_f32 v192, v192\n"
    "v_mfma_f32_16x16x32_bf16 v[16:19], a[232:235], v[212:215], v[16:19]\n"
    "s_cmp_lt_i32 s99, 0\n"
    "s_cbranch_scc1 L_pw4\n"
    "L_pb4:\n"
    "v_mfma_f32_16x16x32_bf16 v[16:19], a[80:83], v[184:187], v[16:19]\n"
    "v_exp_f32 v193, v193\n"
    "v_mfma_f32_16x16x32_bf16 v[20:23], a[236:239], v[212:215], v[20:23]\n"
    "s_add_u32 s25, s98, 130\n"
    "v_cvt_pk_bf16_f32 v208, v192, v193\n"
    "v_mfma_f32_16x16x32_bf16 v[20:23], a[84:87], v[184:187], v[20:23]\n"
    "v_exp_f32 v194, v194\n"
    "v_mfma_f32_16x16x32_bf16 v[24:27], a[240:243], v[212:215], v[24:27]\n"
    "s_waitcnt vmcnt(17)\n"
    "s_barrier\n"
    "v_mfma_f32_16x16x32_bf16 v[24:27], a[88:91], v[184:187], v[24:27]\n"
    "v_exp_f32 v195, v195\n"
    "v_mfma_f32_16x16x32_bf16 v[28:31], a[244:247], v[212:215], v[28:31]\n"
    "buffer_load_dwordx4 v240, s[56:59], s81 offen offset:2048 nt lds\n"
    "v_cvt_pk_bf16_f32 v209, v194, v195\n"
    "v_mfma_f32_16x16x32_bf16 v[28:31], a[92:95], v[184:187], v[28:31]\n"
    "v_exp_f32 v196, v196\n"
    "v_mfma_f32_16x16x32_bf16 v[200:203], a[0:3], v[160:163], v[224:227]\n"
    "ds_read_b64_tr_b16 a[96:97], v236 offset:49152\n"
    "v_mfma_f32_16x16x32_bf16 v[200:203], a[4:7], v[164:167], v[200:203]\n"
    "v_exp_f32 v197, v197\n"
    "v_mfma_f32_16x16x32_bf16 v[200:203], a[8:11], v[168:171], v[200:203]\n"
    "ds_read_b64_tr_b16 a[98:99], v236 offset:53248\n"
    "v_cvt_pk_bf16_f32 v210, v196, v197\n"
    "v_mfma_f32_16x16x32_bf16 v[200:203], a[12:15], v[172:175], v[200:203]\n"
    "v_exp_f32 v198, v198\n"
    "v_mfma_f32_16x16x32_bf16 v[204:207], a[28:31], v[172:175], v[224:227]\n"
    "ds_read_b64_tr_b16 a[100:101], v237 offset:49152\n"
    "v_mfma_f32_16x16x32_bf16 v[204:207], a[24:27], v[168:171], v[204:207]\n"
    "v_exp_f32 v199, v199\n"
    "v_mfma_f32_16x16x32_bf16 v[204:207], a[20:23], v[164:167], v[204:207]\n"
    "ds_read_b64_tr_b16 a[102:103], v237 offset:53248\n"
    "v_cvt_pk_bf16_f32 v211, v198, v199\n"
    "v_mfma_f32_16x16x32_bf16 v[204:207], a[16:19], v[160:163], v[204:207]\n"
    "v_exp_f32 v200, v200\n"
    "v_mfma_f32_16x16x32_bf16 v[32:35], a[216:219], v[188:191], v[32:35]\n"
    "ds_read_b64_tr_b16 a[104:105], v236 offset:49408\n"
    "v_mfma_f32_16x16x32_bf16 v[32:35], a[64:67], v[208:211], v[32:35]\n"
    "v_exp_f32 v201, v201\n"
    "v_mfma_f32_16x16x32_bf16 v[36:39], a[220:223], v[188:191], v[36:39]\n"
    "buffer_load_dwordx4 v241, s[56:59], s81 offen offset:3072 nt lds\n"
    "v_cvt_pk_bf16_f32 v212, v200, v201\n"
    "v_mfma_f32_16x16x32_bf16 v[36:39], a[68:71], v[208:211], v[36:39]\n"
    "v_exp_f32 v202, v202\n"
    "v_mfma_f32_16x16x32_bf16 v[40:43], a[224:227], v[188:191], v[40:43]\n"
    "ds_read_b64_tr_b16 a[106:107], v236 offset:53504\n"
    "s_add_u32 m0, s83, 32768\n"
    "v_mfma_f32_16x16x32_bf16 v[40:43], a[72:75], v[208:211], v[40:43]\n"
    "v_exp_f32 v203, v203\n"
    "v_mfma_f32_16x16x32_bf16 v[44:47], a[228:231], v[188:191], v[44:47]\n"
    "ds_read_b128 a[0:3], v232 offset:49152\n"
    "v_cvt_pk_bf16_f32 v213, v202, v203\n"
    "v_mfma_f32_16x16x32_bf16 v[44:47], a[76:79], v[208:211], v[44:47]\n"
    "v_exp_f32 v204, v204\n"
    "v_mfma_f32_16x16x32_bf16 a[196:199], a[208:211], v[188:191], a[196:199]\n"
    "ds_read_b128 a[4:7], v233 offset:49152\n"
    "s_xor_b32 s81, s4, 131072\n"
    "v_mfma_f32_16x16x32_bf16 a[196:199], a[208:211], v[208:211], a[196:199]\n"
    "v_exp_f32 v205, v205\n"
    "v_mfma_f32_16x16x32_bf16 v[192:195], a[32:35], v[160:163], v[224:227]\n"
    "ds_read_b128 a[8:11], v232 offset:49664\n"
    "v_cvt_pk_bf16_f32 v214, v204, v205\n"
    "v_mfma_f32_16x16x32_bf16 v[192:195], a[36:39], v[164:167], v[192:195]\n"
    "v_exp_f32 v206, v206\n"
    "v_mfma_f32_16x16x32_bf16 v[192:195], a[40:43], v[168:171], v[192:195]\n"
    "ds_read_b128 a[12:15], v233 offset:49664\n"
    "v_mfma_f32_16x16x32_bf16 v[192:195], a[44:47], v[172:175], v[192:195]\n"
    "v_exp_f32 v207, v207\n"
    "v_mfma_f32_16x16x32_bf16 v[196:199], a[60:63], v[172:175], v[224:227]\n"
    "ds_read_b128 a[28:31], v233 offset:53760\n"
    "v_cvt_pk_bf16_f32 v215, v206, v207\n"
    "v_mfma_f32_16x16x32_bf16 v[196:199], a[56:59], v[168:171], v[196:199]\n"
    "buffer_load_dwordx4 v240, s[52:55], s81 offen offset:0 nt lds\n"
    "v_mfma_f32_16x16x32_bf16 v[196:199], a[52:55], v[164:167], v[196:199]\n"
    "ds_read_b128 a[24:27], v232 offset:53760\n"
    "v_mfma_f32_16x16x32_bf16 v[196:199], a[48:51], v[160:163], v[196:199]\n"
    "v_exp_f32 v192, v192\n"
    "v_mfma_f32_16x16x32_bf16 v[48:51], a[232:235], v[188:191], v[48:51]\n"
    "ds_read_b128 a[20:23], v233 offset:53248\n"
    "v_mfma_f32_16x16x32_bf16 v[48:51], a[80:83], v[208:211], v[48:51]\n"
    "v_exp_f32 v193, v193\n"
    "v_mfma_f32_16x16x32_bf16 v[52:55], a[236:239], v[188:191], v[52:55]\n"
    "ds_read_b128 a[16:19], v232 offset:53248\n"
    "buffer_load_dword a215, v242, s[64:67], s87 offen offset:1024\n"
    "v_cvt_pk_bf16_f32 v184, v192, v193\n"
    "v_mfma_f32_16x16x32_bf16 v[52:55], a[84:87], v[208:211], v[52:55]\n"
    "v_exp_f32 v194, v194\n"
    "v_mfma_f32_16x16x32_bf16 v[56:59], a[240:243], v[188:191], v[56:59]\n"
    "ds_read_b64_tr_b16 a[108:109], v237 offset:49408\n"
    "s_waitcnt lgkmcnt(4)\n"
    "v_mfma_f32_16x16x32_bf16 v[56:59], a[88:91], v[208:211], v[56:59]\n"
    "v_exp_f32 v195, v195\n"
    "v_mfma_f32_16x16x32_bf16 v[60:63], a[244:247], v[188:191], v[60:63]\n"
    "ds_read_b64_tr_b16 a[110:111], v237 offset:53504\n"
    "v_cvt_pk_bf16_f32 v185, v194, v195\n"
    "v_mfma_f32_16x16x32_bf16 v[60:63], a[92:95], v[208:211], v[60:63]\n"
    "v_exp_f32 v196, v196\n"
    "v_mfma_f32_16x16x32_bf16 v[200:203], a[0:3], v[176:179], v[228:231]\n"
    "ds_read_b64_tr_b16 a[112:113], v236 offset:49664\n"
    "v_mfma_f32_16x16x32_bf16 v[200:203], a[4:7], a[248:251], v[200:203]\n"
    "v_exp_f32 v197, v197\n"
    "v_mfma_f32_16x16x32_bf16 v[200:203], a[8:11], v[180:183], v[200:203]\n"
    "ds_read_b64_tr_b16 a[114:115], v236 offset:53760\n"
    "v_cvt_pk_bf16_f32 v186, v196, v197\n"
    "v_mfma_f32_16x16x32_bf16 v[200:203], a[12:15], a[252:255], v[200:203]\n"
    "v_exp_f32 v198, v198\n"
    "v_mfma_f32_16x16x32_bf16 v[204:207], a[28:31], a[252:255], v[228:231]\n"
    "buffer_load_dwordx4 v241, s[52:55], s81 offen offset:1024 nt lds\n"
    "s_waitcnt lgkmcnt(3)\n"
    "v_mfma_f32_16x16x32_bf16 v[204:207], a[24:27], v[180:183], v[204:207]\n"
    "v_exp_f32 v199, v199\n"
    "v_mfma_f32_16x16x32_bf16 v[204:207], a[20:23], a[248:251], v[204:207]\n"
    "ds_read_b64_tr_b16 a[116:117], v237 offset:49664\n"
    "v_cvt_pk_bf16_f32 v187, v198, v199\n"
    "v_mfma_f32_16x16x32_bf16 v[204:207], a[16:19], v[176:179], v[204:207]\n"
    "v_exp_f32 v200, v200\n"
    "v_mfma_f32_16x16x32_bf16 v[64:67], a[216:219], v[212:215], v[64:67]\n"
    "ds_read_b64_tr_b16 a[118:119], v237 offset:53760\n"
    "v_mfma_f32_16x16x32_bf16 v[64:67], a[64:67], v[184:187], v[64:67]\n"
    "v_exp_f32 v201, v201\n"
    "v_mfma_f32_16x16x32_bf16 v[68:71], a[220:223], v[212:215], v[68:71]\n"
    "ds_read_b64_tr_b16 a[120:121], v236 offset:49920\n"
    "v_cvt_pk_bf16_f32 v188, v200, v201\n"
    "v_mfma_f32_16x16x32_bf16 v[68:71], a[68:71], v[184:187], v[68:71]\n"
    "v_exp_f32 v202, v202\n"
    "v_mfma_f32_16x16x32_bf16 v[72:75], a[224:227], v[212:215], v[72:75]\n"
    "ds_read_b64_tr_b16 a[122:123], v236 offset:54016\n"
    "v_mfma_f32_16x16x32_bf16 v[72:75], a[72:75], v[184:187], v[72:75]\n"
    "v_exp_f32 v203, v203\n"
    "v_mfma_f32_16x16x32_bf16 v[76:79], a[228:231], v[212:215], v[76:79]\n"
    "ds_read_b64_tr_b16 a[124:125], v237 offset:49920\n"
    "v_cvt_pk_bf16_f32 v189, v202, v203\n"
    "v_mfma_f32_16x16x32_bf16 v[76:79], a[76:79], v[184:187], v[76:79]\n"
    "v_exp_f32 v204, v204\n"
    "v_mfma_f32_16x16x32_bf16 a[200:203], a[208:211], v[212:215], a[200:203]\n"
    "ds_read_b64_tr_b16 a[126:127], v237 offset:54016\n"
    "v_mfma_f32_16x16x32_bf16 a[200:203], a[208:211], v[184:187], a[200:203]\n"
    "v_exp_f32 v205, v205\n"
    "v_mfma_f32_16x16x32_bf16 v[192:195], a[0:3], v[128:131], v[216:219]\n"
    "buffer_load_dwordx4 v240, s[52:55], s81 offen offset:2048 nt lds\n"
    "v_cvt_pk_bf16_f32 v190, v204, v205\n"
    "v_mfma_f32_16x16x32_bf16 v[192:195], a[4:7], v[132:135], v[192:195]\n"
    "v_exp_f32 v206, v206\n"
    "v_mfma_f32_16x16x32_bf16 v[192:195], a[8:11], v[136:139], v[192:195]\n"
    "ds_read_b128 a[32:35], v234 offset:49152\n"
    "ds_read_b128 a[36:39], v235 offset:49152\n"
    "v_mfma_f32_16x16x32_bf16 v[192:195], a[12:15], v[140:143], v[192:195]\n"
    "v_exp_f32 v207, v207\n"
    "v_mfma_f32_16x16x32_bf16 v[196:199], a[28:31], v[140:143], v[216:219]\n"
    "ds_read_b128 a[40:43], v234 offset:49664\n"
    "v_cvt_pk_bf16_f32 v191, v206, v207\n"
    "v_mfma_f32_16x16x32_bf16 v[196:199], a[24:27], v[136:139], v[196:199]\n"
    "ds_read_b128 a[44:47], v235 offset:49664\n"
    "v_mfma_f32_16x16x32_bf16 v[196:199], a[20:23], v[132:135], v[196:199]\n"
    "ds_read_b128 a[60:63], v235 offset:53760\n"
    "v_mfma_f32_16x16x32_bf16 v[196:199], a[16:19], v[128:131], v[196:199]\n"
    "v_exp_f32 v192, v192\n"
    "v_mfma_f32_16x16x32_bf16 v[80:83], a[232:235], v[212:215], v[80:83]\n"
    "ds_read_b128 a[56:59], v234 offset:53760\n"
    "v_mfma_f32_16x16x32_bf16 v[80:83], a[80:83], v[184:187], v[80:83]\n"
    "v_exp_f32 v193, v193\n"
    "v_mfma_f32_16x16x32_bf16 v[84:87], a[236:239], v[212:215], v[84:87]\n"
    "ds_read_b128 a[52:55], v235 offset:53248\n"
    "v_cvt_pk_bf16_f32 v208, v192, v193\n"
    "v_mfma_f32_16x16x32_bf16 v[84:87], a[84:87], v[184:187], v[84:87]\n"
    "v_exp_f32 v194, v194\n"
    "v_mfma_f32_16x16x32_bf16 v[88:91], a[240:243], v[212:215], v[88:91]\n"
    "ds_read_b128 a[48:51], v234 offset:53248\n"
    "s_waitcnt lgkmcnt(5)\n"
    "v_mfma_f32_16x16x32_bf16 v[88:91], a[88:91], v[184:187], v[88:91]\n"
    "v_exp_f32 v195, v195\n"
    "v_mfma_f32_16x16x32_bf16 v[92:95], a[244:247], v[212:215], v[92:95]\n"
    "ds_read_b64_tr_b16 a[216:217], v238 offset:49152\n"
    "v_cvt_pk_bf16_f32 v209, v194, v195\n"
    "v_mfma_f32_16x16x32_bf16 v[92:95], a[92:95], v[184:187], v[92:95]\n"
    "v_exp_f32 v196, v196\n"
    "v_mfma_f32_16x16x32_bf16 v[200:203], a[32:35], v[128:131], v[216:219]\n"
    "buffer_load_dwordx4 v241, s[52:55], s81 offen offset:3072 nt lds\n"
    "s_waitcnt lgkmcnt(3)\n"
    "v_mfma_f32_16x16x32_bf16 v[200:203], a[36:39], v[132:135], v[200:203]\n"
    "v_exp_f32 v197, v197\n"
    "v_mfma_f32_16x16x32_bf16 v[200:203], a[40:43], v[136:139], v[200:203]\n"
    "ds_read_b64_tr_b16 a[218:219], v238 offset:53248\n"
    "v_cvt_pk_bf16_f32 v210, v196, v197\n"
    "v_mfma_f32_16x16x32_bf16 v[200:203], a[44:47], v[140:143], v[200:203]\n"
    "v_exp_f32 v198, v198\n"
    "v_mfma_f32_16x16x32_bf16 v[204:207], a[60:63], v[140:143], v[216:219]\n"
    "ds_read_b64_tr_b16 a[220:221], v239 offset:49152\n"
    "s_add_u32 m0, s83, 98304\n"
    "v_mfma_f32_16x16x32_bf16 v[204:207], a[56:59], v[136:139], v[204:207]\n"
    "v_exp_f32 v199, v199\n"
    "s_waitcnt lgkmcnt(3)\n"
    "v_mfma_f32_16x16x32_bf16 v[204:207], a[52:55], v[132:135], v[204:207]\n"
    "ds_read_b64_tr_b16 a[222:223], v239 offset:53248\n"
    "v_cvt_pk_bf16_f32 v211, v198, v199\n"
    "v_mfma_f32_16x16x32_bf16 v[204:207], a[48:51], v[128:131], v[204:207]\n"
    "v_exp_f32 v200, v200\n"
    "v_mfma_f32_16x16x32_bf16 v[96:99], a[96:99], v[188:191], v[96:99]\n"
    "ds_read_b64_tr_b16 a[224:225], v238 offset:49408\n"
    "s_xor_b32 s81, s4, 131072\n"
    "v_mfma_f32_16x16x32_bf16 v[100:103], a[100:103], v[188:191], v[100:103]\n"
    "v_exp_f32 v201, v201\n"
    "v_mfma_f32_16x16x32_bf16 v[104:107], a[104:107], v[188:191], v[104:107]\n"
    "ds_read_b64_tr_b16 a[226:227], v238 offset:53504\n"
    "v_cvt_pk_bf16_f32 v212, v200, v201\n"
    "v_mfma_f32_16x16x32_bf16 v[108:111], a[108:111], v[188:191], v[108:111]\n"
    "v_exp_f32 v202, v202\n"
    "v_mfma_f32_16x16x32_bf16 v[112:115], a[112:115], v[188:191], v[112:115]\n"
    "ds_read_b64_tr_b16 a[228:229], v239 offset:49408\n"
    "v_mfma_f32_16x16x32_bf16 v[116:119], a[116:119], v[188:191], v[116:119]\n"
    "v_exp_f32 v203, v203\n"
    "v_mfma_f32_16x16x32_bf16 v[120:123], a[120:123], v[188:191], v[120:123]\n"
    "buffer_load_dwordx4 v240, s[56:59], s81 offen offset:0 nt lds\n"
    "v_cvt_pk_bf16_f32 v213, v202, v203\n"
    "v_mfma_f32_16x16x32_bf16 v[124:127], a[124:127], v[188:191], v[124:127]\n"
    "v_exp_f32 v204, v204\n"
    "v_mfma_f32_16x16x32_bf16 a[204:207], a[208:211], v[188:191], a[204:207]\n"
    "ds_read_b64_tr_b16 a[230:231], v239 offset:53504\n"
    "v_mfma_f32_16x16x32_bf16 v[192:195], a[0:3], v[144:147], v[220:223]\n"
    "v_exp_f32 v205, v205\n"
    "v_mfma_f32_16x16x32_bf16 v[192:195], a[4:7], v[148:151], v[192:195]\n"
    "ds_read_b64_tr_b16 a[232:233], v238 offset:49664\n"
    "v_cvt_pk_bf16_f32 v214, v204, v205\n"
    "v_mfma_f32_16x16x32_bf16 v[192:195], a[8:11], v[152:155], v[192:195]\n"
    "v_exp_f32 v206, v206\n"
    "v_mfma_f32_16x16x32_bf16 v[192:195], a[12:15], v[156:159], v[192:195]\n"
    "ds_read_b64_tr_b16 a[234:235], v238 offset:53760\n"
    "v_mfma_f32_16x16x32_bf16 v[196:199], a[28:31], v[156:159], v[220:223]\n"
    "v_exp_f32 v207, v207\n"
    "v_mfma_f32_16x16x32_bf16 v[196:199], a[24:27], v[152:155], v[196:199]\n"
    "ds_read_b64_tr_b16 a[236:237], v239 offset:49664\n"
    "v_cvt_pk_bf16_f32 v215, v206, v207\n"
    "v_mfma_f32_16x16x32_bf16 v[196:199], a[20:23], v[148:151], v[196:199]\n"
    "ds_read_b64_tr_b16 a[238:239], v239 offset:53760\n"
    "v_mfma_f32_16x16x32_bf16 v[196:199], a[16:19], v[144:147], v[196:199]\n"
    "v_exp_f32 v192, v192\n"
    "v_mfma_f32_16x16x32_bf16 v[0:3], a[96:99], v[208:211], v[0:3]\n"
    "ds_read_b64_tr_b16 a[240:241], v238 offset:49920\n"
    "s_waitcnt lgkmcnt(5)\n"
    "v_mfma_f32_16x16x32_bf16 v[0:3], a[216:219], v[212:215], v[0:3]\n"
    "v_exp_f32 v193, v193\n"
    "v_mfma_f32_16x16x32_bf16 v[4:7], a[100:103], v[208:211], v[4:7]\n"
    "ds_read_b64_tr_b16 a[242:243], v238 offset:54016\n"
    "v_cvt_pk_bf16_f32 v184, v192, v193\n"
    "v_mfma_f32_16x16x32_bf16 v[4:7], a[220:223], v[212:215], v[4:7]\n"
    "v_exp_f32 v194, v194\n"
    "v_mfma_f32_16x16x32_bf16 v[8:11], a[104:107], v[208:211], v[8:11]\n"
    "buffer_load_dwordx4 v241, s[56:59], s81 offen offset:1024 nt lds\n"
    "v_mfma_f32_16x16x32_bf16 v[8:11], a[224:227], v[212:215], v[8:11]\n"
    "v_exp_f32 v195, v195\n"
    "v_mfma_f32_16x16x32_bf16 v[12:15], a[108:111], v[208:211], v[12:15]\n"
    "ds_read_b64_tr_b16 a[244:245], v239 offset:49920\n"
    "v_cvt_pk_bf16_f32 v185, v194, v195\n"
    "v_mfma_f32_16x16x32_bf16 v[12:15], a[228:231], v[212:215], v[12:15]\n"
    "v_exp_f32 v196, v196\n"
    "v_mfma_f32_16x16x32_bf16 a[192:195], a[208:211], v[208:211], a[192:195]\n"
    "ds_read_b64_tr_b16 a[246:247], v239 offset:54016\n"
    "v_mfma_f32_16x16x32_bf16 a[192:195], a[208:211], v[212:215], a[192:195]\n"
    "v_exp_f32 v197, v197\n"
    "v_mfma_f32_16x16x32_bf16 v[200:203], a[32:35], v[144:147], v[220:223]\n"
    "v_cvt_pk_bf16_f32 v186, v196, v197\n"
    "v_mfma_f32_16x16x32_bf16 v[200:203], a[36:39], v[148:151], v[200:203]\n"
    "L_h8:\n"
    "s_cbranch_execz L_h12\n"
    "v_exp_f32 v198, v198\n"
    "v_mfma_f32_16x16x32_bf16 v[200:203], a[40:43], v[152:155], v[200:203]\n"
    "s_memrealtime s[98:99]\n"
    "v_mfma_f32_16x16x32_bf16 v[200:203], a[44:47], v[156:159], v[200:203]\n"
    "v_exp_f32 v199, v199\n"
    "v_mfma_f32_16x16x32_bf16 v[204:207], a[60:63], v[156:159], v[220:223]\n"
    "v_cvt_pk_bf16_f32 v187, v198, v199\n"
    "v_mfma_f32_16x16x32_bf16 v[204:207], a[56:59], v[152:155], v[204:207]\n"
    "s_waitcnt lgkmcnt(1)\n"
    "s_waitcnt lgkmcnt(0)\n"
    "v_mfma_f32_16x16x32_bf16 v[204:207], a[52:55], v[148:151], v[204:207]\n"
    "s_waitcnt lgkmcnt(0)\n"
    "s_sub_u32 s99, s98, s25\n"
    "v_mfma_f32_16x16x32_bf16 v[204:207], a[48:51], v[144:147], v[204:207]\n"
    "v_exp_f32 v200, v200\n"
    "v_mfma_f32_16x16x32_bf16 v[16:19], a[112:115], v[208:211], v[16:19]\n"
    "s_cmp_lt_i32 s99, 0\n"
    "s_cbranch_scc1 L_pw5\n"
    "L_pb5:\n"
    "v_mfma_f32_16x16x32_bf16 v[16:19], a[232:235], v[212:215], v[16:19]\n"
    "v_exp_f32 v201, v201\n"
    "v_mfma_f32_16x16x32_bf16 v[20:23], a[116:119], v[208:211], v[20:23]\n"
    "buffer_load_dwordx4 v240, s[56:59], s81 offen offset:2048 nt lds\n"
    "s_add_u32 s25, s98, 130\n"
    "v_cvt_pk_bf16_f32 v188, v200, v201\n"
    "v_mfma_f32_16x16x32_bf16 v[20:23], a[236:239], v[212:215], v[20:23]\n"
    "v_exp_f32 v202, v202\n"
    "v_mfma_f32_16x16x32_bf16 v[24:27], a[120:123], v[208:211], v[24:27]\n"
    "s_waitcnt vmcnt(17)\n"
    "s_barrier\n"
    "v_mfma_f32_16x16x32_bf16 v[24:27], a[240:243], v[212:215], v[24:27]\n"
    "v_exp_f32 v203, v203\n"
    "v_mfma_f32_16x16x32_bf16 v[28:31], a[124:127], v[208:211], v[28:31]\n"
    "ds_read_b64_tr_b16 a[64:65], v236 offset:0\n"
    "v_cvt_pk_bf16_f32 v189, v202, v203\n"
    "v_mfma_f32_16x16x32_bf16 v[28:31], a[244:247], v[212:215], v[28:31]\n"
    "v_exp_f32 v204, v204\n"
    "v_mfma_f32_16x16x32_bf16 v[192:195], a[0:3], v[160:163], v[224:227]\n"
    "ds_read_b64_tr_b16 a[66:67], v236 offset:4096\n"
    "v_mfma_f32_16x16x32_bf16 v[192:195], a[4:7], v[164:167], v[192:195]\n"
    "v_exp_f32 v205, v205\n"
    "v_mfma_f32_16x16x32_bf16 v[192:195], a[8:11], v[168:171], v[192:195]\n"
    "ds_read_b64_tr_b16 a[68:69], v237 offset:0\n"
    "v_cvt_pk_bf16_f32 v190, v204, v205\n"
    "v_mfma_f32_16x16x32_bf16 v[192:195], a[12:15], v[172:175], v[192:195]\n"
    "v_exp_f32 v206, v206\n"
    "v_mfma_f32_16x16x32_bf16 v[196:199], a[28:31], v[172:175], v[224:227]\n"
    "ds_read_b64_tr_b16 a[70:71], v237 offset:4096\n"
    "v_mfma_f32_16x16x32_bf16 v[196:199], a[24:27], v[168:171], v[196:199]\n"
    "v_exp_f32 v207, v207\n"
    "v_mfma_f32_16x16x32_bf16 v[196:199], a[20:23], v[164:167], v[196:199]\n"
    "buffer_load_dwordx4 v241, s[56:59], s81 offen offset:3072 nt lds\n"
    "v_cvt_pk_bf16_f32 v191, v206, v207\n"
    "v_mfma_f32_16x16x32_bf16 v[196:199], a[16:19], v[160:163], v[196:199]\n"
    "v_exp_f32 v192, v192\n"
    "v_mfma_f32_16x16x32_bf16 v[32:35], a[96:99], v[184:187], v[32:35]\n"
    "ds_read_b64_tr_b16 a[72:73], v236 offset:256\n"
    "s_add_u32 m0, s83, 49152\n"
    "v_mfma_f32_16x16x32_bf16 v[32:35], a[216:219], v[188:191], v[32:35]\n"
    "v_exp_f32 v193, v193\n"
    "v_mfma_f32_16x16x32_bf16 v[36:39], a[100:103], v[184:187], v[36:39]\n"
    "ds_read_b64_tr_b16 a[74:75], v236 offset:4352\n"
    "buffer_load_dword a215, v243, s[64:67], s87 offen offset:0\n"
    "v_cvt_pk_bf16_f32 v208, v192, v193\n"
    "v_mfma_f32_16x16x32_bf16 v[36:39], a[220:223], v[188:191], v[36:39]\n"
    "v_exp_f32 v194, v194\n"
    "v_mfma_f32_16x16x32_bf16 v[40:43], a[104:107], v[184:187], v[40:43]\n"
    "ds_read_b128 a[0:3], v232 offset:0\n"
    "s_xor_b32 s81, s4, 147456\n"
    "v_mfma_f32_16x16x32_bf16 v[40:43], a[224:227], v[188:191], v[40:43]\n"
    "v_exp_f32 v195, v195\n"
    "v_mfma_f32_16x16x32_bf16 v[44:47], a[108:111], v[184:187], v[44:47]\n"
    "ds_read_b128 a[4:7], v233 offset:0\n"
    "v_cvt_pk_bf16_f32 v209, v194, v195\n"
    "v_mfma_f32_16x16x32_bf16 v[44:47], a[228:231], v[188:191], v[44:47]\n"
    "v_exp_f32 v196, v196\n"
    "v_mfma_f32_16x16x32_bf16 a[196:199], a[208:211], v[184:187], a[196:199]\n"
    "ds_read_b128 a[8:11], v232 offset:512\n"
    "v_mfma_f32_16x16x32_bf16 a[196:199], a[208:211], v[188:191], a[196:199]\n"
    "v_exp_f32 v197, v197\n"
    "v_mfma_f32_16x16x32_bf16 v[200:203], a[32:35], v[160:163], v[224:227]\n"
    "ds_read_b128 a[12:15], v233 offset:512\n"
    "v_cvt_pk_bf16_f32 v210, v196, v197\n"
    "v_mfma_f32_16x16x32_bf16 v[200:203], a[36:39], v[164:167], v[200:203]\n"
    "v_exp_f32 v198, v198\n"
    "v_mfma_f32_16x16x32_bf16 v[200:203], a[40:43], v[168:171], v[200:203]\n"
    "buffer_load_dwordx4 v240, s[52:55], s81 offen offset:0 nt lds\n"
    "v_mfma_f32_16x16x32_bf16 v[200:203], a[44:47], v[172:175], v[200:203]\n"
    "v_exp_f32 v199, v199\n"
    "v_mfma_f32_16x16x32_bf16 v[204:207], a[60:63], v[172:175], v[224:227]\n"
    "ds_read_b128 a[28:31], v233 offset:4608\n"
    "v_cvt_pk_bf16_f32 v211, v198, v199\n"
    "v_mfma_f32_16x16x32_bf16 v[204:207], a[56:59], v[168:171], v[204:207]\n"
    "ds_read_b128 a[24:27], v232 offset:4608\n"
    "v_mfma_f32_16x16x32_bf16 v[204:207], a[52:55], v[164:167], v[204:207]\n"
    "ds_read_b128 a[20:23], v233 offset:4096\n"
    "v_mfma_f32_16x16x32_bf16 v[204:207], a[48:51], v[160:163], v[204:207]\n"
    "v_exp_f32 v200, v200\n"
    "v_mfma_f32_16x16x32_bf16 v[48:51], a[112:115], v[184:187], v[48:51]\n"
    "ds_read_b128 a[16:19], v232 offset:4096\n"
    "v_mfma_f32_16x16x32_bf16 v[48:51], a[232:235], v[188:191], v[48:51]\n"
    "v_exp_f32 v201, v201\n"
    "v_mfma_f32_16x16x32_bf16 v[52:55], a[116:119], v[184:187], v[52:55]\n"
    "ds_read_b64_tr_b16 a[76:77], v237 offset:256\n"
    "v_cvt_pk_bf16_f32 v212, v200, v201\n"
    "v_mfma_f32_16x16x32_bf16 v[52:55], a[236:239], v[188:191], v[52:55]\n"
    "v_exp_f32 v202, v202\n"
    "v_mfma_f32_16x16x32_bf16 v[56:59], a[120:123], v[184:187], v[56:59]\n"
    "ds_read_b64_tr_b16 a[78:79], v237 offset:4352\n"
    "s_waitcnt lgkmcnt(5)\n"
    "v_mfma_f32_16x16x32_bf16 v[56:59], a[240:243], v[188:191], v[56:59]\n"
    "v_exp_f32 v203, v203\n"
    "v_mfma_f32_16x16x32_bf16 v[60:63], a[124:127], v[184:187], v[60:63]\n"
    "ds_read_b64_tr_b16 a[80:81], v236 offset:512\n"
    "v_cvt_pk_bf16_f32 v213, v202, v203\n"
    "v_mfma_f32_16x16x32_bf16 v[60:63], a[244:247], v[188:191], v[60:63]\n"
    "v_exp_f32 v204, v204\n"
    "v_mfma_f32_16x16x32_bf16 v[192:195], a[0:3], v[176:179], v[228:231]\n"
    "buffer_load_dwordx4 v241, s[52:55], s81 offen offset:1024 nt lds\n"
    "v_mfma_f32_16x16x32_bf16 v[192:195], a[4:7], a[248:251], v[192:195]\n"
    "v_exp_f32 v205, v205\n"
    "v_mfma_f32_16x16x32_bf16 v[192:195], a[8:11], v[180:183], v[192:195]\n"
    "ds_read_b64_tr_b16 a[82:83], v236 offset:4608\n"
    "v_cvt_pk_bf16_f32 v214, v204, v205\n"
    "v_mfma_f32_16x16x32_bf16 v[192:195], a[12:15], a[252:255], v[192:195]\n"
    "v_exp_f32 v206, v206\n"
    "v_mfma_f32_16x16x32_bf16 v[196:199], a[28:31], a[252:255], v[228:231]\n"
    "ds_read_b64_tr_b16 a[84:85], v237 offset:512\n"
    "s_waitcnt lgkmcnt(3)\n"
    "v_mfma_f32_16x16x32_bf16 v[196:199], a[24:27], v[180:183], v[196:199]\n"
    "v_exp_f32 v207, v207\n"
    "v_mfma_f32_16x16x32_bf16 v[196:199], a[20:23], a[248:251], v[196:199]\n"
    "ds_read_b64_tr_b16 a[86:87], v237 offset:4608\n"
    "v_cvt_pk_bf16_f32 v215, v206, v207\n"
    "v_mfma_f32_16x16x32_bf16 v[196:199], a[16:19], v[176:179], v[196:199]\n"
    "v_exp_f32 v192, v192\n"
    "v_mfma_f32_16x16x32_bf16 v[64:67], a[96:99], v[208:211], v[64:67]\n"
    "ds_read_b64_tr_b16 a[88:89], v236 offset:768\n"
    "v_mfma_f32_16x16x32_bf16 v[64:67], a[216:219], v[212:215], v[64:67]\n"
    "v_exp_f32 v193, v193\n"
    "v_mfma_f32_16x16x32_bf16 v[68:71], a[100:103], v[208:211], v[68:71]\n"
    "ds_read_b64_tr_b16 a[90:91], v236 offset:4864\n"
    "v_cvt_pk_bf16_f32 v184, v192, v193\n"
    "v_mfma_f32_16x16x32_bf16 v[68:71], a[220:223], v[212:215], v[68:71]\n"
    "v_exp_f32 v194, v194\n"
    "v_mfma_f32_16x16x32_bf16 v[72:75], a[104:107], v[208:211], v[72:75]\n"
    "ds_read_b64_tr_b16 a[92:93], v237 offset:768\n"
    "v_mfma_f32_16x16x32_bf16 v[72:75], a[224:227], v[212:215], v[72:75]\n"
    "v_exp_f32 v195, v195\n"
    "v_mfma_f32_16x16x32_bf16 v[76:79], a[108:111], v[208:211], v[76:79]\n"
    "ds_read_b64_tr_b16 a[94:95], v237 offset:4864\n"
    "v_cvt_pk_bf16_f32 v185, v194, v195\n"
    "v_mfma_f32_16x16x32_bf16 v[76:79], a[228:231], v[212:215], v[76:79]\n"
    "v_exp_f32 v196, v196\n"
    "v_mfma_f32_16x16x32_bf16 a[200:203], a[208:211], v[208:211], a[200:203]\n"
    "buffer_load_dwordx4 v240, s[52:55], s81 offen offset:2048 nt lds\n"
    "v_mfma_f32_16x16x32_bf16 a[200:203], a[208:211], v[212:215], a[200:203]\n"
    "v_exp_f32 v197, v197\n"
    "v_mfma_f32_16x16x32_bf16 v[200:203], a[0:3], v[128:131], v[216:219]\n"
    "ds_read_b128 a[32:35], v234 offset:0\n"
    "v_cvt_pk_bf16_f32 v186, v196, v197\n"
    "v_mfma_f32_16x16x32_bf16 v[200:203], a[4:7], v[132:135], v[200:203]\n"
    "v_exp_f32 v198, v198\n"
    "v_mfma_f32_16x16x32_bf16 v[200:203], a[8:11], v[136:139], v[200:203]\n"
    "ds_read_b128 a[36:39], v235 offset:0\n"
    "v_mfma_f32_16x16x32_bf16 v[200:203], a[12:15], v[140:143], v[200:203]\n"
    "v_exp_f32 v199, v199\n"
    "v_mfma_f32_16x16x32_bf16 v[204:207], a[28:31], v[140:143], v[216:219]\n"
    "ds_read_b128 a[40:43], v234 offset:512\n"
    "v_cvt_pk_bf16_f32 v187, v198, v199\n"
    "v_mfma_f32_16x16x32_bf16 v[204:207], a[24:27], v[136:139], v[204:207]\n"
    "ds_read_b128 a[44:47], v235 offset:512\n"
    "v_mfma_f32_16x16x32_bf16 v[204:207], a[20:23], v[132:135], v[204:207]\n"
    "ds_read_b128 a[60:63], v235 offset:4608\n"
    "v_mfma_f32_16x16x32_bf16 v[204:207], a[16:19], v[128:131], v[204:207]\n"
    "v_exp_f32 v200, v200\n"
    "v_mfma_f32_16x16x32_bf16 v[80:83], a[112:115], v[208:211], v[80:83]\n"
    "ds_read_b128 a[56:59], v234 offset:4608\n"
    "v_mfma_f32_16x16x32_bf16 v[80:83], a[232:235], v[212:215], v[80:83]\n"
    "v_exp_f32 v201, v201\n"
    "v_mfma_f32_16x16x32_bf16 v[84:87], a[116:119], v[208:211], v[84:87]\n"
    "ds_read_b128 a[52:55], v235 offset:4096\n"
    "v_cvt_pk_bf16_f32 v188, v200, v201\n"
    "v_mfma_f32_16x16x32_bf16 v[84:87], a[236:239], v[212:215], v[84:87]\n"
    "v_exp_f32 v202, v202\n"
    "v_mfma_f32_16x16x32_bf16 v[88:91], a[120:123], v[208:211], v[88:91]\n"
    "buffer_load_dwordx4 v241, s[52:55], s81 offen offset:3072 nt lds\n"
    "s_waitcnt lgkmcnt(4)\n"
    "v_mfma_f32_16x16x32_bf16 v[88:91], a[240:243], v[212:215], v[88:91]\n"
    "v_exp_f32 v203, v203\n"
    "v_mfma_f32_16x16x32_bf16 v[92:95], a[124:127], v[208:211], v[92:95]\n"
    "ds_read_b128 a[48:51], v234 offset:4096\n"
    "v_cvt_pk_bf16_f32 v189, v202, v203\n"
    "v_mfma_f32_16x16x32_bf16 v[92:95], a[244:247], v[212:215], v[92:95]\n"
    "v_exp_f32 v204, v204\n"
    "v_mfma_f32_16x16x32_bf16 v[192:195], a[32:35], v[128:131], v[216:219]\n"
    "ds_read_b64_tr_b16 a[96:97], v238 offset:0\n"
    "s_add_u32 m0, s83, 114688\n"
    "v_mfma_f32_16x16x32_bf16 v[192:195], a[36:39], v[132:135], v[192:195]\n"
    "v_exp_f32 v205, v205\n"
    "v_mfma_f32_16x16x32_bf16 v[192:195], a[40:43], v[136:139], v[192:195]\n"
    "ds_read_b64_tr_b16 a[98:99], v238 offset:4096\n"
    "v_cvt_pk_bf16_f32 v190, v204, v205\n"
    "s_waitcnt lgkmcnt(3)\n"
    "v_mfma_f32_16x16x32_bf16 v[192:195], a[44:47], v[140:143], v[192:195]\n"
    "v_exp_f32 v206, v206\n"
    "v_mfma_f32_16x16x32_bf16 v[196:199], a[60:63], v[140:143], v[216:219]\n"
    "ds_read_b64_tr_b16 a[100:101], v239 offset:0\n"
    "s_xor_b32 s81, s4, 147456\n"
    "v_mfma_f32_16x16x32_bf16 v[196:199], a[56:59], v[136:139], v[196:199]\n"
    "v_exp_f32 v207, v207\n"
    "v_mfma_f32_16x16x32_bf16 v[196:199], a[52:55], v[132:135], v[196:199]\n"
    "ds_read_b64_tr_b16 a[102:103], v239 offset:4096\n"
    "v_cvt_pk_bf16_f32 v191, v206, v207\n"
    "s_waitcnt lgkmcnt(4)\n"
    "v_mfma_f32_16x16x32_bf16 v[196:199], a[48:51], v[128:131], v[196:199]\n"
    "v_exp_f32 v192, v192\n"
    "v_mfma_f32_16x16x32_bf16 v[96:99], a[64:67], v[184:187], v[96:99]\n"
    "ds_read_b64_tr_b16 a[104:105], v238 offset:256\n"
    "v_mfma_f32_16x16x32_bf16 v[100:103], a[68:71], v[184:187], v[100:103]\n"
    "v_exp_f32 v193, v193\n"
    "v_mfma_f32_16x16x32_bf16 v[104:107], a[72:75], v[184:187], v[104:107]\n"
    "buffer_load_dwordx4 v240, s[56:59], s81 offen offset:0 nt lds\n"
    "v_cvt_pk_bf16_f32 v208, v192, v193\n"
    "v_mfma_f32_16x16x32_bf16 v[108:111], a[76:79], v[184:187], v[108:111]\n"
    "v_exp_f32 v194, v194\n"
    "v_mfma_f32_16x16x32_bf16 v[112:115], a[80:83], v[184:187], v[112:115]\n"
    "ds_read_b64_tr_b16 a[106:107], v238 offset:4352\n"
    "v_mfma_f32_16x16x32_bf16 v[116:119], a[84:87], v[184:187], v[116:119]\n"
    "v_exp_f32 v195, v195\n"
    "v_mfma_f32_16x16x32_bf16 v[120:123], a[88:91], v[184:187], v[120:123]\n"
    "ds_read_b64_tr_b16 a[108:109], v239 offset:256\n"
    "v_cvt_pk_bf16_f32 v209, v194, v195\n"
    "v_mfma_f32_16x16x32_bf16 v[124:127], a[92:95], v[184:187], v[124:127]\n"
    "v_exp_f32 v196, v196\n"
    "v_mfma_f32_16x16x32_bf16 a[204:207], a[208:211], v[184:187], a[204:207]\n"
    "ds_read_b64_tr_b16 a[110:111], v239 offset:4352\n"
    "v_mfma_f32_16x16x32_bf16 v[200:203], a[0:3], v[144:147], v[220:223]\n"
    "v_exp_f32 v197, v197\n"
    "v_mfma_f32_16x16x32_bf16 v[200:203], a[4:7], v[148:151], v[200:203]\n"
    "ds_read_b64_tr_b16 a[112:113], v238 offset:512\n"
    "v_cvt_pk_bf16_f32 v210, v196, v197\n"
    "v_mfma_f32_16x16x32_bf16 v[200:203], a[8:11], v[152:155], v[200:203]\n"
    "v_exp_f32 v198, v198\n"
    "v_mfma_f32_16x16x32_bf16 v[200:203], a[12:15], v[156:159], v[200:203]\n"
    "ds_read_b64_tr_b16 a[114:115], v238 offset:4608\n"
    "v_mfma_f32_16x16x32_bf16 v[204:207], a[28:31], v[156:159], v[220:223]\n"
    "v_exp_f32 v199, v199\n"
    "v_mfma_f32_16x16x32_bf16 v[204:207], a[24:27], v[152:155], v[204:207]\n"
    "ds_read_b64_tr_b16 a[116:117], v239 offset:512\n"
    "v_cvt_pk_bf16_f32 v211, v198, v199\n"
    "v_mfma_f32_16x16x32_bf16 v[204:207], a[20:23], v[148:151], v[204:207]\n"
    "ds_read_b64_tr_b16 a[118:119], v239 offset:4608\n"
    "v_mfma_f32_16x16x32_bf16 v[204:207], a[16:19], v[144:147], v[204:207]\n"
    "v_exp_f32 v200, v200\n"
    "v_mfma_f32_16x16x32_bf16 v[0:3], a[64:67], v[188:191], v[0:3]\n"
    "buffer_load_dwordx4 v241, s[56:59], s81 offen offset:1024 nt lds\n"
    "s_waitcnt lgkmcnt(4)\n"
    "v_mfma_f32_16x16x32_bf16 v[0:3], a[96:99], v[208:211], v[0:3]\n"
    "v_exp_f32 v201, v201\n"
    "v_mfma_f32_16x16x32_bf16 v[4:7], a[68:71], v[188:191], v[4:7]\n"
    "ds_read_b64_tr_b16 a[120:121], v238 offset:768\n"
    "v_cvt_pk_bf16_f32 v212, v200, v201\n"
    "v_mfma_f32_16x16x32_bf16 v[4:7], a[100:103], v[208:211], v[4:7]\n"
    "v_exp_f32 v202, v202\n"
    "v_mfma_f32_16x16x32_bf16 v[8:11], a[72:75], v[188:191], v[8:11]\n"
    "ds_read_b64_tr_b16 a[122:123], v238 offset:4864\n"
    "v_mfma_f32_16x16x32_bf16 v[8:11], a[104:107], v[208:211], v[8:11]\n"
    "v_exp_f32 v203, v203\n"
    "v_mfma_f32_16x16x32_bf16 v[12:15], a[76:79], v[188:191], v[12:15]\n"
    "ds_read_b64_tr_b16 a[124:125], v239 offset:768\n"
    "v_cvt_pk_bf16_f32 v213, v202, v203\n"
    "v_mfma_f32_16x16x32_bf16 v[12:15], a[108:111], v[208:211], v[12:15]\n"
    "v_exp_f32 v204, v204\n"
    "v_mfma_f32_16x16x32_bf16 a[192:195], a[208:211], v[188:191], a[192:195]\n"
    "ds_read_b64_tr_b16 a[126:127], v239 offset:4864\n"
    "v_mfma_f32_16x16x32_bf16 a[192:195], a[208:211], v[208:211], a[192:195]\n"
    "v_exp_f32 v205, v205\n"
    "v_mfma_f32_16x16x32_bf16 v[192:195], a[32:35], v[144:147], v[220:223]\n"
    "v_cvt_pk_bf16_f32 v214, v204, v205\n"
    "v_mfma_f32_16x16x32_bf16 v[192:195], a[36:39], v[148:151], v[192:195]\n"
    "v_exp_f32 v206, v206\n"
    "v_mfma_f32_16x16x32_bf16 v[192:195], a[40:43], v[152:155], v[192:195]\n"
    "s_memrealtime s[98:99]\n"
    "v_mfma_f32_16x16x32_bf16 v[192:195], a[44:47], v[156:159], v[192:195]\n"
    "v_exp_f32 v207, v207\n"
    "v_mfma_f32_16x16x32_bf16 v[196:199], a[60:63], v[156:159], v[220:223]\n"
    "buffer_load_dwordx4 v240, s[56:59], s81 offen offset:2048 nt lds\n"
    "v_cvt_pk_bf16_f32 v215, v206, v207\n"
    "v_mfma_f32_16x16x32_bf16 v[196:199], a[56:59], v[152:155], v[196:199]\n"
    "s_waitcnt lgkmcnt(1)\n"
    "s_waitcnt lgkmcnt(0)\n"
    "v_mfma_f32_16x16x32_bf16 v[196:199], a[52:55], v[148:151], v[196:199]\n"
    "s_waitcnt lgkmcnt(0)\n"
    "s_sub_u32 s99, s98, s25\n"
    "v_mfma_f32_16x16x32_bf16 v[196:199], a[48:51], v[144:147], v[196:199]\n"
    "v_exp_f32 v192, v192\n"
    "v_mfma_f32_16x16x32_bf16 v[16:19], a[80:83], v[188:191], v[16:19]\n"
    "s_cmp_lt_i32 s99, 0\n"
    "s_cbranch_scc1 L_pw6\n"
    "L_pb6:\n"
    "v_mfma_f32_16x16x32_bf16 v[16:19], a[112:115], v[208:211], v[16:19]\n"
    "v_exp_f32 v193, v193\n"
    "v_mfma_f32_16x16x32_bf16 v[20:23], a[84:87], v[188:191], v[20:23]\n"
    "s_add_u32 s25, s98, 130\n"
    "v_cvt_pk_bf16_f32 v184, v192, v193\n"
    "v_mfma_f32_16x16x32_bf16 v[20:23], a[116:119], v[208:211], v[20:23]\n"
    "v_exp_f32 v194, v194\n"
    "v_mfma_f32_16x16x32_bf16 v[24:27], a[88:91], v[188:191], v[24:27]\n"
    "s_waitcnt vmcnt(17)\n"
    "s_barrier\n"
    "v_accvgpr_read_b32 v250, a214\n"
    "v_accvgpr_read_b32 v251, a212\n"
    "v_and_b32 v250, 1, v250\n"
    "v_lshl_add_u32 v251, v250, 13, v251\n"
    "v_cmp_ne_u32 vcc, 0, v250\n"
    "v_accvgpr_write_b32 a212, v251\n"
    "s_cmp_lg_u32 s9, 0\n"
    "s_cselect_b32 s10, -1, 0\n"
    "s_xor_b32 s10, s10, vcc_lo\n"
    "s_mov_b32 s96, 0xfefc\n"
    "s_mov_b32 s97, 0x0\n"
    "s_mov_b64 exec, s[96:97]\n"
    "buffer_store_dword a214, v251, s[68:71], 0 offen offset:0 sc1\n"
    "s_mov_b64 exec, -1\n"
    "v_mfma_f32_16x16x32_bf16 v[24:27], a[120:123], v[208:211], v[24:27]\n"
    "v_exp_f32 v195, v195\n"
    "v_mfma_f32_16x16x32_bf16 v[28:31], a[92:95], v[188:191], v[28:31]\n"
    "ds_read_b64_tr_b16 a[216:217], v236 offset:16384\n"
    "buffer_load_dword a215, v243, s[64:67], s87 offen offset:1024\n"
    "v_cvt_pk_bf16_f32 v185, v194, v195\n"
    "v_mfma_f32_16x16x32_bf16 v[28:31], a[124:127], v[208:211], v[28:31]\n"
    "v_exp_f32 v196, v196\n"
    "v_mfma_f32_16x16x32_bf16 v[200:203], a[0:3], v[160:163], v[224:227]\n"
    "ds_read_b64_tr_b16 a[218:219], v236 offset:20480\n"
    "v_accvgpr_read_b32 v250, a214\n"
    "v_mfma_f32_16x16x32_bf16 v[200:203], a[4:7], v[164:167], v[200:203]\n"
    "v_exp_f32 v197, v197\n"
    "v_mfma_f32_16x16x32_bf16 v[200:203], a[8:11], v[168:171], v[200:203]\n"
    "buffer_load_dwordx4 v241, s[56:59], s81 offen offset:3072 nt lds\n"
    "v_cvt_pk_bf16_f32 v186, v196, v197\n"
    "v_mfma_f32_16x16x32_bf16 v[200:203], a[12:15], v[172:175], v[200:203]\n"
    "v_exp_f32 v198, v198\n"
    "v_mfma_f32_16x16x32_bf16 v[204:207], a[28:31], v[172:175], v[224:227]\n"
    "ds_read_b64_tr_b16 a[220:221], v237 offset:16384\n"
    "s_add_u32 m0, s83, 0\n"
    "v_mfma_f32_16x16x32_bf16 v[204:207], a[24:27], v[168:171], v[204:207]\n"
    "v_exp_f32 v199, v199\n"
    "v_mfma_f32_16x16x32_bf16 v[204:207], a[20:23], v[164:167], v[204:207]\n"
    "ds_read_b64_tr_b16 a[222:223], v237 offset:20480\n"
    "v_cvt_pk_bf16_f32 v187, v198, v199\n"
    "v_mfma_f32_16x16x32_bf16 v[204:207], a[16:19], v[160:163], v[204:207]\n"
    "v_exp_f32 v200, v200\n"
    "v_mfma_f32_16x16x32_bf16 v[32:35], a[64:67], v[212:215], v[32:35]\n"
    "ds_read_b64_tr_b16 a[224:225], v236 offset:16640\n"
    "s_xor_b32 s81, s4, 163840\n"
    "v_mfma_f32_16x16x32_bf16 v[32:35], a[96:99], v[184:187], v[32:35]\n"
    "v_exp_f32 v201, v201\n"
    "v_mfma_f32_16x16x32_bf16 v[36:39], a[68:71], v[212:215], v[36:39]\n"
    "ds_read_b64_tr_b16 a[226:227], v236 offset:20736\n"
    "v_cvt_pk_bf16_f32 v188, v200, v201\n"
    "v_mfma_f32_16x16x32_bf16 v[36:39], a[100:103], v[184:187], v[36:39]\n"
    "v_exp_f32 v202, v202\n"
    "v_mfma_f32_16x16x32_bf16 v[40:43], a[72:75], v[212:215], v[40:43]\n"
    "ds_read_b128 a[0:3], v232 offset:16384\n"
    "v_bfe_i32 v251, v250, 0, 1\n"
    "v_mfma_f32_16x16x32_bf16 v[40:43], a[104:107], v[184:187], v[40:43]\n"
    "v_exp_f32 v203, v203\n"
    "v_mfma_f32_16x16x32_bf16 v[44:47], a[76:79], v[212:215], v[44:47]\n"
    "ds_read_b128 a[4:7], v233 offset:16384\n"
    "v_cvt_pk_bf16_f32 v189, v202, v203\n"
    "v_mfma_f32_16x16x32_bf16 v[44:47], a[108:111], v[184:187], v[44:47]\n"
    "v_exp_f32 v204, v204\n"
    "v_mfma_f32_16x16x32_bf16 a[196:199], a[208:211], v[212:215], a[196:199]\n"
    "ds_read_b128 a[8:11], v232 offset:16896\n"
    "v_sub_u32 v250, s18, v250\n"
    "v_mfma_f32_16x16x32_bf16 a[196:199], a[208:211], v[184:187], a[196:199]\n"
    "v_exp_f32 v205, v205\n"
    "v_and_b32 v250, v250, v251\n"
    "v_mfma_f32_16x16x32_bf16 v[192:195], a[32:35], v[160:163], v[224:227]\n"
    "buffer_load_dwordx4 v240, s[52:55], s81 offen offset:0 nt lds\n"
    "v_cvt_pk_bf16_f32 v190, v204, v205\n"
    "v_mfma_f32_16x16x32_bf16 v[192:195], a[36:39], v[164:167], v[192:195]\n"
    "v_exp_f32 v206, v206\n"
    "v_mfma_f32_16x16x32_bf16 v[192:195], a[40:43], v[168:171], v[192:195]\n"
    "ds_read_b128 a[12:15], v233 offset:16896\n"
    "v_readlane_b32 s12, v250, 41\n"
    "v_mfma_f32_16x16x32_bf16 v[192:195], a[44:47], v[172:175], v[192:195]\n"
    "v_exp_f32 v207, v207\n"
    "v_mfma_f32_16x16x32_bf16 v[196:199], a[60:63], v[172:175], v[224:227]\n"
    "ds_read_b128 a[28:31], v233 offset:20992\n"
    "v_cvt_pk_bf16_f32 v191, v206, v207\n"
    "v_mfma_f32_16x16x32_bf16 v[196:199], a[56:59], v[168:171], v[196:199]\n"
    "ds_read_b128 a[24:27], v232 offset:20992\n"
    "v_readlane_b32 s13, v250, 42\n"
    "v_mfma_f32_16x16x32_bf16 v[196:199], a[52:55], v[164:167], v[196:199]\n"
    "ds_read_b128 a[20:23], v233 offset:20480\n"
    "v_readlane_b32 s14, v250, 43\n"
    "v_mfma_f32_16x16x32_bf16 v[196:199], a[48:51], v[160:163], v[196:199]\n"
    "v_exp_f32 v192, v192\n"
    "v_mfma_f32_16x16x32_bf16 v[48:51], a[80:83], v[212:215], v[48:51]\n"
    "ds_read_b128 a[16:19], v232 offset:20480\n"
    "v_readlane_b32 s15, v250, 44\n"
    "v_mfma_f32_16x16x32_bf16 v[48:51], a[112:115], v[184:187], v[48:51]\n"
    "v_exp_f32 v193, v193\n"
    "v_mfma_f32_16x16x32_bf16 v[52:55], a[84:87], v[212:215], v[52:55]\n"
    "ds_read_b64_tr_b16 a[228:229], v237 offset:16640\n"
    "v_cvt_pk_bf16_f32 v208, v192, v193\n"
    "v_mfma_f32_16x16x32_bf16 v[52:55], a[116:119], v[184:187], v[52:55]\n"
    "v_exp_f32 v194, v194\n"
    "v_mfma_f32_16x16x32_bf16 v[56:59], a[88:91], v[212:215], v[56:59]\n"
    "ds_read_b64_tr_b16 a[230:231], v237 offset:20736\n"
    "v_readlane_b32 s16, v250, 45\n"
    "v_mfma_f32_16x16x32_bf16 v[56:59], a[120:123], v[184:187], v[56:59]\n"
    "v_exp_f32 v195, v195\n"
    "v_mfma_f32_16x16x32_bf16 v[60:63], a[92:95], v[212:215], v[60:63]\n"
    "buffer_load_dwordx4 v241, s[52:55], s81 offen offset:1024 nt lds\n"
    "v_cvt_pk_bf16_f32 v209, v194, v195\n"
    "v_mfma_f32_16x16x32_bf16 v[60:63], a[124:127], v[184:187], v[60:63]\n"
    "v_exp_f32 v196, v196\n"
    "s_waitcnt lgkmcnt(3)\n"
    "v_mfma_f32_16x16x32_bf16 v[200:203], a[0:3], v[176:179], v[228:231]\n"
    "ds_read_b64_tr_b16 a[232:233], v236 offset:16896\n"
    "v_readlane_b32 s17, v250, 46\n"
    "v_mfma_f32_16x16x32_bf16 v[200:203], a[4:7], a[248:251], v[200:203]\n"
    "v_exp_f32 v197, v197\n"
    "v_mfma_f32_16x16x32_bf16 v[200:203], a[8:11], v[180:183], v[200:203]\n"
    "ds_read_b64_tr_b16 a[234:235], v236 offset:20992\n"
    "v_cvt_pk_bf16_f32 v210, v196, v197\n"
    "v_mfma_f32_16x16x32_bf16 v[200:203], a[12:15], a[252:255], v[200:203]\n"
    "v_exp_f32 v198, v198\n"
    "v_readlane_b32 s19, v250, 47\n"
    "v_mfma_f32_16x16x32_bf16 v[204:207], a[28:31], a[252:255], v[228:231]\n"
    "ds_read_b64_tr_b16 a[236:237], v237 offset:16896\n"
    "s_add_i32 s19, s19, s12\n"
    "v_mfma_f32_16x16x32_bf16 v[204:207], a[24:27], v[180:183], v[204:207]\n"
    "v_exp_f32 v199, v199\n"
    "v_mfma_f32_16x16x32_bf16 v[204:207], a[20:23], a[248:251], v[204:207]\n"
    "ds_read_b64_tr_b16 a[238:239], v237 offset:20992\n"
    "v_cvt_pk_bf16_f32 v211, v198, v199\n"
    "s_waitcnt lgkmcnt(4)\n"
    "v_mfma_f32_16x16x32_bf16 v[204:207], a[16:19], v[176:179], v[204:207]\n"
    "v_exp_f32 v200, v200\n"
    "v_mfma_f32_16x16x32_bf16 v[64:67], a[64:67], v[188:191], v[64:67]\n"
    "ds_read_b64_tr_b16 a[240:241], v236 offset:17152\n"
    "s_add_i32 s19, s19, s13\n"
    "v_mfma_f32_16x16x32_bf16 v[64:67], a[96:99], v[208:211], v[64:67]\n"
    "v_exp_f32 v201, v201\n"
    "v_mfma_f32_16x16x32_bf16 v[68:71], a[68:71], v[188:191], v[68:71]\n"
    "ds_read_b64_tr_b16 a[242:243], v236 offset:21248\n"
    "v_cvt_pk_bf16_f32 v212, v200, v201\n"
    "v_mfma_f32_16x16x32_bf16 v[68:71], a[100:103], v[208:211], v[68:71]\n"
    "v_exp_f32 v202, v202\n"
    "v_mfma_f32_16x16x32_bf16 v[72:75], a[72:75], v[188:191], v[72:75]\n"
    "buffer_load_dwordx4 v240, s[52:55], s81 offen offset:2048 nt lds\n"
    "s_add_i32 s19, s19, s14\n"
    "v_mfma_f32_16x16x32_bf16 v[72:75], a[104:107], v[208:211], v[72:75]\n"
    "v_exp_f32 v203, v203\n"
    "v_mfma_f32_16x16x32_bf16 v[76:79], a[76:79], v[188:191], v[76:79]\n"
    "ds_read_b64_tr_b16 a[244:245], v237 offset:17152\n"
    "v_cvt_pk_bf16_f32 v213, v202, v203\n"
    "v_mfma_f32_16x16x32_bf16 v[76:79], a[108:111], v[208:211], v[76:79]\n"
    "v_exp_f32 v204, v204\n"
    "v_mfma_f32_16x16x32_bf16 a[200:203], a[208:211], v[188:191], a[200:203]\n"
    "ds_read_b64_tr_b16 a[246:247], v237 offset:21248\n"
    "s_add_i32 s19, s19, s15\n"
    "v_mfma_f32_16x16x32_bf16 a[200:203], a[208:211], v[208:211], a[200:203]\n"
    "v_exp_f32 v205, v205\n"
    "v_mfma_f32_16x16x32_bf16 v[192:195], a[0:3], v[128:131], v[216:219]\n"
    "ds_read_b128 a[32:35], v234 offset:16384\n"
    "v_cvt_pk_bf16_f32 v214, v204, v205\n"
    "v_mfma_f32_16x16x32_bf16 v[192:195], a[4:7], v[132:135], v[192:195]\n"
    "v_exp_f32 v206, v206\n"
    "v_mfma_f32_16x16x32_bf16 v[192:195], a[8:11], v[136:139], v[192:195]\n"
    "ds_read_b128 a[36:39], v235 offset:16384\n"
    "s_add_i32 s19, s19, s16\n"
    "v_mfma_f32_16x16x32_bf16 v[192:195], a[12:15], v[140:143], v[192:195]\n"
    "v_exp_f32 v207, v207\n"
    "v_mfma_f32_16x16x32_bf16 v[196:199], a[28:31], v[140:143], v[216:219]\n"
    "ds_read_b128 a[40:43], v234 offset:16896\n"
    "v_cvt_pk_bf16_f32 v215, v206, v207\n"
    "v_mfma_f32_16x16x32_bf16 v[196:199], a[24:27], v[136:139], v[196:199]\n"
    "ds_read_b128 a[44:47], v235 offset:16896\n"
    "s_add_i32 s19, s19, s17\n"
    "s_ashr_i32 s19, s19, 2\n"
    "v_mfma_f32_16x16x32_bf16 v[196:199], a[20:23], v[132:135], v[196:199]\n"
    "ds_read_b128 a[60:63], v235 offset:20992\n"
    "s_min_i32 s19, s19, 200\n"
    "v_mfma_f32_16x16x32_bf16 v[196:199], a[16:19], v[128:131], v[196:199]\n"
    "v_exp_f32 v192, v192\n"
    "v_mfma_f32_16x16x32_bf16 v[80:83], a[80:83], v[188:191], v[80:83]\n"
    "buffer_load_dwordx4 v241, s[52:55], s81 offen offset:3072 nt lds\n"
    "s_max_i32 s19, s19, -50\n"
    "v_mfma_f32_16x16x32_bf16 v[80:83], a[112:115], v[208:211], v[80:83]\n"
    "v_exp_f32 v193, v193\n"
    "v_mfma_f32_16x16x32_bf16 v[84:87], a[84:87], v[188:191], v[84:87]\n"
    "ds_read_b128 a[56:59], v234 offset:20992\n"
    "v_cvt_pk_bf16_f32 v184, v192, v193\n"
    "v_mfma_f32_16x16x32_bf16 v[84:87], a[116:119], v[208:211], v[84:87]\n"
    "v_exp_f32 v194, v194\n"
    "v_mfma_f32_16x16x32_bf16 v[88:91], a[88:91], v[188:191], v[88:91]\n"
    "ds_read_b128 a[52:55], v235 offset:20480\n"
    "s_add_u32 m0, s83, 65536\n"
    "v_mfma_f32_16x16x32_bf16 v[88:91], a[120:123], v[208:211], v[88:91]\n"
    "v_exp_f32 v195, v195\n"
    "v_mfma_f32_16x16x32_bf16 v[92:95], a[92:95], v[188:191], v[92:95]\n"
    "ds_read_b128 a[48:51], v234 offset:20480\n"
    "v_cvt_pk_bf16_f32 v185, v194, v195\n"
    "v_mfma_f32_16x16x32_bf16 v[92:95], a[124:127], v[208:211], v[92:95]\n"
    "v_exp_f32 v196, v196\n"
    "s_waitcnt lgkmcnt(3)\n"
    "v_mfma_f32_16x16x32_bf16 v[200:203], a[32:35], v[128:131], v[216:219]\n"
    "ds_read_b64_tr_b16 a[64:65], v238 offset:16384\n"
    "s_xor_b32 s81, s4, 163840\n"
    "v_mfma_f32_16x16x32_bf16 v[200:203], a[36:39], v[132:135], v[200:203]\n"
    "v_exp_f32 v197, v197\n"
    "v_mfma_f32_16x16x32_bf16 v[200:203], a[40:43], v[136:139], v[200:203]\n"
    "ds_read_b64_tr_b16 a[66:67], v238 offset:20480\n"
    "v_cvt_pk_bf16_f32 v186, v196, v197\n"
    "v_mfma_f32_16x16x32_bf16 v[200:203], a[44:47], v[140:143], v[200:203]\n"
    "v_exp_f32 v198, v198\n"
    "v_mfma_f32_16x16x32_bf16 v[204:207], a[60:63], v[140:143], v[216:219]\n"
    "ds_read_b64_tr_b16 a[68:69], v239 offset:16384\n"
    "s_waitcnt lgkmcnt(4)\n"
    "v_mfma_f32_16x16x32_bf16 v[204:207], a[56:59], v[136:139], v[204:207]\n"
    "v_exp_f32 v199, v199\n"
    "v_mfma_f32_16x16x32_bf16 v[204:207], a[52:55], v[132:135], v[204:207]\n"
    "ds_read_b64_tr_b16 a[70:71], v239 offset:20480\n"
    "v_cvt_pk_bf16_f32 v187, v198, v199\n"
    "s_waitcnt lgkmcnt(4)\n"
    "v_mfma_f32_16x16x32_bf16 v[204:207], a[48:51], v[128:131], v[204:207]\n"
    "v_exp_f32 v200, v200\n"
    "v_mfma_f32_16x16x32_bf16 v[96:99], a[216:219], v[212:215], v[96:99]\n"
    "buffer_load_dwordx4 v240, s[56:59], s81 offen offset:0 nt lds\n"
    "v_mfma_f32_16x16x32_bf16 v[100:103], a[220:223], v[212:215], v[100:103]\n"
    "v_exp_f32 v201, v201\n"
    "v_mfma_f32_16x16x32_bf16 v[104:107], a[224:227], v[212:215], v[104:107]\n"
    "ds_read_b64_tr_b16 a[72:73], v238 offset:16640\n"
    "v_cvt_pk_bf16_f32 v188, v200, v201\n"
    "v_mfma_f32_16x16x32_bf16 v[108:111], a[228:231], v[212:215], v[108:111]\n"
    "v_exp_f32 v202, v202\n"
    "v_mfma_f32_16x16x32_bf16 v[112:115], a[232:235], v[212:215], v[112:115]\n"
    "ds_read_b64_tr_b16 a[74:75], v238 offset:20736\n"
    "v_mfma_f32_16x16x32_bf16 v[116:119], a[236:239], v[212:215], v[116:119]\n"
    "v_exp_f32 v203, v203\n"
    "v_mfma_f32_16x16x32_bf16 v[120:123], a[240:243], v[212:215], v[120:123]\n"
    "ds_read_b64_tr_b16 a[76:77], v239 offset:16640\n"
    "v_cvt_pk_bf16_f32 v189, v202, v203\n"
    "v_mfma_f32_16x16x32_bf16 v[124:127], a[244:247], v[212:215], v[124:127]\n"
    "v_exp_f32 v204, v204\n"
    "v_mfma_f32_16x16x32_bf16 a[204:207], a[208:211], v[212:215], a[204:207]\n"
    "ds_read_b64_tr_b16 a[78:79], v239 offset:20736\n"
    "s_waitcnt lgkmcnt(4)\n"
    "v_mfma_f32_16x16x32_bf16 v[192:195], a[0:3], v[144:147], v[220:223]\n"
    "v_exp_f32 v205, v205\n"
    "v_mfma_f32_16x16x32_bf16 v[192:195], a[4:7], v[148:151], v[192:195]\n"
    "ds_read_b64_tr_b16 a[80:81], v238 offset:16896\n"
    "v_cvt_pk_bf16_f32 v190, v204, v205\n"
    "v_mfma_f32_16x16x32_bf16 v[192:195], a[8:11], v[152:155], v[192:195]\n"
    "v_exp_f32 v206, v206\n"
    "v_mfma_f32_16x16x32_bf16 v[192:195], a[12:15], v[156:159], v[192:195]\n"
    "ds_read_b64_tr_b16 a[82:83], v238 offset:20992\n"
    "v_accvgpr_read_b32 v251, a204\n"
    "v_mfma_f32_16x16x32_bf16 v[196:199], a[28:31], v[156:159], v[220:223]\n"
    "v_exp_f32 v207, v207\n"
    "v_mov_b32 v250, v228\n"
    "v_mfma_f32_16x16x32_bf16 v[196:199], a[24:27], v[152:155], v[196:199]\n"
    "buffer_load_dwordx4 v241, s[56:59], s81 offen offset:1024 nt lds\n"
    "v_cvt_pk_bf16_f32 v191, v206, v207\n"
    "v_mfma_f32_16x16x32_bf16 v[196:199], a[20:23], v[148:151], v[196:199]\n"
    "ds_read_b64_tr_b16 a[84:85], v239 offset:16896\n"
    "ds_write_b128 v244, v[100:103] offset:0\n"
    "v_mfma_f32_16x16x32_bf16 v[196:199], a[16:19], v[144:147], v[196:199]\n"
    "v_exp_f32 v192, v192\n"
    "v_mfma_f32_16x16x32_bf16 v[0:3], a[216:219], v[184:187], v[0:3]\n"
    "ds_read_b64_tr_b16 a[86:87], v239 offset:20992\n"
    "ds_write_b128 v244, v[108:111] offset:1024\n"
    "v_mfma_f32_16x16x32_bf16 v[0:3], a[64:67], v[188:191], v[0:3]\n"
    "v_exp_f32 v193, v193\n"
    "v_mfma_f32_16x16x32_bf16 v[4:7], a[220:223], v[184:187], v[4:7]\n"
    "ds_read_b64_tr_b16 a[88:89], v238 offset:17152\n"
    "v_cvt_pk_bf16_f32 v208, v192, v193\n"
    "v_mfma_f32_16x16x32_bf16 v[4:7], a[68:71], v[188:191], v[4:7]\n"
    "v_exp_f32 v194, v194\n"
    "v_mfma_f32_16x16x32_bf16 v[8:11], a[224:227], v[184:187], v[8:11]\n"
    "ds_read_b64_tr_b16 a[90:91], v238 offset:21248\n"
    "ds_write_b128 v244, v[116:119] offset:2048\n"
    "s_waitcnt lgkmcnt(7)\n"
    "v_mfma_f32_16x16x32_bf16 v[8:11], a[72:75], v[188:191], v[8:11]\n"
    "v_exp_f32 v195, v195\n"
    "v_mfma_f32_16x16x32_bf16 v[12:15], a[228:231], v[184:187], v[12:15]\n"
    "ds_read_b64_tr_b16 a[92:93], v239 offset:17152\n"
    "ds_write_b128 v244, v[124:127] offset:3072\n"
    "L_h9:\n"
    "s_cbranch_execz L_ret3\n"
    "v_cvt_pk_bf16_f32 v209, v194, v195\n"
    "v_mfma_f32_16x16x32_bf16 v[12:15], a[76:79], v[188:191], v[12:15]\n"
    "v_exp_f32 v196, v196\n"
    "v_mfma_f32_16x16x32_bf16 a[192:195], a[208:211], v[184:187], a[192:195]\n"
    "ds_read_b64_tr_b16 a[94:95], v239 offset:21248\n"
    "ds_write_b64 v244, v[250:251] offset:4096\n"
    "v_mfma_f32_16x16x32_bf16 a[192:195], a[208:211], v[188:191], a[192:195]\n"
    "v_exp_f32 v197, v197\n"
    "v_mfma_f32_16x16x32_bf16 v[200:203], a[32:35], v[144:147], v[220:223]\n"
    "v_cvt_pk_bf16_f32 v210, v196, v197\n"
    "v_mfma_f32_16x16x32_bf16 v[200:203], a[36:39], v[148:151], v[200:203]\n"
    "v_exp_f32 v198, v198\n"
    "v_mfma_f32_16x16x32_bf16 v[200:203], a[40:43], v[152:155], v[200:203]\n"
    "buffer_load_dwordx4 v240, s[56:59], s81 offen offset:2048 nt lds\n"
    "s_memrealtime s[98:99]\n"
    "v_mfma_f32_16x16x32_bf16 v[200:203], a[44:47], v[156:159], v[200:203]\n"
    "v_exp_f32 v199, v199\n"
    "v_mfma_f32_16x16x32_bf16 v[204:207], a[60:63], v[156:159], v[220:223]\n"
    "v_cvt_pk_bf16_f32 v211, v198, v199\n"
    "v_mfma_f32_16x16x32_bf16 v[204:207], a[56:59], v[152:155], v[204:207]\n"
    "s_waitcnt lgkmcnt(2)\n"
    "s_waitcnt lgkmcnt(1)\n"
    "v_mfma_f32_16x16x32_bf16 v[204:207], a[52:55], v[148:151], v[204:207]\n"
    "s_waitcnt lgkmcnt(0)\n"
    "s_sub_u32 s99, s98, s25\n"
    "v_mfma_f32_16x16x32_bf16 v[204:207], a[48:51], v[144:147], v[204:207]\n"
    "v_exp_f32 v200, v200\n"
    "v_mfma_f32_16x16x32_bf16 v[16:19], a[232:235], v[184:187], v[16:19]\n"
    "s_cmp_lt_i32 s99, 0\n"
    "s_cbranch_scc1 L_pw7\n"
    "L_pb7:\n"
    "v_mfma_f32_16x16x32_bf16 v[16:19], a[80:83], v[188:191], v[16:19]\n"
    "v_exp_f32 v201, v201\n"
    "v_mfma_f32_16x16x32_bf16 v[20:23], a[236:239], v[184:187], v[20:23]\n"
    "s_add_u32 s25, s98, 130\n"
    "v_cvt_pk_bf16_f32 v212, v200, v201\n"
    "v_mfma_f32_16x16x32_bf16 v[20:23], a[84:87], v[188:191], v[20:23]\n"
    "v_exp_f32 v202, v202\n"
    "v_mfma_f32_16x16x32_bf16 v[24:27], a[240:243], v[184:187], v[24:27]\n"
    "s_waitcnt vmcnt(18)\n"
    "s_barrier\n"
    "v_mfma_f32_16x16x32_bf16 v[24:27], a[88:91], v[188:191], v[24:27]\n"
    "v_exp_f32 v203, v203\n"
    "v_mfma_f32_16x16x32_bf16 v[28:31], a[244:247], v[184:187], v[28:31]\n"
    "ds_read_b64_tr_b16 a[96:97], v236 offset:32768\n"
    "ds_read_b64 v[252:253], v245 offset:4096\n"
    "v_cvt_pk_bf16_f32 v213, v202, v203\n"
    "v_mfma_f32_16x16x32_bf16 v[28:31], a[92:95], v[188:191], v[28:31]\n"
    "v_exp_f32 v204, v204\n"
    "v_mfma_f32_16x16x32_bf16 v[192:195], a[0:3], v[160:163], v[224:227]\n"
    "buffer_load_dwordx4 v241, s[56:59], s81 offen offset:3072 nt lds\n"
    "ds_read_b128 v[100:103], v245 offset:0\n"
    "ds_read_b128 v[108:111], v245 offset:1024\n"
    "v_mfma_f32_16x16x32_bf16 v[192:195], a[4:7], v[164:167], v[192:195]\n"
    "v_exp_f32 v205, v205\n"
    "v_mfma_f32_16x16x32_bf16 v[192:195], a[8:11], v[168:171], v[192:195]\n"
    "ds_read_b64_tr_b16 a[98:99], v236 offset:36864\n"
    "ds_read_b128 v[116:119], v245 offset:2048\n"
    "v_cvt_pk_bf16_f32 v214, v204, v205\n"
    "v_mfma_f32_16x16x32_bf16 v[192:195], a[12:15], v[172:175], v[192:195]\n"
    "v_exp_f32 v206, v206\n"
    "v_mfma_f32_16x16x32_bf16 v[196:199], a[28:31], v[172:175], v[224:227]\n"
    "ds_read_b64_tr_b16 a[100:101], v237 offset:32768\n"
    "ds_read_b128 v[124:127], v245 offset:3072\n"
    "s_add_u32 m0, s83, 16384\n"
    "v_mfma_f32_16x16x32_bf16 v[196:199], a[24:27], v[168:171], v[196:199]\n"
    "v_exp_f32 v207, v207\n"
    "v_mfma_f32_16x16x32_bf16 v[196:199], a[20:23], v[164:167], v[196:199]\n"
    "ds_read_b64_tr_b16 a[102:103], v237 offset:36864\n"
    "v_cvt_pk_bf16_f32 v215, v206, v207\n"
    "v_mfma_f32_16x16x32_bf16 v[196:199], a[16:19], v[160:163], v[196:199]\n"
    "v_exp_f32 v192, v192\n"
    "v_mfma_f32_16x16x32_bf16 v[32:35], a[216:219], v[208:211], v[32:35]\n"
    "ds_read_b64_tr_b16 a[104:105], v236 offset:33024\n"
    "s_xor_b32 s81, s4, 180224\n"
    "v_mfma_f32_16x16x32_bf16 v[32:35], a[64:67], v[212:215], v[32:35]\n"
    "v_exp_f32 v193, v193\n"
    "v_mfma_f32_16x16x32_bf16 v[36:39], a[220:223], v[208:211], v[36:39]\n"
    "ds_read_b64_tr_b16 a[106:107], v236 offset:37120\n"
    "v_cvt_pk_bf16_f32 v184, v192, v193\n"
    "s_waitcnt lgkmcnt(5)\n"
    "v_min_f32 v254, v250, v252\n"
    "v_mfma_f32_16x16x32_bf16 v[36:39], a[68:71], v[212:215], v[36:39]\n"
    "v_exp_f32 v194, v194\n"
    "v_sub_f32 v250, v254, v250\n"
    "v_sub_f32 v252, v254, v252\n"
    "v_mfma_f32_16x16x32_bf16 v[40:43], a[224:227], v[208:211], v[40:43]\n"
    "ds_read_b128 a[0:3], v232 offset:32768\n"
    "v_exp_f32 v250, v250\n"
    "v_mfma_f32_16x16x32_bf16 v[40:43], a[72:75], v[212:215], v[40:43]\n"
    "v_exp_f32 v195, v195\n"
    "v_exp_f32 v252, v252\n"
    "v_mfma_f32_16x16x32_bf16 v[44:47], a[228:231], v[208:211], v[44:47]\n"
    "buffer_load_dwordx4 v240, s[52:55], s81 offen offset:0 nt lds\n"
    "v_cvt_pk_bf16_f32 v185, v194, v195\n"
    "v_mul_f32 v254, v250, v251\n"
    "v_mfma_f32_16x16x32_bf16 v[44:47], a[76:79], v[212:215], v[44:47]\n"
    "v_exp_f32 v196, v196\n"
    "v_fma_f32 v254, v252, v253, v254\n"
    "v_max_f32 v251, v251, v253\n"
    "v_mfma_f32_16x16x32_bf16 a[196:199], a[208:211], v[208:211], a[196:199]\n"
    "ds_read_b128 a[4:7], v233 offset:32768\n"
    "v_mul_f32 v251, s43, v251\n"
    "v_mfma_f32_16x16x32_bf16 a[196:199], a[208:211], v[212:215], a[196:199]\n"
    "v_exp_f32 v197, v197\n"
    "v_rcp_f32 v254, v254\n"
    "v_sub_f32 v251, v251, v251\n"
    "v_mfma_f32_16x16x32_bf16 v[200:203], a[32:35], v[160:163], v[224:227]\n"
    "ds_read_b128 a[8:11], v232 offset:33280\n"
    "v_cvt_pk_bf16_f32 v186, v196, v197\n"
    "v_add_f32 v254, v254, v251\n"
    "v_mfma_f32_16x16x32_bf16 v[200:203], a[36:39], v[164:167], v[200:203]\n"
    "v_exp_f32 v198, v198\n"
    "v_mul_f32 v250, v250, v254\n"
    "v_mul_f32 v252, v252, v254\n"
    "v_mfma_f32_16x16x32_bf16 v[200:203], a[40:43], v[168:171], v[200:203]\n"
    "ds_read_b128 a[12:15], v233 offset:33280\n"
    "v_mul_f32 v96, v96, v250\n"
    "v_mul_f32 v97, v97, v250\n"
    "v_mul_f32 v98, v98, v250\n"
    "v_mfma_f32_16x16x32_bf16 v[200:203], a[44:47], v[172:175], v[200:203]\n"
    "v_exp_f32 v199, v199\n"
    "v_mul_f32 v99, v99, v250\n"
    "v_mfma_f32_16x16x32_bf16 v[204:207], a[60:63], v[172:175], v[224:227]\n"
    "ds_read_b128 a[28:31], v233 offset:37376\n"
    "v_cvt_pk_bf16_f32 v187, v198, v199\n"
    "v_fma_f32 v96, v100, v252, v96\n"
    "v_fma_f32 v97, v101, v252, v97\n"
    "v_mfma_f32_16x16x32_bf16 v[204:207], a[56:59], v[168:171], v[204:207]\n"
    "ds_read_b128 a[24:27], v232 offset:37376\n"
    "v_fma_f32 v98, v102, v252, v98\n"
    "v_fma_f32 v99, v103, v252, v99\n"
    "v_mfma_f32_16x16x32_bf16 v[204:207], a[52:55], v[164:167], v[204:207]\n"
    "ds_read_b128 a[20:23], v233 offset:36864\n"
    "v_mul_f32 v104, v104, v250\n"
    "v_mul_f32 v105, v105, v250\n"
    "v_mul_f32 v106, v106, v250\n"
    "v_mfma_f32_16x16x32_bf16 v[204:207], a[48:51], v[160:163], v[204:207]\n"
    "v_exp_f32 v200, v200\n"
    "v_mul_f32 v107, v107, v250\n"
    "v_mfma_f32_16x16x32_bf16 v[48:51], a[232:235], v[208:211], v[48:51]\n"
    "ds_read_b128 a[16:19], v232 offset:36864\n"
    "v_fma_f32 v104, v108, v252, v104\n"
    "v_fma_f32 v105, v109, v252, v105\n"
    "v_fma_f32 v106, v110, v252, v106\n"
    "v_mfma_f32_16x16x32_bf16 v[48:51], a[80:83], v[212:215], v[48:51]\n"
    "v_exp_f32 v201, v201\n"
    "v_fma_f32 v107, v111, v252, v107\n"
    "v_mfma_f32_16x16x32_bf16 v[52:55], a[236:239], v[208:211], v[52:55]\n"
    "ds_read_b64_tr_b16 a[108:109], v237 offset:33024\n"
    "v_cvt_pk_bf16_f32 v188, v200, v201\n"
    "v_cvt_pk_bf16_f32 v176, v96, v97\n"
    "v_cvt_pk_bf16_f32 v177, v98, v99\n"
    "v_mfma_f32_16x16x32_bf16 v[52:55], a[84:87], v[212:215], v[52:55]\n"
    "v_exp_f32 v202, v202\n"
    "v_cvt_pk_bf16_f32 v178, v104, v105\n"
    "v_cvt_pk_bf16_f32 v179, v106, v107\n"
    "v_mfma_f32_16x16x32_bf16 v[56:59], a[240:243], v[208:211], v[56:59]\n"
    "ds_read_b64_tr_b16 a[110:111], v237 offset:37120\n"
    "ds_write_b128 v244, v[176:179] offset:5120\n"
    "v_mfma_f32_16x16x32_bf16 v[56:59], a[88:91], v[212:215], v[56:59]\n"
    "v_exp_f32 v203, v203\n"
    "v_mul_f32 v112, v112, v250\n"
    "v_mul_f32 v113, v113, v250\n"
    "v_mfma_f32_16x16x32_bf16 v[60:63], a[244:247], v[208:211], v[60:63]\n"
    "ds_read_b64_tr_b16 a[112:113], v236 offset:33280\n"
    "v_cvt_pk_bf16_f32 v189, v202, v203\n"
    "v_mul_f32 v114, v114, v250\n"
    "v_mfma_f32_16x16x32_bf16 v[60:63], a[92:95], v[212:215], v[60:63]\n"
    "v_exp_f32 v204, v204\n"
    "v_mul_f32 v115, v115, v250\n"
    "v_fma_f32 v112, v116, v252, v112\n"
    "s_waitcnt lgkmcnt(3)\n"
    "v_mfma_f32_16x16x32_bf16 v[192:195], a[0:3], a[176:179], 0\n"
    "buffer_load_dwordx4 v241, s[52:55], s81 offen offset:1024 nt lds\n"
    "v_fma_f32 v113, v117, v252, v113\n"
    "v_fma_f32 v114, v118, v252, v114\n"
    "v_mfma_f32_16x16x32_bf16 v[192:195], a[4:7], a[180:183], v[192:195]\n"
    "v_exp_f32 v205, v205\n"
    "v_fma_f32 v115, v119, v252, v115\n"
    "v_mul_f32 v120, v120, v250\n"
    "v_mfma_f32_16x16x32_bf16 v[192:195], a[8:11], a[184:187], v[192:195]\n"
    "ds_read_b64_tr_b16 a[114:115], v236 offset:37376\n"
    "v_cvt_pk_bf16_f32 v190, v204, v205\n"
    "v_mul_f32 v121, v121, v250\n"
    "v_mfma_f32_16x16x32_bf16 v[192:195], a[12:15], a[188:191], v[192:195]\n"
    "v_exp_f32 v206, v206\n"
    "v_mul_f32 v122, v122, v250\n"
    "v_mul_f32 v123, v123, v250\n"
    "v_mfma_f32_16x16x32_bf16 v[196:199], a[28:31], a[188:191], 0\n"
    "ds_read_b64_tr_b16 a[116:117], v237 offset:33280\n"
    "v_fma_f32 v120, v124, v252, v120\n"
    "v_fma_f32 v121, v125, v252, v121\n"
    "v_mfma_f32_16x16x32_bf16 v[196:199], a[24:27], a[184:187], v[196:199]\n"
    "v_exp_f32 v207, v207\n"
    "v_fma_f32 v122, v126, v252, v122\n"
    "v_fma_f32 v123, v127, v252, v123\n"
    "v_mfma_f32_16x16x32_bf16 v[196:199], a[20:23], a[180:183], v[196:199]\n"
    "ds_read_b64_tr_b16 a[118:119], v237 offset:37376\n"
    "v_cvt_pk_bf16_f32 v191, v206, v207\n"
    "v_cvt_pk_bf16_f32 v180, v112, v113\n"
    "v_cvt_pk_bf16_f32 v181, v114, v115\n"
    "v_mfma_f32_16x16x32_bf16 v[196:199], a[16:19], a[176:179], v[196:199]\n"
    "ds_read_b64_tr_b16 a[120:121], v236 offset:33536\n"
    "v_cvt_pk_bf16_f32 v182, v120, v121\n"
    "v_cvt_pk_bf16_f32 v183, v122, v123\n"
    "v_mfma_f32_16x16x32_bf16 v[64:67], a[216:219], v[184:187], v[64:67]\n"
    "ds_read_b64_tr_b16 a[122:123], v236 offset:37632\n"
    "ds_write_b128 v244, v[180:183] offset:6144\n"
    "v_mfma_f32_16x16x32_bf16 v[64:67], a[64:67], v[188:191], v[64:67]\n"
    "ds_read_b64_tr_b16 a[124:125], v237 offset:33536\n"
    "v_accvgpr_read_b32 v248, a192\n"
    "v_mfma_f32_16x16x32_bf16 v[68:71], a[220:223], v[184:187], v[68:71]\n"
    "ds_read_b64_tr_b16 a[126:127], v237 offset:37632\n"
    "v_max3_f32 v246, v192, v193, v194\n"
    "v_mul_f32 v128, s43, v248\n"
    "v_mfma_f32_16x16x32_bf16 v[68:71], a[68:71], v[188:191], v[68:71]\n"
    "ds_read_b128 a[32:35], v234 offset:32768\n"
    "v_max3_f32 v247, v195, v196, v197\n"
    "v_max3_f32 v246, v246, v198, v199\n"
    "v_mfma_f32_16x16x32_bf16 v[72:75], a[224:227], v[184:187], v[72:75]\n"
    "ds_read_b128 a[36:39], v235 offset:32768\n"
    "v_max_f32 v246, v246, v247\n"
    "v_mov_b32 v247, v246\n"
    "v_mfma_f32_16x16x32_bf16 v[72:75], a[72:75], v[188:191], v[72:75]\n"
    "ds_read_b128 a[40:43], v234 offset:33280\n"
    "v_permlane32_swap_b32 v246, v247\n"
    "v_mfma_f32_16x16x32_bf16 v[76:79], a[228:231], v[184:187], v[76:79]\n"
    "ds_read_b128 a[44:47], v235 offset:33280\n"
    "v_max_f32 v246, v246, v247\n"
    "v_mov_b32 v247, v246\n"
    "v_rcp_f32 v248, v248\n"
    "v_mfma_f32_16x16x32_bf16 v[76:79], a[76:79], v[188:191], v[76:79]\n"
    "ds_read_b128 a[60:63], v235 offset:37376\n"
    "v_permlane16_swap_b32 v246, v247\n"
    "v_mfma_f32_16x16x32_bf16 a[200:203], a[208:211], v[184:187], a[200:203]\n"
    "ds_read_b128 a[56:59], v234 offset:37376\n"
    "v_max_f32 v228, v246, v247\n"
    "v_sub_f32 v128, v128, v128\n"
    "v_mfma_f32_16x16x32_bf16 a[200:203], a[208:211], v[188:191], a[200:203]\n"
    "ds_read_b128 a[52:55], v235 offset:36864\n"
    "v_sub_f32 v228, 0, v228\n"
    "v_add_f32 v192, v192, v228\n"
    "v_mfma_f32_16x16x32_bf16 v[200:203], a[0:3], a[128:131], 0\n"
    "ds_read_b128 a[48:51], v234 offset:36864\n"
    "v_add_f32 v193, v193, v228\n"
    "v_add_f32 v194, v194, v228\n"
    "v_add_f32 v248, v248, v128\n"
    "v_mfma_f32_16x16x32_bf16 v[200:203], a[4:7], a[132:135], v[200:203]\n"
    "buffer_load_dwordx4 v240, s[52:55], s81 offen offset:2048 nt lds\n"
    "v_add_f32 v195, v195, v228\n"
    "v_add_f32 v196, v196, v228\n"
    "v_mfma_f32_16x16x32_bf16 v[200:203], a[8:11], a[136:139], v[200:203]\n"
    "ds_read_b64_tr_b16 a[216:217], v238 offset:32768\n"
    "v_add_f32 v197, v197, v228\n"
    "v_add_f32 v198, v198, v228\n"
    "v_mfma_f32_16x16x32_bf16 v[200:203], a[12:15], a[140:143], v[200:203]\n"
    "ds_read_b64_tr_b16 a[218:219], v238 offset:36864\n"
    "v_add_f32 v199, v199, v228\n"
    "v_mfma_f32_16x16x32_bf16 v[204:207], a[28:31], a[140:143], 0\n"
    "ds_read_b64_tr_b16 a[220:221], v239 offset:32768\n"
    "v_exp_f32 v192, v192\n"
    "v_pk_mul_f32 v[0:1], v[0:1], v[248:249] op_sel_hi:[1,0]\n"
    "v_pk_mul_f32 v[2:3], v[2:3], v[248:249] op_sel_hi:[1,0]\n"
    "v_pk_mul_f32 v[4:5], v[4:5], v[248:249] op_sel_hi:[1,0]\n"
    "v_pk_mul_f32 v[6:7], v[6:7], v[248:249] op_sel_hi:[1,0]\n"
    "v_pk_mul_f32 v[8:9], v[8:9], v[248:249] op_sel_hi:[1,0]\n"
    "v_pk_mul_f32 v[10:11], v[10:11], v[248:249] op_sel_hi:[1,0]\n"
    "v_pk_mul_f32 v[12:13], v[12:13], v[248:249] op_sel_hi:[1,0]\n"
    "v_pk_mul_f32 v[14:15], v[14:15], v[248:249] op_sel_hi:[1,0]\n"
    "v_pk_mul_f32 v[16:17], v[16:17], v[248:249] op_sel_hi:[1,0]\n"
    "v_pk_mul_f32 v[18:19], v[18:19], v[248:249] op_sel_hi:[1,0]\n"
    "v_pk_mul_f32 v[20:21], v[20:21], v[248:249] op_sel_hi:[1,0]\n"
    "v_pk_mul_f32 v[22:23], v[22:23], v[248:249] op_sel_hi:[1,0]\n"
    "v_pk_mul_f32 v[24:25], v[24:25], v[248:249] op_sel_hi:[1,0]\n"
    "v_pk_mul_f32 v[26:27], v[26:27], v[248:249] op_sel_hi:[1,0]\n"
    "v_pk_mul_f32 v[28:29], v[28:29], v[248:249] op_sel_hi:[1,0]\n"
    "v_pk_mul_f32 v[30:31], v[30:31], v[248:249] op_sel_hi:[1,0]\n"
    "v_mfma_f32_16x16x32_bf16 v[204:207], a[24:27], a[136:139], v[204:207]\n"
    "ds_read_b64_tr_b16 a[222:223], v239 offset:36864\n"
    "v_exp_f32 v193, v193\n"
    "v_mfma_f32_16x16x32_bf16 v[204:207], a[20:23], a[132:135], v[204:207]\n"
    "ds_read_b64_tr_b16 a[224:225], v238 offset:33024\n"
    "v_mov_b32 v229, v228\n"
    "v_cvt_pk_bf16_f32 v128, v0, v1\n"
    "v_mfma_f32_16x16x32_bf16 v[204:207], a[16:19], a[128:131], v[204:207]\n"
    "ds_read_b64_tr_b16 a[226:227], v238 offset:37120\n"
    "v_exp_f32 v194, v194\n"
    "v_cvt_pk_bf16_f32 v208, v192, v193\n"
    "v_mfma_f32_16x16x32_bf16 v[80:83], a[232:235], v[184:187], v[80:83]\n"
    "ds_read_b64_tr_b16 a[228:229], v239 offset:33024\n"
    "v_exp_f32 v195, v195\n"
    "v_cvt_pk_bf16_f32 v129, v2, v3\n"
    "v_mfma_f32_16x16x32_bf16 v[80:83], a[80:83], v[188:191], v[80:83]\n"
    "ds_read_b64_tr_b16 a[230:231], v239 offset:37120\n"
    "v_mov_b32 v230, v228\n"
    "v_mfma_f32_16x16x32_bf16 v[84:87], a[236:239], v[184:187], v[84:87]\n"
    "ds_read_b64_tr_b16 a[232:233], v238 offset:33280\n"
    "v_exp_f32 v196, v196\n"
    "v_cvt_pk_bf16_f32 v209, v194, v195\n"
    "v_cvt_pk_bf16_f32 v130, v8, v9\n"
    "v_mfma_f32_16x16x32_bf16 v[84:87], a[84:87], v[188:191], v[84:87]\n"
    "ds_read_b64_tr_b16 a[234:235], v238 offset:37376\n"
    "v_exp_f32 v197, v197\n"
    "v_mfma_f32_16x16x32_bf16 v[88:91], a[240:243], v[184:187], v[88:91]\n"
    "ds_read_b64_tr_b16 a[236:237], v239 offset:33280\n"
    "v_mov_b32 v231, v228\n"
    "v_cvt_pk_bf16_f32 v131, v10, v11\n"
    "v_mfma_f32_16x16x32_bf16 v[88:91], a[88:91], v[188:191], v[88:91]\n"
    "ds_read_b64_tr_b16 a[238:239], v239 offset:37376\n"
    "v_exp_f32 v198, v198\n"
    "v_mfma_f32_16x16x32_bf16 v[92:95], a[244:247], v[184:187], v[92:95]\n"
    "ds_read_b64_tr_b16 a[240:241], v238 offset:33536\n"
    "v_cvt_pk_bf16_f32 v210, v196, v197\n"
    "v_cvt_pk_bf16_f32 v136, v4, v5\n"
    "v_mfma_f32_16x16x32_bf16 v[92:95], a[92:95], v[188:191], v[92:95]\n"
    "ds_read_b64_tr_b16 a[242:243], v238 offset:37632\n"
    "v_exp_f32 v199, v199\n"
    "s_nop 0\n"
    "v_cvt_pk_bf16_f32 v211, v198, v199\n"
    "s_waitcnt lgkmcnt(5)\n"
    "v_mfma_f32_16x16x32_bf16 v[192:195], a[32:35], a[128:131], 0\n"
    "ds_read_b64_tr_b16 a[244:245], v239 offset:33536\n"
    "v_max3_f32 v246, v200, v201, v202\n"
    "v_max3_f32 v247, v203, v204, v205\n"
    "v_cvt_pk_bf16_f32 v137, v6, v7\n"
    "v_mfma_f32_16x16x32_bf16 v[192:195], a[36:39], a[132:135], v[192:195]\n"
    "ds_read_b64_tr_b16 a[246:247], v239 offset:37632\n"
    "v_max3_f32 v246, v246, v206, v207\n"
    "v_max_f32 v246, v246, v247\n"
    "v_mov_b32 v247, v246\n"
    "v_mfma_f32_16x16x32_bf16 v[192:195], a[40:43], a[136:139], v[192:195]\n"
    "s_nop 0\n"
    "v_permlane32_swap_b32 v246, v247\n"
    "v_max_f32 v246, v246, v247\n"
    "v_mov_b32 v247, v246\n"
    "v_cvt_pk_bf16_f32 v138, v12, v13\n"
    "v_mfma_f32_16x16x32_bf16 v[192:195], a[44:47], a[140:143], v[192:195]\n"
    "v_permlane16_swap_b32 v246, v247\n"
    "v_max_f32 v216, v246, v247\n"
    "v_sub_f32 v216, 0, v216\n"
    "v_mfma_f32_16x16x32_bf16 v[196:199], a[60:63], a[140:143], 0\n"
    "v_add_f32 v200, v200, v216\n"
    "v_add_f32 v201, v201, v216\n"
    "v_add_f32 v202, v202, v216\n"
    "v_add_f32 v203, v203, v216\n"
    "v_cvt_pk_bf16_f32 v139, v14, v15\n"
    "v_mfma_f32_16x16x32_bf16 v[196:199], a[56:59], a[136:139], v[196:199]\n"
    "buffer_load_dwordx4 v241, s[52:55], s81 offen offset:3072 nt lds\n"
    "v_add_f32 v204, v204, v216\n"
    "v_add_f32 v205, v205, v216\n"
    "v_mfma_f32_16x16x32_bf16 v[196:199], a[52:55], a[132:135], v[196:199]\n"
    "s_add_u32 m0, s83, 81920\n"
    "s_xor_b32 s81, s4, 180224\n"
    "v_add_f32 v206, v206, v216\n"
    "v_add_f32 v207, v207, v216\n"
    "v_cvt_pk_bf16_f32 v132, v16, v17\n"
    "v_mfma_f32_16x16x32_bf16 v[196:199], a[48:51], a[128:131], v[196:199]\n"
    "v_exp_f32 v200, v200\n"
    "v_exp_f32 v201, v201\n"
    "v_mfma_f32_16x16x32_bf16 v[96:99], a[96:99], v[208:211], 0\n"
    "v_mov_b32 v217, v216\n"
    "v_exp_f32 v202, v202\n"
    "v_cvt_pk_bf16_f32 v212, v200, v201\n"
    "v_cvt_pk_bf16_f32 v133, v18, v19\n"
    "v_mfma_f32_16x16x32_bf16 v[100:103], a[100:103], v[208:211], 0\n"
    "v_exp_f32 v203, v203\n"
    "v_mov_b32 v218, v216\n"
    "v_mfma_f32_16x16x32_bf16 v[104:107], a[104:107], v[208:211], 0\n"
    "v_exp_f32 v204, v204\n"
    "v_cvt_pk_bf16_f32 v213, v202, v203\n"
    "v_cvt_pk_bf16_f32 v134, v24, v25\n"
    "v_mfma_f32_16x16x32_bf16 v[108:111], a[108:111], v[208:211], 0\n"
    "v_exp_f32 v205, v205\n"
    "v_mov_b32 v219, v216\n"
    "v_exp_f32 v206, v206\n"
    "v_mfma_f32_16x16x32_bf16 v[112:115], a[112:115], v[208:211], 0\n"
    "v_cvt_pk_bf16_f32 v214, v204, v205\n"
    "v_exp_f32 v207, v207\n"
    "s_nop 0\n"
    "v_cvt_pk_bf16_f32 v215, v206, v207\n"
    "v_cvt_pk_bf16_f32 v135, v26, v27\n"
    "v_mfma_f32_16x16x32_bf16 v[116:119], a[116:119], v[208:211], 0\n"
    "v_add_f32 v192, v192, v216\n"
    "v_add_f32 v193, v193, v216\n"
    "v_mfma_f32_16x16x32_bf16 v[120:123], a[120:123], v[208:211], 0\n"
    "v_add_f32 v194, v194, v216\n"
    "v_add_f32 v195, v195, v216\n"
    "v_add_f32 v196, v196, v216\n"
    "v_cvt_pk_bf16_f32 v140, v20, v21\n"
    "v_mfma_f32_16x16x32_bf16 v[124:127], a[124:127], v[208:211], 0\n"
    "v_add_f32 v197, v197, v216\n"
    "v_add_f32 v198, v198, v216\n"
    "v_mfma_f32_16x16x32_bf16 a[204:207], a[208:211], v[208:211], 0\n"
    "v_add_f32 v199, v199, v216\n"
    "v_exp_f32 v192, v192\n"
    "v_cvt_pk_bf16_f32 v141, v22, v23\n"
    "v_mfma_f32_16x16x32_bf16 v[200:203], a[0:3], a[144:147], 0\n"
    "v_exp_f32 v193, v193\n"
    "v_mfma_f32_16x16x32_bf16 v[200:203], a[4:7], a[148:151], v[200:203]\n"
    "v_exp_f32 v194, v194\n"
    "v_cvt_pk_bf16_f32 v184, v192, v193\n"
    "v_cvt_pk_bf16_f32 v142, v28, v29\n"
    "v_mfma_f32_16x16x32_bf16 v[200:203], a[8:11], a[152:155], v[200:203]\n"
    "v_exp_f32 v195, v195\n"
    "v_mfma_f32_16x16x32_bf16 v[200:203], a[12:15], a[156:159], v[200:203]\n"
    "v_exp_f32 v196, v196\n"
    "v_cvt_pk_bf16_f32 v185, v194, v195\n"
    "v_cvt_pk_bf16_f32 v143, v30, v31\n"
    "v_mfma_f32_16x16x32_bf16 v[204:207], a[28:31], a[156:159], 0\n"
    "v_exp_f32 v197, v197\n"
    "v_mfma_f32_16x16x32_bf16 v[204:207], a[24:27], a[152:155], v[204:207]\n"
    "v_exp_f32 v198, v198\n"
    "v_cvt_pk_bf16_f32 v186, v196, v197\n"
    "v_accvgpr_read_b32 v248, a196\n"
    "v_mfma_f32_16x16x32_bf16 v[204:207], a[20:23], a[148:151], v[204:207]\n"
    "v_exp_f32 v199, v199\n"
    "s_nop 0\n"
    "v_cvt_pk_bf16_f32 v187, v198, v199\n"
    "v_mfma_f32_16x16x32_bf16 v[204:207], a[16:19], a[144:147], v[204:207]\n"
    "v_mul_f32 v144, s43, v248\n"
    "v_rcp_f32 v248, v248\n"
    "v_mfma_f32_16x16x32_bf16 v[0:3], a[96:99], v[212:215], 0\n"
    "buffer_load_dwordx4 v240, s[56:59], s81 offen offset:0 nt lds\n"
    "v_sub_f32 v144, v144, v144\n"
    "v_mfma_f32_16x16x32_bf16 v[0:3], a[216:219], v[184:187], v[0:3]\n"
    "v_add_f32 v248, v248, v144\n"
    "v_pk_mul_f32 v[32:33], v[32:33], v[248:249] op_sel_hi:[1,0]\n"
    "v_pk_mul_f32 v[34:35], v[34:35], v[248:249] op_sel_hi:[1,0]\n"
    "v_pk_mul_f32 v[36:37], v[36:37], v[248:249] op_sel_hi:[1,0]\n"
    "v_pk_mul_f32 v[38:39], v[38:39], v[248:249] op_sel_hi:[1,0]\n"
    "v_pk_mul_f32 v[40:41], v[40:41], v[248:249] op_sel_hi:[1,0]\n"
    "v_pk_mul_f32 v[42:43], v[42:43], v[248:249] op_sel_hi:[1,0]\n"
    "v_pk_mul_f32 v[44:45], v[44:45], v[248:249] op_sel_hi:[1,0]\n"
    "v_pk_mul_f32 v[46:47], v[46:47], v[248:249] op_sel_hi:[1,0]\n"
    "v_pk_mul_f32 v[48:49], v[48:49], v[248:249] op_sel_hi:[1,0]\n"
    "v_pk_mul_f32 v[50:51], v[50:51], v[248:249] op_sel_hi:[1,0]\n"
    "v_pk_mul_f32 v[52:53], v[52:53], v[248:249] op_sel_hi:[1,0]\n"
    "v_pk_mul_f32 v[54:55], v[54:55], v[248:249] op_sel_hi:[1,0]\n"
    "v_pk_mul_f32 v[56:57], v[56:57], v[248:249] op_sel_hi:[1,0]\n"
    "v_pk_mul_f32 v[58:59], v[58:59], v[248:249] op_sel_hi:[1,0]\n"
    "v_pk_mul_f32 v[60:61], v[60:61], v[248:249] op_sel_hi:[1,0]\n"
    "v_pk_mul_f32 v[62:63], v[62:63], v[248:249] op_sel_hi:[1,0]\n"
    "v_mfma_f32_16x16x32_bf16 v[4:7], a[100:103], v[212:215], 0\n"
    "v_max3_f32 v246, v200, v201, v202\n"
    "v_max3_f32 v247, v203, v204, v205\n"
    "v_mfma_f32_16x16x32_bf16 v[4:7], a[220:223], v[184:187], v[4:7]\n"
    "v_max3_f32 v246, v246, v206, v207\n"
    "v_max_f32 v246, v246, v247\n"
    "v_mov_b32 v247, v246\n"
    "v_cvt_pk_bf16_f32 v144, v32, v33\n"
    "v_mfma_f32_16x16x32_bf16 v[8:11], a[104:107], v[212:215], 0\n"
    "v_permlane32_swap_b32 v246, v247\n"
    "v_mfma_f32_16x16x32_bf16 v[8:11], a[224:227], v[184:187], v[8:11]\n"
    "v_max_f32 v246, v246, v247\n"
    "v_mov_b32 v247, v246\n"
    "v_cvt_pk_bf16_f32 v145, v34, v35\n"
    "v_mfma_f32_16x16x32_bf16 v[12:15], a[108:111], v[212:215], 0\n"
    "v_permlane16_swap_b32 v246, v247\n"
    "v_max_f32 v220, v246, v247\n"
    "v_sub_f32 v220, 0, v220\n"
    "v_mfma_f32_16x16x32_bf16 v[12:15], a[228:231], v[184:187], v[12:15]\n"
    "v_add_f32 v200, v200, v220\n"
    "v_add_f32 v201, v201, v220\n"
    "v_cvt_pk_bf16_f32 v146, v40, v41\n"
    "v_mfma_f32_16x16x32_bf16 a[192:195], a[208:211], v[212:215], 0\n"
    "v_add_f32 v202, v202, v220\n"
    "v_add_f32 v203, v203, v220\n"
    "v_add_f32 v204, v204, v220\n"
    "v_mfma_f32_16x16x32_bf16 a[192:195], a[208:211], v[184:187], a[192:195]\n"
    "v_add_f32 v205, v205, v220\n"
    "v_add_f32 v206, v206, v220\n"
    "v_cvt_pk_bf16_f32 v147, v42, v43\n"
    "v_mfma_f32_16x16x32_bf16 v[192:195], a[32:35], a[144:147], 0\n"
    "v_add_f32 v207, v207, v220\n"
    "v_exp_f32 v200, v200\n"
    "v_mfma_f32_16x16x32_bf16 v[192:195], a[36:39], a[148:151], v[192:195]\n"
    "v_exp_f32 v201, v201\n"
    "v_mov_b32 v221, v220\n"
    "v_cvt_pk_bf16_f32 v152, v36, v37\n"
    "v_mfma_f32_16x16x32_bf16 v[192:195], a[40:43], a[152:155], v[192:195]\n"
    "s_memrealtime s[98:99]\n"
    "v_cvt_pk_bf16_f32 v153, v38, v39\n"
    "v_mfma_f32_16x16x32_bf16 v[192:195], a[44:47], a[156:159], v[192:195]\n"
    "v_exp_f32 v202, v202\n"
    "v_cvt_pk_bf16_f32 v188, v200, v201\n"
    "v_cvt_pk_bf16_f32 v154, v44, v45\n"
    "v_mfma_f32_16x16x32_bf16 v[196:199], a[60:63], a[156:159], 0\n"
    "v_exp_f32 v203, v203\n"
    "v_mfma_f32_16x16x32_bf16 v[196:199], a[56:59], a[152:155], v[196:199]\n"
    "v_mov_b32 v222, v220\n"
    "v_exp_f32 v204, v204\n"
    "v_cvt_pk_bf16_f32 v155, v46, v47\n"
    "v_mfma_f32_16x16x32_bf16 v[196:199], a[52:55], a[148:151], v[196:199]\n"
    "s_waitcnt lgkmcnt(0)\n"
    "s_sub_u32 s99, s98, s25\n"
    "v_cvt_pk_bf16_f32 v189, v202, v203\n"
    "v_mfma_f32_16x16x32_bf16 v[196:199], a[48:51], a[144:147], v[196:199]\n"
    "v_exp_f32 v205, v205\n"
    "v_cvt_pk_bf16_f32 v148, v48, v49\n"
    "v_mfma_f32_16x16x32_bf16 v[16:19], a[112:115], v[212:215], 0\n"
    "s_cmp_lt_i32 s99, 0\n"
    "s_cbranch_scc1 L_pw8\n"
    "L_pb8:\n"
    "v_mov_b32 v223, v220\n"
    "v_mfma_f32_16x16x32_bf16 v[16:19], a[232:235], v[184:187], v[16:19]\n"
    "v_exp_f32 v206, v206\n"
    "v_cvt_pk_bf16_f32 v149, v50, v51\n"
    "v_mfma_f32_16x16x32_bf16 v[20:23], a[116:119], v[212:215], 0\n"
    "buffer_load_dwordx4 v241, s[56:59], s81 offen offset:1024 nt lds\n"
    "s_add_u32 s25, s98, 143\n"
    "v_cvt_pk_bf16_f32 v190, v204, v205\n"
    "v_mfma_f32_16x16x32_bf16 v[20:23], a[236:239], v[184:187], v[20:23]\n"
    "v_exp_f32 v207, v207\n"
    "s_nop 0\n"
    "v_cvt_pk_bf16_f32 v191, v206, v207\n"
    "v_cvt_pk_bf16_f32 v150, v56, v57\n"
    "v_mfma_f32_16x16x32_bf16 v[24:27], a[120:123], v[212:215], 0\n"
    "s_waitcnt vmcnt(14)\n"
    "s_barrier\n"
    "v_add_f32 v192, v192, v220\n"
    "v_mfma_f32_16x16x32_bf16 v[24:27], a[240:243], v[184:187], v[24:27]\n"
    "ds_read_b64_tr_b16 a[64:65], v236 offset:49152\n"
    "v_add_f32 v193, v193, v220\n"
    "v_add_f32 v194, v194, v220\n"
    "v_add_f32 v195, v195, v220\n"
    "v_cvt_pk_bf16_f32 v151, v58, v59\n"
    "v_mfma_f32_16x16x32_bf16 v[28:31], a[124:127], v[212:215], 0\n"
    "ds_read_b64_tr_b16 a[66:67], v236 offset:53248\n"
    "v_add_f32 v196, v196, v220\n"
    "v_add_f32 v197, v197, v220\n"
    "s_min_i32 s88, s19, 0\n"
    "s_sub_i32 s25, s25, s88\n"
    "s_max_i32 s19, s19, 0\n"
    "v_mfma_f32_16x16x32_bf16 v[28:31], a[244:247], v[184:187], v[28:31]\n"
    "ds_read_b64_tr_b16 a[68:69], v237 offset:49152\n"
    "v_add_f32 v198, v198, v220\n"
    "v_add_f32 v199, v199, v220\n"
    "v_cvt_pk_bf16_f32 v156, v52, v53\n"
    "v_mfma_f32_16x16x32_bf16 v[200:203], a[0:3], a[160:163], 0\n"
    "ds_read_b64_tr_b16 a[70:71], v237 offset:53248\n"
    "v_exp_f32 v192, v192\n"
    "v_exp_f32 v193, v193\n"
    "v_mfma_f32_16x16x32_bf16 v[200:203], a[4:7], a[164:167], v[200:203]\n"
    "ds_read_b64_tr_b16 a[72:73], v236 offset:49408\n"
    "v_exp_f32 v194, v194\n"
    "v_cvt_pk_bf16_f32 v157, v54, v55\n"
    "v_mfma_f32_16x16x32_bf16 v[200:203], a[8:11], a[168:171], v[200:203]\n"
    "ds_read_b64_tr_b16 a[74:75], v236 offset:53504\n"
    "v_cvt_pk_bf16_f32 v208, v192, v193\n"
    "v_exp_f32 v195, v195\n"
    "v_mfma_f32_16x16x32_bf16 v[200:203], a[12:15], a[172:175], v[200:203]\n"
    "ds_read_b128 a[0:3], v232 offset:49152\n"
    "v_exp_f32 v196, v196\n"
    "v_cvt_pk_bf16_f32 v209, v194, v195\n"
    "v_cvt_pk_bf16_f32 v158, v60, v61\n"
    "v_mfma_f32_16x16x32_bf16 v[204:207], a[28:31], a[172:175], 0\n"
    "ds_read_b128 a[4:7], v233 offset:49152\n"
    "v_exp_f32 v197, v197\n"
    "v_mfma_f32_16x16x32_bf16 v[204:207], a[24:27], a[168:171], v[204:207]\n"
    "ds_read_b128 a[8:11], v232 offset:49664\n"
    "v_exp_f32 v198, v198\n"
    "v_cvt_pk_bf16_f32 v210, v196, v197\n"
    "v_cvt_pk_bf16_f32 v159, v62, v63\n"
    "v_mfma_f32_16x16x32_bf16 v[204:207], a[20:23], a[164:167], v[204:207]\n"
    "ds_read_b128 a[12:15], v233 offset:49664\n"
    "v_exp_f32 v199, v199\n"
    "s_nop 0\n"
    "v_cvt_pk_bf16_f32 v211, v198, v199\n"
    "v_mfma_f32_16x16x32_bf16 v[204:207], a[16:19], a[160:163], v[204:207]\n"
    "ds_read_b128 a[28:31], v233 offset:53760\n"
    "v_accvgpr_read_b32 v248, a200\n"
    "v_mul_f32 v160, s43, v248\n"
    "v_mfma_f32_16x16x32_bf16 v[32:35], a[96:99], v[188:191], 0\n"
    "ds_read_b128 a[24:27], v232 offset:53760\n"
    "v_mfma_f32_16x16x32_bf16 v[32:35], a[216:219], v[208:211], v[32:35]\n"
    "ds_read_b128 a[20:23], v233 offset:53248\n"
    "v_rcp_f32 v248, v248\n"
    "v_mfma_f32_16x16x32_bf16 v[36:39], a[100:103], v[188:191], 0\n"
    "ds_read_b128 a[16:19], v232 offset:53248\n"
    "v_max3_f32 v246, v200, v201, v202\n"
    "v_sub_f32 v160, v160, v160\n"
    "v_mfma_f32_16x16x32_bf16 v[36:39], a[220:223], v[208:211], v[36:39]\n"
    "ds_read_b64_tr_b16 a[76:77], v237 offset:49408\n"
    "v_max3_f32 v247, v203, v204, v205\n"
    "v_max3_f32 v246, v246, v206, v207\n"
    "v_mfma_f32_16x16x32_bf16 v[40:43], a[104:107], v[188:191], 0\n"
    "ds_read_b64_tr_b16 a[78:79], v237 offset:53504\n"
    "v_max_f32 v246, v246, v247\n"
    "v_mov_b32 v247, v246\n"
    "v_add_f32 v248, v248, v160\n"
    "v_mfma_f32_16x16x32_bf16 v[40:43], a[224:227], v[208:211], v[40:43]\n"
    "ds_read_b64_tr_b16 a[80:81], v236 offset:49664\n"
    "v_permlane32_swap_b32 v246, v247\n"
    "v_mfma_f32_16x16x32_bf16 v[44:47], a[108:111], v[188:191], 0\n"
    "buffer_load_dwordx4 v240, s[56:59], s81 offen offset:2048 nt lds\n"
    "v_max_f32 v246, v246, v247\n"
    "v_mov_b32 v247, v246\n"
    "v_mfma_f32_16x16x32_bf16 v[44:47], a[228:231], v[208:211], v[44:47]\n"
    "ds_read_b64_tr_b16 a[82:83], v236 offset:53760\n"
    "v_permlane16_swap_b32 v246, v247\n"
    "v_pk_mul_f32 v[64:65], v[64:65], v[248:249] op_sel_hi:[1,0]\n"
    "v_pk_mul_f32 v[66:67], v[66:67], v[248:249] op_sel_hi:[1,0]\n"
    "v_pk_mul_f32 v[68:69], v[68:69], v[248:249] op_sel_hi:[1,0]\n"
    "v_pk_mul_f32 v[70:71], v[70:71], v[248:249] op_sel_hi:[1,0]\n"
    "v_pk_mul_f32 v[72:73], v[72:73], v[248:249] op_sel_hi:[1,0]\n"
    "v_pk_mul_f32 v[74:75], v[74:75], v[248:249] op_sel_hi:[1,0]\n"
    "v_pk_mul_f32 v[76:77], v[76:77], v[248:249] op_sel_hi:[1,0]\n"
    "v_pk_mul_f32 v[78:79], v[78:79], v[248:249] op_sel_hi:[1,0]\n"
    "v_pk_mul_f32 v[80:81], v[80:81], v[248:249] op_sel_hi:[1,0]\n"
    "v_pk_mul_f32 v[82:83], v[82:83], v[248:249] op_sel_hi:[1,0]\n"
    "v_pk_mul_f32 v[84:85], v[84:85], v[248:249] op_sel_hi:[1,0]\n"
    "v_pk_mul_f32 v[86:87], v[86:87], v[248:249] op_sel_hi:[1,0]\n"
    "v_pk_mul_f32 v[88:89], v[88:89], v[248:249] op_sel_hi:[1,0]\n"
    "v_pk_mul_f32 v[90:91], v[90:91], v[248:249] op_sel_hi:[1,0]\n"
    "v_pk_mul_f32 v[92:93], v[92:93], v[248:249] op_sel_hi:[1,0]\n"
    "v_pk_mul_f32 v[94:95], v[94:95], v[248:249] op_sel_hi:[1,0]\n"
    "v_mfma_f32_16x16x32_bf16 a[196:199], a[208:211], v[188:191], 0\n"
    "ds_read_b64_tr_b16 a[84:85], v237 offset:49664\n"
    "v_max_f32 v224, v246, v247\n"
    "v_sub_f32 v224, 0, v224\n"
    "L_h10:\n"
    "s_cbranch_execz L_ret3\n"
    "v_cvt_pk_bf16_f32 v160, v64, v65\n"
    "v_mfma_f32_16x16x32_bf16 a[196:199], a[208:211], v[208:211], a[196:199]\n"
    "ds_read_b64_tr_b16 a[86:87], v237 offset:53760\n"
    "v_add_f32 v200, v200, v224\n"
    "v_add_f32 v201, v201, v224\n"
    "v_mfma_f32_16x16x32_bf16 v[192:195], a[32:35], a[160:163], 0\n"
    "ds_read_b64_tr_b16 a[88:89], v236 offset:49920\n"
    "v_add_f32 v202, v202, v224\n"
    "v_add_f32 v203, v203, v224\n"
    "v_cvt_pk_bf16_f32 v161, v66, v67\n"
    "v_mfma_f32_16x16x32_bf16 v[192:195], a[36:39], a[164:167], v[192:195]\n"
    "ds_read_b64_tr_b16 a[90:91], v236 offset:54016\n"
    "v_add_f32 v204, v204, v224\n"
    "v_add_f32 v205, v205, v224\n"
    "v_cvt_pk_bf16_f32 v162, v72, v73\n"
    "v_mfma_f32_16x16x32_bf16 v[192:195], a[40:43], a[168:171], v[192:195]\n"
    "ds_read_b64_tr_b16 a[92:93], v237 offset:49920\n"
    "v_add_f32 v206, v206, v224\n"
    "v_add_f32 v207, v207, v224\n"
    "v_mfma_f32_16x16x32_bf16 v[192:195], a[44:47], a[172:175], v[192:195]\n"
    "ds_read_b64_tr_b16 a[94:95], v237 offset:54016\n"
    "v_exp_f32 v200, v200\n"
    "v_cvt_pk_bf16_f32 v163, v74, v75\n"
    "v_mfma_f32_16x16x32_bf16 v[196:199], a[60:63], a[172:175], 0\n"
    "v_exp_f32 v201, v201\n"
    "v_mov_b32 v225, v224\n"
    "v_cvt_pk_bf16_f32 v168, v68, v69\n"
    "v_mfma_f32_16x16x32_bf16 v[196:199], a[56:59], a[168:171], v[196:199]\n"
    "v_exp_f32 v202, v202\n"
    "v_cvt_pk_bf16_f32 v212, v200, v201\n"
    "v_mfma_f32_16x16x32_bf16 v[196:199], a[52:55], a[164:167], v[196:199]\n"
    "v_exp_f32 v203, v203\n"
    "v_mov_b32 v226, v224\n"
    "v_cvt_pk_bf16_f32 v169, v70, v71\n"
    "v_mfma_f32_16x16x32_bf16 v[196:199], a[48:51], a[160:163], v[196:199]\n"
    "v_exp_f32 v204, v204\n"
    "v_cvt_pk_bf16_f32 v213, v202, v203\n"
    "v_cvt_pk_bf16_f32 v170, v76, v77\n"
    "v_mfma_f32_16x16x32_bf16 v[48:51], a[112:115], v[188:191], 0\n"
    "ds_read_b128 a[32:35], v234 offset:49152\n"
    "v_exp_f32 v205, v205\n"
    "v_mfma_f32_16x16x32_bf16 v[48:51], a[232:235], v[208:211], v[48:51]\n"
    "ds_read_b128 a[36:39], v235 offset:49152\n"
    "v_mov_b32 v227, v224\n"
    "v_cvt_pk_bf16_f32 v171, v78, v79\n"
    "v_mfma_f32_16x16x32_bf16 v[52:55], a[116:119], v[188:191], 0\n"
    "ds_read_b128 a[40:43], v234 offset:49664\n"
    "v_exp_f32 v206, v206\n"
    "v_cvt_pk_bf16_f32 v214, v204, v205\n"
    "v_mfma_f32_16x16x32_bf16 v[52:55], a[236:239], v[208:211], v[52:55]\n"
    "ds_read_b128 a[44:47], v235 offset:49664\n"
    "v_exp_f32 v207, v207\n"
    "v_cvt_pk_bf16_f32 v164, v80, v81\n"
    "v_mfma_f32_16x16x32_bf16 v[56:59], a[120:123], v[188:191], 0\n"
    "ds_read_b128 a[60:63], v235 offset:53760\n"
    "v_cvt_pk_bf16_f32 v215, v206, v207\n"
    "v_cvt_pk_bf16_f32 v165, v82, v83\n"
    "v_mfma_f32_16x16x32_bf16 v[56:59], a[240:243], v[208:211], v[56:59]\n"
    "ds_read_b128 a[56:59], v234 offset:53760\n"
    "v_add_f32 v192, v192, v224\n"
    "v_add_f32 v193, v193, v224\n"
    "v_mfma_f32_16x16x32_bf16 v[60:63], a[124:127], v[188:191], 0\n"
    "ds_read_b128 a[52:55], v235 offset:53248\n"
    "v_add_f32 v194, v194, v224\n"
    "v_add_f32 v195, v195, v224\n"
    "v_cvt_pk_bf16_f32 v166, v88, v89\n"
    "v_mfma_f32_16x16x32_bf16 v[60:63], a[244:247], v[208:211], v[60:63]\n"
    "buffer_load_dwordx4 v241, s[56:59], s81 offen offset:3072 nt lds\n"
    "v_add_f32 v196, v196, v224\n"
    "v_add_f32 v197, v197, v224\n"
    "v_add_f32 v198, v198, v224\n"
    "v_cvt_pk_bf16_f32 v167, v90, v91\n"
    "s_waitcnt lgkmcnt(4)\n"
    "v_mfma_f32_16x16x32_bf16 v[200:203], a[0:3], a[176:179], v[228:231]\n"
    "ds_read_b128 a[48:51], v234 offset:53248\n"
    "s_add_u32 m0, s83, 32768\n"
    "v_add_f32 v199, v199, v224\n"
    "v_mfma_f32_16x16x32_bf16 v[200:203], a[4:7], a[180:183], v[200:203]\n"
    "s_xor_b32 s81, s4, 196608\n"
    "v_exp_f32 v192, v192\n"
    "v_cvt_pk_bf16_f32 v172, v84, v85\n"
    "v_mfma_f32_16x16x32_bf16 v[200:203], a[8:11], a[184:187], v[200:203]\n"
    "v_exp_f32 v193, v193\n"
    "v_exp_f32 v194, v194\n"
    "v_cvt_pk_bf16_f32 v173, v86, v87\n"
    "v_mfma_f32_16x16x32_bf16 v[200:203], a[12:15], a[188:191], v[200:203]\n"
    "v_cvt_pk_bf16_f32 v184, v192, v193\n"
    "v_exp_f32 v195, v195\n"
    "v_mfma_f32_16x16x32_bf16 v[204:207], a[28:31], a[188:191], v[228:231]\n"
    "v_exp_f32 v196, v196\n"
    "v_cvt_pk_bf16_f32 v185, v194, v195\n"
    "v_cvt_pk_bf16_f32 v174, v92, v93\n"
    "v_mfma_f32_16x16x32_bf16 v[204:207], a[24:27], a[184:187], v[204:207]\n"
    "v_exp_f32 v197, v197\n"
    "v_exp_f32 v198, v198\n"
    "v_cvt_pk_bf16_f32 v175, v94, v95\n"
    "v_mfma_f32_16x16x32_bf16 v[204:207], a[20:23], a[180:183], v[204:207]\n"
    "v_cvt_pk_bf16_f32 v186, v196, v197\n"
    "v_exp_f32 v199, v199\n"
    "s_nop 0\n"
    "v_cvt_pk_bf16_f32 v187, v198, v199\n"
    "v_mfma_f32_16x16x32_bf16 v[204:207], a[16:19], a[176:179], v[204:207]\n"
    "v_exp_f32 v200, v200\n"
    "v_mfma_f32_16x16x32_bf16 v[64:67], a[96:99], v[212:215], 0\n"
    "ds_read_b128 a[248:251], v245 offset:5120\n"
    "v_mfma_f32_16x16x32_bf16 v[64:67], a[216:219], v[184:187], v[64:67]\n"
    "v_exp_f32 v201, v201\n"
    "v_mfma_f32_16x16x32_bf16 v[68:71], a[100:103], v[212:215], 0\n"
    "ds_read_b64_tr_b16 a[96:97], v238 offset:49152\n"
    "v_cvt_pk_bf16_f32 v188, v200, v201\n"
    "v_mfma_f32_16x16x32_bf16 v[68:71], a[220:223], v[184:187], v[68:71]\n"
    "v_exp_f32 v202, v202\n"
    "v_mfma_f32_16x16x32_bf16 v[72:75], a[104:107], v[212:215], 0\n"
    "ds_read_b64_tr_b16 a[98:99], v238 offset:53248\n"
    "ds_read_b128 a[252:255], v245 offset:6144\n"
    "v_mfma_f32_16x16x32_bf16 v[72:75], a[224:227], v[184:187], v[72:75]\n"
    "v_exp_f32 v203, v203\n"
    "v_mfma_f32_16x16x32_bf16 v[76:79], a[108:111], v[212:215], 0\n"
    "ds_read_b64_tr_b16 a[100:101], v239 offset:49152\n"
    "v_cvt_pk_bf16_f32 v189, v202, v203\n"
    "v_mfma_f32_16x16x32_bf16 v[76:79], a[228:231], v[184:187], v[76:79]\n"
    "v_exp_f32 v204, v204\n"
    "v_mfma_f32_16x16x32_bf16 a[200:203], a[208:211], v[212:215], 0\n"
    "ds_read_b64_tr_b16 a[102:103], v239 offset:53248\n"
    "v_mfma_f32_16x16x32_bf16 a[200:203], a[208:211], v[184:187], a[200:203]\n"
    "v_exp_f32 v205, v205\n"
    "v_mfma_f32_16x16x32_bf16 v[192:195], a[0:3], a[128:131], v[216:219]\n"
    "ds_read_b64_tr_b16 a[104:105], v238 offset:49408\n"
    "v_cvt_pk_bf16_f32 v190, v204, v205\n"
    "v_mfma_f32_16x16x32_bf16 v[192:195], a[4:7], a[132:135], v[192:195]\n"
    "v_exp_f32 v206, v206\n"
    "v_mfma_f32_16x16x32_bf16 v[192:195], a[8:11], a[136:139], v[192:195]\n"
    "buffer_load_dwordx4 v240, s[52:55], s81 offen offset:0 nt lds\n"
    "v_mfma_f32_16x16x32_bf16 v[192:195], a[12:15], a[140:143], v[192:195]\n"
    "v_exp_f32 v207, v207\n"
    "v_mfma_f32_16x16x32_bf16 v[196:199], a[28:31], a[140:143], v[216:219]\n"
    "ds_read_b64_tr_b16 a[106:107], v238 offset:53504\n"
    "v_cvt_pk_bf16_f32 v191, v206, v207\n"
    "v_mfma_f32_16x16x32_bf16 v[196:199], a[24:27], a[136:139], v[196:199]\n"
    "ds_read_b64_tr_b16 a[108:109], v239 offset:49408\n"
    "v_mfma_f32_16x16x32_bf16 v[196:199], a[20:23], a[132:135], v[196:199]\n"
    "ds_read_b64_tr_b16 a[110:111], v239 offset:53504\n"
    "v_mfma_f32_16x16x32_bf16 v[196:199], a[16:19], a[128:131], v[196:199]\n"
    "v_exp_f32 v192, v192\n"
    "v_mfma_f32_16x16x32_bf16 v[80:83], a[112:115], v[212:215], 0\n"
    "v_mfma_f32_16x16x32_bf16 v[80:83], a[232:235], v[184:187], v[80:83]\n"
    "v_exp_f32 v193, v193\n"
    "v_mfma_f32_16x16x32_bf16 v[84:87], a[116:119], v[212:215], 0\n"
    "ds_read_b64_tr_b16 a[112:113], v238 offset:49664\n"
    "v_cvt_pk_bf16_f32 v208, v192, v193\n"
    "v_mfma_f32_16x16x32_bf16 v[84:87], a[236:239], v[184:187], v[84:87]\n"
    "v_exp_f32 v194, v194\n"
    "v_mfma_f32_16x16x32_bf16 v[88:91], a[120:123], v[212:215], 0\n"
    "ds_read_b64_tr_b16 a[114:115], v238 offset:53760\n"
    "buffer_store_dwordx4 v[176:179], v242, s[60:63], s79 offen offset:0 sc1\n"
    "v_mfma_f32_16x16x32_bf16 v[88:91], a[240:243], v[184:187], v[88:91]\n"
    "v_exp_f32 v195, v195\n"
    "v_mfma_f32_16x16x32_bf16 v[92:95], a[124:127], v[212:215], 0\n"
    "ds_read_b64_tr_b16 a[116:117], v239 offset:49664\n"
    "v_cvt_pk_bf16_f32 v209, v194, v195\n"
    "v_mfma_f32_16x16x32_bf16 v[92:95], a[244:247], v[184:187], v[92:95]\n"
    "v_exp_f32 v196, v196\n"
    "v_mfma_f32_16x16x32_bf16 v[200:203], a[32:35], a[128:131], v[216:219]\n"
    "buffer_load_dwordx4 v241, s[52:55], s81 offen offset:1024 nt lds\n"
    "v_mfma_f32_16x16x32_bf16 v[200:203], a[36:39], a[132:135], v[200:203]\n"
    "v_exp_f32 v197, v197\n"
    "v_mfma_f32_16x16x32_bf16 v[200:203], a[40:43], a[136:139], v[200:203]\n"
    "ds_read_b64_tr_b16 a[118:119], v239 offset:53760\n"
    "v_cvt_pk_bf16_f32 v210, v196, v197\n"
    "s_waitcnt lgkmcnt(3)\n"
    "v_mfma_f32_16x16x32_bf16 v[200:203], a[44:47], a[140:143], v[200:203]\n"
    "v_exp_f32 v198, v198\n"
    "v_mfma_f32_16x16x32_bf16 v[204:207], a[60:63], a[140:143], v[216:219]\n"
    "ds_read_b64_tr_b16 a[120:121], v238 offset:49920\n"
    "v_mfma_f32_16x16x32_bf16 v[204:207], a[56:59], a[136:139], v[204:207]\n"
    "v_exp_f32 v199, v199\n"
    "v_mfma_f32_16x16x32_bf16 v[204:207], a[52:55], a[132:135], v[204:207]\n"
    "ds_read_b64_tr_b16 a[122:123], v238 offset:54016\n"
    "v_cvt_pk_bf16_f32 v211, v198, v199\n"
    "v_mfma_f32_16x16x32_bf16 v[204:207], a[48:51], a[128:131], v[204:207]\n"
    "v_exp_f32 v200, v200\n"
    "v_mfma_f32_16x16x32_bf16 v[96:99], a[64:67], v[188:191], v[96:99]\n"
    "ds_read_b64_tr_b16 a[124:125], v239 offset:49920\n"
    "v_mfma_f32_16x16x32_bf16 v[100:103], a[68:71], v[188:191], v[100:103]\n"
    "v_exp_f32 v201, v201\n"
    "v_mfma_f32_16x16x32_bf16 v[104:107], a[72:75], v[188:191], v[104:107]\n"
    "ds_read_b64_tr_b16 a[126:127], v239 offset:54016\n"
    "v_cvt_pk_bf16_f32 v212, v200, v201\n"
    "v_mfma_f32_16x16x32_bf16 v[108:111], a[76:79], v[188:191], v[108:111]\n"
    "v_exp_f32 v202, v202\n"
    "v_mfma_f32_16x16x32_bf16 v[112:115], a[80:83], v[188:191], v[112:115]\n"
    "v_mfma_f32_16x16x32_bf16 v[116:119], a[84:87], v[188:191], v[116:119]\n"
    "v_exp_f32 v203, v203\n"
    "v_mfma_f32_16x16x32_bf16 v[120:123], a[88:91], v[188:191], v[120:123]\n"
    "v_cvt_pk_bf16_f32 v213, v202, v203\n"
    "v_mfma_f32_16x16x32_bf16 v[124:127], a[92:95], v[188:191], v[124:127]\n"
    "v_exp_f32 v204, v204\n"
    "v_mfma_f32_16x16x32_bf16 a[204:207], a[208:211], v[188:191], a[204:207]\n"
    "v_mfma_f32_16x16x32_bf16 v[192:195], a[0:3], a[144:147], v[220:223]\n"
    "v_exp_f32 v205, v205\n"
    "v_mfma_f32_16x16x32_bf16 v[192:195], a[4:7], a[148:151], v[192:195]\n"
    "buffer_load_dwordx4 v240, s[52:55], s81 offen offset:2048 nt lds\n"
    "v_cvt_pk_bf16_f32 v214, v204, v205\n"
    "v_mfma_f32_16x16x32_bf16 v[192:195], a[8:11], a[152:155], v[192:195]\n"
    "v_exp_f32 v206, v206\n"
    "v_mfma_f32_16x16x32_bf16 v[192:195], a[12:15], a[156:159], v[192:195]\n"
    "v_mfma_f32_16x16x32_bf16 v[196:199], a[28:31], a[156:159], v[220:223]\n"
    "v_exp_f32 v207, v207\n"
    "v_mfma_f32_16x16x32_bf16 v[196:199], a[24:27], a[152:155], v[196:199]\n"
    "v_cvt_pk_bf16_f32 v215, v206, v207\n"
    "v_mfma_f32_16x16x32_bf16 v[196:199], a[20:23], a[148:151], v[196:199]\n"
    "v_mfma_f32_16x16x32_bf16 v[196:199], a[16:19], a[144:147], v[196:199]\n"
    "s_nop 1\n"
    "v_exp_f32 v192, v192\n"
    "v_mfma_f32_16x16x32_bf16 v[0:3], a[64:67], v[208:211], v[0:3]\n"
    "v_mfma_f32_16x16x32_bf16 v[0:3], a[96:99], v[212:215], v[0:3]\n"
    "v_exp_f32 v193, v193\n"
    "v_mfma_f32_16x16x32_bf16 v[4:7], a[68:71], v[208:211], v[4:7]\n"
    "v_cvt_pk_bf16_f32 v184, v192, v193\n"
    "v_mfma_f32_16x16x32_bf16 v[4:7], a[100:103], v[212:215], v[4:7]\n"
    "v_exp_f32 v194, v194\n"
    "v_mfma_f32_16x16x32_bf16 v[8:11], a[72:75], v[208:211], v[8:11]\n"
    "v_mfma_f32_16x16x32_bf16 v[8:11], a[104:107], v[212:215], v[8:11]\n"
    "v_exp_f32 v195, v195\n"
    "v_mfma_f32_16x16x32_bf16 v[12:15], a[76:79], v[208:211], v[12:15]\n"
    "v_cvt_pk_bf16_f32 v185, v194, v195\n"
    "v_mfma_f32_16x16x32_bf16 v[12:15], a[108:111], v[212:215], v[12:15]\n"
    "v_exp_f32 v196, v196\n"
    "v_mfma_f32_16x16x32_bf16 a[192:195], a[208:211], v[208:211], a[192:195]\n"
    "v_mfma_f32_16x16x32_bf16 a[192:195], a[208:211], v[212:215], a[192:195]\n"
    "v_exp_f32 v197, v197\n"
    "v_mfma_f32_16x16x32_bf16 v[200:203], a[32:35], a[144:147], v[220:223]\n"
    "buffer_load_dwordx4 v241, s[52:55], s81 offen offset:3072 nt lds\n"
    "v_cvt_pk_bf16_f32 v186, v196, v197\n"
    "v_mfma_f32_16x16x32_bf16 v[200:203], a[36:39], a[148:151], v[200:203]\n"
    "v_exp_f32 v198, v198\n"
    "v_mfma_f32_16x16x32_bf16 v[200:203], a[40:43], a[152:155], v[200:203]\n"
    "s_memrealtime s[98:99]\n"
    "s_add_u32 m0, s83, 98304\n"
    "v_mfma_f32_16x16x32_bf16 v[200:203], a[44:47], a[156:159], v[200:203]\n"
    "v_exp_f32 v199, v199\n"
    "v_mfma_f32_16x16x32_bf16 v[204:207], a[60:63], a[156:159], v[220:223]\n"
    "s_xor_b32 s81, s4, 196608\n"
    "v_cvt_pk_bf16_f32 v187, v198, v199\n"
    "v_mfma_f32_16x16x32_bf16 v[204:207], a[56:59], a[152:155], v[204:207]\n"
    "v_mfma_f32_16x16x32_bf16 v[204:207], a[52:55], a[148:151], v[204:207]\n"
    "s_waitcnt lgkmcnt(0)\n"
    "s_sub_u32 s99, s98, s25\n"
    "v_mfma_f32_16x16x32_bf16 v[204:207], a[48:51], a[144:147], v[204:207]\n"
    "v_exp_f32 v200, v200\n"
    "v_mfma_f32_16x16x32_bf16 v[16:19], a[80:83], v[208:211], v[16:19]\n"
    "s_cmp_lt_i32 s99, 0\n"
    "s_cbranch_scc1 L_pw9\n"
    "L_pb9:\n"
    "v_mfma_f32_16x16x32_bf16 v[16:19], a[112:115], v[212:215], v[16:19]\n"
    "v_exp_f32 v201, v201\n"
    "v_mfma_f32_16x16x32_bf16 v[20:23], a[84:87], v[208:211], v[20:23]\n"
    "buffer_store_dwordx4 v[180:183], v242, s[60:63], s79 offen offset:1024 sc1\n"
    "s_add_u32 s25, s98, 143\n"
    "v_cvt_pk_bf16_f32 v188, v200, v201\n"
    "v_mfma_f32_16x16x32_bf16 v[20:23], a[116:119], v[212:215], v[20:23]\n"
    "v_exp_f32 v202, v202\n"
    "v_mfma_f32_16x16x32_bf16 v[24:27], a[88:91], v[208:211], v[24:27]\n"
    "s_waitcnt vmcnt(14)\n"
    "s_barrier\n"
    "v_mfma_f32_16x16x32_bf16 v[24:27], a[120:123], v[212:215], v[24:27]\n"
    "v_exp_f32 v203, v203\n"
    "v_mfma_f32_16x16x32_bf16 v[28:31], a[92:95], v[208:211], v[28:31]\n"
    "ds_read_b64_tr_b16 a[216:217], v236 offset:0\n"
    "v_cvt_pk_bf16_f32 v189, v202, v203\n"
    "v_mfma_f32_16x16x32_bf16 v[28:31], a[124:127], v[212:215], v[28:31]\n"
    "v_exp_f32 v204, v204\n"
    "v_mfma_f32_16x16x32_bf16 v[192:195], a[0:3], a[160:163], v[224:227]\n"
    "buffer_load_dwordx4 v240, s[56:59], s81 offen offset:0 nt lds\n"
    "v_mfma_f32_16x16x32_bf16 v[192:195], a[4:7], a[164:167], v[192:195]\n"
    "v_exp_f32 v205, v205\n"
    "v_mfma_f32_16x16x32_bf16 v[192:195], a[8:11], a[168:171], v[192:195]\n"
    "ds_read_b64_tr_b16 a[218:219], v236 offset:4096\n"
    "v_cvt_pk_bf16_f32 v190, v204, v205\n"
    "v_mfma_f32_16x16x32_bf16 v[192:195], a[12:15], a[172:175], v[192:195]\n"
    "v_exp_f32 v206, v206\n"
    "v_mfma_f32_16x16x32_bf16 v[196:199], a[28:31], a[172:175], v[224:227]\n"
    "ds_read_b64_tr_b16 a[220:221], v237 offset:0\n"
    "v_mfma_f32_16x16x32_bf16 v[196:199], a[24:27], a[168:171], v[196:199]\n"
    "v_exp_f32 v207, v207\n"
    "v_mfma_f32_16x16x32_bf16 v[196:199], a[20:23], a[164:167], v[196:199]\n"
    "ds_read_b64_tr_b16 a[222:223], v237 offset:4096\n"
    "v_cvt_pk_bf16_f32 v191, v206, v207\n"
    "v_mfma_f32_16x16x32_bf16 v[196:199], a[16:19], a[160:163], v[196:199]\n"
    "v_exp_f32 v192, v192\n"
    "v_mfma_f32_16x16x32_bf16 v[32:35], a[64:67], v[184:187], v[32:35]\n"
    "ds_read_b64_tr_b16 a[224:225], v236 offset:256\n"
    "v_mfma_f32_16x16x32_bf16 v[32:35], a[96:99], v[188:191], v[32:35]\n"
    "v_exp_f32 v193, v193\n"
    "v_mfma_f32_16x16x32_bf16 v[36:39], a[68:71], v[184:187], v[36:39]\n"
    "ds_read_b64_tr_b16 a[226:227], v236 offset:4352\n"
    "v_cvt_pk_bf16_f32 v208, v192, v193\n"
    "v_mfma_f32_16x16x32_bf16 v[36:39], a[100:103], v[188:191], v[36:39]\n"
    "v_exp_f32 v194, v194\n"
    "v_mfma_f32_16x16x32_bf16 v[40:43], a[72:75], v[184:187], v[40:43]\n"
    "ds_read_b128 a[0:3], v232 offset:0\n"
    "v_mfma_f32_16x16x32_bf16 v[40:43], a[104:107], v[188:191], v[40:43]\n"
    "v_exp_f32 v195, v195\n"
    "v_mfma_f32_16x16x32_bf16 v[44:47], a[76:79], v[184:187], v[44:47]\n"
    "ds_read_b128 a[4:7], v233 offset:0\n"
    "v_cvt_pk_bf16_f32 v209, v194, v195\n"
    "v_mfma_f32_16x16x32_bf16 v[44:47], a[108:111], v[188:191], v[44:47]\n"
    "v_exp_f32 v196, v196\n"
    "v_mfma_f32_16x16x32_bf16 a[196:199], a[208:211], v[184:187], a[196:199]\n"
    "ds_read_b128 a[8:11], v232 offset:512\n"
    "v_mfma_f32_16x16x32_bf16 a[196:199], a[208:211], v[188:191], a[196:199]\n"
    "v_exp_f32 v197, v197\n"
    "v_mfma_f32_16x16x32_bf16 v[200:203], a[32:35], a[160:163], v[224:227]\n"
    "buffer_load_dwordx4 v241, s[56:59], s81 offen offset:1024 nt lds\n"
    "v_cvt_pk_bf16_f32 v210, v196, v197\n"
    "v_mfma_f32_16x16x32_bf16 v[200:203], a[36:39], a[164:167], v[200:203]\n"
    "v_exp_f32 v198, v198\n"
    "v_mfma_f32_16x16x32_bf16 v[200:203], a[40:43], a[168:171], v[200:203]\n"
    "ds_read_b128 a[12:15], v233 offset:512\n"
    "v_mfma_f32_16x16x32_bf16 v[200:203], a[44:47], a[172:175], v[200:203]\n"
    "v_exp_f32 v199, v199\n"
    "v_mfma_f32_16x16x32_bf16 v[204:207], a[60:63], a[172:175], v[224:227]\n"
    "ds_read_b128 a[28:31], v233 offset:4608\n"
    "v_cvt_pk_bf16_f32 v211, v198, v199\n"
    "v_mfma_f32_16x16x32_bf16 v[204:207], a[56:59], a[168:171], v[204:207]\n"
    "ds_read_b128 a[24:27], v232 offset:4608\n"
    "v_mfma_f32_16x16x32_bf16 v[204:207], a[52:55], a[164:167], v[204:207]\n"
    "ds_read_b128 a[20:23], v233 offset:4096\n"
    "v_mfma_f32_16x16x32_bf16 v[204:207], a[48:51], a[160:163], v[204:207]\n"
    "v_exp_f32 v200, v200\n"
    "v_mfma_f32_16x16x32_bf16 v[48:51], a[80:83], v[184:187], v[48:51]\n"
    "ds_read_b128 a[16:19], v232 offset:4096\n"
    "v_mfma_f32_16x16x32_bf16 v[48:51], a[112:115], v[188:191], v[48:51]\n"
    "v_exp_f32 v201, v201\n"
    "v_mfma_f32_16x16x32_bf16 v[52:55], a[84:87], v[184:187], v[52:55]\n"
    "ds_read_b64_tr_b16 a[228:229], v237 offset:256\n"
    "v_cvt_pk_bf16_f32 v212, v200, v201\n"
    "v_mfma_f32_16x16x32_bf16 v[52:55], a[116:119], v[188:191], v[52:55]\n"
    "v_exp_f32 v202, v202\n"
    "v_mfma_f32_16x16x32_bf16 v[56:59], a[88:91], v[184:187], v[56:59]\n"
    "ds_read_b64_tr_b16 a[230:231], v237 offset:4352\n"
    "s_waitcnt lgkmcnt(5)\n"
    "v_mfma_f32_16x16x32_bf16 v[56:59], a[120:123], v[188:191], v[56:59]\n"
    "v_exp_f32 v203, v203\n"
    "v_mfma_f32_16x16x32_bf16 v[60:63], a[92:95], v[184:187], v[60:63]\n"
    "ds_read_b64_tr_b16 a[232:233], v236 offset:512\n"
    "v_cvt_pk_bf16_f32 v213, v202, v203\n"
    "v_mfma_f32_16x16x32_bf16 v[60:63], a[124:127], v[188:191], v[60:63]\n"
    "v_exp_f32 v204, v204\n"
    "v_mfma_f32_16x16x32_bf16 v[192:195], a[0:3], a[176:179], v[228:231]\n"
    "buffer_load_dwordx4 v240, s[56:59], s81 offen offset:2048 nt lds\n"
    "v_mfma_f32_16x16x32_bf16 v[192:195], a[4:7], a[180:183], v[192:195]\n"
    "v_exp_f32 v205, v205\n"
    "v_mfma_f32_16x16x32_bf16 v[192:195], a[8:11], a[184:187], v[192:195]\n"
    "ds_read_b64_tr_b16 a[234:235], v236 offset:4608\n"
    "v_cvt_pk_bf16_f32 v214, v204, v205\n"
    "v_mfma_f32_16x16x32_bf16 v[192:195], a[12:15], a[188:191], v[192:195]\n"
    "v_exp_f32 v206, v206\n"
    "v_mfma_f32_16x16x32_bf16 v[196:199], a[28:31], a[188:191], v[228:231]\n"
    "ds_read_b64_tr_b16 a[236:237], v237 offset:512\n"
    "s_waitcnt lgkmcnt(3)\n"
    "v_mfma_f32_16x16x32_bf16 v[196:199], a[24:27], a[184:187], v[196:199]\n"
    "v_exp_f32 v207, v207\n"
    "v_mfma_f32_16x16x32_bf16 v[196:199], a[20:23], a[180:183], v[196:199]\n"
    "ds_read_b64_tr_b16 a[238:239], v237 offset:4608\n"
    "v_cvt_pk_bf16_f32 v215, v206, v207\n"
    "v_mfma_f32_16x16x32_bf16 v[196:199], a[16:19], a[176:179], v[196:199]\n"
    "v_exp_f32 v192, v192\n"
    "v_mfma_f32_16x16x32_bf16 v[64:67], a[64:67], v[208:211], v[64:67]\n"
    "ds_read_b64_tr_b16 a[240:241], v236 offset:768\n"
    "buffer_store_dwordx4 v[128:131], v242, s[60:63], s76 offen offset:0 sc1\n"
    "v_mfma_f32_16x16x32_bf16 v[64:67], a[96:99], v[212:215], v[64:67]\n"
    "v_exp_f32 v193, v193\n"
    "v_mfma_f32_16x16x32_bf16 v[68:71], a[68:71], v[208:211], v[68:71]\n"
    "ds_read_b64_tr_b16 a[242:243], v236 offset:4864\n"
    "v_cvt_pk_bf16_f32 v184, v192, v193\n"
    "v_mfma_f32_16x16x32_bf16 v[68:71], a[100:103], v[212:215], v[68:71]\n"
    "v_exp_f32 v194, v194\n"
    "v_mfma_f32_16x16x32_bf16 v[72:75], a[72:75], v[208:211], v[72:75]\n"
    "ds_read_b64_tr_b16 a[244:245], v237 offset:768\n"
    "v_mfma_f32_16x16x32_bf16 v[72:75], a[104:107], v[212:215], v[72:75]\n"
    "v_exp_f32 v195, v195\n"
    "v_mfma_f32_16x16x32_bf16 v[76:79], a[76:79], v[208:211], v[76:79]\n"
    "ds_read_b64_tr_b16 a[246:247], v237 offset:4864\n"
    "v_cvt_pk_bf16_f32 v185, v194, v195\n"
    "v_mfma_f32_16x16x32_bf16 v[76:79], a[108:111], v[212:215], v[76:79]\n"
    "v_exp_f32 v196, v196\n"
    "v_mfma_f32_16x16x32_bf16 a[200:203], a[208:211], v[208:211], a[200:203]\n"
    "ds_read_b128 a[32:35], v234 offset:0\n"
    "v_mfma_f32_16x16x32_bf16 a[200:203], a[208:211], v[212:215], a[200:203]\n"
    "v_exp_f32 v197, v197\n"
    "v_mfma_f32_16x16x32_bf16 v[200:203], a[0:3], a[128:131], v[216:219]\n"
    "buffer_load_dwordx4 v241, s[56:59], s81 offen offset:3072 nt lds\n"
    "v_cvt_pk_bf16_f32 v186, v196, v197\n"
    "v_mfma_f32_16x16x32_bf16 v[200:203], a[4:7], a[132:135], v[200:203]\n"
    "v_exp_f32 v198, v198\n"
    "v_mfma_f32_16x16x32_bf16 v[200:203], a[8:11], a[136:139], v[200:203]\n"
    "ds_read_b128 a[36:39], v235 offset:0\n"
    "s_add_u32 m0, s83, 49152\n"
    "v_mfma_f32_16x16x32_bf16 v[200:203], a[12:15], a[140:143], v[200:203]\n"
    "v_exp_f32 v199, v199\n"
    "v_mfma_f32_16x16x32_bf16 v[204:207], a[28:31], a[140:143], v[216:219]\n"
    "ds_read_b128 a[40:43], v234 offset:512\n"
    "v_cvt_pk_bf16_f32 v187, v198, v199\n"
    "v_mfma_f32_16x16x32_bf16 v[204:207], a[24:27], a[136:139], v[204:207]\n"
    "ds_read_b128 a[44:47], v235 offset:512\n"
    "s_xor_b32 s81, s4, 212992\n"
    "v_mfma_f32_16x16x32_bf16 v[204:207], a[20:23], a[132:135], v[204:207]\n"
    "ds_read_b128 a[60:63], v235 offset:4608\n"
    "v_mfma_f32_16x16x32_bf16 v[204:207], a[16:19], a[128:131], v[204:207]\n"
    "v_exp_f32 v200, v200\n"
    "v_mfma_f32_16x16x32_bf16 v[80:83], a[80:83], v[208:211], v[80:83]\n"
    "ds_read_b128 a[56:59], v234 offset:4608\n"
    "v_mfma_f32_16x16x32_bf16 v[80:83], a[112:115], v[212:215], v[80:83]\n"
    "v_exp_f32 v201, v201\n"
    "v_mfma_f32_16x16x32_bf16 v[84:87], a[84:87], v[208:211], v[84:87]\n"
    "ds_read_b128 a[52:55], v235 offset:4096\n"
    "v_cvt_pk_bf16_f32 v188, v200, v201\n"
    "v_mfma_f32_16x16x32_bf16 v[84:87], a[116:119], v[212:215], v[84:87]\n"
    "v_exp_f32 v202, v202\n"
    "v_mfma_f32_16x16x32_bf16 v[88:91], a[88:91], v[208:211], v[88:91]\n"
    "ds_read_b128 a[48:51], v234 offset:4096\n"
    "s_waitcnt lgkmcnt(5)\n"
    "v_mfma_f32_16x16x32_bf16 v[88:91], a[120:123], v[212:215], v[88:91]\n"
    "v_exp_f32 v203, v203\n"
    "v_mfma_f32_16x16x32_bf16 v[92:95], a[92:95], v[208:211], v[92:95]\n"
    "ds_read_b64_tr_b16 a[64:65], v238 offset:0\n"
    "v_cvt_pk_bf16_f32 v189, v202, v203\n"
    "v_mfma_f32_16x16x32_bf16 v[92:95], a[124:127], v[212:215], v[92:95]\n"
    "v_exp_f32 v204, v204\n"
    "v_mfma_f32_16x16x32_bf16 v[192:195], a[32:35], a[128:131], v[216:219]\n"
    "buffer_load_dwordx4 v240, s[52:55], s81 offen offset:0 nt lds\n"
    "s_waitcnt lgkmcnt(3)\n"
    "v_mfma_f32_16x16x32_bf16 v[192:195], a[36:39], a[132:135], v[192:195]\n"
    "v_exp_f32 v205, v205\n"
    "v_mfma_f32_16x16x32_bf16 v[192:195], a[40:43], a[136:139], v[192:195]\n"
    "ds_read_b64_tr_b16 a[66:67], v238 offset:4096\n"
    "v_cvt_pk_bf16_f32 v190, v204, v205\n"
    "v_mfma_f32_16x16x32_bf16 v[192:195], a[44:47], a[140:143], v[192:195]\n"
    "v_exp_f32 v206, v206\n"
    "v_mfma_f32_16x16x32_bf16 v[196:199], a[60:63], a[140:143], v[216:219]\n"
    "ds_read_b64_tr_b16 a[68:69], v239 offset:0\n"
    "s_waitcnt lgkmcnt(3)\n"
    "v_mfma_f32_16x16x32_bf16 v[196:199], a[56:59], a[136:139], v[196:199]\n"
    "v_exp_f32 v207, v207\n"
    "v_mfma_f32_16x16x32_bf16 v[196:199], a[52:55], a[132:135], v[196:199]\n"
    "ds_read_b64_tr_b16 a[70:71], v239 offset:4096\n"
    "v_cvt_pk_bf16_f32 v191, v206, v207\n"
    "v_mfma_f32_16x16x32_bf16 v[196:199], a[48:51], a[128:131], v[196:199]\n"
    "v_exp_f32 v192, v192\n"
    "v_mfma_f32_16x16x32_bf16 v[96:99], a[216:219], v[184:187], v[96:99]\n"
    "ds_read_b64_tr_b16 a[72:73], v238 offset:256\n"
    "v_mfma_f32_16x16x32_bf16 v[100:103], a[220:223], v[184:187], v[100:103]\n"
    "v_exp_f32 v193, v193\n"
    "v_mfma_f32_16x16x32_bf16 v[104:107], a[224:227], v[184:187], v[104:107]\n"
    "ds_read_b64_tr_b16 a[74:75], v238 offset:4352\n"
    "v_cvt_pk_bf16_f32 v208, v192, v193\n"
    "v_mfma_f32_16x16x32_bf16 v[108:111], a[228:231], v[184:187], v[108:111]\n"
    "v_exp_f32 v194, v194\n"
    "v_mfma_f32_16x16x32_bf16 v[112:115], a[232:235], v[184:187], v[112:115]\n"
    "ds_read_b64_tr_b16 a[76:77], v239 offset:256\n"
    "v_mfma_f32_16x16x32_bf16 v[116:119], a[236:239], v[184:187], v[116:119]\n"
    "v_exp_f32 v195, v195\n"
    "v_mfma_f32_16x16x32_bf16 v[120:123], a[240:243], v[184:187], v[120:123]\n"
    "ds_read_b64_tr_b16 a[78:79], v239 offset:4352\n"
    "v_cvt_pk_bf16_f32 v209, v194, v195\n"
    "v_mfma_f32_16x16x32_bf16 v[124:127], a[244:247], v[184:187], v[124:127]\n"
    "v_exp_f32 v196, v196\n"
    "v_mfma_f32_16x16x32_bf16 a[204:207], a[208:211], v[184:187], a[204:207]\n"
    "ds_read_b64_tr_b16 a[80:81], v238 offset:512\n"
    "v_mfma_f32_16x16x32_bf16 v[200:203], a[0:3], a[144:147], v[220:223]\n"
    "v_exp_f32 v197, v197\n"
    "v_mfma_f32_16x16x32_bf16 v[200:203], a[4:7], a[148:151], v[200:203]\n"
    "buffer_load_dwordx4 v241, s[52:55], s81 offen offset:1024 nt lds\n"
    "v_cvt_pk_bf16_f32 v210, v196, v197\n"
    "v_mfma_f32_16x16x32_bf16 v[200:203], a[8:11], a[152:155], v[200:203]\n"
    "v_exp_f32 v198, v198\n"
    "v_mfma_f32_16x16x32_bf16 v[200:203], a[12:15], a[156:159], v[200:203]\n"
    "ds_read_b64_tr_b16 a[82:83], v238 offset:4608\n"
    "v_mfma_f32_16x16x32_bf16 v[204:207], a[28:31], a[156:159], v[220:223]\n"
    "v_exp_f32 v199, v199\n"
    "v_mfma_f32_16x16x32_bf16 v[204:207], a[24:27], a[152:155], v[204:207]\n"
    "ds_read_b64_tr_b16 a[84:85], v239 offset:512\n"
    "v_cvt_pk_bf16_f32 v211, v198, v199\n"
    "v_mfma_f32_16x16x32_bf16 v[204:207], a[20:23], a[148:151], v[204:207]\n"
    "ds_read_b64_tr_b16 a[86:87], v239 offset:4608\n"
    "buffer_store_dwordx4 v[132:135], v242, s[60:63], s76 offen offset:1024 sc1\n"
    "v_mfma_f32_16x16x32_bf16 v[204:207], a[16:19], a[144:147], v[204:207]\n"
    "v_exp_f32 v200, v200\n"
    "v_mfma_f32_16x16x32_bf16 v[0:3], a[216:219], v[188:191], v[0:3]\n"
    "ds_read_b64_tr_b16 a[88:89], v238 offset:768\n"
    "s_waitcnt lgkmcnt(4)\n"
    "v_mfma_f32_16x16x32_bf16 v[0:3], a[64:67], v[208:211], v[0:3]\n"
    "v_exp_f32 v201, v201\n"
    "v_mfma_f32_16x16x32_bf16 v[4:7], a[220:223], v[188:191], v[4:7]\n"
    "ds_read_b64_tr_b16 a[90:91], v238 offset:4864\n"
    "v_cvt_pk_bf16_f32 v212, v200, v201\n"
    "v_mfma_f32_16x16x32_bf16 v[4:7], a[68:71], v[208:211], v[4:7]\n"
    "v_exp_f32 v202, v202\n"
    "v_mfma_f32_16x16x32_bf16 v[8:11], a[224:227], v[188:191], v[8:11]\n"
    "ds_read_b64_tr_b16 a[92:93], v239 offset:768\n"
    "v_mfma_f32_16x16x32_bf16 v[8:11], a[72:75], v[208:211], v[8:11]\n"
    "v_exp_f32 v203, v203\n"
    "v_mfma_f32_16x16x32_bf16 v[12:15], a[228:231], v[188:191], v[12:15]\n"
    "ds_read_b64_tr_b16 a[94:95], v239 offset:4864\n"
    "v_cvt_pk_bf16_f32 v213, v202, v203\n"
    "v_mfma_f32_16x16x32_bf16 v[12:15], a[76:79], v[208:211], v[12:15]\n"
    "v_exp_f32 v204, v204\n"
    "v_mfma_f32_16x16x32_bf16 a[192:195], a[208:211], v[188:191], a[192:195]\n"
    "v_mfma_f32_16x16x32_bf16 a[192:195], a[208:211], v[208:211], a[192:195]\n"
    "v_exp_f32 v205, v205\n"
    "v_mfma_f32_16x16x32_bf16 v[192:195], a[32:35], a[144:147], v[220:223]\n"
    "buffer_load_dwordx4 v240, s[52:55], s81 offen offset:2048 nt lds\n"
    "v_cvt_pk_bf16_f32 v214, v204, v205\n"
    "v_mfma_f32_16x16x32_bf16 v[192:195], a[36:39], a[148:151], v[192:195]\n"
    "v_exp_f32 v206, v206\n"
    "v_mfma_f32_16x16x32_bf16 v[192:195], a[40:43], a[152:155], v[192:195]\n"
    "s_memrealtime s[98:99]\n"
    "v_mfma_f32_16x16x32_bf16 v[192:195], a[44:47], a[156:159], v[192:195]\n"
    "v_exp_f32 v207, v207\n"
    "v_mfma_f32_16x16x32_bf16 v[196:199], a[60:63], a[156:159], v[220:223]\n"
    "v_cvt_pk_bf16_f32 v215, v206, v207\n"
    "v_mfma_f32_16x16x32_bf16 v[196:199], a[56:59], a[152:155], v[196:199]\n"
    "s_waitcnt lgkmcnt(0)\n"
    "v_mfma_f32_16x16x32_bf16 v[196:199], a[52:55], a[148:151], v[196:199]\n"
    "s_waitcnt lgkmcnt(0)\n"
    "s_sub_u32 s99, s98, s25\n"
    "v_mfma_f32_16x16x32_bf16 v[196:199], a[48:51], a[144:147], v[196:199]\n"
    "v_exp_f32 v192, v192\n"
    "v_mfma_f32_16x16x32_bf16 v[16:19], a[232:235], v[188:191], v[16:19]\n"
    "s_cmp_lt_i32 s99, 0\n"
    "s_cbranch_scc1 L_pw10\n"
    "L_pb10:\n"
    "v_mfma_f32_16x16x32_bf16 v[16:19], a[80:83], v[208:211], v[16:19]\n"
    "v_exp_f32 v193, v193\n"
    "v_mfma_f32_16x16x32_bf16 v[20:23], a[236:239], v[188:191], v[20:23]\n"
    "s_add_u32 s25, s98, 143\n"
    "v_cvt_pk_bf16_f32 v184, v192, v193\n"
    "v_mfma_f32_16x16x32_bf16 v[20:23], a[84:87], v[208:211], v[20:23]\n"
    "v_exp_f32 v194, v194\n"
    "v_mfma_f32_16x16x32_bf16 v[24:27], a[240:243], v[188:191], v[24:27]\n"
    "s_waitcnt vmcnt(15)\n"
    "s_barrier\n"
    "v_bfe_u32 v246, v242, 2, 8\n"
    "v_mfma_f32_16x16x32_bf16 v[24:27], a[88:91], v[208:211], v[24:27]\n"
    "v_exp_f32 v195, v195\n"
    "v_mfma_f32_16x16x32_bf16 v[28:31], a[244:247], v[188:191], v[28:31]\n"
    "ds_read_b64_tr_b16 a[96:97], v236 offset:16384\n"
    "v_cvt_pk_bf16_f32 v185, v194, v195\n"
    "v_mfma_f32_16x16x32_bf16 v[28:31], a[92:95], v[208:211], v[28:31]\n"
    "v_exp_f32 v196, v196\n"
    "v_mfma_f32_16x16x32_bf16 v[200:203], a[0:3], a[160:163], v[224:227]\n"
    "buffer_load_dwordx4 v241, s[52:55], s81 offen offset:3072 nt lds\n"
    "s_bfe_u32 s12, s10, 0x10002\n"
    "v_mfma_f32_16x16x32_bf16 v[200:203], a[4:7], a[164:167], v[200:203]\n"
    "v_exp_f32 v197, v197\n"
    "v_mfma_f32_16x16x32_bf16 v[200:203], a[8:11], a[168:171], v[200:203]\n"
    "ds_read_b64_tr_b16 a[98:99], v236 offset:20480\n"
    "v_cvt_pk_bf16_f32 v186, v196, v197\n"
    "v_mfma_f32_16x16x32_bf16 v[200:203], a[12:15], a[172:175], v[200:203]\n"
    "v_exp_f32 v198, v198\n"
    "v_mfma_f32_16x16x32_bf16 v[204:207], a[28:31], a[172:175], v[224:227]\n"
    "ds_read_b64_tr_b16 a[100:101], v237 offset:16384\n"
    "s_add_u32 m0, s83, 114688\n"
    "v_bfe_u32 v249, v242, 4, 6\n"
    "s_getpc_b64 s[90:91]\n"
    "L_pc2:\n"
    "s_lshr_b32 s88, s95, 5\n"
    "s_lshl2_add_u32 s88, s88, s40\n"
    "s_lshl_b32 s88, s88, 8\n"
    "s_add_u32 s88, s88, 4096\n"
    "v_lshl_add_u32 v255, v249, 2, s88\n"
    "v_and_b32 v255, -4, v255\n"
    "v_min_u32 v255, L_end-L_pc2-4, v255\n"
    "global_load_dword a215, v255, s[90:91]\n"
    "v_mfma_f32_16x16x32_bf16 v[204:207], a[24:27], a[168:171], v[204:207]\n"
    "v_exp_f32 v199, v199\n"
    "v_mfma_f32_16x16x32_bf16 v[204:207], a[20:23], a[164:167], v[204:207]\n"
    "ds_read_b64_tr_b16 a[102:103], v237 offset:20480\n"
    "v_cvt_pk_bf16_f32 v187, v198, v199\n"
    "v_mfma_f32_16x16x32_bf16 v[204:207], a[16:19], a[160:163], v[204:207]\n"
    "v_exp_f32 v200, v200\n"
    "v_mfma_f32_16x16x32_bf16 v[32:35], a[216:219], v[212:215], v[32:35]\n"
    "ds_read_b64_tr_b16 a[104:105], v236 offset:16640\n"
    "s_xor_b32 s81, s4, 212992\n"
    "v_mfma_f32_16x16x32_bf16 v[32:35], a[64:67], v[184:187], v[32:35]\n"
    "v_exp_f32 v201, v201\n"
    "v_mfma_f32_16x16x32_bf16 v[36:39], a[220:223], v[212:215], v[36:39]\n"
    "ds_read_b64_tr_b16 a[106:107], v236 offset:20736\n"
    "v_cvt_pk_bf16_f32 v188, v200, v201\n"
    "v_mfma_f32_16x16x32_bf16 v[36:39], a[68:71], v[184:187], v[36:39]\n"
    "v_exp_f32 v202, v202\n"
    "v_mfma_f32_16x16x32_bf16 v[40:43], a[224:227], v[212:215], v[40:43]\n"
    "ds_read_b128 a[0:3], v232 offset:16384\n"
    "s_lshl_b32 s12, s12, 13\n"
    "v_mfma_f32_16x16x32_bf16 v[40:43], a[72:75], v[184:187], v[40:43]\n"
    "L_h11:\n"
    "s_cbranch_execz L_ret3\n"
    "v_exp_f32 v203, v203\n"
    "v_mfma_f32_16x16x32_bf16 v[44:47], a[228:231], v[212:215], v[44:47]\n"
    "ds_read_b128 a[4:7], v233 offset:16384\n"
    "v_cvt_pk_bf16_f32 v189, v202, v203\n"
    "v_mfma_f32_16x16x32_bf16 v[44:47], a[76:79], v[184:187], v[44:47]\n"
    "v_exp_f32 v204, v204\n"
    "v_mfma_f32_16x16x32_bf16 a[196:199], a[208:211], v[212:215], a[196:199]\n"
    "ds_read_b128 a[8:11], v232 offset:16896\n"
    "s_or_b32 s12, s12, 655360\n"
    "v_mfma_f32_16x16x32_bf16 a[196:199], a[208:211], v[184:187], a[196:199]\n"
    "v_exp_f32 v205, v205\n"
    "v_mfma_f32_16x16x32_bf16 v[192:195], a[32:35], a[160:163], v[224:227]\n"
    "buffer_load_dwordx4 v240, s[56:59], s81 offen offset:0 nt lds\n"
    "v_cvt_pk_bf16_f32 v190, v204, v205\n"
    "v_mfma_f32_16x16x32_bf16 v[192:195], a[36:39], a[164:167], v[192:195]\n"
    "v_exp_f32 v206, v206\n"
    "v_mfma_f32_16x16x32_bf16 v[192:195], a[40:43], a[168:171], v[192:195]\n"
    "ds_read_b128 a[12:15], v233 offset:16896\n"
    "s_xor_b32 s15, s12, 131072\n"
    "v_mfma_f32_16x16x32_bf16 v[192:195], a[44:47], a[172:175], v[192:195]\n"
    "v_exp_f32 v207, v207\n"
    "v_mfma_f32_16x16x32_bf16 v[196:199], a[60:63], a[172:175], v[224:227]\n"
    "ds_read_b128 a[28:31], v233 offset:20992\n"
    "buffer_store_dwordx4 v[136:139], v243, s[60:63], s76 offen offset:0 sc1\n"
    "v_cvt_pk_bf16_f32 v191, v206, v207\n"
    "v_mfma_f32_16x16x32_bf16 v[196:199], a[56:59], a[168:171], v[196:199]\n"
    "ds_read_b128 a[24:27], v232 offset:20992\n"
    "s_bfe_u32 s13, s10, 0x10004\n"
    "v_mfma_f32_16x16x32_bf16 v[196:199], a[52:55], a[164:167], v[196:199]\n"
    "ds_read_b128 a[20:23], v233 offset:20480\n"
    "s_lshl_b32 s13, s13, 13\n"
    "v_mfma_f32_16x16x32_bf16 v[196:199], a[48:51], a[160:163], v[196:199]\n"
    "v_exp_f32 v192, v192\n"
    "v_mfma_f32_16x16x32_bf16 v[48:51], a[232:235], v[212:215], v[48:51]\n"
    "ds_read_b128 a[16:19], v232 offset:20480\n"
    "s_or_b32 s13, s13, 1310720\n"
    "v_mfma_f32_16x16x32_bf16 v[48:51], a[80:83], v[184:187], v[48:51]\n"
    "v_exp_f32 v193, v193\n"
    "v_mfma_f32_16x16x32_bf16 v[52:55], a[236:239], v[212:215], v[52:55]\n"
    "ds_read_b64_tr_b16 a[108:109], v237 offset:16640\n"
    "v_cvt_pk_bf16_f32 v208, v192, v193\n"
    "v_mfma_f32_16x16x32_bf16 v[52:55], a[84:87], v[184:187], v[52:55]\n"
    "v_exp_f32 v194, v194\n"
    "v_mfma_f32_16x16x32_bf16 v[56:59], a[240:243], v[212:215], v[56:59]\n"
    "ds_read_b64_tr_b16 a[110:111], v237 offset:20736\n"
    "s_xor_b32 s16, s13, 131072\n"
    "v_mfma_f32_16x16x32_bf16 v[56:59], a[88:91], v[184:187], v[56:59]\n"
    "v_exp_f32 v195, v195\n"
    "v_mfma_f32_16x16x32_bf16 v[60:63], a[244:247], v[212:215], v[60:63]\n"
    "ds_read_b64_tr_b16 a[112:113], v236 offset:16896\n"
    "v_cvt_pk_bf16_f32 v209, v194, v195\n"
    "v_mfma_f32_16x16x32_bf16 v[60:63], a[92:95], v[184:187], v[60:63]\n"
    "v_exp_f32 v196, v196\n"
    "s_waitcnt lgkmcnt(4)\n"
    "v_mfma_f32_16x16x32_bf16 v[200:203], a[0:3], a[176:179], v[228:231]\n"
    "buffer_load_dwordx4 v241, s[56:59], s81 offen offset:1024 nt lds\n"
    "s_bfe_u32 s14, s10, 0x10006\n"
    "v_mfma_f32_16x16x32_bf16 v[200:203], a[4:7], a[180:183], v[200:203]\n"
    "v_exp_f32 v197, v197\n"
    "v_mfma_f32_16x16x32_bf16 v[200:203], a[8:11], a[184:187], v[200:203]\n"
    "ds_read_b64_tr_b16 a[114:115], v236 offset:20992\n"
    "v_cvt_pk_bf16_f32 v210, v196, v197\n"
    "v_mfma_f32_16x16x32_bf16 v[200:203], a[12:15], a[188:191], v[200:203]\n"
    "v_exp_f32 v198, v198\n"
    "v_mfma_f32_16x16x32_bf16 v[204:207], a[28:31], a[188:191], v[228:231]\n"
    "ds_read_b64_tr_b16 a[116:117], v237 offset:16896\n"
    "s_lshl_b32 s14, s14, 13\n"
    "v_mfma_f32_16x16x32_bf16 v[204:207], a[24:27], a[184:187], v[204:207]\n"
    "v_exp_f32 v199, v199\n"
    "v_mfma_f32_16x16x32_bf16 v[204:207], a[20:23], a[180:183], v[204:207]\n"
    "ds_read_b64_tr_b16 a[118:119], v237 offset:20992\n"
    "v_cvt_pk_bf16_f32 v211, v198, v199\n"
    "s_waitcnt lgkmcnt(3)\n"
    "v_mfma_f32_16x16x32_bf16 v[204:207], a[16:19], a[176:179], v[204:207]\n"
    "v_exp_f32 v200, v200\n"
    "v_mfma_f32_16x16x32_bf16 v[64:67], a[216:219], v[188:191], v[64:67]\n"
    "ds_read_b64_tr_b16 a[120:121], v236 offset:17152\n"
    "s_or_b32 s14, s14, 1966080\n"
    "v_mfma_f32_16x16x32_bf16 v[64:67], a[64:67], v[208:211], v[64:67]\n"
    "v_exp_f32 v201, v201\n"
    "v_mfma_f32_16x16x32_bf16 v[68:71], a[220:223], v[188:191], v[68:71]\n"
    "ds_read_b64_tr_b16 a[122:123], v236 offset:21248\n"
    "v_cvt_pk_bf16_f32 v212, v200, v201\n"
    "v_mfma_f32_16x16x32_bf16 v[68:71], a[68:71], v[208:211], v[68:71]\n"
    "v_exp_f32 v202, v202\n"
    "v_mfma_f32_16x16x32_bf16 v[72:75], a[224:227], v[188:191], v[72:75]\n"
    "ds_read_b64_tr_b16 a[124:125], v237 offset:17152\n"
    "s_xor_b32 s17, s14, 131072\n"
    "v_mfma_f32_16x16x32_bf16 v[72:75], a[72:75], v[208:211], v[72:75]\n"
    "v_exp_f32 v203, v203\n"
    "v_mfma_f32_16x16x32_bf16 v[76:79], a[228:231], v[188:191], v[76:79]\n"
    "ds_read_b64_tr_b16 a[126:127], v237 offset:21248\n"
    "v_cvt_pk_bf16_f32 v213, v202, v203\n"
    "v_mfma_f32_16x16x32_bf16 v[76:79], a[76:79], v[208:211], v[76:79]\n"
    "v_exp_f32 v204, v204\n"
    "v_mfma_f32_16x16x32_bf16 a[200:203], a[208:211], v[188:191], a[200:203]\n"
    "ds_read_b128 a[32:35], v234 offset:16384\n"
    "v_mfma_f32_16x16x32_bf16 a[200:203], a[208:211], v[208:211], a[200:203]\n"
    "v_exp_f32 v205, v205\n"
    "v_mfma_f32_16x16x32_bf16 v[192:195], a[0:3], a[128:131], v[216:219]\n"
    "buffer_load_dwordx4 v240, s[56:59], s81 offen offset:2048 nt lds\n"
    "v_cvt_pk_bf16_f32 v214, v204, v205\n"
    "v_mfma_f32_16x16x32_bf16 v[192:195], a[4:7], a[132:135], v[192:195]\n"
    "v_exp_f32 v206, v206\n"
    "v_mfma_f32_16x16x32_bf16 v[192:195], a[8:11], a[136:139], v[192:195]\n"
    "ds_read_b128 a[36:39], v235 offset:16384\n"
    "v_mfma_f32_16x16x32_bf16 v[192:195], a[12:15], a[140:143], v[192:195]\n"
    "v_exp_f32 v207, v207\n"
    "v_mfma_f32_16x16x32_bf16 v[196:199], a[28:31], a[140:143], v[216:219]\n"
    "ds_read_b128 a[40:43], v234 offset:16896\n"
    "v_cvt_pk_bf16_f32 v215, v206, v207\n"
    "v_mfma_f32_16x16x32_bf16 v[196:199], a[24:27], a[136:139], v[196:199]\n"
    "ds_read_b128 a[44:47], v235 offset:16896\n"
    "v_mfma_f32_16x16x32_bf16 v[196:199], a[20:23], a[132:135], v[196:199]\n"
    "ds_read_b128 a[60:63], v235 offset:20992\n"
    "v_mfma_f32_16x16x32_bf16 v[196:199], a[16:19], a[128:131], v[196:199]\n"
    "v_exp_f32 v192, v192\n"
    "v_mfma_f32_16x16x32_bf16 v[80:83], a[232:235], v[188:191], v[80:83]\n"
    "ds_read_b128 a[56:59], v234 offset:20992\n"
    "v_mfma_f32_16x16x32_bf16 v[80:83], a[80:83], v[208:211], v[80:83]\n"
    "v_exp_f32 v193, v193\n"
    "v_mfma_f32_16x16x32_bf16 v[84:87], a[236:239], v[188:191], v[84:87]\n"
    "ds_read_b128 a[52:55], v235 offset:20480\n"
    "v_cvt_pk_bf16_f32 v184, v192, v193\n"
    "v_mfma_f32_16x16x32_bf16 v[84:87], a[84:87], v[208:211], v[84:87]\n"
    "v_exp_f32 v194, v194\n"
    "v_mfma_f32_16x16x32_bf16 v[88:91], a[240:243], v[188:191], v[88:91]\n"
    "ds_read_b128 a[48:51], v234 offset:20480\n"
    "s_waitcnt lgkmcnt(5)\n"
    "v_mfma_f32_16x16x32_bf16 v[88:91], a[88:91], v[208:211], v[88:91]\n"
    "v_exp_f32 v195, v195\n"
    "v_mfma_f32_16x16x32_bf16 v[92:95], a[244:247], v[188:191], v[92:95]\n"
    "ds_read_b64_tr_b16 a[216:217], v238 offset:16384\n"
    "v_cvt_pk_bf16_f32 v185, v194, v195\n"
    "v_mfma_f32_16x16x32_bf16 v[92:95], a[92:95], v[208:211], v[92:95]\n"
    "v_exp_f32 v196, v196\n"
    "v_mfma_f32_16x16x32_bf16 v[200:203], a[32:35], a[128:131], v[216:219]\n"
    "ds_read_b64_tr_b16 a[218:219], v238 offset:20480\n"
    "s_waitcnt lgkmcnt(4)\n"
    "v_mfma_f32_16x16x32_bf16 v[200:203], a[36:39], a[132:135], v[200:203]\n"
    "v_exp_f32 v197, v197\n"
    "v_mfma_f32_16x16x32_bf16 v[200:203], a[40:43], a[136:139], v[200:203]\n"
    "buffer_load_dwordx4 v241, s[56:59], s81 offen offset:3072 nt lds\n"
    "v_cvt_pk_bf16_f32 v186, v196, v197\n"
    "v_mfma_f32_16x16x32_bf16 v[200:203], a[44:47], a[140:143], v[200:203]\n"
    "v_exp_f32 v198, v198\n"
    "v_mfma_f32_16x16x32_bf16 v[204:207], a[60:63], a[140:143], v[216:219]\n"
    "ds_read_b64_tr_b16 a[220:221], v239 offset:16384\n"
    "s_add_u32 m0, s83, 0\n"
    "v_mfma_f32_16x16x32_bf16 v[204:207], a[56:59], a[136:139], v[204:207]\n"
    "v_exp_f32 v199, v199\n"
    "s_waitcnt lgkmcnt(3)\n"
    "v_mfma_f32_16x16x32_bf16 v[204:207], a[52:55], a[132:135], v[204:207]\n"
    "ds_read_b64_tr_b16 a[222:223], v239 offset:20480\n"
    "buffer_store_dwordx4 v[140:143], v243, s[60:63], s76 offen offset:1024 sc1\n"
    "v_cvt_pk_bf16_f32 v187, v198, v199\n"
    "v_mfma_f32_16x16x32_bf16 v[204:207], a[48:51], a[128:131], v[204:207]\n"
    "v_exp_f32 v200, v200\n"
    "v_mfma_f32_16x16x32_bf16 v[96:99], a[96:99], v[212:215], v[96:99]\n"
    "ds_read_b64_tr_b16 a[224:225], v238 offset:16640\n"
    "s_xor_b32 s81, s4, 229376\n"
    "v_mfma_f32_16x16x32_bf16 v[100:103], a[100:103], v[212:215], v[100:103]\n"
    "v_exp_f32 v201, v201\n"
    "v_mfma_f32_16x16x32_bf16 v[104:107], a[104:107], v[212:215], v[104:107]\n"
    "ds_read_b64_tr_b16 a[226:227], v238 offset:20736\n"
    "v_cvt_pk_bf16_f32 v188, v200, v201\n"
    "v_mfma_f32_16x16x32_bf16 v[108:111], a[108:111], v[212:215], v[108:111]\n"
    "v_exp_f32 v202, v202\n"
    "v_mfma_f32_16x16x32_bf16 v[112:115], a[112:115], v[212:215], v[112:115]\n"
    "ds_read_b64_tr_b16 a[228:229], v239 offset:16640\n"
    "v_mfma_f32_16x16x32_bf16 v[116:119], a[116:119], v[212:215], v[116:119]\n"
    "v_exp_f32 v203, v203\n"
    "v_mfma_f32_16x16x32_bf16 v[120:123], a[120:123], v[212:215], v[120:123]\n"
    "ds_read_b64_tr_b16 a[230:231], v239 offset:20736\n"
    "v_cvt_pk_bf16_f32 v189, v202, v203\n"
    "v_mfma_f32_16x16x32_bf16 v[124:127], a[124:127], v[212:215], v[124:127]\n"
    "v_exp_f32 v204, v204\n"
    "v_mfma_f32_16x16x32_bf16 a[204:207], a[208:211], v[212:215], a[204:207]\n"
    "ds_read_b64_tr_b16 a[232:233], v238 offset:16896\n"
    "v_mfma_f32_16x16x32_bf16 v[192:195], a[0:3], a[144:147], v[220:223]\n"
    "v_exp_f32 v205, v205\n"
    "v_mfma_f32_16x16x32_bf16 v[192:195], a[4:7], a[148:151], v[192:195]\n"
    "buffer_load_dwordx4 v240, s[52:55], s81 offen offset:0 nt lds\n"
    "v_cvt_pk_bf16_f32 v190, v204, v205\n"
    "v_mfma_f32_16x16x32_bf16 v[192:195], a[8:11], a[152:155], v[192:195]\n"
    "v_exp_f32 v206, v206\n"
    "v_mfma_f32_16x16x32_bf16 v[192:195], a[12:15], a[156:159], v[192:195]\n"
    "ds_read_b64_tr_b16 a[234:235], v238 offset:20992\n"
    "v_mfma_f32_16x16x32_bf16 v[196:199], a[28:31], a[156:159], v[220:223]\n"
    "v_exp_f32 v207, v207\n"
    "v_mfma_f32_16x16x32_bf16 v[196:199], a[24:27], a[152:155], v[196:199]\n"
    "ds_read_b64_tr_b16 a[236:237], v239 offset:16896\n"
    "v_cvt_pk_bf16_f32 v191, v206, v207\n"
    "v_mfma_f32_16x16x32_bf16 v[196:199], a[20:23], a[148:151], v[196:199]\n"
    "ds_read_b64_tr_b16 a[238:239], v239 offset:20992\n"
    "v_mfma_f32_16x16x32_bf16 v[196:199], a[16:19], a[144:147], v[196:199]\n"
    "v_exp_f32 v192, v192\n"
    "v_mfma_f32_16x16x32_bf16 v[0:3], a[96:99], v[184:187], v[0:3]\n"
    "ds_read_b64_tr_b16 a[240:241], v238 offset:17152\n"
    "s_waitcnt lgkmcnt(4)\n"
    "v_mfma_f32_16x16x32_bf16 v[0:3], a[216:219], v[188:191], v[0:3]\n"
    "v_exp_f32 v193, v193\n"
    "v_mfma_f32_16x16x32_bf16 v[4:7], a[100:103], v[184:187], v[4:7]\n"
    "ds_read_b64_tr_b16 a[242:243], v238 offset:21248\n"
    "v_cvt_pk_bf16_f32 v208, v192, v193\n"
    "v_mfma_f32_16x16x32_bf16 v[4:7], a[220:223], v[188:191], v[4:7]\n"
    "v_exp_f32 v194, v194\n"
    "v_mfma_f32_16x16x32_bf16 v[8:11], a[104:107], v[184:187], v[8:11]\n"
    "ds_read_b64_tr_b16 a[244:245], v239 offset:17152\n"
    "v_mfma_f32_16x16x32_bf16 v[8:11], a[224:227], v[188:191], v[8:11]\n"
    "v_exp_f32 v195, v195\n"
    "v_mfma_f32_16x16x32_bf16 v[12:15], a[108:111], v[184:187], v[12:15]\n"
    "ds_read_b64_tr_b16 a[246:247], v239 offset:21248\n"
    "v_cvt_pk_bf16_f32 v209, v194, v195\n"
    "v_mfma_f32_16x16x32_bf16 v[12:15], a[228:231], v[188:191], v[12:15]\n"
    "v_exp_f32 v196, v196\n"
    "v_mfma_f32_16x16x32_bf16 a[192:195], a[208:211], v[184:187], a[192:195]\n"
    "v_mfma_f32_16x16x32_bf16 a[192:195], a[208:211], v[188:191], a[192:195]\n"
    "v_exp_f32 v197, v197\n"
    "v_mfma_f32_16x16x32_bf16 v[200:203], a[32:35], a[144:147], v[220:223]\n"
    "buffer_load_dwordx4 v241, s[52:55], s81 offen offset:1024 nt lds\n"
    "v_cvt_pk_bf16_f32 v210, v196, v197\n"
    "v_mfma_f32_16x16x32_bf16 v[200:203], a[36:39], a[148:151], v[200:203]\n"
    "v_exp_f32 v198, v198\n"
    "v_mfma_f32_16x16x32_bf16 v[200:203], a[40:43], a[152:155], v[200:203]\n"
    "s_memrealtime s[98:99]\n"
    "v_mfma_f32_16x16x32_bf16 v[200:203], a[44:47], a[156:159], v[200:203]\n"
    "v_exp_f32 v199, v199\n"
    "v_mfma_f32_16x16x32_bf16 v[204:207], a[60:63], a[156:159], v[220:223]\n"
    "v_cvt_pk_bf16_f32 v211, v198, v199\n"
    "v_mfma_f32_16x16x32_bf16 v[204:207], a[56:59], a[152:155], v[204:207]\n"
    "s_waitcnt lgkmcnt(0)\n"
    "v_mfma_f32_16x16x32_bf16 v[204:207], a[52:55], a[148:151], v[204:207]\n"
    "s_waitcnt lgkmcnt(0)\n"
    "s_sub_u32 s99, s98, s25\n"
    "v_mfma_f32_16x16x32_bf16 v[204:207], a[48:51], a[144:147], v[204:207]\n"
    "v_exp_f32 v200, v200\n"
    "v_mfma_f32_16x16x32_bf16 v[16:19], a[112:115], v[184:187], v[16:19]\n"
    "s_cmp_lt_i32 s99, 0\n"
    "s_cbranch_scc1 L_pw11\n"
    "L_pb11:\n"
    "v_mfma_f32_16x16x32_bf16 v[16:19], a[232:235], v[188:191], v[16:19]\n"
    "v_exp_f32 v201, v201\n"
    "v_mfma_f32_16x16x32_bf16 v[20:23], a[116:119], v[184:187], v[20:23]\n"
    "s_add_u32 s25, s98, 143\n"
    "v_cvt_pk_bf16_f32 v212, v200, v201\n"
    "v_mfma_f32_16x16x32_bf16 v[20:23], a[236:239], v[188:191], v[20:23]\n"
    "v_exp_f32 v202, v202\n"
    "v_mfma_f32_16x16x32_bf16 v[24:27], a[120:123], v[184:187], v[24:27]\n"
    "s_waitcnt vmcnt(14)\n"
    "s_barrier\n"
    "v_mfma_f32_16x16x32_bf16 v[24:27], a[240:243], v[188:191], v[24:27]\n"
    "v_exp_f32 v203, v203\n"
    "v_mfma_f32_16x16x32_bf16 v[28:31], a[124:127], v[184:187], v[28:31]\n"
    "ds_read_b64_tr_b16 a[64:65], v236 offset:32768\n"
    "v_cvt_pk_bf16_f32 v213, v202, v203\n"
    "v_mfma_f32_16x16x32_bf16 v[28:31], a[244:247], v[188:191], v[28:31]\n"
    "v_exp_f32 v204, v204\n"
    "v_mfma_f32_16x16x32_bf16 v[192:195], a[0:3], a[160:163], v[224:227]\n"
    "ds_read_b64_tr_b16 a[66:67], v236 offset:36864\n"
    "v_mfma_f32_16x16x32_bf16 v[192:195], a[4:7], a[164:167], v[192:195]\n"
    "v_exp_f32 v205, v205\n"
    "v_mfma_f32_16x16x32_bf16 v[192:195], a[8:11], a[168:171], v[192:195]\n"
    "buffer_load_dwordx4 v240, s[52:55], s81 offen offset:2048 nt lds\n"
    "v_cvt_pk_bf16_f32 v214, v204, v205\n"
    "v_mfma_f32_16x16x32_bf16 v[192:195], a[12:15], a[172:175], v[192:195]\n"
    "v_exp_f32 v206, v206\n"
    "v_mfma_f32_16x16x32_bf16 v[196:199], a[28:31], a[172:175], v[224:227]\n"
    "ds_read_b64_tr_b16 a[68:69], v237 offset:32768\n"
    "v_mfma_f32_16x16x32_bf16 v[196:199], a[24:27], a[168:171], v[196:199]\n"
    "v_exp_f32 v207, v207\n"
    "v_mfma_f32_16x16x32_bf16 v[196:199], a[20:23], a[164:167], v[196:199]\n"
    "ds_read_b64_tr_b16 a[70:71], v237 offset:36864\n"
    "buffer_store_dwordx4 v[144:147], v242, s[60:63], s77 offen offset:0 sc1\n"
    "v_cvt_pk_bf16_f32 v215, v206, v207\n"
    "v_mfma_f32_16x16x32_bf16 v[196:199], a[16:19], a[160:163], v[196:199]\n"
    "v_exp_f32 v192, v192\n"
    "v_mfma_f32_16x16x32_bf16 v[32:35], a[96:99], v[208:211], v[32:35]\n"
    "ds_read_b64_tr_b16 a[72:73], v236 offset:33024\n"
    "v_mfma_f32_16x16x32_bf16 v[32:35], a[216:219], v[212:215], v[32:35]\n"
    "v_exp_f32 v193, v193\n"
    "v_mfma_f32_16x16x32_bf16 v[36:39], a[100:103], v[208:211], v[36:39]\n"
    "ds_read_b64_tr_b16 a[74:75], v236 offset:37120\n"
    "v_cvt_pk_bf16_f32 v184, v192, v193\n"
    "v_mfma_f32_16x16x32_bf16 v[36:39], a[220:223], v[212:215], v[36:39]\n"
    "v_exp_f32 v194, v194\n"
    "v_mfma_f32_16x16x32_bf16 v[40:43], a[104:107], v[208:211], v[40:43]\n"
    "ds_read_b128 a[0:3], v232 offset:32768\n"
    "v_mfma_f32_16x16x32_bf16 v[40:43], a[224:227], v[212:215], v[40:43]\n"
    "v_exp_f32 v195, v195\n"
    "v_mfma_f32_16x16x32_bf16 v[44:47], a[108:111], v[208:211], v[44:47]\n"
    "ds_read_b128 a[4:7], v233 offset:32768\n"
    "v_cvt_pk_bf16_f32 v185, v194, v195\n"
    "v_mfma_f32_16x16x32_bf16 v[44:47], a[228:231], v[212:215], v[44:47]\n"
    "v_exp_f32 v196, v196\n"
    "v_mfma_f32_16x16x32_bf16 a[196:199], a[208:211], v[208:211], a[196:199]\n"
    "ds_read_b128 a[8:11], v232 offset:33280\n"
    "v_mfma_f32_16x16x32_bf16 a[196:199], a[208:211], v[212:215], a[196:199]\n"
    "v_exp_f32 v197, v197\n"
    "v_mfma_f32_16x16x32_bf16 v[200:203], a[32:35], a[160:163], v[224:227]\n"
    "buffer_load_dwordx4 v241, s[52:55], s81 offen offset:3072 nt lds\n"
    "v_cvt_pk_bf16_f32 v186, v196, v197\n"
    "v_mfma_f32_16x16x32_bf16 v[200:203], a[36:39], a[164:167], v[200:203]\n"
    "v_exp_f32 v198, v198\n"
    "v_mfma_f32_16x16x32_bf16 v[200:203], a[40:43], a[168:171], v[200:203]\n"
    "ds_read_b128 a[12:15], v233 offset:33280\n"
    "s_add_u32 m0, s83, 65536\n"
    "v_mfma_f32_16x16x32_bf16 v[200:203], a[44:47], a[172:175], v[200:203]\n"
    "v_exp_f32 v199, v199\n"
    "v_mfma_f32_16x16x32_bf16 v[204:207], a[60:63], a[172:175], v[224:227]\n"
    "ds_read_b128 a[28:31], v233 offset:37376\n"
    "v_cvt_pk_bf16_f32 v187, v198, v199\n"
    "v_mfma_f32_16x16x32_bf16 v[204:207], a[56:59], a[168:171], v[204:207]\n"
    "ds_read_b128 a[24:27], v232 offset:37376\n"
    "s_xor_b32 s81, s4, 229376\n"
    "v_mfma_f32_16x16x32_bf16 v[204:207], a[52:55], a[164:167], v[204:207]\n"
    "ds_read_b128 a[20:23], v233 offset:36864\n"
    "v_mfma_f32_16x16x32_bf16 v[204:207], a[48:51], a[160:163], v[204:207]\n"
    "v_exp_f32 v200, v200\n"
    "v_mfma_f32_16x16x32_bf16 v[48:51], a[112:115], v[208:211], v[48:51]\n"
    "ds_read_b128 a[16:19], v232 offset:36864\n"
    "v_mfma_f32_16x16x32_bf16 v[48:51], a[232:235], v[212:215], v[48:51]\n"
    "v_exp_f32 v201, v201\n"
    "v_mfma_f32_16x16x32_bf16 v[52:55], a[116:119], v[208:211], v[52:55]\n"
    "ds_read_b64_tr_b16 a[76:77], v237 offset:33024\n"
    "v_cvt_pk_bf16_f32 v188, v200, v201\n"
    "v_mfma_f32_16x16x32_bf16 v[52:55], a[236:239], v[212:215], v[52:55]\n"
    "v_exp_f32 v202, v202\n"
    "v_mfma_f32_16x16x32_bf16 v[56:59], a[120:123], v[208:211], v[56:59]\n"
    "ds_read_b64_tr_b16 a[78:79], v237 offset:37120\n"
    "s_waitcnt lgkmcnt(5)\n"
    "v_mfma_f32_16x16x32_bf16 v[56:59], a[240:243], v[212:215], v[56:59]\n"
    "v_exp_f32 v203, v203\n"
    "v_mfma_f32_16x16x32_bf16 v[60:63], a[124:127], v[208:211], v[60:63]\n"
    "ds_read_b64_tr_b16 a[80:81], v236 offset:33280\n"
    "v_cvt_pk_bf16_f32 v189, v202, v203\n"
    "v_mfma_f32_16x16x32_bf16 v[60:63], a[244:247], v[212:215], v[60:63]\n"
    "v_exp_f32 v204, v204\n"
    "v_mfma_f32_16x16x32_bf16 v[192:195], a[0:3], a[176:179], v[228:231]\n"
    "ds_read_b64_tr_b16 a[82:83], v236 offset:37376\n"
    "v_mfma_f32_16x16x32_bf16 v[192:195], a[4:7], a[180:183], v[192:195]\n"
    "v_exp_f32 v205, v205\n"
    "v_mfma_f32_16x16x32_bf16 v[192:195], a[8:11], a[184:187], v[192:195]\n"
    "buffer_load_dwordx4 v240, s[56:59], s81 offen offset:0 nt lds\n"
    "v_cvt_pk_bf16_f32 v190, v204, v205\n"
    "v_mfma_f32_16x16x32_bf16 v[192:195], a[12:15], a[188:191], v[192:195]\n"
    "v_exp_f32 v206, v206\n"
    "v_mfma_f32_16x16x32_bf16 v[196:199], a[28:31], a[188:191], v[228:231]\n"
    "ds_read_b64_tr_b16 a[84:85], v237 offset:33280\n"
    "s_waitcnt lgkmcnt(3)\n"
    "v_mfma_f32_16x16x32_bf16 v[196:199], a[24:27], a[184:187], v[196:199]\n"
    "v_exp_f32 v207, v207\n"
    "v_mfma_f32_16x16x32_bf16 v[196:199], a[20:23], a[180:183], v[196:199]\n"
    "ds_read_b64_tr_b16 a[86:87], v237 offset:37376\n"
    "v_cvt_pk_bf16_f32 v191, v206, v207\n"
    "v_mfma_f32_16x16x32_bf16 v[196:199], a[16:19], a[176:179], v[196:199]\n"
    "v_exp_f32 v192, v192\n"
    "v_mfma_f32_16x16x32_bf16 v[64:67], a[96:99], v[184:187], v[64:67]\n"
    "ds_read_b64_tr_b16 a[88:89], v236 offset:33536\n"
    "v_mfma_f32_16x16x32_bf16 v[64:67], a[216:219], v[188:191], v[64:67]\n"
    "v_exp_f32 v193, v193\n"
    "v_mfma_f32_16x16x32_bf16 v[68:71], a[100:103], v[184:187], v[68:71]\n"
    "ds_read_b64_tr_b16 a[90:91], v236 offset:37632\n"
    "v_cvt_pk_bf16_f32 v208, v192, v193\n"
    "v_mfma_f32_16x16x32_bf16 v[68:71], a[220:223], v[188:191], v[68:71]\n"
    "v_exp_f32 v194, v194\n"
    "v_mfma_f32_16x16x32_bf16 v[72:75], a[104:107], v[184:187], v[72:75]\n"
    "ds_read_b64_tr_b16 a[92:93], v237 offset:33536\n"
    "v_mfma_f32_16x16x32_bf16 v[72:75], a[224:227], v[188:191], v[72:75]\n"
    "v_exp_f32 v195, v195\n"
    "v_mfma_f32_16x16x32_bf16 v[76:79], a[108:111], v[184:187], v[76:79]\n"
    "ds_read_b64_tr_b16 a[94:95], v237 offset:37632\n"
    "v_cvt_pk_bf16_f32 v209, v194, v195\n"
    "v_mfma_f32_16x16x32_bf16 v[76:79], a[228:231], v[188:191], v[76:79]\n"
    "v_exp_f32 v196, v196\n"
    "v_mfma_f32_16x16x32_bf16 a[200:203], a[208:211], v[184:187], a[200:203]\n"
    "ds_read_b128 a[32:35], v234 offset:32768\n"
    "v_mfma_f32_16x16x32_bf16 a[200:203], a[208:211], v[188:191], a[200:203]\n"
    "v_exp_f32 v197, v197\n"
    "v_mfma_f32_16x16x32_bf16 v[200:203], a[0:3], a[128:131], v[216:219]\n"
    "buffer_load_dwordx4 v241, s[56:59], s81 offen offset:1024 nt lds\n"
    "v_cvt_pk_bf16_f32 v210, v196, v197\n"
    "v_mfma_f32_16x16x32_bf16 v[200:203], a[4:7], a[132:135], v[200:203]\n"
    "v_exp_f32 v198, v198\n"
    "v_mfma_f32_16x16x32_bf16 v[200:203], a[8:11], a[136:139], v[200:203]\n"
    "ds_read_b128 a[36:39], v235 offset:32768\n"
    "v_mfma_f32_16x16x32_bf16 v[200:203], a[12:15], a[140:143], v[200:203]\n"
    "v_exp_f32 v199, v199\n"
    "v_mfma_f32_16x16x32_bf16 v[204:207], a[28:31], a[140:143], v[216:219]\n"
    "ds_read_b128 a[40:43], v234 offset:33280\n"
    "buffer_store_dwordx4 v[148:151], v242, s[60:63], s77 offen offset:1024 sc1\n"
    "v_cvt_pk_bf16_f32 v211, v198, v199\n"
    "v_mfma_f32_16x16x32_bf16 v[204:207], a[24:27], a[136:139], v[204:207]\n"
    "ds_read_b128 a[44:47], v235 offset:33280\n"
    "v_mfma_f32_16x16x32_bf16 v[204:207], a[20:23], a[132:135], v[204:207]\n"
    "ds_read_b128 a[60:63], v235 offset:37376\n"
    "v_mfma_f32_16x16x32_bf16 v[204:207], a[16:19], a[128:131], v[204:207]\n"
    "v_exp_f32 v200, v200\n"
    "v_mfma_f32_16x16x32_bf16 v[80:83], a[112:115], v[184:187], v[80:83]\n"
    "ds_read_b128 a[56:59], v234 offset:37376\n"
    "v_mfma_f32_16x16x32_bf16 v[80:83], a[232:235], v[188:191], v[80:83]\n"
    "v_exp_f32 v201, v201\n"
    "v_mfma_f32_16x16x32_bf16 v[84:87], a[116:119], v[184:187], v[84:87]\n"
    "ds_read_b128 a[52:55], v235 offset:36864\n"
    "v_cvt_pk_bf16_f32 v212, v200, v201\n"
    "v_mfma_f32_16x16x32_bf16 v[84:87], a[236:239], v[188:191], v[84:87]\n"
    "v_exp_f32 v202, v202\n"
    "v_mfma_f32_16x16x32_bf16 v[88:91], a[120:123], v[184:187], v[88:91]\n"
    "ds_read_b128 a[48:51], v234 offset:36864\n"
    "s_waitcnt lgkmcnt(5)\n"
    "v_mfma_f32_16x16x32_bf16 v[88:91], a[240:243], v[188:191], v[88:91]\n"
    "v_exp_f32 v203, v203\n"
    "v_mfma_f32_16x16x32_bf16 v[92:95], a[124:127], v[184:187], v[92:95]\n"
    "ds_read_b64_tr_b16 a[96:97], v238 offset:32768\n"
    "v_cvt_pk_bf16_f32 v213, v202, v203\n"
    "v_mfma_f32_16x16x32_bf16 v[92:95], a[244:247], v[188:191], v[92:95]\n"
    "v_exp_f32 v204, v204\n"
    "v_mfma_f32_16x16x32_bf16 v[192:195], a[32:35], a[128:131], v[216:219]\n"
    "ds_read_b64_tr_b16 a[98:99], v238 offset:36864\n"
    "s_waitcnt lgkmcnt(4)\n"
    "v_mfma_f32_16x16x32_bf16 v[192:195], a[36:39], a[132:135], v[192:195]\n"
    "v_exp_f32 v205, v205\n"
    "v_mfma_f32_16x16x32_bf16 v[192:195], a[40:43], a[136:139], v[192:195]\n"
    "buffer_load_dwordx4 v240, s[56:59], s81 offen offset:2048 nt lds\n"
    "v_cvt_pk_bf16_f32 v214, v204, v205\n"
    "v_mfma_f32_16x16x32_bf16 v[192:195], a[44:47], a[140:143], v[192:195]\n"
    "v_exp_f32 v206, v206\n"
    "v_mfma_f32_16x16x32_bf16 v[196:199], a[60:63], a[140:143], v[216:219]\n"
    "ds_read_b64_tr_b16 a[100:101], v239 offset:32768\n"
    "s_waitcnt lgkmcnt(3)\n"
    "v_mfma_f32_16x16x32_bf16 v[196:199], a[56:59], a[136:139], v[196:199]\n"
    "v_exp_f32 v207, v207\n"
    "v_mfma_f32_16x16x32_bf16 v[196:199], a[52:55], a[132:135], v[196:199]\n"
    "ds_read_b64_tr_b16 a[102:103], v239 offset:36864\n"
    "v_cvt_pk_bf16_f32 v215, v206, v207\n"
    "v_mfma_f32_16x16x32_bf16 v[196:199], a[48:51], a[128:131], v[196:199]\n"
    "v_exp_f32 v192, v192\n"
    "v_mfma_f32_16x16x32_bf16 v[96:99], a[64:67], v[208:211], v[96:99]\n"
    "ds_read_b64_tr_b16 a[104:105], v238 offset:33024\n"
    "v_mfma_f32_16x16x32_bf16 v[100:103], a[68:71], v[208:211], v[100:103]\n"
    "v_exp_f32 v193, v193\n"
    "v_mfma_f32_16x16x32_bf16 v[104:107], a[72:75], v[208:211], v[104:107]\n"
    "ds_read_b64_tr_b16 a[106:107], v238 offset:37120\n"
    "v_cvt_pk_bf16_f32 v184, v192, v193\n"
    "v_mfma_f32_16x16x32_bf16 v[108:111], a[76:79], v[208:211], v[108:111]\n"
    "v_exp_f32 v194, v194\n"
    "v_mfma_f32_16x16x32_bf16 v[112:115], a[80:83], v[208:211], v[112:115]\n"
    "ds_read_b64_tr_b16 a[108:109], v239 offset:33024\n"
    "v_mfma_f32_16x16x32_bf16 v[116:119], a[84:87], v[208:211], v[116:119]\n"
    "v_exp_f32 v195, v195\n"
    "v_mfma_f32_16x16x32_bf16 v[120:123], a[88:91], v[208:211], v[120:123]\n"
    "ds_read_b64_tr_b16 a[110:111], v239 offset:37120\n"
    "v_cvt_pk_bf16_f32 v185, v194, v195\n"
    "v_mfma_f32_16x16x32_bf16 v[124:127], a[92:95], v[208:211], v[124:127]\n"
    "v_exp_f32 v196, v196\n"
    "v_mfma_f32_16x16x32_bf16 a[204:207], a[208:211], v[208:211], a[204:207]\n"
    "ds_read_b64_tr_b16 a[112:113], v238 offset:33280\n"
    "v_mfma_f32_16x16x32_bf16 v[200:203], a[0:3], a[144:147], v[220:223]\n"
    "v_exp_f32 v197, v197\n"
    "v_mfma_f32_16x16x32_bf16 v[200:203], a[4:7], a[148:151], v[200:203]\n"
    "buffer_load_dwordx4 v241, s[56:59], s81 offen offset:3072 nt lds\n"
    "v_cvt_pk_bf16_f32 v186, v196, v197\n"
    "v_mfma_f32_16x16x32_bf16 v[200:203], a[8:11], a[152:155], v[200:203]\n"
    "v_exp_f32 v198, v198\n"
    "v_mfma_f32_16x16x32_bf16 v[200:203], a[12:15], a[156:159], v[200:203]\n"
    "ds_read_b64_tr_b16 a[114:115], v238 offset:37376\n"
    "s_add_u32 m0, s83, 16384\n"
    "v_mfma_f32_16x16x32_bf16 v[204:207], a[28:31], a[156:159], v[220:223]\n"
    "v_exp_f32 v199, v199\n"
    "v_mfma_f32_16x16x32_bf16 v[204:207], a[24:27], a[152:155], v[204:207]\n"
    "ds_read_b64_tr_b16 a[116:117], v239 offset:33280\n"
    "v_cvt_pk_bf16_f32 v187, v198, v199\n"
    "v_mfma_f32_16x16x32_bf16 v[204:207], a[20:23], a[148:151], v[204:207]\n"
    "ds_read_b64_tr_b16 a[118:119], v239 offset:37376\n"
    "s_xor_b32 s81, s4, 245760\n"
    "v_mfma_f32_16x16x32_bf16 v[204:207], a[16:19], a[144:147], v[204:207]\n"
    "v_exp_f32 v200, v200\n"
    "v_mfma_f32_16x16x32_bf16 v[0:3], a[64:67], v[212:215], v[0:3]\n"
    "ds_read_b64_tr_b16 a[120:121], v238 offset:33536\n"
    "s_waitcnt lgkmcnt(4)\n"
    "v_mfma_f32_16x16x32_bf16 v[0:3], a[96:99], v[184:187], v[0:3]\n"
    "v_exp_f32 v201, v201\n"
    "v_mfma_f32_16x16x32_bf16 v[4:7], a[68:71], v[212:215], v[4:7]\n"
    "ds_read_b64_tr_b16 a[122:123], v238 offset:37632\n"
    "v_cvt_pk_bf16_f32 v188, v200, v201\n"
    "v_mfma_f32_16x16x32_bf16 v[4:7], a[100:103], v[184:187], v[4:7]\n"
    "v_exp_f32 v202, v202\n"
    "v_mfma_f32_16x16x32_bf16 v[8:11], a[72:75], v[212:215], v[8:11]\n"
    "ds_read_b64_tr_b16 a[124:125], v239 offset:33536\n"
    "v_mfma_f32_16x16x32_bf16 v[8:11], a[104:107], v[184:187], v[8:11]\n"
    "v_exp_f32 v203, v203\n"
    "v_mfma_f32_16x16x32_bf16 v[12:15], a[76:79], v[212:215], v[12:15]\n"
    "ds_read_b64_tr_b16 a[126:127], v239 offset:37632\n"
    "v_cvt_pk_bf16_f32 v189, v202, v203\n"
    "v_mfma_f32_16x16x32_bf16 v[12:15], a[108:111], v[184:187], v[12:15]\n"
    "v_exp_f32 v204, v204\n"
    "v_mfma_f32_16x16x32_bf16 a[192:195], a[208:211], v[212:215], a[192:195]\n"
    "v_mfma_f32_16x16x32_bf16 a[192:195], a[208:211], v[184:187], a[192:195]\n"
    "v_exp_f32 v205, v205\n"
    "v_mfma_f32_16x16x32_bf16 v[192:195], a[32:35], a[144:147], v[220:223]\n"
    "buffer_load_dwordx4 v240, s[52:55], s81 offen offset:0 nt lds\n"
    "v_cvt_pk_bf16_f32 v190, v204, v205\n"
    "v_mfma_f32_16x16x32_bf16 v[192:195], a[36:39], a[148:151], v[192:195]\n"
    "v_exp_f32 v206, v206\n"
    "v_mfma_f32_16x16x32_bf16 v[192:195], a[40:43], a[152:155], v[192:195]\n"
    "s_memrealtime s[98:99]\n"
    "v_mfma_f32_16x16x32_bf16 v[192:195], a[44:47], a[156:159], v[192:195]\n"
    "v_exp_f32 v207, v207\n"
    "v_mfma_f32_16x16x32_bf16 v[196:199], a[60:63], a[156:159], v[220:223]\n"
    "buffer_store_dwordx4 v[152:155], v243, s[60:63], s77 offen offset:0 sc1\n"
    "v_cvt_pk_bf16_f32 v191, v206, v207\n"
    "v_mfma_f32_16x16x32_bf16 v[196:199], a[56:59], a[152:155], v[196:199]\n"
    "s_waitcnt lgkmcnt(0)\n"
    "v_mfma_f32_16x16x32_bf16 v[196:199], a[52:55], a[148:151], v[196:199]\n"
    "s_waitcnt lgkmcnt(0)\n"
    "s_sub_u32 s99, s98, s25\n"
    "v_mfma_f32_16x16x32_bf16 v[196:199], a[48:51], a[144:147], v[196:199]\n"
    "v_exp_f32 v192, v192\n"
    "v_mfma_f32_16x16x32_bf16 v[16:19], a[80:83], v[212:215], v[16:19]\n"
    "s_cmp_lt_i32 s99, 0\n"
    "s_cbranch_scc1 L_pw12\n"
    "L_pb12:\n"
    "v_mfma_f32_16x16x32_bf16 v[16:19], a[112:115], v[184:187], v[16:19]\n"
    "v_exp_f32 v193, v193\n"
    "v_mfma_f32_16x16x32_bf16 v[20:23], a[84:87], v[212:215], v[20:23]\n"
    "s_add_u32 s25, s98, 143\n"
    "v_cvt_pk_bf16_f32 v208, v192, v193\n"
    "v_mfma_f32_16x16x32_bf16 v[20:23], a[116:119], v[184:187], v[20:23]\n"
    "v_exp_f32 v194, v194\n"
    "v_mfma_f32_16x16x32_bf16 v[24:27], a[88:91], v[212:215], v[24:27]\n"
    "s_waitcnt vmcnt(13)\n"
    "s_barrier\n"
    "v_mfma_f32_16x16x32_bf16 v[24:27], a[120:123], v[184:187], v[24:27]\n"
    "v_exp_f32 v195, v195\n"
    "v_mfma_f32_16x16x32_bf16 v[28:31], a[92:95], v[212:215], v[28:31]\n"
    "ds_read_b64_tr_b16 a[216:217], v236 offset:49152\n"
    "v_cvt_pk_bf16_f32 v209, v194, v195\n"
    "v_mfma_f32_16x16x32_bf16 v[28:31], a[124:127], v[184:187], v[28:31]\n"
    "v_exp_f32 v196, v196\n"
    "v_mfma_f32_16x16x32_bf16 v[200:203], a[0:3], a[160:163], v[224:227]\n"
    "ds_read_b64_tr_b16 a[218:219], v236 offset:53248\n"
    "v_mfma_f32_16x16x32_bf16 v[200:203], a[4:7], a[164:167], v[200:203]\n"
    "v_exp_f32 v197, v197\n"
    "v_mfma_f32_16x16x32_bf16 v[200:203], a[8:11], a[168:171], v[200:203]\n"
    "buffer_load_dwordx4 v241, s[52:55], s81 offen offset:1024 nt lds\n"
    "v_cvt_pk_bf16_f32 v210, v196, v197\n"
    "v_mfma_f32_16x16x32_bf16 v[200:203], a[12:15], a[172:175], v[200:203]\n"
    "v_exp_f32 v198, v198\n"
    "v_mfma_f32_16x16x32_bf16 v[204:207], a[28:31], a[172:175], v[224:227]\n"
    "ds_read_b64_tr_b16 a[220:221], v237 offset:49152\n"
    "v_mfma_f32_16x16x32_bf16 v[204:207], a[24:27], a[168:171], v[204:207]\n"
    "v_exp_f32 v199, v199\n"
    "v_mfma_f32_16x16x32_bf16 v[204:207], a[20:23], a[164:167], v[204:207]\n"
    "ds_read_b64_tr_b16 a[222:223], v237 offset:53248\n"
    "v_cvt_pk_bf16_f32 v211, v198, v199\n"
    "v_mfma_f32_16x16x32_bf16 v[204:207], a[16:19], a[160:163], v[204:207]\n"
    "v_exp_f32 v200, v200\n"
    "v_mfma_f32_16x16x32_bf16 v[32:35], a[64:67], v[188:191], v[32:35]\n"
    "ds_read_b64_tr_b16 a[224:225], v236 offset:49408\n"
    "v_mfma_f32_16x16x32_bf16 v[32:35], a[96:99], v[208:211], v[32:35]\n"
    "v_exp_f32 v201, v201\n"
    "v_mfma_f32_16x16x32_bf16 v[36:39], a[68:71], v[188:191], v[36:39]\n"
    "ds_read_b64_tr_b16 a[226:227], v236 offset:53504\n"
    "v_cvt_pk_bf16_f32 v212, v200, v201\n"
    "v_mfma_f32_16x16x32_bf16 v[36:39], a[100:103], v[208:211], v[36:39]\n"
    "v_exp_f32 v202, v202\n"
    "v_mfma_f32_16x16x32_bf16 v[40:43], a[72:75], v[188:191], v[40:43]\n"
    "ds_read_b128 a[0:3], v232 offset:49152\n"
    "v_mfma_f32_16x16x32_bf16 v[40:43], a[104:107], v[208:211], v[40:43]\n"
    "v_exp_f32 v203, v203\n"
    "v_mfma_f32_16x16x32_bf16 v[44:47], a[76:79], v[188:191], v[44:47]\n"
    "ds_read_b128 a[4:7], v233 offset:49152\n"
    "v_cvt_pk_bf16_f32 v213, v202, v203\n"
    "v_mfma_f32_16x16x32_bf16 v[44:47], a[108:111], v[208:211], v[44:47]\n"
    "v_exp_f32 v204, v204\n"
    "v_mfma_f32_16x16x32_bf16 a[196:199], a[208:211], v[188:191], a[196:199]\n"
    "ds_read_b128 a[8:11], v232 offset:49664\n"
    "v_mfma_f32_16x16x32_bf16 a[196:199], a[208:211], v[208:211], a[196:199]\n"
    "v_exp_f32 v205, v205\n"
    "v_mfma_f32_16x16x32_bf16 v[192:195], a[32:35], a[160:163], v[224:227]\n"
    "buffer_load_dwordx4 v240, s[52:55], s81 offen offset:2048 nt lds\n"
    "v_cvt_pk_bf16_f32 v214, v204, v205\n"
    "v_mfma_f32_16x16x32_bf16 v[192:195], a[36:39], a[164:167], v[192:195]\n"
    "v_exp_f32 v206, v206\n"
    "v_mfma_f32_16x16x32_bf16 v[192:195], a[40:43], a[168:171], v[192:195]\n"
    "ds_read_b128 a[12:15], v233 offset:49664\n"
    "L_h12:\n"
    "s_cbranch_execz L_ret3\n"
    "v_mfma_f32_16x16x32_bf16 v[192:195], a[44:47], a[172:175], v[192:195]\n"
    "v_exp_f32 v207, v207\n"
    "v_mfma_f32_16x16x32_bf16 v[196:199], a[60:63], a[172:175], v[224:227]\n"
    "ds_read_b128 a[28:31], v233 offset:53760\n"
    "v_cvt_pk_bf16_f32 v215, v206, v207\n"
    "v_mfma_f32_16x16x32_bf16 v[196:199], a[56:59], a[168:171], v[196:199]\n"
    "ds_read_b128 a[24:27], v232 offset:53760\n"
    "v_mfma_f32_16x16x32_bf16 v[196:199], a[52:55], a[164:167], v[196:199]\n"
    "ds_read_b128 a[20:23], v233 offset:53248\n"
    "v_mfma_f32_16x16x32_bf16 v[196:199], a[48:51], a[160:163], v[196:199]\n"
    "v_exp_f32 v192, v192\n"
    "v_mfma_f32_16x16x32_bf16 v[48:51], a[80:83], v[188:191], v[48:51]\n"
    "ds_read_b128 a[16:19], v232 offset:53248\n"
    "v_mfma_f32_16x16x32_bf16 v[48:51], a[112:115], v[208:211], v[48:51]\n"
    "v_exp_f32 v193, v193\n"
    "v_mfma_f32_16x16x32_bf16 v[52:55], a[84:87], v[188:191], v[52:55]\n"
    "ds_read_b64_tr_b16 a[228:229], v237 offset:49408\n"
    "v_cvt_pk_bf16_f32 v184, v192, v193\n"
    "v_mfma_f32_16x16x32_bf16 v[52:55], a[116:119], v[208:211], v[52:55]\n"
    "v_exp_f32 v194, v194\n"
    "v_mfma_f32_16x16x32_bf16 v[56:59], a[88:91], v[188:191], v[56:59]\n"
    "ds_read_b64_tr_b16 a[230:231], v237 offset:53504\n"
    "s_waitcnt lgkmcnt(5)\n"
    "v_mfma_f32_16x16x32_bf16 v[56:59], a[120:123], v[208:211], v[56:59]\n"
    "v_exp_f32 v195, v195\n"
    "v_mfma_f32_16x16x32_bf16 v[60:63], a[92:95], v[188:191], v[60:63]\n"
    "ds_read_b64_tr_b16 a[232:233], v236 offset:49664\n"
    "v_cvt_pk_bf16_f32 v185, v194, v195\n"
    "v_mfma_f32_16x16x32_bf16 v[60:63], a[124:127], v[208:211], v[60:63]\n"
    "v_exp_f32 v196, v196\n"
    "v_mfma_f32_16x16x32_bf16 v[200:203], a[0:3], a[176:179], v[228:231]\n"
    "ds_read_b64_tr_b16 a[234:235], v236 offset:53760\n"
    "s_waitcnt lgkmcnt(4)\n"
    "v_mfma_f32_16x16x32_bf16 v[200:203], a[4:7], a[180:183], v[200:203]\n"
    "v_exp_f32 v197, v197\n"
    "v_mfma_f32_16x16x32_bf16 v[200:203], a[8:11], a[184:187], v[200:203]\n"
    "buffer_load_dwordx4 v241, s[52:55], s81 offen offset:3072 nt lds\n"
    "v_cvt_pk_bf16_f32 v186, v196, v197\n"
    "v_mfma_f32_16x16x32_bf16 v[200:203], a[12:15], a[188:191], v[200:203]\n"
    "v_exp_f32 v198, v198\n"
    "v_mfma_f32_16x16x32_bf16 v[204:207], a[28:31], a[188:191], v[228:231]\n"
    "ds_read_b64_tr_b16 a[236:237], v237 offset:49664\n"
    "s_add_u32 m0, s83, 81920\n"
    "v_mfma_f32_16x16x32_bf16 v[204:207], a[24:27], a[184:187], v[204:207]\n"
    "v_exp_f32 v199, v199\n"
    "v_mfma_f32_16x16x32_bf16 v[204:207], a[20:23], a[180:183], v[204:207]\n"
    "ds_read_b64_tr_b16 a[238:239], v237 offset:53760\n"
    "buffer_store_dwordx4 v[156:159], v243, s[60:63], s77 offen offset:1024 sc1\n"
    "v_cvt_pk_bf16_f32 v187, v198, v199\n"
    "v_mfma_f32_16x16x32_bf16 v[204:207], a[16:19], a[176:179], v[204:207]\n"
    "v_exp_f32 v200, v200\n"
    "v_mfma_f32_16x16x32_bf16 v[64:67], a[64:67], v[212:215], v[64:67]\n"
    "ds_read_b64_tr_b16 a[240:241], v236 offset:49920\n"
    "s_xor_b32 s81, s4, 245760\n"
    "v_mfma_f32_16x16x32_bf16 v[64:67], a[96:99], v[184:187], v[64:67]\n"
    "v_exp_f32 v201, v201\n"
    "v_mfma_f32_16x16x32_bf16 v[68:71], a[68:71], v[212:215], v[68:71]\n"
    "ds_read_b64_tr_b16 a[242:243], v236 offset:54016\n"
    "v_cvt_pk_bf16_f32 v188, v200, v201\n"
    "v_mfma_f32_16x16x32_bf16 v[68:71], a[100:103], v[184:187], v[68:71]\n"
    "v_exp_f32 v202, v202\n"
    "v_mfma_f32_16x16x32_bf16 v[72:75], a[72:75], v[212:215], v[72:75]\n"
    "ds_read_b64_tr_b16 a[244:245], v237 offset:49920\n"
    "v_mfma_f32_16x16x32_bf16 v[72:75], a[104:107], v[184:187], v[72:75]\n"
    "v_exp_f32 v203, v203\n"
    "v_mfma_f32_16x16x32_bf16 v[76:79], a[76:79], v[212:215], v[76:79]\n"
    "ds_read_b64_tr_b16 a[246:247], v237 offset:54016\n"
    "v_cvt_pk_bf16_f32 v189, v202, v203\n"
    "v_mfma_f32_16x16x32_bf16 v[76:79], a[108:111], v[184:187], v[76:79]\n"
    "v_exp_f32 v204, v204\n"
    "v_mfma_f32_16x16x32_bf16 a[200:203], a[208:211], v[212:215], a[200:203]\n"
    "ds_read_b128 a[32:35], v234 offset:49152\n"
    "v_mfma_f32_16x16x32_bf16 a[200:203], a[208:211], v[184:187], a[200:203]\n"
    "v_exp_f32 v205, v205\n"
    "v_mfma_f32_16x16x32_bf16 v[192:195], a[0:3], a[128:131], v[216:219]\n"
    "buffer_load_dwordx4 v240, s[56:59], s81 offen offset:0 nt lds\n"
    "v_cvt_pk_bf16_f32 v190, v204, v205\n"
    "v_mfma_f32_16x16x32_bf16 v[192:195], a[4:7], a[132:135], v[192:195]\n"
    "v_exp_f32 v206, v206\n"
    "v_mfma_f32_16x16x32_bf16 v[192:195], a[8:11], a[136:139], v[192:195]\n"
    "ds_read_b128 a[36:39], v235 offset:49152\n"
    "v_bfe_u32 v252, v242, 6, 3\n"
    "v_mfma_f32_16x16x32_bf16 v[192:195], a[12:15], a[140:143], v[192:195]\n"
    "v_exp_f32 v207, v207\n"
    "v_mfma_f32_16x16x32_bf16 v[196:199], a[28:31], a[140:143], v[216:219]\n"
    "ds_read_b128 a[40:43], v234 offset:49664\n"
    "v_cvt_pk_bf16_f32 v191, v206, v207\n"
    "v_mfma_f32_16x16x32_bf16 v[196:199], a[24:27], a[136:139], v[196:199]\n"
    "ds_read_b128 a[44:47], v235 offset:49664\n"
    "v_lshlrev_b32 v252, 10, v252\n"
    "v_mfma_f32_16x16x32_bf16 v[196:199], a[20:23], a[132:135], v[196:199]\n"
    "ds_read_b128 a[60:63], v235 offset:53760\n"
    "v_and_b32 v253, 0x30, v242\n"
    "v_mfma_f32_16x16x32_bf16 v[196:199], a[16:19], a[128:131], v[196:199]\n"
    "v_exp_f32 v192, v192\n"
    "v_mfma_f32_16x16x32_bf16 v[80:83], a[80:83], v[212:215], v[80:83]\n"
    "ds_read_b128 a[56:59], v234 offset:53760\n"
    "v_or_b32 v252, v252, v253\n"
    "v_mfma_f32_16x16x32_bf16 v[80:83], a[112:115], v[184:187], v[80:83]\n"
    "v_exp_f32 v193, v193\n"
    "v_mfma_f32_16x16x32_bf16 v[84:87], a[84:87], v[212:215], v[84:87]\n"
    "ds_read_b128 a[52:55], v235 offset:53248\n"
    "v_cvt_pk_bf16_f32 v208, v192, v193\n"
    "v_mfma_f32_16x16x32_bf16 v[84:87], a[116:119], v[184:187], v[84:87]\n"
    "v_exp_f32 v194, v194\n"
    "v_mfma_f32_16x16x32_bf16 v[88:91], a[88:91], v[212:215], v[88:91]\n"
    "ds_read_b128 a[48:51], v234 offset:53248\n"
    "v_lshrrev_b32 v253, 3, v242\n"
    "v_mfma_f32_16x16x32_bf16 v[88:91], a[120:123], v[184:187], v[88:91]\n"
    "v_exp_f32 v195, v195\n"
    "s_mov_b64 exec, 0\n"
    "s_cmp_eq_u32 s40, 0\n"
    "s_cbranch_scc1 L_h13\n"
    "s_cmp_eq_u32 s40, 1\n"
    "s_cbranch_scc1 L_h14\n"
    "s_cmp_eq_u32 s40, 2\n"
    "s_cbranch_scc1 L_h15\n"
    "s_cmp_eq_u32 s40, 3\n"
    "s_cbranch_scc1 L_h16\n"
    "L_ret2:\n"
    "s_mov_b64 exec, -1\n"
    "v_mfma_f32_16x16x32_bf16 v[92:95], a[92:95], v[212:215], v[92:95]\n"
    "ds_read_b64_tr_b16 a[64:65], v238 offset:49152\n"
    "v_cvt_pk_bf16_f32 v209, v194, v195\n"
    "v_mfma_f32_16x16x32_bf16 v[92:95], a[124:127], v[184:187], v[92:95]\n"
    "v_exp_f32 v196, v196\n"
    "s_waitcnt lgkmcnt(4)\n"
    "v_mfma_f32_16x16x32_bf16 v[200:203], a[32:35], a[128:131], v[216:219]\n"
    "ds_read_b64_tr_b16 a[66:67], v238 offset:53248\n"
    "v_and_b32 v253, 0x60, v253\n"
    "v_mfma_f32_16x16x32_bf16 v[200:203], a[36:39], a[132:135], v[200:203]\n"
    "v_exp_f32 v197, v197\n"
    "v_mfma_f32_16x16x32_bf16 v[200:203], a[40:43], a[136:139], v[200:203]\n"
    "buffer_load_dwordx4 v241, s[56:59], s81 offen offset:1024 nt lds\n"
    "v_cvt_pk_bf16_f32 v210, v196, v197\n"
    "v_mfma_f32_16x16x32_bf16 v[200:203], a[44:47], a[140:143], v[200:203]\n"
    "v_exp_f32 v198, v198\n"
    "v_mfma_f32_16x16x32_bf16 v[204:207], a[60:63], a[140:143], v[216:219]\n"
    "ds_read_b64_tr_b16 a[68:69], v239 offset:49152\n"
    "v_xor_b32 v252, v252, v253\n"
    "s_waitcnt lgkmcnt(3)\n"
    "v_mfma_f32_16x16x32_bf16 v[204:207], a[56:59], a[136:139], v[204:207]\n"
    "v_exp_f32 v199, v199\n"
    "v_mfma_f32_16x16x32_bf16 v[204:207], a[52:55], a[132:135], v[204:207]\n"
    "ds_read_b64_tr_b16 a[70:71], v239 offset:53248\n"
    "v_cvt_pk_bf16_f32 v211, v198, v199\n"
    "v_mfma_f32_16x16x32_bf16 v[204:207], a[48:51], a[128:131], v[204:207]\n"
    "v_exp_f32 v200, v200\n"
    "v_mfma_f32_16x16x32_bf16 v[96:99], a[216:219], v[188:191], v[96:99]\n"
    "ds_read_b64_tr_b16 a[72:73], v238 offset:49408\n"
    "s_and_b32 s89, s7, 1\n"
    "v_mfma_f32_16x16x32_bf16 v[100:103], a[220:223], v[188:191], v[100:103]\n"
    "v_exp_f32 v201, v201\n"
    "v_mfma_f32_16x16x32_bf16 v[104:107], a[224:227], v[188:191], v[104:107]\n"
    "ds_read_b64_tr_b16 a[74:75], v238 offset:53504\n"
    "v_cvt_pk_bf16_f32 v212, v200, v201\n"
    "v_mfma_f32_16x16x32_bf16 v[108:111], a[228:231], v[188:191], v[108:111]\n"
    "v_exp_f32 v202, v202\n"
    "v_mfma_f32_16x16x32_bf16 v[112:115], a[232:235], v[188:191], v[112:115]\n"
    "ds_read_b64_tr_b16 a[76:77], v239 offset:49408\n"
    "s_lshl_b32 s89, s89, 6\n"
    "v_mfma_f32_16x16x32_bf16 v[116:119], a[236:239], v[188:191], v[116:119]\n"
    "v_exp_f32 v203, v203\n"
    "v_mfma_f32_16x16x32_bf16 v[120:123], a[240:243], v[188:191], v[120:123]\n"
    "ds_read_b64_tr_b16 a[78:79], v239 offset:53504\n"
    "v_cvt_pk_bf16_f32 v213, v202, v203\n"
    "v_mfma_f32_16x16x32_bf16 v[124:127], a[244:247], v[188:191], v[124:127]\n"
    "v_exp_f32 v204, v204\n"
    "v_mfma_f32_16x16x32_bf16 a[204:207], a[208:211], v[188:191], a[204:207]\n"
    "ds_read_b64_tr_b16 a[80:81], v238 offset:49664\n"
    "v_mfma_f32_16x16x32_bf16 v[192:195], a[0:3], a[144:147], v[220:223]\n"
    "v_exp_f32 v205, v205\n"
    "v_mfma_f32_16x16x32_bf16 v[192:195], a[4:7], a[148:151], v[192:195]\n"
    "buffer_load_dwordx4 v240, s[56:59], s81 offen offset:2048 nt lds\n"
    "v_cvt_pk_bf16_f32 v214, v204, v205\n"
    "v_mfma_f32_16x16x32_bf16 v[192:195], a[8:11], a[152:155], v[192:195]\n"
    "v_exp_f32 v206, v206\n"
    "v_mfma_f32_16x16x32_bf16 v[192:195], a[12:15], a[156:159], v[192:195]\n"
    "ds_read_b64_tr_b16 a[82:83], v238 offset:53760\n"
    "v_mfma_f32_16x16x32_bf16 v[196:199], a[28:31], a[156:159], v[220:223]\n"
    "v_exp_f32 v207, v207\n"
    "v_mfma_f32_16x16x32_bf16 v[196:199], a[24:27], a[152:155], v[196:199]\n"
    "ds_read_b64_tr_b16 a[84:85], v239 offset:49664\n"
    "buffer_store_dwordx4 v[160:163], v242, s[60:63], s78 offen offset:0 sc1\n"
    "v_cvt_pk_bf16_f32 v215, v206, v207\n"
    "v_mfma_f32_16x16x32_bf16 v[196:199], a[20:23], a[148:151], v[196:199]\n"
    "ds_read_b64_tr_b16 a[86:87], v239 offset:53760\n"
    "v_mfma_f32_16x16x32_bf16 v[196:199], a[16:19], a[144:147], v[196:199]\n"
    "v_exp_f32 v192, v192\n"
    "v_mfma_f32_16x16x32_bf16 v[0:3], a[216:219], v[208:211], v[0:3]\n"
    "ds_read_b64_tr_b16 a[88:89], v238 offset:49920\n"
    "s_waitcnt lgkmcnt(4)\n"
    "v_mfma_f32_16x16x32_bf16 v[0:3], a[64:67], v[212:215], v[0:3]\n"
    "v_exp_f32 v193, v193\n"
    "v_mfma_f32_16x16x32_bf16 v[4:7], a[220:223], v[208:211], v[4:7]\n"
    "ds_read_b64_tr_b16 a[90:91], v238 offset:54016\n"
    "v_cvt_pk_bf16_f32 v184, v192, v193\n"
    "v_mfma_f32_16x16x32_bf16 v[4:7], a[68:71], v[212:215], v[4:7]\n"
    "v_exp_f32 v194, v194\n"
    "v_mfma_f32_16x16x32_bf16 v[8:11], a[224:227], v[208:211], v[8:11]\n"
    "ds_read_b64_tr_b16 a[92:93], v239 offset:49920\n"
    "v_mfma_f32_16x16x32_bf16 v[8:11], a[72:75], v[212:215], v[8:11]\n"
    "v_exp_f32 v195, v195\n"
    "v_mfma_f32_16x16x32_bf16 v[12:15], a[228:231], v[208:211], v[12:15]\n"
    "ds_read_b64_tr_b16 a[94:95], v239 offset:54016\n"
    "v_cvt_pk_bf16_f32 v185, v194, v195\n"
    "v_mfma_f32_16x16x32_bf16 v[12:15], a[76:79], v[212:215], v[12:15]\n"
    "v_exp_f32 v196, v196\n"
    "v_mfma_f32_16x16x32_bf16 a[192:195], a[208:211], v[208:211], a[192:195]\n"
    "v_mfma_f32_16x16x32_bf16 a[192:195], a[208:211], v[212:215], a[192:195]\n"
    "v_exp_f32 v197, v197\n"
    "v_mfma_f32_16x16x32_bf16 v[200:203], a[32:35], a[144:147], v[220:223]\n"
    "buffer_load_dwordx4 v241, s[56:59], s81 offen offset:3072 nt lds\n"
    "v_cvt_pk_bf16_f32 v186, v196, v197\n"
    "v_mfma_f32_16x16x32_bf16 v[200:203], a[36:39], a[148:151], v[200:203]\n"
    "v_exp_f32 v198, v198\n"
    "v_mfma_f32_16x16x32_bf16 v[200:203], a[40:43], a[152:155], v[200:203]\n"
    "s_memrealtime s[98:99]\n"
    "v_xor_b32 v240, s89, v252\n"
    "v_mfma_f32_16x16x32_bf16 v[200:203], a[44:47], a[156:159], v[200:203]\n"
    "v_exp_f32 v199, v199\n"
    "v_mfma_f32_16x16x32_bf16 v[204:207], a[60:63], a[156:159], v[220:223]\n"
    "v_cvt_pk_bf16_f32 v187, v198, v199\n"
    "v_add_u32 v241, 7168, v240\n"
    "v_mfma_f32_16x16x32_bf16 v[204:207], a[56:59], a[152:155], v[204:207]\n"
    "s_waitcnt lgkmcnt(0)\n"
    "v_mfma_f32_16x16x32_bf16 v[204:207], a[52:55], a[148:151], v[204:207]\n"
    "s_waitcnt lgkmcnt(0)\n"
    "s_sub_u32 s99, s98, s25\n"
    "v_mfma_f32_16x16x32_bf16 v[204:207], a[48:51], a[144:147], v[204:207]\n"
    "v_exp_f32 v200, v200\n"
    "v_mfma_f32_16x16x32_bf16 v[16:19], a[232:235], v[208:211], v[16:19]\n"
    "s_cmp_lt_i32 s99, 0\n"
    "s_cbranch_scc1 L_pw13\n"
    "L_pb13:\n"
    "v_mfma_f32_16x16x32_bf16 v[16:19], a[80:83], v[212:215], v[16:19]\n"
    "v_exp_f32 v201, v201\n"
    "v_mfma_f32_16x16x32_bf16 v[20:23], a[236:239], v[208:211], v[20:23]\n"
    "s_add_u32 s25, s98, 143\n"
    "v_cvt_pk_bf16_f32 v188, v200, v201\n"
    "v_mfma_f32_16x16x32_bf16 v[20:23], a[84:87], v[212:215], v[20:23]\n"
    "v_exp_f32 v202, v202\n"
    "v_mfma_f32_16x16x32_bf16 v[24:27], a[240:243], v[208:211], v[24:27]\n"
    "s_waitcnt vmcnt(11)\n"
    "s_barrier\n"
    "v_mfma_f32_16x16x32_bf16 v[24:27], a[88:91], v[212:215], v[24:27]\n"
    "v_exp_f32 v203, v203\n"
    "v_mfma_f32_16x16x32_bf16 v[28:31], a[244:247], v[208:211], v[28:31]\n"
    "ds_read_b64_tr_b16 a[96:97], v236 offset:0\n"
    "v_cvt_pk_bf16_f32 v189, v202, v203\n"
    "v_mfma_f32_16x16x32_bf16 v[28:31], a[92:95], v[212:215], v[28:31]\n"
    "v_exp_f32 v204, v204\n"
    "v_mfma_f32_16x16x32_bf16 v[192:195], a[0:3], a[160:163], v[224:227]\n"
    "ds_read_b64_tr_b16 a[98:99], v236 offset:4096\n"
    "v_mfma_f32_16x16x32_bf16 v[192:195], a[4:7], a[164:167], v[192:195]\n"
    "v_exp_f32 v205, v205\n"
    "v_mfma_f32_16x16x32_bf16 v[192:195], a[8:11], a[168:171], v[192:195]\n"
    "ds_read_b64_tr_b16 a[100:101], v237 offset:0\n"
    "v_cvt_pk_bf16_f32 v190, v204, v205\n"
    "v_mfma_f32_16x16x32_bf16 v[192:195], a[12:15], a[172:175], v[192:195]\n"
    "v_exp_f32 v206, v206\n"
    "v_mfma_f32_16x16x32_bf16 v[196:199], a[28:31], a[172:175], v[224:227]\n"
    "ds_read_b64_tr_b16 a[102:103], v237 offset:4096\n"
    "s_add_u32 m0, s83, 32768\n"
    "s_xor_b32 s81, s93, 0\n"
    "v_mfma_f32_16x16x32_bf16 v[196:199], a[24:27], a[168:171], v[196:199]\n"
    "s_add_u32 s82, s81, 14336\n"
    "v_exp_f32 v207, v207\n"
    "v_mfma_f32_16x16x32_bf16 v[196:199], a[20:23], a[164:167], v[196:199]\n"
    "ds_read_b64_tr_b16 a[104:105], v236 offset:256\n"
    "buffer_load_dwordx4 v240, s[64:67], s81 offen offset:0 lds\n"
    "v_cvt_pk_bf16_f32 v191, v206, v207\n"
    "v_mfma_f32_16x16x32_bf16 v[196:199], a[16:19], a[160:163], v[196:199]\n"
    "v_exp_f32 v192, v192\n"
    "v_mfma_f32_16x16x32_bf16 v[32:35], a[216:219], v[184:187], v[32:35]\n"
    "ds_read_b64_tr_b16 a[106:107], v236 offset:4352\n"
    "v_mfma_f32_16x16x32_bf16 v[32:35], a[64:67], v[188:191], v[32:35]\n"
    "v_exp_f32 v193, v193\n"
    "v_mfma_f32_16x16x32_bf16 v[36:39], a[220:223], v[184:187], v[36:39]\n"
    "ds_read_b128 a[0:3], v232 offset:0\n"
    "v_cvt_pk_bf16_f32 v208, v192, v193\n"
    "v_mfma_f32_16x16x32_bf16 v[36:39], a[68:71], v[188:191], v[36:39]\n"
    "v_exp_f32 v194, v194\n"
    "v_mfma_f32_16x16x32_bf16 v[40:43], a[224:227], v[184:187], v[40:43]\n"
    "ds_read_b128 a[4:7], v233 offset:0\n"
    "v_mfma_f32_16x16x32_bf16 v[40:43], a[72:75], v[188:191], v[40:43]\n"
    "v_exp_f32 v195, v195\n"
    "v_mfma_f32_16x16x32_bf16 v[44:47], a[228:231], v[184:187], v[44:47]\n"
    "ds_read_b128 a[8:11], v232 offset:512\n"
    "buffer_load_dwordx4 v241, s[64:67], s81 offen offset:1024 lds\n"
    "v_cvt_pk_bf16_f32 v209, v194, v195\n"
    "v_mfma_f32_16x16x32_bf16 v[44:47], a[76:79], v[188:191], v[44:47]\n"
    "v_exp_f32 v196, v196\n"
    "v_mfma_f32_16x16x32_bf16 a[196:199], a[208:211], v[184:187], a[196:199]\n"
    "ds_read_b128 a[12:15], v233 offset:512\n"
    "v_mfma_f32_16x16x32_bf16 a[196:199], a[208:211], v[188:191], a[196:199]\n"
    "v_exp_f32 v197, v197\n"
    "v_mfma_f32_16x16x32_bf16 v[200:203], a[32:35], a[160:163], v[224:227]\n"
    "ds_read_b128 a[28:31], v233 offset:4608\n"
    "v_cvt_pk_bf16_f32 v210, v196, v197\n"
    "v_mfma_f32_16x16x32_bf16 v[200:203], a[36:39], a[164:167], v[200:203]\n"
    "v_exp_f32 v198, v198\n"
    "v_mfma_f32_16x16x32_bf16 v[200:203], a[40:43], a[168:171], v[200:203]\n"
    "ds_read_b128 a[24:27], v232 offset:4608\n"
    "buffer_store_dwordx4 v[164:167], v242, s[60:63], s78 offen offset:1024 sc1\n"
    "v_mfma_f32_16x16x32_bf16 v[200:203], a[44:47], a[172:175], v[200:203]\n"
    "v_exp_f32 v199, v199\n"
    "v_mfma_f32_16x16x32_bf16 v[204:207], a[60:63], a[172:175], v[224:227]\n"
    "ds_read_b128 a[20:23], v233 offset:4096\n"
    "v_cvt_pk_bf16_f32 v211, v198, v199\n"
    "v_mfma_f32_16x16x32_bf16 v[204:207], a[56:59], a[168:171], v[204:207]\n"
    "ds_read_b128 a[16:19], v232 offset:4096\n"
    "v_mfma_f32_16x16x32_bf16 v[204:207], a[52:55], a[164:167], v[204:207]\n"
    "ds_read_b64_tr_b16 a[108:109], v237 offset:256\n"
    "v_mfma_f32_16x16x32_bf16 v[204:207], a[48:51], a[160:163], v[204:207]\n"
    "v_exp_f32 v200, v200\n"
    "v_mfma_f32_16x16x32_bf16 v[48:51], a[232:235], v[184:187], v[48:51]\n"
    "ds_read_b64_tr_b16 a[110:111], v237 offset:4352\n"
    "buffer_load_dwordx4 v240, s[64:67], s82 offen offset:2048 lds\n"
    "v_mfma_f32_16x16x32_bf16 v[48:51], a[80:83], v[188:191], v[48:51]\n"
    "v_exp_f32 v201, v201\n"
    "v_mfma_f32_16x16x32_bf16 v[52:55], a[236:239], v[184:187], v[52:55]\n"
    "ds_read_b64_tr_b16 a[112:113], v236 offset:512\n"
    "v_cvt_pk_bf16_f32 v212, v200, v201\n"
    "v_mfma_f32_16x16x32_bf16 v[52:55], a[84:87], v[188:191], v[52:55]\n"
    "v_exp_f32 v202, v202\n"
    "v_mfma_f32_16x16x32_bf16 v[56:59], a[240:243], v[184:187], v[56:59]\n"
    "ds_read_b64_tr_b16 a[114:115], v236 offset:4608\n"
    "s_waitcnt lgkmcnt(5)\n"
    "v_mfma_f32_16x16x32_bf16 v[56:59], a[88:91], v[188:191], v[56:59]\n"
    "v_exp_f32 v203, v203\n"
    "v_mfma_f32_16x16x32_bf16 v[60:63], a[244:247], v[184:187], v[60:63]\n"
    "ds_read_b64_tr_b16 a[116:117], v237 offset:512\n"
    "v_cvt_pk_bf16_f32 v213, v202, v203\n"
    "v_mfma_f32_16x16x32_bf16 v[60:63], a[92:95], v[188:191], v[60:63]\n"
    "v_exp_f32 v204, v204\n"
    "v_mfma_f32_16x16x32_bf16 v[192:195], a[0:3], a[176:179], v[228:231]\n"
    "ds_read_b64_tr_b16 a[118:119], v237 offset:4608\n"
    "buffer_load_dwordx4 v241, s[64:67], s82 offen offset:3072 lds\n"
    "v_mfma_f32_16x16x32_bf16 v[192:195], a[4:7], a[180:183], v[192:195]\n"
    "v_exp_f32 v205, v205\n"
    "v_mfma_f32_16x16x32_bf16 v[192:195], a[8:11], a[184:187], v[192:195]\n"
    "ds_read_b64_tr_b16 a[120:121], v236 offset:768\n"
    "v_cvt_pk_bf16_f32 v214, v204, v205\n"
    "v_mfma_f32_16x16x32_bf16 v[192:195], a[12:15], a[188:191], v[192:195]\n"
    "v_exp_f32 v206, v206\n"
    "v_mfma_f32_16x16x32_bf16 v[196:199], a[28:31], a[188:191], v[228:231]\n"
    "ds_read_b64_tr_b16 a[122:123], v236 offset:4864\n"
    "s_waitcnt lgkmcnt(4)\n"
    "v_mfma_f32_16x16x32_bf16 v[196:199], a[24:27], a[184:187], v[196:199]\n"
    "v_exp_f32 v207, v207\n"
    "v_mfma_f32_16x16x32_bf16 v[196:199], a[20:23], a[180:183], v[196:199]\n"
    "ds_read_b64_tr_b16 a[124:125], v237 offset:768\n"
    "s_add_u32 m0, s83, 98304\n"
    "v_cvt_pk_bf16_f32 v215, v206, v207\n"
    "v_mfma_f32_16x16x32_bf16 v[196:199], a[16:19], a[176:179], v[196:199]\n"
    "s_xor_b32 s81, s93, 128\n"
    "s_add_u32 s82, s81, 14336\n"
    "v_exp_f32 v192, v192\n"
    "v_mfma_f32_16x16x32_bf16 v[64:67], a[216:219], v[208:211], v[64:67]\n"
    "ds_read_b64_tr_b16 a[126:127], v237 offset:4864\n"
    "buffer_load_dwordx4 v240, s[64:67], s81 offen offset:0 lds\n"
    "v_mfma_f32_16x16x32_bf16 v[64:67], a[64:67], v[212:215], v[64:67]\n"
    "v_exp_f32 v193, v193\n"
    "v_mfma_f32_16x16x32_bf16 v[68:71], a[220:223], v[208:211], v[68:71]\n"
    "ds_read_b128 a[32:35], v234 offset:0\n"
    "v_cvt_pk_bf16_f32 v184, v192, v193\n"
    "v_mfma_f32_16x16x32_bf16 v[68:71], a[68:71], v[212:215], v[68:71]\n"
    "v_exp_f32 v194, v194\n"
    "v_mfma_f32_16x16x32_bf16 v[72:75], a[224:227], v[208:211], v[72:75]\n"
    "ds_read_b128 a[36:39], v235 offset:0\n"
    "v_mfma_f32_16x16x32_bf16 v[72:75], a[72:75], v[212:215], v[72:75]\n"
    "v_exp_f32 v195, v195\n"
    "v_mfma_f32_16x16x32_bf16 v[76:79], a[228:231], v[208:211], v[76:79]\n"
    "ds_read_b128 a[40:43], v234 offset:512\n"
    "v_cvt_pk_bf16_f32 v185, v194, v195\n"
    "v_mfma_f32_16x16x32_bf16 v[76:79], a[76:79], v[212:215], v[76:79]\n"
    "v_exp_f32 v196, v196\n"
    "v_mfma_f32_16x16x32_bf16 a[200:203], a[208:211], v[208:211], a[200:203]\n"
    "ds_read_b128 a[44:47], v235 offset:512\n"
    "buffer_load_dwordx4 v241, s[64:67], s81 offen offset:1024 lds\n"
    "v_mfma_f32_16x16x32_bf16 a[200:203], a[208:211], v[212:215], a[200:203]\n"
    "v_exp_f32 v197, v197\n"
    "v_mfma_f32_16x16x32_bf16 v[200:203], a[0:3], a[128:131], v[216:219]\n"
    "ds_read_b128 a[60:63], v235 offset:4608\n"
    "v_cvt_pk_bf16_f32 v186, v196, v197\n"
    "v_mfma_f32_16x16x32_bf16 v[200:203], a[4:7], a[132:135], v[200:203]\n"
    "v_exp_f32 v198, v198\n"
    "v_mfma_f32_16x16x32_bf16 v[200:203], a[8:11], a[136:139], v[200:203]\n"
    "ds_read_b128 a[56:59], v234 offset:4608\n"
    "v_mfma_f32_16x16x32_bf16 v[200:203], a[12:15], a[140:143], v[200:203]\n"
    "v_exp_f32 v199, v199\n"
    "v_mfma_f32_16x16x32_bf16 v[204:207], a[28:31], a[140:143], v[216:219]\n"
    "ds_read_b128 a[52:55], v235 offset:4096\n"
    "v_cvt_pk_bf16_f32 v187, v198, v199\n"
    "v_mfma_f32_16x16x32_bf16 v[204:207], a[24:27], a[136:139], v[204:207]\n"
    "ds_read_b128 a[48:51], v234 offset:4096\n"
    "v_mfma_f32_16x16x32_bf16 v[204:207], a[20:23], a[132:135], v[204:207]\n"
    "ds_read_b64_tr_b16 a[216:217], v238 offset:0\n"
    "buffer_load_dwordx4 v240, s[64:67], s82 offen offset:2048 lds\n"
    "v_mfma_f32_16x16x32_bf16 v[204:207], a[16:19], a[128:131], v[204:207]\n"
    "v_exp_f32 v200, v200\n"
    "v_mfma_f32_16x16x32_bf16 v[80:83], a[232:235], v[208:211], v[80:83]\n"
    "ds_read_b64_tr_b16 a[218:219], v238 offset:4096\n"
    "v_mfma_f32_16x16x32_bf16 v[80:83], a[80:83], v[212:215], v[80:83]\n"
    "v_exp_f32 v201, v201\n"
    "v_mfma_f32_16x16x32_bf16 v[84:87], a[236:239], v[208:211], v[84:87]\n"
    "ds_read_b64_tr_b16 a[220:221], v239 offset:0\n"
    "v_cvt_pk_bf16_f32 v188, v200, v201\n"
    "v_mfma_f32_16x16x32_bf16 v[84:87], a[84:87], v[212:215], v[84:87]\n"
    "v_exp_f32 v202, v202\n"
    "v_mfma_f32_16x16x32_bf16 v[88:91], a[240:243], v[208:211], v[88:91]\n"
    "ds_read_b64_tr_b16 a[222:223], v239 offset:4096\n"
    "s_waitcnt lgkmcnt(5)\n"
    "v_mfma_f32_16x16x32_bf16 v[88:91], a[88:91], v[212:215], v[88:91]\n"
    "v_exp_f32 v203, v203\n"
    "v_mfma_f32_16x16x32_bf16 v[92:95], a[244:247], v[208:211], v[92:95]\n"
    "ds_read_b64_tr_b16 a[224:225], v238 offset:256\n"
    "v_cvt_pk_bf16_f32 v189, v202, v203\n"
    "v_mfma_f32_16x16x32_bf16 v[92:95], a[92:95], v[212:215], v[92:95]\n"
    "v_exp_f32 v204, v204\n"
    "v_mfma_f32_16x16x32_bf16 v[192:195], a[32:35], a[128:131], v[216:219]\n"
    "ds_read_b64_tr_b16 a[226:227], v238 offset:4352\n"
    "buffer_store_dwordx4 v[168:171], v243, s[60:63], s78 offen offset:0 sc1\n"
    "v_mfma_f32_16x16x32_bf16 v[192:195], a[36:39], a[132:135], v[192:195]\n"
    "v_exp_f32 v205, v205\n"
    "v_mfma_f32_16x16x32_bf16 v[192:195], a[40:43], a[136:139], v[192:195]\n"
    "ds_read_b64_tr_b16 a[228:229], v239 offset:256\n"
    "v_cvt_pk_bf16_f32 v190, v204, v205\n"
    "v_mfma_f32_16x16x32_bf16 v[192:195], a[44:47], a[140:143], v[192:195]\n"
    "v_exp_f32 v206, v206\n"
    "v_mfma_f32_16x16x32_bf16 v[196:199], a[60:63], a[140:143], v[216:219]\n"
    "ds_read_b64_tr_b16 a[230:231], v239 offset:4352\n"
    "s_waitcnt lgkmcnt(4)\n"
    "v_mfma_f32_16x16x32_bf16 v[196:199], a[56:59], a[136:139], v[196:199]\n"
    "v_exp_f32 v207, v207\n"
    "v_mfma_f32_16x16x32_bf16 v[196:199], a[52:55], a[132:135], v[196:199]\n"
    "ds_read_b64_tr_b16 a[232:233], v238 offset:512\n"
    "buffer_load_dwordx4 v241, s[64:67], s82 offen offset:3072 lds\n"
    "v_cvt_pk_bf16_f32 v191, v206, v207\n"
    "v_mfma_f32_16x16x32_bf16 v[196:199], a[48:51], a[128:131], v[196:199]\n"
    "v_exp_f32 v192, v192\n"
    "v_mfma_f32_16x16x32_bf16 v[96:99], a[96:99], v[184:187], v[96:99]\n"
    "ds_read_b64_tr_b16 a[234:235], v238 offset:4608\n"
    "v_mfma_f32_16x16x32_bf16 v[100:103], a[100:103], v[184:187], v[100:103]\n"
    "v_exp_f32 v193, v193\n"
    "v_mfma_f32_16x16x32_bf16 v[104:107], a[104:107], v[184:187], v[104:107]\n"
    "ds_read_b64_tr_b16 a[236:237], v239 offset:512\n"
    "v_cvt_pk_bf16_f32 v208, v192, v193\n"
    "v_mfma_f32_16x16x32_bf16 v[108:111], a[108:111], v[184:187], v[108:111]\n"
    "v_exp_f32 v194, v194\n"
    "v_mfma_f32_16x16x32_bf16 v[112:115], a[112:115], v[184:187], v[112:115]\n"
    "ds_read_b64_tr_b16 a[238:239], v239 offset:4608\n"
    "s_add_u32 m0, s83, 49152\n"
    "s_xor_b32 s81, s93, 131072\n"
    "v_mfma_f32_16x16x32_bf16 v[116:119], a[116:119], v[184:187], v[116:119]\n"
    "s_add_u32 s82, s81, 14336\n"
    "v_exp_f32 v195, v195\n"
    "v_mfma_f32_16x16x32_bf16 v[120:123], a[120:123], v[184:187], v[120:123]\n"
    "ds_read_b64_tr_b16 a[240:241], v238 offset:768\n"
    "buffer_load_dwordx4 v240, s[64:67], s81 offen offset:0 lds\n"
    "v_cvt_pk_bf16_f32 v209, v194, v195\n"
    "v_mfma_f32_16x16x32_bf16 v[124:127], a[124:127], v[184:187], v[124:127]\n"
    "v_exp_f32 v196, v196\n"
    "v_mfma_f32_16x16x32_bf16 a[204:207], a[208:211], v[184:187], a[204:207]\n"
    "ds_read_b64_tr_b16 a[242:243], v238 offset:4864\n"
    "v_mfma_f32_16x16x32_bf16 v[200:203], a[0:3], a[144:147], v[220:223]\n"
    "v_exp_f32 v197, v197\n"
    "v_mfma_f32_16x16x32_bf16 v[200:203], a[4:7], a[148:151], v[200:203]\n"
    "ds_read_b64_tr_b16 a[244:245], v239 offset:768\n"
    "v_cvt_pk_bf16_f32 v210, v196, v197\n"
    "v_mfma_f32_16x16x32_bf16 v[200:203], a[8:11], a[152:155], v[200:203]\n"
    "v_exp_f32 v198, v198\n"
    "v_mfma_f32_16x16x32_bf16 v[200:203], a[12:15], a[156:159], v[200:203]\n"
    "ds_read_b64_tr_b16 a[246:247], v239 offset:4864\n"
    "v_mfma_f32_16x16x32_bf16 v[204:207], a[28:31], a[156:159], v[220:223]\n"
    "v_exp_f32 v199, v199\n"
    "v_mfma_f32_16x16x32_bf16 v[204:207], a[24:27], a[152:155], v[204:207]\n"
    "buffer_load_dwordx4 v241, s[64:67], s81 offen offset:1024 lds\n"
    "v_cvt_pk_bf16_f32 v211, v198, v199\n"
    "v_mfma_f32_16x16x32_bf16 v[204:207], a[20:23], a[148:151], v[204:207]\n"
    "v_mfma_f32_16x16x32_bf16 v[204:207], a[16:19], a[144:147], v[204:207]\n"
    "v_exp_f32 v200, v200\n"
    "v_mfma_f32_16x16x32_bf16 v[0:3], a[96:99], v[188:191], v[0:3]\n"
    "v_mfma_f32_16x16x32_bf16 v[0:3], a[216:219], v[208:211], v[0:3]\n"
    "v_exp_f32 v201, v201\n"
    "v_mfma_f32_16x16x32_bf16 v[4:7], a[100:103], v[188:191], v[4:7]\n"
    "v_cvt_pk_bf16_f32 v212, v200, v201\n"
    "v_mfma_f32_16x16x32_bf16 v[4:7], a[220:223], v[208:211], v[4:7]\n"
    "v_exp_f32 v202, v202\n"
    "v_mfma_f32_16x16x32_bf16 v[8:11], a[104:107], v[188:191], v[8:11]\n"
    "s_waitcnt lgkmcnt(0)\n"
    "v_mfma_f32_16x16x32_bf16 v[8:11], a[224:227], v[208:211], v[8:11]\n"
    "v_exp_f32 v203, v203\n"
    "v_mfma_f32_16x16x32_bf16 v[12:15], a[108:111], v[188:191], v[12:15]\n"
    "buffer_load_dwordx4 v240, s[64:67], s82 offen offset:2048 lds\n"
    "v_cvt_pk_bf16_f32 v213, v202, v203\n"
    "v_mfma_f32_16x16x32_bf16 v[12:15], a[228:231], v[208:211], v[12:15]\n"
    "v_exp_f32 v204, v204\n"
    "v_mfma_f32_16x16x32_bf16 a[192:195], a[208:211], v[188:191], a[192:195]\n"
    "v_mfma_f32_16x16x32_bf16 a[192:195], a[208:211], v[208:211], a[192:195]\n"
    "v_exp_f32 v205, v205\n"
    "v_mfma_f32_16x16x32_bf16 v[192:195], a[32:35], a[144:147], v[220:223]\n"
    "v_cvt_pk_bf16_f32 v214, v204, v205\n"
    "v_mfma_f32_16x16x32_bf16 v[192:195], a[36:39], a[148:151], v[192:195]\n"
    "v_exp_f32 v206, v206\n"
    "v_mfma_f32_16x16x32_bf16 v[192:195], a[40:43], a[152:155], v[192:195]\n"
    "v_mfma_f32_16x16x32_bf16 v[192:195], a[44:47], a[156:159], v[192:195]\n"
    "v_exp_f32 v207, v207\n"
    "v_mfma_f32_16x16x32_bf16 v[196:199], a[60:63], a[156:159], v[220:223]\n"
    "buffer_load_dwordx4 v241, s[64:67], s82 offen offset:3072 lds\n"
    "v_cvt_pk_bf16_f32 v215, v206, v207\n"
    "v_mfma_f32_16x16x32_bf16 v[196:199], a[56:59], a[152:155], v[196:199]\n"
    "v_mfma_f32_16x16x32_bf16 v[196:199], a[52:55], a[148:151], v[196:199]\n"
    "v_mfma_f32_16x16x32_bf16 v[196:199], a[48:51], a[144:147], v[196:199]\n"
    "s_nop 0\n"
    "v_exp_f32 v192, v192\n"
    "v_mfma_f32_16x16x32_bf16 v[16:19], a[112:115], v[188:191], v[16:19]\n"
    "v_mfma_f32_16x16x32_bf16 v[16:19], a[232:235], v[208:211], v[16:19]\n"
    "v_exp_f32 v193, v193\n"
    "v_mfma_f32_16x16x32_bf16 v[20:23], a[116:119], v[188:191], v[20:23]\n"
    "v_cvt_pk_bf16_f32 v184, v192, v193\n"
    "v_mfma_f32_16x16x32_bf16 v[20:23], a[236:239], v[208:211], v[20:23]\n"
    "v_exp_f32 v194, v194\n"
    "v_mfma_f32_16x16x32_bf16 v[24:27], a[120:123], v[188:191], v[24:27]\n"
    "s_waitcnt vmcnt(14)\n"
    "s_barrier\n"
    "v_mfma_f32_16x16x32_bf16 v[24:27], a[240:243], v[208:211], v[24:27]\n"
    "v_exp_f32 v195, v195\n"
    "v_mfma_f32_16x16x32_bf16 v[28:31], a[124:127], v[188:191], v[28:31]\n"
    "ds_read_b64_tr_b16 a[64:65], v236 offset:16384\n"
    "buffer_store_dwordx4 v[172:175], v243, s[60:63], s78 offen offset:1024 sc1\n"
    "v_cvt_pk_bf16_f32 v185, v194, v195\n"
    "v_mfma_f32_16x16x32_bf16 v[28:31], a[244:247], v[208:211], v[28:31]\n"
    "v_exp_f32 v196, v196\n"
    "v_mfma_f32_16x16x32_bf16 v[200:203], a[0:3], a[160:163], v[224:227]\n"
    "ds_read_b64_tr_b16 a[66:67], v236 offset:20480\n"
    "v_mfma_f32_16x16x32_bf16 v[200:203], a[4:7], a[164:167], v[200:203]\n"
    "s_add_u32 m0, s83, 114688\n"
    "v_exp_f32 v197, v197\n"
    "v_mfma_f32_16x16x32_bf16 v[200:203], a[8:11], a[168:171], v[200:203]\n"
    "ds_read_b64_tr_b16 a[68:69], v237 offset:16384\n"
    "s_xor_b32 s81, s93, 131200\n"
    "s_add_u32 s82, s81, 14336\n"
    "v_cvt_pk_bf16_f32 v186, v196, v197\n"
    "v_mfma_f32_16x16x32_bf16 v[200:203], a[12:15], a[172:175], v[200:203]\n"
    "v_exp_f32 v198, v198\n"
    "v_mfma_f32_16x16x32_bf16 v[204:207], a[28:31], a[172:175], v[224:227]\n"
    "ds_read_b64_tr_b16 a[70:71], v237 offset:20480\n"
    "buffer_load_dwordx4 v240, s[64:67], s81 offen offset:0 lds\n"
    "v_mfma_f32_16x16x32_bf16 v[204:207], a[24:27], a[168:171], v[204:207]\n"
    "v_exp_f32 v199, v199\n"
    "v_mfma_f32_16x16x32_bf16 v[204:207], a[20:23], a[164:167], v[204:207]\n"
    "ds_read_b64_tr_b16 a[72:73], v236 offset:16640\n"
    "v_cvt_pk_bf16_f32 v187, v198, v199\n"
    "v_mfma_f32_16x16x32_bf16 v[204:207], a[16:19], a[160:163], v[204:207]\n"
    "v_exp_f32 v200, v200\n"
    "v_mfma_f32_16x16x32_bf16 v[32:35], a[96:99], v[212:215], v[32:35]\n"
    "ds_read_b64_tr_b16 a[74:75], v236 offset:20736\n"
    "v_mfma_f32_16x16x32_bf16 v[32:35], a[216:219], v[184:187], v[32:35]\n"
    "v_exp_f32 v201, v201\n"
    "v_mfma_f32_16x16x32_bf16 v[36:39], a[100:103], v[212:215], v[36:39]\n"
    "ds_read_b128 a[0:3], v232 offset:16384\n"
    "v_cvt_pk_bf16_f32 v188, v200, v201\n"
    "v_mfma_f32_16x16x32_bf16 v[36:39], a[220:223], v[184:187], v[36:39]\n"
    "v_exp_f32 v202, v202\n"
    "v_mfma_f32_16x16x32_bf16 v[40:43], a[104:107], v[212:215], v[40:43]\n"
    "ds_read_b128 a[4:7], v233 offset:16384\n"
    "buffer_load_dwordx4 v241, s[64:67], s81 offen offset:1024 lds\n"
    "v_mfma_f32_16x16x32_bf16 v[40:43], a[224:227], v[184:187], v[40:43]\n"
    "v_exp_f32 v203, v203\n"
    "v_mfma_f32_16x16x32_bf16 v[44:47], a[108:111], v[212:215], v[44:47]\n"
    "ds_read_b128 a[8:11], v232 offset:16896\n"
    "v_cvt_pk_bf16_f32 v189, v202, v203\n"
    "v_mfma_f32_16x16x32_bf16 v[44:47], a[228:231], v[184:187], v[44:47]\n"
    "v_exp_f32 v204, v204\n"
    "v_mfma_f32_16x16x32_bf16 a[196:199], a[208:211], v[212:215], a[196:199]\n"
    "ds_read_b128 a[12:15], v233 offset:16896\n"
    "v_mfma_f32_16x16x32_bf16 a[196:199], a[208:211], v[184:187], a[196:199]\n"
    "v_exp_f32 v205, v205\n"
    "v_mfma_f32_16x16x32_bf16 v[192:195], a[32:35], a[160:163], v[224:227]\n"
    "ds_read_b128 a[28:31], v233 offset:20992\n"
    "v_cvt_pk_bf16_f32 v190, v204, v205\n"
    "v_mfma_f32_16x16x32_bf16 v[192:195], a[36:39], a[164:167], v[192:195]\n"
    "v_exp_f32 v206, v206\n"
    "v_mfma_f32_16x16x32_bf16 v[192:195], a[40:43], a[168:171], v[192:195]\n"
    "ds_read_b128 a[24:27], v232 offset:20992\n"
    "buffer_load_dwordx4 v240, s[64:67], s82 offen offset:2048 lds\n"
    "v_mfma_f32_16x16x32_bf16 v[192:195], a[44:47], a[172:175], v[192:195]\n"
    "L_h13:\n"
    "s_cbranch_execz L_h17\n"
    "v_exp_f32 v207, v207\n"
    "v_mfma_f32_16x16x32_bf16 v[196:199], a[60:63], a[172:175], v[224:227]\n"
    "ds_read_b128 a[20:23], v233 offset:20480\n"
    "v_cvt_pk_bf16_f32 v191, v206, v207\n"
    "v_mfma_f32_16x16x32_bf16 v[196:199], a[56:59], a[168:171], v[196:199]\n"
    "ds_read_b128 a[16:19], v232 offset:20480\n"
    "v_mfma_f32_16x16x32_bf16 v[196:199], a[52:55], a[164:167], v[196:199]\n"
    "ds_read_b64_tr_b16 a[76:77], v237 offset:16640\n"
    "v_mfma_f32_16x16x32_bf16 v[196:199], a[48:51], a[160:163], v[196:199]\n"
    "v_exp_f32 v192, v192\n"
    "v_mfma_f32_16x16x32_bf16 v[48:51], a[112:115], v[212:215], v[48:51]\n"
    "ds_read_b64_tr_b16 a[78:79], v237 offset:20736\n"
    "v_mfma_f32_16x16x32_bf16 v[48:51], a[232:235], v[184:187], v[48:51]\n"
    "buffer_load_dwordx4 v241, s[64:67], s82 offen offset:3072 lds\n"
    "v_exp_f32 v193, v193\n"
    "v_mfma_f32_16x16x32_bf16 v[52:55], a[116:119], v[212:215], v[52:55]\n"
    "ds_read_b64_tr_b16 a[80:81], v236 offset:16896\n"
    "v_cvt_pk_bf16_f32 v208, v192, v193\n"
    "v_mfma_f32_16x16x32_bf16 v[52:55], a[236:239], v[184:187], v[52:55]\n"
    "v_exp_f32 v194, v194\n"
    "v_mfma_f32_16x16x32_bf16 v[56:59], a[120:123], v[212:215], v[56:59]\n"
    "ds_read_b64_tr_b16 a[82:83], v236 offset:20992\n"
    "s_waitcnt lgkmcnt(5)\n"
    "v_mfma_f32_16x16x32_bf16 v[56:59], a[240:243], v[184:187], v[56:59]\n"
    "v_exp_f32 v195, v195\n"
    "v_mfma_f32_16x16x32_bf16 v[60:63], a[124:127], v[212:215], v[60:63]\n"
    "ds_read_b64_tr_b16 a[84:85], v237 offset:16896\n"
    "v_cvt_pk_bf16_f32 v209, v194, v195\n"
    "v_mfma_f32_16x16x32_bf16 v[60:63], a[244:247], v[184:187], v[60:63]\n"
    "v_exp_f32 v196, v196\n"
    "v_mfma_f32_16x16x32_bf16 v[200:203], a[0:3], a[176:179], v[228:231]\n"
    "ds_read_b64_tr_b16 a[86:87], v237 offset:20992\n"
    "s_add_u32 m0, s83, 0\n"
    "s_xor_b32 s81, s93, 262144\n"
    "s_add_u32 s82, s81, 14336\n"
    "v_mfma_f32_16x16x32_bf16 v[200:203], a[4:7], a[180:183], v[200:203]\n"
    "buffer_load_dwordx4 v240, s[64:67], s81 offen offset:0 lds\n"
    "v_exp_f32 v197, v197\n"
    "v_mfma_f32_16x16x32_bf16 v[200:203], a[8:11], a[184:187], v[200:203]\n"
    "ds_read_b64_tr_b16 a[88:89], v236 offset:17152\n"
    "v_cvt_pk_bf16_f32 v210, v196, v197\n"
    "v_mfma_f32_16x16x32_bf16 v[200:203], a[12:15], a[188:191], v[200:203]\n"
    "v_exp_f32 v198, v198\n"
    "v_mfma_f32_16x16x32_bf16 v[204:207], a[28:31], a[188:191], v[228:231]\n"
    "ds_read_b64_tr_b16 a[90:91], v236 offset:21248\n"
    "s_waitcnt lgkmcnt(4)\n"
    "v_mfma_f32_16x16x32_bf16 v[204:207], a[24:27], a[184:187], v[204:207]\n"
    "v_exp_f32 v199, v199\n"
    "v_mfma_f32_16x16x32_bf16 v[204:207], a[20:23], a[180:183], v[204:207]\n"
    "ds_read_b64_tr_b16 a[92:93], v237 offset:17152\n"
    "v_cvt_pk_bf16_f32 v211, v198, v199\n"
    "v_mfma_f32_16x16x32_bf16 v[204:207], a[16:19], a[176:179], v[204:207]\n"
    "v_exp_f32 v200, v200\n"
    "v_mfma_f32_16x16x32_bf16 v[64:67], a[96:99], v[188:191], v[64:67]\n"
    "ds_read_b64_tr_b16 a[94:95], v237 offset:21248\n"
    "v_mfma_f32_16x16x32_bf16 v[64:67], a[216:219], v[208:211], v[64:67]\n"
    "buffer_load_dwordx4 v241, s[64:67], s81 offen offset:1024 lds\n"
    "v_exp_f32 v201, v201\n"
    "v_mfma_f32_16x16x32_bf16 v[68:71], a[100:103], v[188:191], v[68:71]\n"
    "ds_read_b128 a[32:35], v234 offset:16384\n"
    "v_cvt_pk_bf16_f32 v212, v200, v201\n"
    "v_mfma_f32_16x16x32_bf16 v[68:71], a[220:223], v[208:211], v[68:71]\n"
    "v_exp_f32 v202, v202\n"
    "v_mfma_f32_16x16x32_bf16 v[72:75], a[104:107], v[188:191], v[72:75]\n"
    "ds_read_b128 a[36:39], v235 offset:16384\n"
    "v_mfma_f32_16x16x32_bf16 v[72:75], a[224:227], v[208:211], v[72:75]\n"
    "v_exp_f32 v203, v203\n"
    "v_mfma_f32_16x16x32_bf16 v[76:79], a[108:111], v[188:191], v[76:79]\n"
    "ds_read_b128 a[40:43], v234 offset:16896\n"
    "v_cvt_pk_bf16_f32 v213, v202, v203\n"
    "v_mfma_f32_16x16x32_bf16 v[76:79], a[228:231], v[208:211], v[76:79]\n"
    "v_exp_f32 v204, v204\n"
    "v_mfma_f32_16x16x32_bf16 a[200:203], a[208:211], v[188:191], a[200:203]\n"
    "ds_read_b128 a[44:47], v235 offset:16896\n"
    "v_mfma_f32_16x16x32_bf16 a[200:203], a[208:211], v[208:211], a[200:203]\n"
    "buffer_load_dwordx4 v240, s[64:67], s82 offen offset:2048 lds\n"
    "v_exp_f32 v205, v205\n"
    "v_mfma_f32_16x16x32_bf16 v[192:195], a[0:3], a[128:131], v[216:219]\n"
    "ds_read_b128 a[60:63], v235 offset:20992\n"
    "v_cvt_pk_bf16_f32 v214, v204, v205\n"
    "v_mfma_f32_16x16x32_bf16 v[192:195], a[4:7], a[132:135], v[192:195]\n"
    "v_exp_f32 v206, v206\n"
    "v_mfma_f32_16x16x32_bf16 v[192:195], a[8:11], a[136:139], v[192:195]\n"
    "ds_read_b128 a[56:59], v234 offset:20992\n"
    "v_mfma_f32_16x16x32_bf16 v[192:195], a[12:15], a[140:143], v[192:195]\n"
    "v_exp_f32 v207, v207\n"
    "s_waitcnt vmcnt(7)\n"
    "v_accvgpr_read_b32 v249, a212\n"
    "s_mov_b32 s96, 0xfc0000\n"
    "s_mov_b32 s97, 0x0\n"
    "s_mov_b64 exec, s[96:97]\n"
    "buffer_store_dword a214, v249, s[68:71], 0 offen offset:0 sc1\n"
    "s_mov_b64 exec, -1\n"
    "v_mfma_f32_16x16x32_bf16 v[196:199], a[28:31], a[140:143], v[216:219]\n"
    "ds_read_b128 a[52:55], v235 offset:20480\n"
    "v_cvt_pk_bf16_f32 v215, v206, v207\n"
    "v_mfma_f32_16x16x32_bf16 v[196:199], a[24:27], a[136:139], v[196:199]\n"
    "ds_read_b128 a[48:51], v234 offset:20480\n"
    "v_mfma_f32_16x16x32_bf16 v[196:199], a[20:23], a[132:135], v[196:199]\n"
    "ds_read_b64_tr_b16 a[96:97], v238 offset:16384\n"
    "s_waitcnt lgkmcnt(4)\n"
    "v_mfma_f32_16x16x32_bf16 v[196:199], a[16:19], a[128:131], v[196:199]\n"
    "buffer_load_dwordx4 v241, s[64:67], s82 offen offset:3072 lds\n"
    "v_exp_f32 v192, v192\n"
    "v_mfma_f32_16x16x32_bf16 v[80:83], a[112:115], v[188:191], v[80:83]\n"
    "ds_read_b64_tr_b16 a[98:99], v238 offset:20480\n"
    "v_and_b32 v248, 0x800, v238\n"
    "v_mfma_f32_16x16x32_bf16 v[80:83], a[232:235], v[208:211], v[80:83]\n"
    "v_exp_f32 v193, v193\n"
    "v_mfma_f32_16x16x32_bf16 v[84:87], a[116:119], v[188:191], v[84:87]\n"
    "ds_read_b64_tr_b16 a[100:101], v239 offset:16384\n"
    "v_cvt_pk_bf16_f32 v184, v192, v193\n"
    "v_mfma_f32_16x16x32_bf16 v[84:87], a[236:239], v[208:211], v[84:87]\n"
    "v_exp_f32 v194, v194\n"
    "v_mfma_f32_16x16x32_bf16 v[88:91], a[120:123], v[188:191], v[88:91]\n"
    "ds_read_b64_tr_b16 a[102:103], v239 offset:20480\n"
    "v_lshrrev_b32 v248, 1, v248\n"
    "v_mfma_f32_16x16x32_bf16 v[88:91], a[240:243], v[208:211], v[88:91]\n"
    "s_add_u32 m0, s83, 65536\n"
    "v_exp_f32 v195, v195\n"
    "v_mfma_f32_16x16x32_bf16 v[92:95], a[124:127], v[188:191], v[92:95]\n"
    "ds_read_b64_tr_b16 a[104:105], v238 offset:16640\n"
    "s_xor_b32 s81, s93, 262272\n"
    "s_add_u32 s82, s81, 14336\n"
    "v_cvt_pk_bf16_f32 v185, v194, v195\n"
    "v_mfma_f32_16x16x32_bf16 v[92:95], a[244:247], v[208:211], v[92:95]\n"
    "buffer_load_dwordx4 v240, s[64:67], s81 offen offset:0 lds\n"
    "v_exp_f32 v196, v196\n"
    "v_mfma_f32_16x16x32_bf16 v[200:203], a[32:35], a[128:131], v[216:219]\n"
    "ds_read_b64_tr_b16 a[106:107], v238 offset:20736\n"
    "v_and_b32 v249, 0xff, v238\n"
    "v_mfma_f32_16x16x32_bf16 v[200:203], a[36:39], a[132:135], v[200:203]\n"
    "v_exp_f32 v197, v197\n"
    "v_mfma_f32_16x16x32_bf16 v[200:203], a[40:43], a[136:139], v[200:203]\n"
    "ds_read_b64_tr_b16 a[108:109], v239 offset:16640\n"
    "v_cvt_pk_bf16_f32 v186, v196, v197\n"
    "v_mfma_f32_16x16x32_bf16 v[200:203], a[44:47], a[140:143], v[200:203]\n"
    "v_exp_f32 v198, v198\n"
    "v_mfma_f32_16x16x32_bf16 v[204:207], a[60:63], a[140:143], v[216:219]\n"
    "ds_read_b64_tr_b16 a[110:111], v239 offset:20736\n"
    "v_or_b32 v248, v248, v249\n"
    "s_waitcnt lgkmcnt(4)\n"
    "v_mfma_f32_16x16x32_bf16 v[204:207], a[56:59], a[136:139], v[204:207]\n"
    "v_exp_f32 v199, v199\n"
    "v_mfma_f32_16x16x32_bf16 v[204:207], a[52:55], a[132:135], v[204:207]\n"
    "ds_read_b64_tr_b16 a[112:113], v238 offset:16896\n"
    "v_cvt_pk_bf16_f32 v187, v198, v199\n"
    "v_mfma_f32_16x16x32_bf16 v[204:207], a[48:51], a[128:131], v[204:207]\n"
    "buffer_load_dwordx4 v241, s[64:67], s81 offen offset:1024 lds\n"
    "v_exp_f32 v200, v200\n"
    "v_mfma_f32_16x16x32_bf16 v[96:99], a[64:67], v[212:215], v[96:99]\n"
    "ds_read_b64_tr_b16 a[114:115], v238 offset:20992\n"
    "v_bfe_u32 v249, v238, 10, 1\n"
    "v_mfma_f32_16x16x32_bf16 v[100:103], a[68:71], v[212:215], v[100:103]\n"
    "v_exp_f32 v201, v201\n"
    "buffer_load_dword v247, v246, s[68:71], s92 offen offset:0 sc1\n"
    "v_mfma_f32_16x16x32_bf16 v[104:107], a[72:75], v[212:215], v[104:107]\n"
    "ds_read_b64_tr_b16 a[116:117], v239 offset:16896\n"
    "v_cvt_pk_bf16_f32 v188, v200, v201\n"
    "v_mfma_f32_16x16x32_bf16 v[108:111], a[76:79], v[212:215], v[108:111]\n"
    "v_exp_f32 v202, v202\n"
    "v_mfma_f32_16x16x32_bf16 v[112:115], a[80:83], v[212:215], v[112:115]\n"
    "ds_read_b64_tr_b16 a[118:119], v239 offset:20992\n"
    "v_lshl_add_u32 v248, v249, 8, v248\n"
    "v_mfma_f32_16x16x32_bf16 v[116:119], a[84:87], v[212:215], v[116:119]\n"
    "v_exp_f32 v203, v203\n"
    "v_mfma_f32_16x16x32_bf16 v[120:123], a[88:91], v[212:215], v[120:123]\n"
    "ds_read_b64_tr_b16 a[120:121], v238 offset:17152\n"
    "v_cvt_pk_bf16_f32 v189, v202, v203\n"
    "v_mfma_f32_16x16x32_bf16 v[124:127], a[92:95], v[212:215], v[124:127]\n"
    "buffer_load_dwordx4 v240, s[64:67], s82 offen offset:2048 lds\n"
    "v_exp_f32 v204, v204\n"
    "v_mfma_f32_16x16x32_bf16 a[204:207], a[208:211], v[212:215], a[204:207]\n"
    "ds_read_b64_tr_b16 a[122:123], v238 offset:21248\n"
    "v_xor_b32 v249, 32, v248\n"
    "v_mfma_f32_16x16x32_bf16 v[192:195], a[0:3], a[144:147], v[220:223]\n"
    "v_exp_f32 v205, v205\n"
    "v_mfma_f32_16x16x32_bf16 v[192:195], a[4:7], a[148:151], v[192:195]\n"
    "ds_read_b64_tr_b16 a[124:125], v239 offset:17152\n"
    "v_cvt_pk_bf16_f32 v190, v204, v205\n"
    "v_mfma_f32_16x16x32_bf16 v[192:195], a[8:11], a[152:155], v[192:195]\n"
    "v_exp_f32 v206, v206\n"
    "v_mfma_f32_16x16x32_bf16 v[192:195], a[12:15], a[156:159], v[192:195]\n"
    "ds_read_b64_tr_b16 a[126:127], v239 offset:21248\n"
    "s_lshl_b32 s90, s41, 11\n"
    "v_mfma_f32_16x16x32_bf16 v[196:199], a[28:31], a[156:159], v[220:223]\n"
    "v_exp_f32 v207, v207\n"
    "v_accvgpr_read_b32 v251, a204\n"
    "v_mfma_f32_16x16x32_bf16 v[196:199], a[24:27], a[152:155], v[196:199]\n"
    "s_xor_b32 s91, s90, 2048\n"
    "v_cvt_pk_bf16_f32 v191, v206, v207\n"
    "v_mov_b32 v250, v228\n"
    "v_mfma_f32_16x16x32_bf16 v[196:199], a[20:23], a[148:151], v[196:199]\n"
    "buffer_load_dwordx4 v241, s[64:67], s82 offen offset:3072 lds\n"
    "v_add_u32 v232, s90, v248\n"
    "v_add_u32 v233, s90, v249\n"
    "v_mfma_f32_16x16x32_bf16 v[196:199], a[16:19], a[144:147], v[196:199]\n"
    "ds_write_b128 v244, v[100:103] offset:0\n"
    "v_exp_f32 v192, v192\n"
    "v_mfma_f32_16x16x32_bf16 v[0:3], a[64:67], v[184:187], v[0:3]\n"
    "v_add_u32 v236, s91, v248\n"
    "v_add_u32 v237, s91, v249\n"
    "v_mfma_f32_16x16x32_bf16 v[0:3], a[96:99], v[188:191], v[0:3]\n"
    "ds_write_b128 v244, v[108:111] offset:1024\n"
    "v_exp_f32 v193, v193\n"
    "v_mfma_f32_16x16x32_bf16 v[4:7], a[68:71], v[184:187], v[4:7]\n"
    "ds_write_b128 v244, v[116:119] offset:2048\n"
    "v_cvt_pk_bf16_f32 v208, v192, v193\n"
    "v_mfma_f32_16x16x32_bf16 v[4:7], a[100:103], v[188:191], v[4:7]\n"
    "v_exp_f32 v194, v194\n"
    "v_mfma_f32_16x16x32_bf16 v[8:11], a[72:75], v[184:187], v[8:11]\n"
    "ds_write_b128 v244, v[124:127] offset:3072\n"
    "ds_write_b64 v244, v[250:251] offset:4096\n"
    "s_waitcnt lgkmcnt(5)\n"
    "v_mfma_f32_16x16x32_bf16 v[8:11], a[104:107], v[188:191], v[8:11]\n"
    "v_exp_f32 v195, v195\n"
    "v_mfma_f32_16x16x32_bf16 v[12:15], a[76:79], v[184:187], v[12:15]\n"
    "v_cvt_pk_bf16_f32 v209, v194, v195\n"
    "v_accvgpr_read_b32 v228, a212\n"
    "v_accvgpr_read_b32 v229, a213\n"
    "v_mfma_f32_16x16x32_bf16 v[12:15], a[108:111], v[188:191], v[12:15]\n"
    "v_exp_f32 v196, v196\n"
    "v_accvgpr_read_b32 v230, a214\n"
    "v_mfma_f32_16x16x32_bf16 a[192:195], a[208:211], v[184:187], a[192:195]\n"
    "s_xor_b32 s72, s76, 131072\n"
    "s_xor_b32 s73, s77, 131072\n"
    "s_xor_b32 s74, s78, 131072\n"
    "v_mfma_f32_16x16x32_bf16 a[192:195], a[208:211], v[188:191], a[192:195]\n"
    "s_xor_b32 s75, s79, 131072\n"
    "v_exp_f32 v197, v197\n"
    "v_mfma_f32_16x16x32_bf16 v[200:203], a[32:35], a[144:147], v[220:223]\n"
    "v_cvt_pk_bf16_f32 v210, v196, v197\n"
    "v_add_u32 v234, 65536, v232\n"
    "v_mfma_f32_16x16x32_bf16 v[200:203], a[36:39], a[148:151], v[200:203]\n"
    "v_exp_f32 v198, v198\n"
    "v_mfma_f32_16x16x32_bf16 v[200:203], a[40:43], a[152:155], v[200:203]\n"
    "v_add_u32 v235, 65536, v233\n"
    "v_add_u32 v238, 65536, v236\n"
    "v_mfma_f32_16x16x32_bf16 v[200:203], a[44:47], a[156:159], v[200:203]\n"
    "v_exp_f32 v199, v199\n"
    "v_mfma_f32_16x16x32_bf16 v[204:207], a[60:63], a[156:159], v[220:223]\n"
    "v_cvt_pk_bf16_f32 v211, v198, v199\n"
    "v_add_u32 v239, 65536, v237\n"
    "v_mfma_f32_16x16x32_bf16 v[204:207], a[56:59], a[152:155], v[204:207]\n"
    "v_mfma_f32_16x16x32_bf16 v[204:207], a[52:55], a[148:151], v[204:207]\n"
    "v_mfma_f32_16x16x32_bf16 v[204:207], a[48:51], a[144:147], v[204:207]\n"
    "s_nop 0\n"
    "v_exp_f32 v200, v200\n"
    "v_mfma_f32_16x16x32_bf16 v[16:19], a[80:83], v[184:187], v[16:19]\n"
    "v_mfma_f32_16x16x32_bf16 v[16:19], a[112:115], v[188:191], v[16:19]\n"
    "v_exp_f32 v201, v201\n"
    "v_mfma_f32_16x16x32_bf16 v[20:23], a[84:87], v[184:187], v[20:23]\n"
    "v_cvt_pk_bf16_f32 v212, v200, v201\n"
    "v_mfma_f32_16x16x32_bf16 v[20:23], a[116:119], v[188:191], v[20:23]\n"
    "v_exp_f32 v202, v202\n"
    "v_mfma_f32_16x16x32_bf16 v[24:27], a[88:91], v[184:187], v[24:27]\n"
    "s_waitcnt lgkmcnt(0)\n"
    "s_barrier\n"
    "v_mfma_f32_16x16x32_bf16 v[24:27], a[120:123], v[188:191], v[24:27]\n"
    "v_exp_f32 v203, v203\n"
    "v_mfma_f32_16x16x32_bf16 v[28:31], a[92:95], v[184:187], v[28:31]\n"
    "ds_read_b64 v[252:253], v245 offset:4096\n"
    "s_add_u32 m0, s83, 16384\n"
    "v_cvt_pk_bf16_f32 v213, v202, v203\n"
    "v_mfma_f32_16x16x32_bf16 v[28:31], a[124:127], v[188:191], v[28:31]\n"
    "s_xor_b32 s81, s93, 524288\n"
    "s_add_u32 s82, s81, 14336\n"
    "v_exp_f32 v204, v204\n"
    "v_mfma_f32_16x16x32_bf16 v[192:195], a[0:3], a[160:163], v[224:227]\n"
    "ds_read_b128 v[100:103], v245 offset:0\n"
    "ds_read_b128 v[108:111], v245 offset:1024\n"
    "buffer_load_dwordx4 v240, s[64:67], s81 offen offset:0 lds\n"
    "v_mfma_f32_16x16x32_bf16 v[192:195], a[4:7], a[164:167], v[192:195]\n"
    "v_exp_f32 v205, v205\n"
    "v_mfma_f32_16x16x32_bf16 v[192:195], a[8:11], a[168:171], v[192:195]\n"
    "ds_read_b128 v[116:119], v245 offset:2048\n"
    "v_cvt_pk_bf16_f32 v214, v204, v205\n"
    "v_mfma_f32_16x16x32_bf16 v[192:195], a[12:15], a[172:175], v[192:195]\n"
    "v_exp_f32 v206, v206\n"
    "v_mfma_f32_16x16x32_bf16 v[196:199], a[28:31], a[172:175], v[224:227]\n"
    "ds_read_b64_tr_b16 a[0:1], v232 offset:32768\n"
    "ds_read_b128 v[124:127], v245 offset:3072\n"
    "buffer_load_dwordx4 v241, s[64:67], s81 offen offset:1024 lds\n"
    "v_mfma_f32_16x16x32_bf16 v[196:199], a[24:27], a[168:171], v[196:199]\n"
    "v_exp_f32 v207, v207\n"
    "v_mfma_f32_16x16x32_bf16 v[196:199], a[20:23], a[164:167], v[196:199]\n"
    "ds_read_b64_tr_b16 a[2:3], v232 offset:36864\n"
    "v_cvt_pk_bf16_f32 v215, v206, v207\n"
    "v_mfma_f32_16x16x32_bf16 v[196:199], a[16:19], a[160:163], v[196:199]\n"
    "v_exp_f32 v192, v192\n"
    "v_mfma_f32_16x16x32_bf16 v[32:35], a[64:67], v[208:211], v[32:35]\n"
    "ds_read_b64_tr_b16 a[4:5], v232 offset:40960\n"
    "buffer_load_dwordx4 v240, s[64:67], s82 offen offset:2048 lds\n"
    "v_mfma_f32_16x16x32_bf16 v[32:35], a[96:99], v[212:215], v[32:35]\n"
    "v_exp_f32 v193, v193\n"
    "v_mfma_f32_16x16x32_bf16 v[36:39], a[68:71], v[208:211], v[36:39]\n"
    "ds_read_b64_tr_b16 a[6:7], v232 offset:45056\n"
    "v_cvt_pk_bf16_f32 v184, v192, v193\n"
    "s_waitcnt lgkmcnt(5)\n"
    "v_min_f32 v254, v250, v252\n"
    "v_mfma_f32_16x16x32_bf16 v[36:39], a[100:103], v[212:215], v[36:39]\n"
    "v_exp_f32 v194, v194\n"
    "v_sub_f32 v250, v254, v250\n"
    "v_sub_f32 v252, v254, v252\n"
    "v_mfma_f32_16x16x32_bf16 v[40:43], a[72:75], v[208:211], v[40:43]\n"
    "ds_read_b64_tr_b16 a[8:9], v236 offset:32768\n"
    "buffer_load_dwordx4 v241, s[64:67], s82 offen offset:3072 lds\n"
    "v_exp_f32 v250, v250\n"
    "v_mfma_f32_16x16x32_bf16 v[40:43], a[104:107], v[212:215], v[40:43]\n"
    "v_exp_f32 v195, v195\n"
    "v_exp_f32 v252, v252\n"
    "v_mfma_f32_16x16x32_bf16 v[44:47], a[76:79], v[208:211], v[44:47]\n"
    "ds_read_b64_tr_b16 a[10:11], v236 offset:36864\n"
    "s_add_u32 m0, s83, 81920\n"
    "s_xor_b32 s81, s93, 524416\n"
    "v_cvt_pk_bf16_f32 v185, v194, v195\n"
    "v_mul_f32 v254, v250, v251\n"
    "v_mfma_f32_16x16x32_bf16 v[44:47], a[108:111], v[212:215], v[44:47]\n"
    "s_add_u32 s82, s81, 14336\n"
    "v_exp_f32 v196, v196\n"
    "v_fma_f32 v254, v252, v253, v254\n"
    "v_max_f32 v251, v251, v253\n"
    "v_mfma_f32_16x16x32_bf16 a[196:199], a[208:211], v[208:211], a[196:199]\n"
    "ds_read_b64_tr_b16 a[12:13], v236 offset:40960\n"
    "buffer_load_dwordx4 v240, s[64:67], s81 offen offset:0 lds\n"
    "v_mul_f32 v251, s43, v251\n"
    "v_mfma_f32_16x16x32_bf16 a[196:199], a[208:211], v[212:215], a[196:199]\n"
    "v_exp_f32 v197, v197\n"
    "v_rcp_f32 v254, v254\n"
    "v_sub_f32 v251, v251, v251\n"
    "v_mfma_f32_16x16x32_bf16 v[200:203], a[32:35], a[160:163], v[224:227]\n"
    "ds_read_b64_tr_b16 a[14:15], v236 offset:45056\n"
    "v_cvt_pk_bf16_f32 v186, v196, v197\n"
    "v_add_f32 v254, v254, v251\n"
    "v_mfma_f32_16x16x32_bf16 v[200:203], a[36:39], a[164:167], v[200:203]\n"
    "v_exp_f32 v198, v198\n"
    "v_mul_f32 v250, v250, v254\n"
    "v_mul_f32 v252, v252, v254\n"
    "v_mfma_f32_16x16x32_bf16 v[200:203], a[40:43], a[168:171], v[200:203]\n"
    "buffer_load_dwordx4 v241, s[64:67], s81 offen offset:1024 lds\n"
    "v_mul_f32 v96, v96, v250\n"
    "v_mul_f32 v97, v97, v250\n"
    "v_mul_f32 v98, v98, v250\n"
    "v_mfma_f32_16x16x32_bf16 v[200:203], a[44:47], a[172:175], v[200:203]\n"
    "v_exp_f32 v199, v199\n"
    "v_mul_f32 v99, v99, v250\n"
    "v_fma_f32 v96, v100, v252, v96\n"
    "v_mfma_f32_16x16x32_bf16 v[204:207], a[60:63], a[172:175], v[224:227]\n"
    "v_cvt_pk_bf16_f32 v187, v198, v199\n"
    "v_fma_f32 v97, v101, v252, v97\n"
    "v_fma_f32 v98, v102, v252, v98\n"
    "v_mfma_f32_16x16x32_bf16 v[204:207], a[56:59], a[168:171], v[204:207]\n"
    "v_fma_f32 v99, v103, v252, v99\n"
    "v_mul_f32 v104, v104, v250\n"
    "v_mul_f32 v105, v105, v250\n"
    "v_mul_f32 v106, v106, v250\n"
    "v_mfma_f32_16x16x32_bf16 v[204:207], a[52:55], a[164:167], v[204:207]\n"
    "buffer_load_dwordx4 v240, s[64:67], s82 offen offset:2048 lds\n"
    "v_mul_f32 v107, v107, v250\n"
    "v_fma_f32 v104, v108, v252, v104\n"
    "v_fma_f32 v105, v109, v252, v105\n"
    "v_mfma_f32_16x16x32_bf16 v[204:207], a[48:51], a[160:163], v[204:207]\n"
    "v_exp_f32 v200, v200\n"
    "v_fma_f32 v106, v110, v252, v106\n"
    "v_fma_f32 v107, v111, v252, v107\n"
    "v_mfma_f32_16x16x32_bf16 v[48:51], a[80:83], v[208:211], v[48:51]\n"
    "v_cvt_pk_bf16_f32 v96, v96, v97\n"
    "v_cvt_pk_bf16_f32 v97, v98, v99\n"
    "v_cvt_pk_bf16_f32 v98, v104, v105\n"
    "v_mfma_f32_16x16x32_bf16 v[48:51], a[112:115], v[212:215], v[48:51]\n"
    "v_exp_f32 v201, v201\n"
    "v_cvt_pk_bf16_f32 v99, v106, v107\n"
    "v_mfma_f32_16x16x32_bf16 v[52:55], a[84:87], v[208:211], v[52:55]\n"
    "ds_write_b128 v244, v[96:99] offset:5120\n"
    "buffer_load_dwordx4 v241, s[64:67], s82 offen offset:3072 lds\n"
    "v_cvt_pk_bf16_f32 v188, v200, v201\n"
    "v_mul_f32 v112, v112, v250\n"
    "s_mov_b32 s96, 0xfc0000\n"
    "s_mov_b32 s97, 0x0\n"
    "s_waitcnt vmcnt(10)\n"
    "v_cmp_eq_u32 vcc, v247, v229\n"
    "s_nop 1\n"
    "s_and_b64 vcc, vcc, s[96:97]\n"
    "s_cmp_eq_u64 vcc, s[96:97]\n"
    "s_cbranch_scc0 L_slow1\n"
    "L_back1:\n"
    "v_mfma_f32_16x16x32_bf16 v[52:55], a[116:119], v[212:215], v[52:55]\n"
    "v_exp_f32 v202, v202\n"
    "v_mul_f32 v113, v113, v250\n"
    "v_mul_f32 v114, v114, v250\n"
    "v_mfma_f32_16x16x32_bf16 v[56:59], a[88:91], v[208:211], v[56:59]\n"
    "s_xor_b32 s88, s76, s12\n"
    "v_mul_f32 v115, v115, v250\n"
    "v_fma_f32 v112, v116, v252, v112\n"
    "v_fma_f32 v113, v117, v252, v113\n"
    "v_mfma_f32_16x16x32_bf16 v[56:59], a[120:123], v[212:215], v[56:59]\n"
    "v_exp_f32 v203, v203\n"
    "v_fma_f32 v114, v118, v252, v114\n"
    "v_fma_f32 v115, v119, v252, v115\n"
    "v_mfma_f32_16x16x32_bf16 v[60:63], a[92:95], v[208:211], v[60:63]\n"
    "buffer_load_dwordx4 a[216:219], v242, s[60:63], s88 offen offset:0 sc1\n"
    "v_cvt_pk_bf16_f32 v189, v202, v203\n"
    "v_mul_f32 v120, v120, v250\n"
    "v_mul_f32 v121, v121, v250\n"
    "v_mfma_f32_16x16x32_bf16 v[60:63], a[124:127], v[212:215], v[60:63]\n"
    "v_exp_f32 v204, v204\n"
    "v_mul_f32 v122, v122, v250\n"
    "v_mul_f32 v123, v123, v250\n"
    "s_waitcnt lgkmcnt(0)\n"
    "v_mfma_f32_16x16x32_bf16 a[176:179], a[0:3], v[128:131], 0\n"
    "v_fma_f32 v120, v124, v252, v120\n"
    "v_fma_f32 v121, v125, v252, v121\n"
    "v_fma_f32 v122, v126, v252, v122\n"
    "v_mfma_f32_16x16x32_bf16 a[176:179], a[4:7], v[132:135], a[176:179]\n"
    "v_exp_f32 v205, v205\n"
    "v_fma_f32 v123, v127, v252, v123\n"
    "v_cvt_pk_bf16_f32 v112, v112, v113\n"
    "v_mfma_f32_16x16x32_bf16 a[176:179], a[8:11], v[136:139], a[176:179]\n"
    "ds_read_b64_tr_b16 a[16:17], v233 offset:32768\n"
    "v_cvt_pk_bf16_f32 v190, v204, v205\n"
    "v_cvt_pk_bf16_f32 v113, v114, v115\n"
    "v_mfma_f32_16x16x32_bf16 a[176:179], a[12:15], v[140:143], a[176:179]\n"
    "v_exp_f32 v206, v206\n"
    "v_cvt_pk_bf16_f32 v114, v120, v121\n"
    "v_cvt_pk_bf16_f32 v115, v122, v123\n"
    "v_mfma_f32_16x16x32_bf16 a[180:183], a[12:15], v[156:159], 0\n"
    "ds_read_b64_tr_b16 a[18:19], v233 offset:36864\n"
    "ds_write_b128 v244, v[112:115] offset:6144\n"
    "v_mfma_f32_16x16x32_bf16 a[180:183], a[8:11], v[152:155], a[180:183]\n"
    "v_exp_f32 v207, v207\n"
    "v_mfma_f32_16x16x32_bf16 a[180:183], a[4:7], v[148:151], a[180:183]\n"
    "ds_read_b64_tr_b16 a[20:21], v233 offset:40960\n"
    "buffer_store_dwordx4 v[96:99], v242, s[60:63], s75 offen offset:0 sc1\n"
    "v_cvt_pk_bf16_f32 v191, v206, v207\n"
    "v_accvgpr_read_b32 v248, a192\n"
    "v_mfma_f32_16x16x32_bf16 a[180:183], a[0:3], v[144:147], a[180:183]\n"
    "ds_read_b64_tr_b16 a[22:23], v233 offset:45056\n"
    "v_mul_f32 v249, s43, v248\n"
    "v_mfma_f32_16x16x32_bf16 v[64:67], a[64:67], v[184:187], v[64:67]\n"
    "ds_read_b64_tr_b16 a[24:25], v237 offset:32768\n"
    "v_rcp_f32 v248, v248\n"
    "v_mfma_f32_16x16x32_bf16 v[64:67], a[96:99], v[188:191], v[64:67]\n"
    "ds_read_b64_tr_b16 a[26:27], v237 offset:36864\n"
    "v_sub_f32 v249, v249, v249\n"
    "v_mfma_f32_16x16x32_bf16 v[68:71], a[68:71], v[184:187], v[68:71]\n"
    "ds_read_b64_tr_b16 a[28:29], v237 offset:40960\n"
    "buffer_load_dwordx4 a[220:223], v242, s[60:63], s88 offen offset:1024 sc1\n"
    "v_add_f32 v248, v248, v249\n"
    "v_mul_f32 v0, v0, v248\n"
    "v_mfma_f32_16x16x32_bf16 v[68:71], a[100:103], v[188:191], v[68:71]\n"
    "ds_read_b64_tr_b16 a[30:31], v237 offset:45056\n"
    "v_mul_f32 v1, v1, v248\n"
    "v_mfma_f32_16x16x32_bf16 v[72:75], a[72:75], v[184:187], v[72:75]\n"
    "v_mul_f32 v2, v2, v248\n"
    "v_mul_f32 v3, v3, v248\n"
    "v_mul_f32 v8, v8, v248\n"
    "v_mfma_f32_16x16x32_bf16 v[72:75], a[104:107], v[188:191], v[72:75]\n"
    "v_mul_f32 v9, v9, v248\n"
    "v_mul_f32 v10, v10, v248\n"
    "v_mfma_f32_16x16x32_bf16 v[76:79], a[76:79], v[184:187], v[76:79]\n"
    "ds_read_b64_tr_b16 a[32:33], v232 offset:33280\n"
    "v_mul_f32 v11, v11, v248\n"
    "v_cvt_pk_bf16_f32 v0, v0, v1\n"
    "v_mfma_f32_16x16x32_bf16 v[76:79], a[108:111], v[188:191], v[76:79]\n"
    "ds_read_b64_tr_b16 a[34:35], v232 offset:37376\n"
    "v_cvt_pk_bf16_f32 v1, v2, v3\n"
    "v_mfma_f32_16x16x32_bf16 a[200:203], a[208:211], v[184:187], a[200:203]\n"
    "ds_read_b64_tr_b16 a[36:37], v232 offset:41472\n"
    "v_cvt_pk_bf16_f32 v2, v8, v9\n"
    "v_cvt_pk_bf16_f32 v3, v10, v11\n"
    "v_mfma_f32_16x16x32_bf16 a[200:203], a[208:211], v[188:191], a[200:203]\n"
    "ds_read_b64_tr_b16 a[38:39], v232 offset:45568\n"
    "v_mul_f32 v4, v4, v248\n"
    "v_mfma_f32_16x16x32_bf16 a[184:187], a[0:3], v[160:163], 0\n"
    "ds_read_b64_tr_b16 a[40:41], v236 offset:33280\n"
    "buffer_load_dwordx4 a[224:227], v243, s[60:63], s88 offen offset:0 sc1\n"
    "v_mul_f32 v5, v5, v248\n"
    "v_mul_f32 v6, v6, v248\n"
    "v_mfma_f32_16x16x32_bf16 a[184:187], a[4:7], v[164:167], a[184:187]\n"
    "ds_read_b64_tr_b16 a[42:43], v236 offset:37376\n"
    "v_mul_f32 v7, v7, v248\n"
    "v_mfma_f32_16x16x32_bf16 a[184:187], a[8:11], v[168:171], a[184:187]\n"
    "ds_read_b64_tr_b16 a[44:45], v236 offset:41472\n"
    "v_mul_f32 v12, v12, v248\n"
    "v_mul_f32 v13, v13, v248\n"
    "v_mfma_f32_16x16x32_bf16 a[184:187], a[12:15], v[172:175], a[184:187]\n"
    "ds_read_b64_tr_b16 a[46:47], v236 offset:45568\n"
    "v_mul_f32 v14, v14, v248\n"
    "v_mfma_f32_16x16x32_bf16 a[188:191], a[12:15], a[252:255], 0\n"
    "buffer_store_dwordx4 v[112:115], v242, s[60:63], s75 offen offset:1024 sc1\n"
    "v_mul_f32 v15, v15, v248\n"
    "v_cvt_pk_bf16_f32 v4, v4, v5\n"
    "v_cvt_pk_bf16_f32 v5, v6, v7\n"
    "v_mfma_f32_16x16x32_bf16 a[188:191], a[8:11], a[248:251], a[188:191]\n"
    "v_cvt_pk_bf16_f32 v6, v12, v13\n"
    "v_cvt_pk_bf16_f32 v7, v14, v15\n"
    "v_mfma_f32_16x16x32_bf16 a[188:191], a[4:7], v[180:183], a[188:191]\n"
    "v_mul_f32 v16, v16, v248\n"
    "v_mul_f32 v17, v17, v248\n"
    "v_mul_f32 v18, v18, v248\n"
    "v_mfma_f32_16x16x32_bf16 a[188:191], a[0:3], v[176:179], a[188:191]\n"
    "v_mul_f32 v19, v19, v248\n"
    "v_mul_f32 v24, v24, v248\n"
    "v_mfma_f32_16x16x32_bf16 v[80:83], a[80:83], v[184:187], v[80:83]\n"
    "buffer_load_dwordx4 a[228:231], v243, s[60:63], s88 offen offset:1024 sc1\n"
    "v_mul_f32 v25, v25, v248\n"
    "v_mul_f32 v26, v26, v248\n"
    "v_mul_f32 v27, v27, v248\n"
    "v_mfma_f32_16x16x32_bf16 v[80:83], a[112:115], v[188:191], v[80:83]\n"
    "v_cvt_pk_bf16_f32 v16, v16, v17\n"
    "v_cvt_pk_bf16_f32 v17, v18, v19\n"
    "v_mfma_f32_16x16x32_bf16 v[84:87], a[84:87], v[184:187], v[84:87]\n"
    "v_cvt_pk_bf16_f32 v18, v24, v25\n"
    "v_cvt_pk_bf16_f32 v19, v26, v27\n"
    "v_mul_f32 v20, v20, v248\n"
    "v_mfma_f32_16x16x32_bf16 v[84:87], a[116:119], v[188:191], v[84:87]\n"
    "v_mul_f32 v21, v21, v248\n"
    "v_mul_f32 v22, v22, v248\n"
    "v_mfma_f32_16x16x32_bf16 v[88:91], a[88:91], v[184:187], v[88:91]\n"
    "ds_read_b64_tr_b16 a[48:49], v233 offset:33280\n"
    "v_mul_f32 v23, v23, v248\n"
    "v_mul_f32 v28, v28, v248\n"
    "v_mfma_f32_16x16x32_bf16 v[88:91], a[120:123], v[188:191], v[88:91]\n"
    "ds_read_b64_tr_b16 a[50:51], v233 offset:37376\n"
    "v_mul_f32 v29, v29, v248\n"
    "v_mfma_f32_16x16x32_bf16 v[92:95], a[92:95], v[184:187], v[92:95]\n"
    "ds_read_b64_tr_b16 a[52:53], v233 offset:41472\n"
    "v_mul_f32 v30, v30, v248\n"
    "v_mul_f32 v31, v31, v248\n"
    "v_mfma_f32_16x16x32_bf16 v[92:95], a[124:127], v[188:191], v[92:95]\n"
    "ds_read_b64_tr_b16 a[54:55], v233 offset:45568\n"
    "s_xor_b32 s88, s77, s12\n"
    "v_cvt_pk_bf16_f32 v20, v20, v21\n"
    "s_waitcnt lgkmcnt(4)\n"
    "v_mfma_f32_16x16x32_bf16 a[128:131], a[16:19], v[128:131], 0\n"
    "ds_read_b64_tr_b16 a[56:57], v237 offset:33280\n"
    "buffer_load_dwordx4 a[232:235], v242, s[60:63], s88 offen offset:0 sc1\n"
    "v_cvt_pk_bf16_f32 v21, v22, v23\n"
    "v_cvt_pk_bf16_f32 v22, v28, v29\n"
    "v_mfma_f32_16x16x32_bf16 a[128:131], a[20:23], v[132:135], a[128:131]\n"
    "ds_read_b64_tr_b16 a[58:59], v237 offset:37376\n"
    "v_cvt_pk_bf16_f32 v23, v30, v31\n"
    "v_mfma_f32_16x16x32_bf16 a[128:131], a[24:27], v[136:139], a[128:131]\n"
    "ds_read_b64_tr_b16 a[60:61], v237 offset:41472\n"
    "v_accvgpr_read_b32 v248, a196\n"
    "v_mfma_f32_16x16x32_bf16 a[128:131], a[28:31], v[140:143], a[128:131]\n"
    "ds_read_b64_tr_b16 a[62:63], v237 offset:45568\n"
    "v_mul_f32 v249, s43, v248\n"
    "v_mfma_f32_16x16x32_bf16 a[132:135], a[28:31], v[156:159], 0\n"
    "buffer_store_dwordx4 v[0:3], v242, s[60:63], s72 offen offset:0 sc1\n"
    "v_rcp_f32 v248, v248\n"
    "v_mfma_f32_16x16x32_bf16 a[132:135], a[24:27], v[152:155], a[132:135]\n"
    "v_sub_f32 v249, v249, v249\n"
    "v_add_f32 v248, v248, v249\n"
    "v_mfma_f32_16x16x32_bf16 a[132:135], a[20:23], v[148:151], a[132:135]\n"
    "v_mul_f32 v32, v32, v248\n"
    "v_mul_f32 v33, v33, v248\n"
    "v_mfma_f32_16x16x32_bf16 a[132:135], a[16:19], v[144:147], a[132:135]\n"
    "v_mul_f32 v34, v34, v248\n"
    "v_mul_f32 v35, v35, v248\n"
    "v_mfma_f32_16x16x32_bf16 a[136:139], a[16:19], v[160:163], 0\n"
    "buffer_load_dwordx4 a[236:239], v242, s[60:63], s88 offen offset:1024 sc1\n"
    "v_mul_f32 v40, v40, v248\n"
    "v_mul_f32 v41, v41, v248\n"
    "v_mfma_f32_16x16x32_bf16 a[136:139], a[20:23], v[164:167], a[136:139]\n"
    "ds_read_b64_tr_b16 a[0:1], v234 offset:32768\n"
    "v_mul_f32 v42, v42, v248\n"
    "v_mfma_f32_16x16x32_bf16 a[136:139], a[24:27], v[168:171], a[136:139]\n"
    "ds_read_b64_tr_b16 a[2:3], v234 offset:36864\n"
    "v_mul_f32 v43, v43, v248\n"
    "v_mfma_f32_16x16x32_bf16 a[136:139], a[28:31], v[172:175], a[136:139]\n"
    "ds_read_b64_tr_b16 a[4:5], v234 offset:40960\n"
    "v_cvt_pk_bf16_f32 v32, v32, v33\n"
    "v_mfma_f32_16x16x32_bf16 a[140:143], a[32:35], v[128:131], 0\n"
    "v_cvt_pk_bf16_f32 v33, v34, v35\n"
    "v_cvt_pk_bf16_f32 v34, v40, v41\n"
    "v_mfma_f32_16x16x32_bf16 a[140:143], a[36:39], v[132:135], a[140:143]\n"
    "ds_read_b64_tr_b16 a[6:7], v234 offset:45056\n"
    "v_cvt_pk_bf16_f32 v35, v42, v43\n"
    "v_mfma_f32_16x16x32_bf16 a[140:143], a[40:43], v[136:139], a[140:143]\n"
    "ds_read_b64_tr_b16 a[8:9], v238 offset:32768\n"
    "v_mul_f32 v36, v36, v248\n"
    "v_mfma_f32_16x16x32_bf16 a[140:143], a[44:47], v[140:143], a[140:143]\n"
    "ds_read_b64_tr_b16 a[10:11], v238 offset:36864\n"
    "v_mul_f32 v37, v37, v248\n"
    "v_mfma_f32_16x16x32_bf16 a[144:147], a[44:47], v[156:159], 0\n"
    "buffer_load_dwordx4 a[240:243], v243, s[60:63], s88 offen offset:0 sc1\n"
    "v_mul_f32 v38, v38, v248\n"
    "v_mul_f32 v39, v39, v248\n"
    "v_mfma_f32_16x16x32_bf16 a[144:147], a[40:43], v[152:155], a[144:147]\n"
    "ds_read_b64_tr_b16 a[12:13], v238 offset:40960\n"
    "v_mul_f32 v44, v44, v248\n"
    "v_mfma_f32_16x16x32_bf16 a[144:147], a[36:39], v[148:151], a[144:147]\n"
    "ds_read_b64_tr_b16 a[14:15], v238 offset:45056\n"
    "v_mul_f32 v45, v45, v248\n"
    "v_mfma_f32_16x16x32_bf16 a[144:147], a[32:35], v[144:147], a[144:147]\n"
    "v_mul_f32 v46, v46, v248\n"
    "v_mul_f32 v47, v47, v248\n"
    "v_mfma_f32_16x16x32_bf16 a[148:151], a[32:35], v[160:163], 0\n"
    "buffer_store_dwordx4 v[16:19], v242, s[60:63], s72 offen offset:1024 sc1\n"
    "v_cvt_pk_bf16_f32 v36, v36, v37\n"
    "v_cvt_pk_bf16_f32 v37, v38, v39\n"
    "v_mfma_f32_16x16x32_bf16 a[148:151], a[36:39], v[164:167], a[148:151]\n"
    "v_cvt_pk_bf16_f32 v38, v44, v45\n"
    "v_cvt_pk_bf16_f32 v39, v46, v47\n"
    "L_h14:\n"
    "s_cbranch_execz L_ret2\n"
    "v_mfma_f32_16x16x32_bf16 a[148:151], a[40:43], v[168:171], a[148:151]\n"
    "v_mul_f32 v48, v48, v248\n"
    "v_mul_f32 v49, v49, v248\n"
    "v_mfma_f32_16x16x32_bf16 a[148:151], a[44:47], v[172:175], a[148:151]\n"
    "v_mul_f32 v50, v50, v248\n"
    "v_mul_f32 v51, v51, v248\n"
    "v_mfma_f32_16x16x32_bf16 a[152:155], a[44:47], a[252:255], 0\n"
    "buffer_load_dwordx4 a[244:247], v243, s[60:63], s88 offen offset:1024 sc1\n"
    "v_mul_f32 v56, v56, v248\n"
    "v_mul_f32 v57, v57, v248\n"
    "v_mfma_f32_16x16x32_bf16 a[152:155], a[40:43], a[248:251], a[152:155]\n"
    "ds_read_b64_tr_b16 a[16:17], v235 offset:32768\n"
    "v_mul_f32 v58, v58, v248\n"
    "v_mfma_f32_16x16x32_bf16 a[152:155], a[36:39], v[180:183], a[152:155]\n"
    "ds_read_b64_tr_b16 a[18:19], v235 offset:36864\n"
    "v_mul_f32 v59, v59, v248\n"
    "v_mfma_f32_16x16x32_bf16 a[152:155], a[32:35], v[176:179], a[152:155]\n"
    "ds_read_b64_tr_b16 a[20:21], v235 offset:40960\n"
    "v_cvt_pk_bf16_f32 v48, v48, v49\n"
    "s_waitcnt lgkmcnt(3)\n"
    "v_mfma_f32_16x16x32_bf16 a[156:159], a[48:51], v[128:131], 0\n"
    "v_cvt_pk_bf16_f32 v49, v50, v51\n"
    "v_cvt_pk_bf16_f32 v50, v56, v57\n"
    "v_mfma_f32_16x16x32_bf16 a[156:159], a[52:55], v[132:135], a[156:159]\n"
    "ds_read_b64_tr_b16 a[22:23], v235 offset:45056\n"
    "v_cvt_pk_bf16_f32 v51, v58, v59\n"
    "v_mfma_f32_16x16x32_bf16 a[156:159], a[56:59], v[136:139], a[156:159]\n"
    "ds_read_b64_tr_b16 a[24:25], v239 offset:32768\n"
    "s_xor_b32 s88, s78, s12\n"
    "v_mul_f32 v52, v52, v248\n"
    "v_mfma_f32_16x16x32_bf16 a[156:159], a[60:63], v[140:143], a[156:159]\n"
    "ds_read_b64_tr_b16 a[26:27], v239 offset:36864\n"
    "v_mul_f32 v53, v53, v248\n"
    "v_mfma_f32_16x16x32_bf16 a[160:163], a[60:63], v[156:159], 0\n"
    "buffer_load_dwordx4 a[96:99], v242, s[60:63], s88 offen offset:0 sc1\n"
    "v_mul_f32 v54, v54, v248\n"
    "v_mul_f32 v55, v55, v248\n"
    "v_mfma_f32_16x16x32_bf16 a[160:163], a[56:59], v[152:155], a[160:163]\n"
    "ds_read_b64_tr_b16 a[28:29], v239 offset:40960\n"
    "v_mul_f32 v60, v60, v248\n"
    "v_mfma_f32_16x16x32_bf16 a[160:163], a[52:55], v[148:151], a[160:163]\n"
    "ds_read_b64_tr_b16 a[30:31], v239 offset:45056\n"
    "v_mul_f32 v61, v61, v248\n"
    "v_mfma_f32_16x16x32_bf16 a[160:163], a[48:51], v[144:147], a[160:163]\n"
    "v_mul_f32 v62, v62, v248\n"
    "v_mul_f32 v63, v63, v248\n"
    "v_mfma_f32_16x16x32_bf16 a[164:167], a[48:51], v[160:163], 0\n"
    "buffer_store_dwordx4 v[4:7], v243, s[60:63], s72 offen offset:0 sc1\n"
    "v_cvt_pk_bf16_f32 v52, v52, v53\n"
    "v_cvt_pk_bf16_f32 v53, v54, v55\n"
    "v_mfma_f32_16x16x32_bf16 a[164:167], a[52:55], v[164:167], a[164:167]\n"
    "ds_read_b64_tr_b16 a[32:33], v234 offset:33280\n"
    "v_cvt_pk_bf16_f32 v54, v60, v61\n"
    "v_mfma_f32_16x16x32_bf16 a[164:167], a[56:59], v[168:171], a[164:167]\n"
    "ds_read_b64_tr_b16 a[34:35], v234 offset:37376\n"
    "v_cvt_pk_bf16_f32 v55, v62, v63\n"
    "v_mfma_f32_16x16x32_bf16 a[164:167], a[60:63], v[172:175], a[164:167]\n"
    "ds_read_b64_tr_b16 a[36:37], v234 offset:41472\n"
    "v_accvgpr_read_b32 v248, a200\n"
    "v_mfma_f32_16x16x32_bf16 a[168:171], a[0:3], v[128:131], 0\n"
    "buffer_load_dwordx4 a[100:103], v242, s[60:63], s88 offen offset:1024 sc1\n"
    "v_mul_f32 v249, s43, v248\n"
    "v_mfma_f32_16x16x32_bf16 a[168:171], a[4:7], v[132:135], a[168:171]\n"
    "ds_read_b64_tr_b16 a[38:39], v234 offset:45568\n"
    "v_mfma_f32_16x16x32_bf16 a[168:171], a[8:11], v[136:139], a[168:171]\n"
    "ds_read_b64_tr_b16 a[40:41], v238 offset:33280\n"
    "v_mfma_f32_16x16x32_bf16 a[168:171], a[12:15], v[140:143], a[168:171]\n"
    "ds_read_b64_tr_b16 a[42:43], v238 offset:37376\n"
    "v_mfma_f32_16x16x32_bf16 a[172:175], a[12:15], v[156:159], 0\n"
    "v_rcp_f32 v248, v248\n"
    "v_mfma_f32_16x16x32_bf16 a[172:175], a[8:11], v[152:155], a[172:175]\n"
    "ds_read_b64_tr_b16 a[44:45], v238 offset:41472\n"
    "v_sub_f32 v249, v249, v249\n"
    "v_mfma_f32_16x16x32_bf16 a[172:175], a[4:7], v[148:151], a[172:175]\n"
    "ds_read_b64_tr_b16 a[46:47], v238 offset:45568\n"
    "v_add_f32 v248, v248, v249\n"
    "v_mfma_f32_16x16x32_bf16 a[172:175], a[0:3], v[144:147], a[172:175]\n"
    "ds_read_b64_tr_b16 a[48:49], v235 offset:33280\n"
    "v_mul_f32 v64, v64, v248\n"
    "v_mfma_f32_16x16x32_bf16 a[64:67], a[0:3], v[160:163], 0\n"
    "buffer_load_dwordx4 a[104:107], v243, s[60:63], s88 offen offset:0 sc1\n"
    "v_mul_f32 v65, v65, v248\n"
    "v_mul_f32 v66, v66, v248\n"
    "v_mfma_f32_16x16x32_bf16 a[64:67], a[4:7], v[164:167], a[64:67]\n"
    "ds_read_b64_tr_b16 a[50:51], v235 offset:37376\n"
    "v_mul_f32 v67, v67, v248\n"
    "v_mfma_f32_16x16x32_bf16 a[64:67], a[8:11], v[168:171], a[64:67]\n"
    "ds_read_b64_tr_b16 a[52:53], v235 offset:41472\n"
    "v_mul_f32 v72, v72, v248\n"
    "v_mfma_f32_16x16x32_bf16 a[64:67], a[12:15], v[172:175], a[64:67]\n"
    "ds_read_b64_tr_b16 a[54:55], v235 offset:45568\n"
    "v_mul_f32 v73, v73, v248\n"
    "v_mfma_f32_16x16x32_bf16 a[68:71], a[12:15], a[252:255], 0\n"
    "buffer_store_dwordx4 v[20:23], v243, s[60:63], s72 offen offset:1024 sc1\n"
    "v_mul_f32 v74, v74, v248\n"
    "v_mul_f32 v75, v75, v248\n"
    "v_mfma_f32_16x16x32_bf16 a[68:71], a[8:11], a[248:251], a[68:71]\n"
    "ds_read_b64_tr_b16 a[56:57], v239 offset:33280\n"
    "v_cvt_pk_bf16_f32 v64, v64, v65\n"
    "v_mfma_f32_16x16x32_bf16 a[68:71], a[4:7], v[180:183], a[68:71]\n"
    "ds_read_b64_tr_b16 a[58:59], v239 offset:37376\n"
    "v_cvt_pk_bf16_f32 v65, v66, v67\n"
    "v_mfma_f32_16x16x32_bf16 a[68:71], a[0:3], v[176:179], a[68:71]\n"
    "ds_read_b64_tr_b16 a[60:61], v239 offset:41472\n"
    "v_cvt_pk_bf16_f32 v66, v72, v73\n"
    "s_waitcnt lgkmcnt(6)\n"
    "v_mfma_f32_16x16x32_bf16 a[72:75], a[16:19], v[128:131], 0\n"
    "buffer_load_dwordx4 a[108:111], v243, s[60:63], s88 offen offset:1024 sc1\n"
    "v_cvt_pk_bf16_f32 v67, v74, v75\n"
    "v_mul_f32 v68, v68, v248\n"
    "v_mfma_f32_16x16x32_bf16 a[72:75], a[20:23], v[132:135], a[72:75]\n"
    "ds_read_b64_tr_b16 a[62:63], v239 offset:45568\n"
    "v_mul_f32 v69, v69, v248\n"
    "v_mfma_f32_16x16x32_bf16 a[72:75], a[24:27], v[136:139], a[72:75]\n"
    "v_mul_f32 v70, v70, v248\n"
    "v_mul_f32 v71, v71, v248\n"
    "v_mfma_f32_16x16x32_bf16 a[72:75], a[28:31], v[140:143], a[72:75]\n"
    "v_mul_f32 v76, v76, v248\n"
    "v_mul_f32 v77, v77, v248\n"
    "v_mfma_f32_16x16x32_bf16 a[76:79], a[28:31], v[156:159], 0\n"
    "v_mul_f32 v78, v78, v248\n"
    "v_mul_f32 v79, v79, v248\n"
    "v_mfma_f32_16x16x32_bf16 a[76:79], a[24:27], v[152:155], a[76:79]\n"
    "v_cvt_pk_bf16_f32 v68, v68, v69\n"
    "v_cvt_pk_bf16_f32 v69, v70, v71\n"
    "v_mfma_f32_16x16x32_bf16 a[76:79], a[20:23], v[148:151], a[76:79]\n"
    "s_xor_b32 s88, s79, s12\n"
    "v_cvt_pk_bf16_f32 v70, v76, v77\n"
    "v_cvt_pk_bf16_f32 v71, v78, v79\n"
    "v_mfma_f32_16x16x32_bf16 a[76:79], a[16:19], v[144:147], a[76:79]\n"
    "s_waitcnt lgkmcnt(0)\n"
    "s_barrier\n"
    "v_mfma_f32_16x16x32_bf16 a[80:83], a[16:19], v[160:163], 0\n"
    "ds_read_b128 v[100:103], v245 offset:5120\n"
    "buffer_load_dwordx4 a[112:115], v242, s[60:63], s88 offen offset:0 sc1\n"
    "v_mul_f32 v80, v80, v248\n"
    "v_mul_f32 v81, v81, v248\n"
    "v_mfma_f32_16x16x32_bf16 a[80:83], a[20:23], v[164:167], a[80:83]\n"
    "ds_read_b64_tr_b16 a[0:1], v232 offset:49152\n"
    "ds_read_b128 v[116:119], v245 offset:6144\n"
    "v_mul_f32 v82, v82, v248\n"
    "v_mfma_f32_16x16x32_bf16 a[80:83], a[24:27], v[168:171], a[80:83]\n"
    "ds_read_b64_tr_b16 a[2:3], v232 offset:53248\n"
    "v_mul_f32 v83, v83, v248\n"
    "v_mfma_f32_16x16x32_bf16 a[80:83], a[28:31], v[172:175], a[80:83]\n"
    "ds_read_b64_tr_b16 a[4:5], v232 offset:57344\n"
    "v_mul_f32 v88, v88, v248\n"
    "v_mfma_f32_16x16x32_bf16 a[84:87], a[32:35], v[128:131], 0\n"
    "buffer_store_dwordx4 v[32:35], v242, s[60:63], s73 offen offset:0 sc1\n"
    "v_mul_f32 v89, v89, v248\n"
    "v_mul_f32 v90, v90, v248\n"
    "v_mfma_f32_16x16x32_bf16 a[84:87], a[36:39], v[132:135], a[84:87]\n"
    "ds_read_b64_tr_b16 a[6:7], v232 offset:61440\n"
    "v_mul_f32 v91, v91, v248\n"
    "v_mfma_f32_16x16x32_bf16 a[84:87], a[40:43], v[136:139], a[84:87]\n"
    "ds_read_b64_tr_b16 a[8:9], v236 offset:49152\n"
    "v_cvt_pk_bf16_f32 v80, v80, v81\n"
    "v_mfma_f32_16x16x32_bf16 a[84:87], a[44:47], v[140:143], a[84:87]\n"
    "ds_read_b64_tr_b16 a[10:11], v236 offset:53248\n"
    "v_cvt_pk_bf16_f32 v81, v82, v83\n"
    "v_mfma_f32_16x16x32_bf16 a[88:91], a[44:47], v[156:159], 0\n"
    "buffer_load_dwordx4 a[116:119], v242, s[60:63], s88 offen offset:1024 sc1\n"
    "v_cvt_pk_bf16_f32 v82, v88, v89\n"
    "v_cvt_pk_bf16_f32 v83, v90, v91\n"
    "v_mfma_f32_16x16x32_bf16 a[88:91], a[40:43], v[152:155], a[88:91]\n"
    "ds_read_b64_tr_b16 a[12:13], v236 offset:57344\n"
    "v_mul_f32 v84, v84, v248\n"
    "v_mfma_f32_16x16x32_bf16 a[88:91], a[36:39], v[148:151], a[88:91]\n"
    "ds_read_b64_tr_b16 a[14:15], v236 offset:61440\n"
    "v_mul_f32 v85, v85, v248\n"
    "v_mfma_f32_16x16x32_bf16 a[88:91], a[32:35], v[144:147], a[88:91]\n"
    "v_mul_f32 v86, v86, v248\n"
    "v_mul_f32 v87, v87, v248\n"
    "v_mfma_f32_16x16x32_bf16 a[92:95], a[32:35], v[160:163], 0\n"
    "v_mul_f32 v92, v92, v248\n"
    "v_mul_f32 v93, v93, v248\n"
    "v_mfma_f32_16x16x32_bf16 a[92:95], a[36:39], v[164:167], a[92:95]\n"
    "v_mul_f32 v94, v94, v248\n"
    "v_mul_f32 v95, v95, v248\n"
    "v_mfma_f32_16x16x32_bf16 a[92:95], a[40:43], v[168:171], a[92:95]\n"
    "v_cvt_pk_bf16_f32 v84, v84, v85\n"
    "v_cvt_pk_bf16_f32 v85, v86, v87\n"
    "v_mfma_f32_16x16x32_bf16 a[92:95], a[44:47], v[172:175], a[92:95]\n"
    "v_cvt_pk_bf16_f32 v86, v92, v93\n"
    "v_cvt_pk_bf16_f32 v87, v94, v95\n"
    "v_mfma_f32_16x16x32_bf16 a[208:211], a[44:47], a[252:255], 0\n"
    "buffer_load_dwordx4 a[120:123], v243, s[60:63], s88 offen offset:0 sc1\n"
    "v_mfma_f32_16x16x32_bf16 a[208:211], a[40:43], a[248:251], a[208:211]\n"
    "ds_read_b64_tr_b16 a[16:17], v233 offset:49152\n"
    "v_mfma_f32_16x16x32_bf16 a[208:211], a[36:39], v[180:183], a[208:211]\n"
    "ds_read_b64_tr_b16 a[18:19], v233 offset:53248\n"
    "v_mfma_f32_16x16x32_bf16 a[208:211], a[32:35], v[176:179], a[208:211]\n"
    "ds_read_b64_tr_b16 a[20:21], v233 offset:57344\n"
    "v_mfma_f32_16x16x32_bf16 a[212:215], a[48:51], v[128:131], 0\n"
    "buffer_store_dwordx4 v[48:51], v242, s[60:63], s73 offen offset:1024 sc1\n"
    "v_mfma_f32_16x16x32_bf16 a[212:215], a[52:55], v[132:135], a[212:215]\n"
    "ds_read_b64_tr_b16 a[22:23], v233 offset:61440\n"
    "v_mfma_f32_16x16x32_bf16 a[212:215], a[56:59], v[136:139], a[212:215]\n"
    "ds_read_b64_tr_b16 a[24:25], v237 offset:49152\n"
    "v_mfma_f32_16x16x32_bf16 a[212:215], a[60:63], v[140:143], a[212:215]\n"
    "ds_read_b64_tr_b16 a[26:27], v237 offset:53248\n"
    "v_mfma_f32_16x16x32_bf16 a[248:251], a[60:63], v[156:159], 0\n"
    "buffer_load_dwordx4 a[124:127], v243, s[60:63], s88 offen offset:1024 sc1\n"
    "v_mfma_f32_16x16x32_bf16 a[248:251], a[56:59], v[152:155], a[248:251]\n"
    "ds_read_b64_tr_b16 a[28:29], v237 offset:57344\n"
    "v_mfma_f32_16x16x32_bf16 a[248:251], a[52:55], v[148:151], a[248:251]\n"
    "ds_read_b64_tr_b16 a[30:31], v237 offset:61440\n"
    "v_mfma_f32_16x16x32_bf16 a[248:251], a[48:51], v[144:147], a[248:251]\n"
    "v_mfma_f32_16x16x32_bf16 a[252:255], a[48:51], v[160:163], 0\n"
    "v_mfma_f32_16x16x32_bf16 a[252:255], a[52:55], v[164:167], a[252:255]\n"
    "ds_read_b64_tr_b16 a[32:33], v232 offset:49664\n"
    "v_mfma_f32_16x16x32_bf16 a[252:255], a[56:59], v[168:171], a[252:255]\n"
    "ds_read_b64_tr_b16 a[34:35], v232 offset:53760\n"
    "s_add_u32 m0, s83, 32768\n"
    "v_mfma_f32_16x16x32_bf16 a[252:255], a[60:63], v[172:175], a[252:255]\n"
    "ds_read_b64_tr_b16 a[36:37], v232 offset:57856\n"
    "s_xor_b32 s81, s93, 786432\n"
    "s_add_u32 s82, s81, 14336\n"
    "s_waitcnt lgkmcnt(5)\n"
    "v_mfma_f32_16x16x32_bf16 a[176:179], a[0:3], v[0:3], a[176:179]\n"
    "buffer_load_dwordx4 v240, s[64:67], s81 offen offset:0 lds\n"
    "v_mfma_f32_16x16x32_bf16 a[176:179], a[4:7], v[16:19], a[176:179]\n"
    "ds_read_b64_tr_b16 a[38:39], v232 offset:61952\n"
    "v_mfma_f32_16x16x32_bf16 a[176:179], a[8:11], v[4:7], a[176:179]\n"
    "ds_read_b64_tr_b16 a[40:41], v236 offset:49664\n"
    "v_mfma_f32_16x16x32_bf16 a[176:179], a[12:15], v[20:23], a[176:179]\n"
    "ds_read_b64_tr_b16 a[42:43], v236 offset:53760\n"
    "v_mfma_f32_16x16x32_bf16 a[180:183], a[12:15], v[52:55], a[180:183]\n"
    "buffer_store_dwordx4 v[36:39], v243, s[60:63], s73 offen offset:0 sc1\n"
    "v_mfma_f32_16x16x32_bf16 a[180:183], a[8:11], v[36:39], a[180:183]\n"
    "ds_read_b64_tr_b16 a[44:45], v236 offset:57856\n"
    "v_mfma_f32_16x16x32_bf16 a[180:183], a[4:7], v[48:51], a[180:183]\n"
    "ds_read_b64_tr_b16 a[46:47], v236 offset:61952\n"
    "v_mfma_f32_16x16x32_bf16 a[180:183], a[0:3], v[32:35], a[180:183]\n"
    "v_mfma_f32_16x16x32_bf16 a[184:187], a[0:3], v[64:67], a[184:187]\n"
    "buffer_load_dwordx4 v241, s[64:67], s81 offen offset:1024 lds\n"
    "v_mfma_f32_16x16x32_bf16 a[184:187], a[4:7], v[80:83], a[184:187]\n"
    "v_mfma_f32_16x16x32_bf16 a[184:187], a[8:11], v[68:71], a[184:187]\n"
    "v_mfma_f32_16x16x32_bf16 a[184:187], a[12:15], v[84:87], a[184:187]\n"
    "v_mfma_f32_16x16x32_bf16 a[188:191], a[12:15], v[116:119], a[188:191]\n"
    "v_mfma_f32_16x16x32_bf16 a[188:191], a[8:11], v[100:103], a[188:191]\n"
    "ds_read_b64_tr_b16 a[48:49], v233 offset:49664\n"
    "v_mfma_f32_16x16x32_bf16 a[188:191], a[4:7], v[112:115], a[188:191]\n"
    "ds_read_b64_tr_b16 a[50:51], v233 offset:53760\n"
    "v_mfma_f32_16x16x32_bf16 a[188:191], a[0:3], v[96:99], a[188:191]\n"
    "ds_read_b64_tr_b16 a[52:53], v233 offset:57856\n"
    "v_mfma_f32_16x16x32_bf16 a[128:131], a[16:19], v[0:3], a[128:131]\n"
    "buffer_load_dwordx4 v240, s[64:67], s82 offen offset:2048 lds\n"
    "v_mfma_f32_16x16x32_bf16 a[128:131], a[20:23], v[16:19], a[128:131]\n"
    "ds_read_b64_tr_b16 a[54:55], v233 offset:61952\n"
    "v_mfma_f32_16x16x32_bf16 a[128:131], a[24:27], v[4:7], a[128:131]\n"
    "ds_read_b64_tr_b16 a[56:57], v237 offset:49664\n"
    "s_waitcnt lgkmcnt(5)\n"
    "v_mfma_f32_16x16x32_bf16 a[128:131], a[28:31], v[20:23], a[128:131]\n"
    "ds_read_b64_tr_b16 a[58:59], v237 offset:53760\n"
    "v_mfma_f32_16x16x32_bf16 a[132:135], a[28:31], v[52:55], a[132:135]\n"
    "buffer_store_dwordx4 v[52:55], v243, s[60:63], s73 offen offset:1024 sc1\n"
    "v_mfma_f32_16x16x32_bf16 a[132:135], a[24:27], v[36:39], a[132:135]\n"
    "ds_read_b64_tr_b16 a[60:61], v237 offset:57856\n"
    "v_mfma_f32_16x16x32_bf16 a[132:135], a[20:23], v[48:51], a[132:135]\n"
    "ds_read_b64_tr_b16 a[62:63], v237 offset:61952\n"
    "v_mfma_f32_16x16x32_bf16 a[132:135], a[16:19], v[32:35], a[132:135]\n"
    "v_mfma_f32_16x16x32_bf16 a[136:139], a[16:19], v[64:67], a[136:139]\n"
    "buffer_load_dwordx4 v241, s[64:67], s82 offen offset:3072 lds\n"
    "v_mfma_f32_16x16x32_bf16 a[136:139], a[20:23], v[80:83], a[136:139]\n"
    "ds_read_b64_tr_b16 a[0:1], v234 offset:49152\n"
    "v_mfma_f32_16x16x32_bf16 a[136:139], a[24:27], v[68:71], a[136:139]\n"
    "ds_read_b64_tr_b16 a[2:3], v234 offset:53248\n"
    "v_mfma_f32_16x16x32_bf16 a[136:139], a[28:31], v[84:87], a[136:139]\n"
    "ds_read_b64_tr_b16 a[4:5], v234 offset:57344\n"
    "v_mfma_f32_16x16x32_bf16 a[140:143], a[32:35], v[0:3], a[140:143]\n"
    "v_mfma_f32_16x16x32_bf16 a[140:143], a[36:39], v[16:19], a[140:143]\n"
    "ds_read_b64_tr_b16 a[6:7], v234 offset:61440\n"
    "v_mfma_f32_16x16x32_bf16 a[140:143], a[40:43], v[4:7], a[140:143]\n"
    "ds_read_b64_tr_b16 a[8:9], v238 offset:49152\n"
    "s_add_u32 m0, s83, 98304\n"
    "v_mfma_f32_16x16x32_bf16 a[140:143], a[44:47], v[20:23], a[140:143]\n"
    "ds_read_b64_tr_b16 a[10:11], v238 offset:53248\n"
    "s_xor_b32 s81, s93, 786560\n"
    "s_add_u32 s82, s81, 14336\n"
    "v_mfma_f32_16x16x32_bf16 a[144:147], a[44:47], v[52:55], a[144:147]\n"
    "buffer_load_dwordx4 v240, s[64:67], s81 offen offset:0 lds\n"
    "v_mfma_f32_16x16x32_bf16 a[144:147], a[40:43], v[36:39], a[144:147]\n"
    "ds_read_b64_tr_b16 a[12:13], v238 offset:57344\n"
    "v_mfma_f32_16x16x32_bf16 a[144:147], a[36:39], v[48:51], a[144:147]\n"
    "ds_read_b64_tr_b16 a[14:15], v238 offset:61440\n"
    "v_mfma_f32_16x16x32_bf16 a[144:147], a[32:35], v[32:35], a[144:147]\n"
    "v_mfma_f32_16x16x32_bf16 a[148:151], a[32:35], v[64:67], a[148:151]\n"
    "buffer_store_dwordx4 v[64:67], v242, s[60:63], s74 offen offset:0 sc1\n"
    "v_mfma_f32_16x16x32_bf16 a[148:151], a[36:39], v[80:83], a[148:151]\n"
    "v_mfma_f32_16x16x32_bf16 a[148:151], a[40:43], v[68:71], a[148:151]\n"
    "v_mfma_f32_16x16x32_bf16 a[148:151], a[44:47], v[84:87], a[148:151]\n"
    "v_mfma_f32_16x16x32_bf16 a[152:155], a[44:47], v[116:119], a[152:155]\n"
    "buffer_load_dwordx4 v241, s[64:67], s81 offen offset:1024 lds\n"
    "v_mfma_f32_16x16x32_bf16 a[152:155], a[40:43], v[100:103], a[152:155]\n"
    "ds_read_b64_tr_b16 a[16:17], v235 offset:49152\n"
    "v_mfma_f32_16x16x32_bf16 a[152:155], a[36:39], v[112:115], a[152:155]\n"
    "ds_read_b64_tr_b16 a[18:19], v235 offset:53248\n"
    "v_mfma_f32_16x16x32_bf16 a[152:155], a[32:35], v[96:99], a[152:155]\n"
    "ds_read_b64_tr_b16 a[20:21], v235 offset:57344\n"
    "s_waitcnt lgkmcnt(3)\n"
    "v_mfma_f32_16x16x32_bf16 a[156:159], a[48:51], v[0:3], a[156:159]\n"
    "v_mfma_f32_16x16x32_bf16 a[156:159], a[52:55], v[16:19], a[156:159]\n"
    "ds_read_b64_tr_b16 a[22:23], v235 offset:61440\n"
    "v_mfma_f32_16x16x32_bf16 a[156:159], a[56:59], v[4:7], a[156:159]\n"
    "ds_read_b64_tr_b16 a[24:25], v239 offset:49152\n"
    "v_mfma_f32_16x16x32_bf16 a[156:159], a[60:63], v[20:23], a[156:159]\n"
    "ds_read_b64_tr_b16 a[26:27], v239 offset:53248\n"
    "v_mfma_f32_16x16x32_bf16 a[160:163], a[60:63], v[52:55], a[160:163]\n"
    "buffer_load_dwordx4 v240, s[64:67], s82 offen offset:2048 lds\n"
    "v_mfma_f32_16x16x32_bf16 a[160:163], a[56:59], v[36:39], a[160:163]\n"
    "ds_read_b64_tr_b16 a[28:29], v239 offset:57344\n"
    "v_mfma_f32_16x16x32_bf16 a[160:163], a[52:55], v[48:51], a[160:163]\n"
    "ds_read_b64_tr_b16 a[30:31], v239 offset:61440\n"
    "v_mfma_f32_16x16x32_bf16 a[160:163], a[48:51], v[32:35], a[160:163]\n"
    "v_mfma_f32_16x16x32_bf16 a[164:167], a[48:51], v[64:67], a[164:167]\n"
    "buffer_store_dwordx4 v[80:83], v242, s[60:63], s74 offen offset:1024 sc1\n"
    "v_mfma_f32_16x16x32_bf16 a[164:167], a[52:55], v[80:83], a[164:167]\n"
    "ds_read_b64_tr_b16 a[32:33], v234 offset:49664\n"
    "v_mfma_f32_16x16x32_bf16 a[164:167], a[56:59], v[68:71], a[164:167]\n"
    "ds_read_b64_tr_b16 a[34:35], v234 offset:53760\n"
    "s_waitcnt vmcnt(18)\n"
    "s_mov_b32 s96, 0x0\n"
    "s_mov_b32 s97, 0xfc0000\n"
    "s_mov_b64 exec, s[96:97]\n"
    "buffer_store_dword v230, v228, s[68:71], 0 offen offset:0 sc1\n"
    "s_mov_b64 exec, -1\n"
    "v_mfma_f32_16x16x32_bf16 a[164:167], a[60:63], v[84:87], a[164:167]\n"
    "ds_read_b64_tr_b16 a[36:37], v234 offset:57856\n"
    "v_mfma_f32_16x16x32_bf16 a[168:171], a[0:3], v[0:3], a[168:171]\n"
    "buffer_load_dwordx4 v241, s[64:67], s82 offen offset:3072 lds\n"
    "v_mfma_f32_16x16x32_bf16 a[168:171], a[4:7], v[16:19], a[168:171]\n"
    "ds_read_b64_tr_b16 a[38:39], v234 offset:61952\n"
    "v_mfma_f32_16x16x32_bf16 a[168:171], a[8:11], v[4:7], a[168:171]\n"
    "ds_read_b64_tr_b16 a[40:41], v238 offset:49664\n"
    "v_mfma_f32_16x16x32_bf16 a[168:171], a[12:15], v[20:23], a[168:171]\n"
    "ds_read_b64_tr_b16 a[42:43], v238 offset:53760\n"
    "v_mfma_f32_16x16x32_bf16 a[172:175], a[12:15], v[52:55], a[172:175]\n"
    "v_mfma_f32_16x16x32_bf16 a[172:175], a[8:11], v[36:39], a[172:175]\n"
    "ds_read_b64_tr_b16 a[44:45], v238 offset:57856\n"
    "v_mfma_f32_16x16x32_bf16 a[172:175], a[4:7], v[48:51], a[172:175]\n"
    "ds_read_b64_tr_b16 a[46:47], v238 offset:61952\n"
    "v_mfma_f32_16x16x32_bf16 a[172:175], a[0:3], v[32:35], a[172:175]\n"
    "ds_read_b64_tr_b16 a[48:49], v235 offset:49664\n"
    "v_mfma_f32_16x16x32_bf16 a[64:67], a[0:3], v[64:67], a[64:67]\n"
    "v_mfma_f32_16x16x32_bf16 a[64:67], a[4:7], v[80:83], a[64:67]\n"
    "ds_read_b64_tr_b16 a[50:51], v235 offset:53760\n"
    "v_mfma_f32_16x16x32_bf16 a[64:67], a[8:11], v[68:71], a[64:67]\n"
    "ds_read_b64_tr_b16 a[52:53], v235 offset:57856\n"
    "v_mfma_f32_16x16x32_bf16 a[64:67], a[12:15], v[84:87], a[64:67]\n"
    "ds_read_b64_tr_b16 a[54:55], v235 offset:61952\n"
    "v_mfma_f32_16x16x32_bf16 a[68:71], a[12:15], v[116:119], a[68:71]\n"
    "buffer_store_dwordx4 v[68:71], v243, s[60:63], s74 offen offset:0 sc1\n"
    "v_mfma_f32_16x16x32_bf16 a[68:71], a[8:11], v[100:103], a[68:71]\n"
    "ds_read_b64_tr_b16 a[56:57], v239 offset:49664\n"
    "v_mfma_f32_16x16x32_bf16 a[68:71], a[4:7], v[112:115], a[68:71]\n"
    "ds_read_b64_tr_b16 a[58:59], v239 offset:53760\n"
    "v_mfma_f32_16x16x32_bf16 a[68:71], a[0:3], v[96:99], a[68:71]\n"
    "ds_read_b64_tr_b16 a[60:61], v239 offset:57856\n"
    "s_waitcnt lgkmcnt(6)\n"
    "v_mfma_f32_16x16x32_bf16 a[72:75], a[16:19], v[0:3], a[72:75]\n"
    "v_mfma_f32_16x16x32_bf16 a[72:75], a[20:23], v[16:19], a[72:75]\n"
    "ds_read_b64_tr_b16 a[62:63], v239 offset:61952\n"
    "v_mfma_f32_16x16x32_bf16 a[72:75], a[24:27], v[4:7], a[72:75]\n"
    "s_xor_b32 s88, s76, s13\n"
    "v_mfma_f32_16x16x32_bf16 a[72:75], a[28:31], v[20:23], a[72:75]\n"
    "v_mfma_f32_16x16x32_bf16 a[76:79], a[28:31], v[52:55], a[76:79]\n"
    "buffer_load_dwordx4 v[8:11], v242, s[60:63], s88 offen offset:0 sc1\n"
    "v_mfma_f32_16x16x32_bf16 a[76:79], a[24:27], v[36:39], a[76:79]\n"
    "v_mfma_f32_16x16x32_bf16 a[76:79], a[20:23], v[48:51], a[76:79]\n"
    "v_mfma_f32_16x16x32_bf16 a[76:79], a[16:19], v[32:35], a[76:79]\n"
    "s_waitcnt lgkmcnt(0)\n"
    "s_barrier\n"
    "v_mfma_f32_16x16x32_bf16 a[80:83], a[16:19], v[64:67], a[80:83]\n"
    "buffer_load_dwordx4 v[24:27], v242, s[60:63], s88 offen offset:1024 sc1\n"
    "v_mfma_f32_16x16x32_bf16 a[80:83], a[20:23], v[80:83], a[80:83]\n"
    "ds_read_b64_tr_b16 a[0:1], v232 offset:0\n"
    "v_mfma_f32_16x16x32_bf16 a[80:83], a[24:27], v[68:71], a[80:83]\n"
    "ds_read_b64_tr_b16 a[2:3], v232 offset:4096\n"
    "v_mfma_f32_16x16x32_bf16 a[80:83], a[28:31], v[84:87], a[80:83]\n"
    "ds_read_b64_tr_b16 a[4:5], v232 offset:8192\n"
    "v_mfma_f32_16x16x32_bf16 a[84:87], a[32:35], v[0:3], a[84:87]\n"
    "buffer_store_dwordx4 v[84:87], v243, s[60:63], s74 offen offset:1024 sc1\n"
    "v_mfma_f32_16x16x32_bf16 a[84:87], a[36:39], v[16:19], a[84:87]\n"
    "ds_read_b64_tr_b16 a[6:7], v232 offset:12288\n"
    "v_mfma_f32_16x16x32_bf16 a[84:87], a[40:43], v[4:7], a[84:87]\n"
    "ds_read_b64_tr_b16 a[8:9], v236 offset:0\n"
    "v_mfma_f32_16x16x32_bf16 a[84:87], a[44:47], v[20:23], a[84:87]\n"
    "ds_read_b64_tr_b16 a[10:11], v236 offset:4096\n"
    "v_mfma_f32_16x16x32_bf16 a[88:91], a[44:47], v[52:55], a[88:91]\n"
    "buffer_load_dwordx4 v[12:15], v243, s[60:63], s88 offen offset:0 sc1\n"
    "v_mfma_f32_16x16x32_bf16 a[88:91], a[40:43], v[36:39], a[88:91]\n"
    "ds_read_b64_tr_b16 a[12:13], v236 offset:8192\n"
    "v_mfma_f32_16x16x32_bf16 a[88:91], a[36:39], v[48:51], a[88:91]\n"
    "ds_read_b64_tr_b16 a[14:15], v236 offset:12288\n"
    "v_mfma_f32_16x16x32_bf16 a[88:91], a[32:35], v[32:35], a[88:91]\n"
    "v_mfma_f32_16x16x32_bf16 a[92:95], a[32:35], v[64:67], a[92:95]\n"
    "buffer_load_dwordx4 v[28:31], v243, s[60:63], s88 offen offset:1024 sc1\n"
    "v_mfma_f32_16x16x32_bf16 a[92:95], a[36:39], v[80:83], a[92:95]\n"
    "v_mfma_f32_16x16x32_bf16 a[92:95], a[40:43], v[68:71], a[92:95]\n"
    "s_xor_b32 s88, s77, s13\n"
    "buffer_load_dword v247, v246, s[68:71], s92 offen offset:0 sc1\n"
    "v_mfma_f32_16x16x32_bf16 a[92:95], a[44:47], v[84:87], a[92:95]\n"
    "v_mfma_f32_16x16x32_bf16 a[208:211], a[44:47], v[116:119], a[208:211]\n"
    "buffer_load_dwordx4 v[40:43], v242, s[60:63], s88 offen offset:0 sc1\n"
    "v_mfma_f32_16x16x32_bf16 a[208:211], a[40:43], v[100:103], a[208:211]\n"
    "ds_read_b64_tr_b16 a[16:17], v233 offset:0\n"
    "v_mfma_f32_16x16x32_bf16 a[208:211], a[36:39], v[112:115], a[208:211]\n"
    "ds_read_b64_tr_b16 a[18:19], v233 offset:4096\n"
    "v_mfma_f32_16x16x32_bf16 a[208:211], a[32:35], v[96:99], a[208:211]\n"
    "ds_read_b64_tr_b16 a[20:21], v233 offset:8192\n"
    "v_mfma_f32_16x16x32_bf16 a[212:215], a[48:51], v[0:3], a[212:215]\n"
    "buffer_load_dwordx4 v[56:59], v242, s[60:63], s88 offen offset:1024 sc1\n"
    "v_mfma_f32_16x16x32_bf16 a[212:215], a[52:55], v[16:19], a[212:215]\n"
    "ds_read_b64_tr_b16 a[22:23], v233 offset:12288\n"
    "v_mfma_f32_16x16x32_bf16 a[212:215], a[56:59], v[4:7], a[212:215]\n"
    "ds_read_b64_tr_b16 a[24:25], v237 offset:0\n"
    "v_mfma_f32_16x16x32_bf16 a[212:215], a[60:63], v[20:23], a[212:215]\n"
    "ds_read_b64_tr_b16 a[26:27], v237 offset:4096\n"
    "v_mfma_f32_16x16x32_bf16 a[248:251], a[60:63], v[52:55], a[248:251]\n"
    "buffer_load_dwordx4 v[44:47], v243, s[60:63], s88 offen offset:0 sc1\n"
    "v_mfma_f32_16x16x32_bf16 a[248:251], a[56:59], v[36:39], a[248:251]\n"
    "ds_read_b64_tr_b16 a[28:29], v237 offset:8192\n"
    "v_mfma_f32_16x16x32_bf16 a[248:251], a[52:55], v[48:51], a[248:251]\n"
    "ds_read_b64_tr_b16 a[30:31], v237 offset:12288\n"
    "v_mfma_f32_16x16x32_bf16 a[248:251], a[48:51], v[32:35], a[248:251]\n"
    "v_mfma_f32_16x16x32_bf16 a[252:255], a[48:51], v[64:67], a[252:255]\n"
    "buffer_load_dwordx4 v[60:63], v243, s[60:63], s88 offen offset:1024 sc1\n"
    "v_mfma_f32_16x16x32_bf16 a[252:255], a[52:55], v[80:83], a[252:255]\n"
    "ds_read_b64_tr_b16 a[32:33], v232 offset:512\n"
    "v_mfma_f32_16x16x32_bf16 a[252:255], a[56:59], v[68:71], a[252:255]\n"
    "ds_read_b64_tr_b16 a[34:35], v232 offset:4608\n"
    "s_xor_b32 s88, s78, s13\n"
    "v_mfma_f32_16x16x32_bf16 a[252:255], a[60:63], v[84:87], a[252:255]\n"
    "ds_read_b64_tr_b16 a[36:37], v232 offset:8704\n"
    "s_waitcnt vmcnt(24)\n"
    "s_waitcnt lgkmcnt(5)\n"
    "v_mfma_f32_16x16x32_bf16 a[176:179], a[0:3], a[216:219], a[176:179]\n"
    "buffer_load_dwordx4 v[72:75], v242, s[60:63], s88 offen offset:0 sc1\n"
    "v_mfma_f32_16x16x32_bf16 a[176:179], a[4:7], a[220:223], a[176:179]\n"
    "ds_read_b64_tr_b16 a[38:39], v232 offset:12800\n"
    "v_mfma_f32_16x16x32_bf16 a[176:179], a[8:11], a[224:227], a[176:179]\n"
    "ds_read_b64_tr_b16 a[40:41], v236 offset:512\n"
    "v_mfma_f32_16x16x32_bf16 a[176:179], a[12:15], a[228:231], a[176:179]\n"
    "ds_read_b64_tr_b16 a[42:43], v236 offset:4608\n"
    "v_mfma_f32_16x16x32_bf16 a[180:183], a[12:15], a[244:247], a[180:183]\n"
    "buffer_load_dwordx4 v[88:91], v242, s[60:63], s88 offen offset:1024 sc1\n"
    "v_mfma_f32_16x16x32_bf16 a[180:183], a[8:11], a[240:243], a[180:183]\n"
    "ds_read_b64_tr_b16 a[44:45], v236 offset:8704\n"
    "v_mfma_f32_16x16x32_bf16 a[180:183], a[4:7], a[236:239], a[180:183]\n"
    "ds_read_b64_tr_b16 a[46:47], v236 offset:12800\n"
    "v_mfma_f32_16x16x32_bf16 a[180:183], a[0:3], a[232:235], a[180:183]\n"
    "v_mfma_f32_16x16x32_bf16 a[184:187], a[0:3], a[96:99], a[184:187]\n"
    "buffer_load_dwordx4 v[76:79], v243, s[60:63], s88 offen offset:0 sc1\n"
    "v_mfma_f32_16x16x32_bf16 a[184:187], a[4:7], a[100:103], a[184:187]\n"
    "v_mfma_f32_16x16x32_bf16 a[184:187], a[8:11], a[104:107], a[184:187]\n"
    "v_mfma_f32_16x16x32_bf16 a[184:187], a[12:15], a[108:111], a[184:187]\n"
    "v_mfma_f32_16x16x32_bf16 a[188:191], a[12:15], a[124:127], a[188:191]\n"
    "buffer_load_dwordx4 v[92:95], v243, s[60:63], s88 offen offset:1024 sc1\n"
    "v_mfma_f32_16x16x32_bf16 a[188:191], a[8:11], a[120:123], a[188:191]\n"
    "ds_read_b64_tr_b16 a[48:49], v233 offset:512\n"
    "v_mfma_f32_16x16x32_bf16 a[188:191], a[4:7], a[116:119], a[188:191]\n"
    "ds_read_b64_tr_b16 a[50:51], v233 offset:4608\n"
    "s_xor_b32 s88, s79, s13\n"
    "v_mfma_f32_16x16x32_bf16 a[188:191], a[0:3], a[112:115], a[188:191]\n"
    "ds_read_b64_tr_b16 a[52:53], v233 offset:8704\n"
    "v_mfma_f32_16x16x32_bf16 a[128:131], a[16:19], a[216:219], a[128:131]\n"
    "buffer_load_dwordx4 v[104:107], v242, s[60:63], s88 offen offset:0 sc1\n"
    "v_mfma_f32_16x16x32_bf16 a[128:131], a[20:23], a[220:223], a[128:131]\n"
    "ds_read_b64_tr_b16 a[54:55], v233 offset:12800\n"
    "v_mfma_f32_16x16x32_bf16 a[128:131], a[24:27], a[224:227], a[128:131]\n"
    "ds_read_b64_tr_b16 a[56:57], v237 offset:512\n"
    "s_waitcnt lgkmcnt(5)\n"
    "v_mfma_f32_16x16x32_bf16 a[128:131], a[28:31], a[228:231], a[128:131]\n"
    "ds_read_b64_tr_b16 a[58:59], v237 offset:4608\n"
    "v_mfma_f32_16x16x32_bf16 a[132:135], a[28:31], a[244:247], a[132:135]\n"
    "buffer_load_dwordx4 v[120:123], v242, s[60:63], s88 offen offset:1024 sc1\n"
    "v_mfma_f32_16x16x32_bf16 a[132:135], a[24:27], a[240:243], a[132:135]\n"
    "ds_read_b64_tr_b16 a[60:61], v237 offset:8704\n"
    "v_mfma_f32_16x16x32_bf16 a[132:135], a[20:23], a[236:239], a[132:135]\n"
    "ds_read_b64_tr_b16 a[62:63], v237 offset:12800\n"
    "v_mfma_f32_16x16x32_bf16 a[132:135], a[16:19], a[232:235], a[132:135]\n"
    "v_mfma_f32_16x16x32_bf16 a[136:139], a[16:19], a[96:99], a[136:139]\n"
    "buffer_load_dwordx4 v[108:111], v243, s[60:63], s88 offen offset:0 sc1\n"
    "v_mfma_f32_16x16x32_bf16 a[136:139], a[20:23], a[100:103], a[136:139]\n"
    "ds_read_b64_tr_b16 a[0:1], v234 offset:0\n"
    "v_mfma_f32_16x16x32_bf16 a[136:139], a[24:27], a[104:107], a[136:139]\n"
    "ds_read_b64_tr_b16 a[2:3], v234 offset:4096\n"
    "v_mfma_f32_16x16x32_bf16 a[136:139], a[28:31], a[108:111], a[136:139]\n"
    "ds_read_b64_tr_b16 a[4:5], v234 offset:8192\n"
    "v_mfma_f32_16x16x32_bf16 a[140:143], a[32:35], a[216:219], a[140:143]\n"
    "buffer_load_dwordx4 v[124:127], v243, s[60:63], s88 offen offset:1024 sc1\n"
    "v_mfma_f32_16x16x32_bf16 a[140:143], a[36:39], a[220:223], a[140:143]\n"
    "ds_read_b64_tr_b16 a[6:7], v234 offset:12288\n"
    "v_mfma_f32_16x16x32_bf16 a[140:143], a[40:43], a[224:227], a[140:143]\n"
    "ds_read_b64_tr_b16 a[8:9], v238 offset:0\n"
    "s_xor_b32 s88, s76, s14\n"
    "v_mfma_f32_16x16x32_bf16 a[140:143], a[44:47], a[228:231], a[140:143]\n"
    "ds_read_b64_tr_b16 a[10:11], v238 offset:4096\n"
    "v_mfma_f32_16x16x32_bf16 a[144:147], a[44:47], a[244:247], a[144:147]\n"
    "buffer_load_dwordx4 v[128:131], v242, s[60:63], s88 offen offset:0 sc1\n"
    "v_mfma_f32_16x16x32_bf16 a[144:147], a[40:43], a[240:243], a[144:147]\n"
    "ds_read_b64_tr_b16 a[12:13], v238 offset:8192\n"
    "v_mfma_f32_16x16x32_bf16 a[144:147], a[36:39], a[236:239], a[144:147]\n"
    "ds_read_b64_tr_b16 a[14:15], v238 offset:12288\n"
    "v_mfma_f32_16x16x32_bf16 a[144:147], a[32:35], a[232:235], a[144:147]\n"
    "v_mfma_f32_16x16x32_bf16 a[148:151], a[32:35], a[96:99], a[148:151]\n"
    "buffer_load_dwordx4 v[132:135], v242, s[60:63], s88 offen offset:1024 sc1\n"
    "v_mfma_f32_16x16x32_bf16 a[148:151], a[36:39], a[100:103], a[148:151]\n"
    "v_mfma_f32_16x16x32_bf16 a[148:151], a[40:43], a[104:107], a[148:151]\n"
    "v_mfma_f32_16x16x32_bf16 a[148:151], a[44:47], a[108:111], a[148:151]\n"
    "v_mfma_f32_16x16x32_bf16 a[152:155], a[44:47], a[124:127], a[152:155]\n"
    "buffer_load_dwordx4 v[136:139], v243, s[60:63], s88 offen offset:0 sc1\n"
    "v_mfma_f32_16x16x32_bf16 a[152:155], a[40:43], a[120:123], a[152:155]\n"
    "ds_read_b64_tr_b16 a[16:17], v235 offset:0\n"
    "v_mfma_f32_16x16x32_bf16 a[152:155], a[36:39], a[116:119], a[152:155]\n"
    "ds_read_b64_tr_b16 a[18:19], v235 offset:4096\n"
    "v_mfma_f32_16x16x32_bf16 a[152:155], a[32:35], a[112:115], a[152:155]\n"
    "ds_read_b64_tr_b16 a[20:21], v235 offset:8192\n"
    "s_waitcnt lgkmcnt(3)\n"
    "v_mfma_f32_16x16x32_bf16 a[156:159], a[48:51], a[216:219], a[156:159]\n"
    "buffer_load_dwordx4 v[140:143], v243, s[60:63], s88 offen offset:1024 sc1\n"
    "v_mfma_f32_16x16x32_bf16 a[156:159], a[52:55], a[220:223], a[156:159]\n"
    "ds_read_b64_tr_b16 a[22:23], v235 offset:12288\n"
    "v_mfma_f32_16x16x32_bf16 a[156:159], a[56:59], a[224:227], a[156:159]\n"
    "ds_read_b64_tr_b16 a[24:25], v239 offset:0\n"
    "s_xor_b32 s88, s77, s14\n"
    "v_mfma_f32_16x16x32_bf16 a[156:159], a[60:63], a[228:231], a[156:159]\n"
    "ds_read_b64_tr_b16 a[26:27], v239 offset:4096\n"
    "s_mov_b32 s96, 0x0\n"
    "s_mov_b32 s97, 0xfc0000\n"
    "s_waitcnt vmcnt(16)\n"
    "v_cmp_eq_u32 vcc, v247, v229\n"
    "s_nop 1\n"
    "s_and_b64 vcc, vcc, s[96:97]\n"
    "s_cmp_eq_u64 vcc, s[96:97]\n"
    "s_cbranch_scc0 L_slow11\n"
    "L_back11:\n"
    "v_mfma_f32_16x16x32_bf16 a[160:163], a[60:63], a[244:247], a[160:163]\n"
    "buffer_load_dwordx4 v[144:147], v242, s[60:63], s88 offen offset:0 sc1\n"
    "v_mfma_f32_16x16x32_bf16 a[160:163], a[56:59], a[240:243], a[160:163]\n"
    "ds_read_b64_tr_b16 a[28:29], v239 offset:8192\n"
    "v_mfma_f32_16x16x32_bf16 a[160:163], a[52:55], a[236:239], a[160:163]\n"
    "ds_read_b64_tr_b16 a[30:31], v239 offset:12288\n"
    "s_xor_b32 s89, s79, s15\n"
    "v_mfma_f32_16x16x32_bf16 a[160:163], a[48:51], a[232:235], a[160:163]\n"
    "v_mfma_f32_16x16x32_bf16 a[164:167], a[48:51], a[96:99], a[164:167]\n"
    "buffer_load_dwordx4 v[96:99], v242, s[60:63], s89 offen offset:0 sc1\n"
    "v_mfma_f32_16x16x32_bf16 a[164:167], a[52:55], a[100:103], a[164:167]\n"
    "ds_read_b64_tr_b16 a[32:33], v234 offset:512\n"
    "v_mfma_f32_16x16x32_bf16 a[164:167], a[56:59], a[104:107], a[164:167]\n"
    "ds_read_b64_tr_b16 a[34:35], v234 offset:4608\n"
    "v_mfma_f32_16x16x32_bf16 a[164:167], a[60:63], a[108:111], a[164:167]\n"
    "ds_read_b64_tr_b16 a[36:37], v234 offset:8704\n"
    "v_mfma_f32_16x16x32_bf16 a[168:171], a[0:3], a[216:219], a[168:171]\n"
    "L_h15:\n"
    "s_cbranch_execz L_ret2\n"
    "buffer_load_dwordx4 v[148:151], v242, s[60:63], s88 offen offset:1024 sc1\n"
    "v_mfma_f32_16x16x32_bf16 a[168:171], a[4:7], a[220:223], a[168:171]\n"
    "ds_read_b64_tr_b16 a[38:39], v234 offset:12800\n"
    "v_mfma_f32_16x16x32_bf16 a[168:171], a[8:11], a[224:227], a[168:171]\n"
    "ds_read_b64_tr_b16 a[40:41], v238 offset:512\n"
    "v_mfma_f32_16x16x32_bf16 a[168:171], a[12:15], a[228:231], a[168:171]\n"
    "ds_read_b64_tr_b16 a[42:43], v238 offset:4608\n"
    "v_mfma_f32_16x16x32_bf16 a[172:175], a[12:15], a[244:247], a[172:175]\n"
    "buffer_load_dwordx4 v[152:155], v243, s[60:63], s88 offen offset:0 sc1\n"
    "v_mfma_f32_16x16x32_bf16 a[172:175], a[8:11], a[240:243], a[172:175]\n"
    "ds_read_b64_tr_b16 a[44:45], v238 offset:8704\n"
    "v_mfma_f32_16x16x32_bf16 a[172:175], a[4:7], a[236:239], a[172:175]\n"
    "ds_read_b64_tr_b16 a[46:47], v238 offset:12800\n"
    "s_mov_b32 s96, 0x0\n"
    "s_mov_b32 s97, 0xfc\n"
    "s_mov_b64 exec, s[96:97]\n"
    "buffer_store_dword v230, v228, s[68:71], 0 offen offset:0 sc1\n"
    "s_mov_b64 exec, -1\n"
    "v_mfma_f32_16x16x32_bf16 a[172:175], a[0:3], a[232:235], a[172:175]\n"
    "ds_read_b64_tr_b16 a[48:49], v235 offset:512\n"
    "v_mfma_f32_16x16x32_bf16 a[64:67], a[0:3], a[96:99], a[64:67]\n"
    "buffer_load_dwordx4 v[156:159], v243, s[60:63], s88 offen offset:1024 sc1\n"
    "v_mfma_f32_16x16x32_bf16 a[64:67], a[4:7], a[100:103], a[64:67]\n"
    "ds_read_b64_tr_b16 a[50:51], v235 offset:4608\n"
    "v_mfma_f32_16x16x32_bf16 a[64:67], a[8:11], a[104:107], a[64:67]\n"
    "ds_read_b64_tr_b16 a[52:53], v235 offset:8704\n"
    "v_mfma_f32_16x16x32_bf16 a[64:67], a[12:15], a[108:111], a[64:67]\n"
    "ds_read_b64_tr_b16 a[54:55], v235 offset:12800\n"
    "v_mfma_f32_16x16x32_bf16 a[68:71], a[12:15], a[124:127], a[68:71]\n"
    "buffer_load_dwordx4 v[112:115], v242, s[60:63], s89 offen offset:1024 sc1\n"
    "v_mfma_f32_16x16x32_bf16 a[68:71], a[8:11], a[120:123], a[68:71]\n"
    "ds_read_b64_tr_b16 a[56:57], v239 offset:512\n"
    "v_mfma_f32_16x16x32_bf16 a[68:71], a[4:7], a[116:119], a[68:71]\n"
    "ds_read_b64_tr_b16 a[58:59], v239 offset:4608\n"
    "s_xor_b32 s88, s78, s14\n"
    "v_mfma_f32_16x16x32_bf16 a[68:71], a[0:3], a[112:115], a[68:71]\n"
    "ds_read_b64_tr_b16 a[60:61], v239 offset:8704\n"
    "s_waitcnt lgkmcnt(6)\n"
    "v_mfma_f32_16x16x32_bf16 a[72:75], a[16:19], a[216:219], a[72:75]\n"
    "buffer_load_dwordx4 v[160:163], v242, s[60:63], s88 offen offset:0 sc1\n"
    "v_mfma_f32_16x16x32_bf16 a[72:75], a[20:23], a[220:223], a[72:75]\n"
    "ds_read_b64_tr_b16 a[62:63], v239 offset:12800\n"
    "v_mfma_f32_16x16x32_bf16 a[72:75], a[24:27], a[224:227], a[72:75]\n"
    "v_mfma_f32_16x16x32_bf16 a[72:75], a[28:31], a[228:231], a[72:75]\n"
    "v_mfma_f32_16x16x32_bf16 a[76:79], a[28:31], a[244:247], a[76:79]\n"
    "buffer_load_dwordx4 v[164:167], v242, s[60:63], s88 offen offset:1024 sc1\n"
    "v_mfma_f32_16x16x32_bf16 a[76:79], a[24:27], a[240:243], a[76:79]\n"
    "v_mfma_f32_16x16x32_bf16 a[76:79], a[20:23], a[236:239], a[76:79]\n"
    "v_mfma_f32_16x16x32_bf16 a[76:79], a[16:19], a[232:235], a[76:79]\n"
    "s_waitcnt lgkmcnt(0)\n"
    "s_barrier\n"
    "v_mfma_f32_16x16x32_bf16 a[80:83], a[16:19], a[96:99], a[80:83]\n"
    "buffer_load_dwordx4 v[168:171], v243, s[60:63], s88 offen offset:0 sc1\n"
    "v_mfma_f32_16x16x32_bf16 a[80:83], a[20:23], a[100:103], a[80:83]\n"
    "ds_read_b64_tr_b16 a[0:1], v232 offset:16384\n"
    "v_mfma_f32_16x16x32_bf16 a[80:83], a[24:27], a[104:107], a[80:83]\n"
    "ds_read_b64_tr_b16 a[2:3], v232 offset:20480\n"
    "v_mfma_f32_16x16x32_bf16 a[80:83], a[28:31], a[108:111], a[80:83]\n"
    "ds_read_b64_tr_b16 a[4:5], v232 offset:24576\n"
    "v_mfma_f32_16x16x32_bf16 a[84:87], a[32:35], a[216:219], a[84:87]\n"
    "buffer_load_dwordx4 v[100:103], v243, s[60:63], s89 offen offset:0 sc1\n"
    "v_mfma_f32_16x16x32_bf16 a[84:87], a[36:39], a[220:223], a[84:87]\n"
    "ds_read_b64_tr_b16 a[6:7], v232 offset:28672\n"
    "v_mfma_f32_16x16x32_bf16 a[84:87], a[40:43], a[224:227], a[84:87]\n"
    "ds_read_b64_tr_b16 a[8:9], v236 offset:16384\n"
    "v_mfma_f32_16x16x32_bf16 a[84:87], a[44:47], a[228:231], a[84:87]\n"
    "ds_read_b64_tr_b16 a[10:11], v236 offset:20480\n"
    "v_mfma_f32_16x16x32_bf16 a[88:91], a[44:47], a[244:247], a[88:91]\n"
    "buffer_load_dwordx4 v[172:175], v243, s[60:63], s88 offen offset:1024 sc1\n"
    "v_mfma_f32_16x16x32_bf16 a[88:91], a[40:43], a[240:243], a[88:91]\n"
    "ds_read_b64_tr_b16 a[12:13], v236 offset:24576\n"
    "v_mfma_f32_16x16x32_bf16 a[88:91], a[36:39], a[236:239], a[88:91]\n"
    "ds_read_b64_tr_b16 a[14:15], v236 offset:28672\n"
    "s_xor_b32 s88, s79, s14\n"
    "v_mfma_f32_16x16x32_bf16 a[88:91], a[32:35], a[232:235], a[88:91]\n"
    "v_mfma_f32_16x16x32_bf16 a[92:95], a[32:35], a[96:99], a[92:95]\n"
    "buffer_load_dwordx4 v[176:179], v242, s[60:63], s88 offen offset:0 sc1\n"
    "v_mfma_f32_16x16x32_bf16 a[92:95], a[36:39], a[100:103], a[92:95]\n"
    "v_mfma_f32_16x16x32_bf16 a[92:95], a[40:43], a[104:107], a[92:95]\n"
    "v_mfma_f32_16x16x32_bf16 a[92:95], a[44:47], a[108:111], a[92:95]\n"
    "v_mfma_f32_16x16x32_bf16 a[208:211], a[44:47], a[124:127], a[208:211]\n"
    "buffer_load_dwordx4 v[180:183], v242, s[60:63], s88 offen offset:1024 sc1\n"
    "v_mfma_f32_16x16x32_bf16 a[208:211], a[40:43], a[120:123], a[208:211]\n"
    "ds_read_b64_tr_b16 a[16:17], v233 offset:16384\n"
    "v_mfma_f32_16x16x32_bf16 a[208:211], a[36:39], a[116:119], a[208:211]\n"
    "ds_read_b64_tr_b16 a[18:19], v233 offset:20480\n"
    "v_mfma_f32_16x16x32_bf16 a[208:211], a[32:35], a[112:115], a[208:211]\n"
    "ds_read_b64_tr_b16 a[20:21], v233 offset:24576\n"
    "v_mfma_f32_16x16x32_bf16 a[212:215], a[48:51], a[216:219], a[212:215]\n"
    "buffer_load_dwordx4 v[116:119], v243, s[60:63], s89 offen offset:1024 sc1\n"
    "v_mfma_f32_16x16x32_bf16 a[212:215], a[52:55], a[220:223], a[212:215]\n"
    "ds_read_b64_tr_b16 a[22:23], v233 offset:28672\n"
    "v_mfma_f32_16x16x32_bf16 a[212:215], a[56:59], a[224:227], a[212:215]\n"
    "ds_read_b64_tr_b16 a[24:25], v237 offset:16384\n"
    "v_mfma_f32_16x16x32_bf16 a[212:215], a[60:63], a[228:231], a[212:215]\n"
    "ds_read_b64_tr_b16 a[26:27], v237 offset:20480\n"
    "v_mfma_f32_16x16x32_bf16 a[248:251], a[60:63], a[244:247], a[248:251]\n"
    "buffer_load_dwordx4 v[184:187], v243, s[60:63], s88 offen offset:0 sc1\n"
    "v_mfma_f32_16x16x32_bf16 a[248:251], a[56:59], a[240:243], a[248:251]\n"
    "ds_read_b64_tr_b16 a[28:29], v237 offset:24576\n"
    "v_mfma_f32_16x16x32_bf16 a[248:251], a[52:55], a[236:239], a[248:251]\n"
    "ds_read_b64_tr_b16 a[30:31], v237 offset:28672\n"
    "v_mfma_f32_16x16x32_bf16 a[248:251], a[48:51], a[232:235], a[248:251]\n"
    "v_mfma_f32_16x16x32_bf16 a[252:255], a[48:51], a[96:99], a[252:255]\n"
    "buffer_load_dwordx4 v[188:191], v243, s[60:63], s88 offen offset:1024 sc1\n"
    "v_mfma_f32_16x16x32_bf16 a[252:255], a[52:55], a[100:103], a[252:255]\n"
    "ds_read_b64_tr_b16 a[32:33], v232 offset:16896\n"
    "v_mfma_f32_16x16x32_bf16 a[252:255], a[56:59], a[104:107], a[252:255]\n"
    "ds_read_b64_tr_b16 a[34:35], v232 offset:20992\n"
    "s_add_u32 m0, s83, 49152\n"
    "v_mfma_f32_16x16x32_bf16 a[252:255], a[60:63], a[108:111], a[252:255]\n"
    "ds_read_b64_tr_b16 a[36:37], v232 offset:25088\n"
    "s_xor_b32 s81, s93, 393216\n"
    "s_add_u32 s82, s81, 14336\n"
    "s_waitcnt vmcnt(21)\n"
    "s_waitcnt lgkmcnt(5)\n"
    "v_mfma_f32_16x16x32_bf16 a[176:179], a[0:3], v[8:11], a[176:179]\n"
    "buffer_load_dwordx4 v240, s[64:67], s81 offen offset:0 lds\n"
    "buffer_load_dword v231, v246, s[68:71], s92 offen offset:0 sc1\n"
    "v_mfma_f32_16x16x32_bf16 a[176:179], a[4:7], v[24:27], a[176:179]\n"
    "ds_read_b64_tr_b16 a[38:39], v232 offset:29184\n"
    "v_mfma_f32_16x16x32_bf16 a[176:179], a[8:11], v[12:15], a[176:179]\n"
    "ds_read_b64_tr_b16 a[40:41], v236 offset:16896\n"
    "s_xor_b32 s89, s76, s15\n"
    "v_mfma_f32_16x16x32_bf16 a[176:179], a[12:15], v[28:31], a[176:179]\n"
    "ds_read_b64_tr_b16 a[42:43], v236 offset:20992\n"
    "v_mfma_f32_16x16x32_bf16 a[180:183], a[12:15], v[60:63], a[180:183]\n"
    "buffer_load_dwordx4 v[0:3], v242, s[60:63], s89 offen offset:0 sc1\n"
    "v_mfma_f32_16x16x32_bf16 a[180:183], a[8:11], v[44:47], a[180:183]\n"
    "ds_read_b64_tr_b16 a[44:45], v236 offset:25088\n"
    "v_mfma_f32_16x16x32_bf16 a[180:183], a[4:7], v[56:59], a[180:183]\n"
    "ds_read_b64_tr_b16 a[46:47], v236 offset:29184\n"
    "v_mfma_f32_16x16x32_bf16 a[180:183], a[0:3], v[40:43], a[180:183]\n"
    "v_mfma_f32_16x16x32_bf16 a[184:187], a[0:3], v[72:75], a[184:187]\n"
    "buffer_load_dwordx4 v241, s[64:67], s81 offen offset:1024 lds\n"
    "v_mfma_f32_16x16x32_bf16 a[184:187], a[4:7], v[88:91], a[184:187]\n"
    "v_mfma_f32_16x16x32_bf16 a[184:187], a[8:11], v[76:79], a[184:187]\n"
    "v_mfma_f32_16x16x32_bf16 a[184:187], a[12:15], v[92:95], a[184:187]\n"
    "v_mfma_f32_16x16x32_bf16 a[188:191], a[12:15], v[124:127], a[188:191]\n"
    "buffer_load_dwordx4 v240, s[64:67], s82 offen offset:2048 lds\n"
    "v_mfma_f32_16x16x32_bf16 a[188:191], a[8:11], v[108:111], a[188:191]\n"
    "ds_read_b64_tr_b16 a[48:49], v233 offset:16896\n"
    "v_mfma_f32_16x16x32_bf16 a[188:191], a[4:7], v[120:123], a[188:191]\n"
    "ds_read_b64_tr_b16 a[50:51], v233 offset:20992\n"
    "v_mfma_f32_16x16x32_bf16 a[188:191], a[0:3], v[104:107], a[188:191]\n"
    "ds_read_b64_tr_b16 a[52:53], v233 offset:25088\n"
    "v_mfma_f32_16x16x32_bf16 a[128:131], a[16:19], v[8:11], a[128:131]\n"
    "buffer_load_dwordx4 v241, s[64:67], s82 offen offset:3072 lds\n"
    "v_mfma_f32_16x16x32_bf16 a[128:131], a[20:23], v[24:27], a[128:131]\n"
    "ds_read_b64_tr_b16 a[54:55], v233 offset:29184\n"
    "v_mfma_f32_16x16x32_bf16 a[128:131], a[24:27], v[12:15], a[128:131]\n"
    "ds_read_b64_tr_b16 a[56:57], v237 offset:16896\n"
    "s_waitcnt lgkmcnt(5)\n"
    "v_mfma_f32_16x16x32_bf16 a[128:131], a[28:31], v[28:31], a[128:131]\n"
    "ds_read_b64_tr_b16 a[58:59], v237 offset:20992\n"
    "v_mfma_f32_16x16x32_bf16 a[132:135], a[28:31], v[60:63], a[132:135]\n"
    "buffer_load_dwordx4 v[16:19], v242, s[60:63], s89 offen offset:1024 sc1\n"
    "v_mfma_f32_16x16x32_bf16 a[132:135], a[24:27], v[44:47], a[132:135]\n"
    "ds_read_b64_tr_b16 a[60:61], v237 offset:25088\n"
    "v_mfma_f32_16x16x32_bf16 a[132:135], a[20:23], v[56:59], a[132:135]\n"
    "ds_read_b64_tr_b16 a[62:63], v237 offset:29184\n"
    "s_add_u32 m0, s83, 114688\n"
    "v_mfma_f32_16x16x32_bf16 a[132:135], a[16:19], v[40:43], a[132:135]\n"
    "s_xor_b32 s81, s93, 393344\n"
    "s_add_u32 s82, s81, 14336\n"
    "v_mfma_f32_16x16x32_bf16 a[136:139], a[16:19], v[72:75], a[136:139]\n"
    "buffer_load_dwordx4 v240, s[64:67], s81 offen offset:0 lds\n"
    "v_mfma_f32_16x16x32_bf16 a[136:139], a[20:23], v[88:91], a[136:139]\n"
    "ds_read_b64_tr_b16 a[0:1], v234 offset:16384\n"
    "v_mfma_f32_16x16x32_bf16 a[136:139], a[24:27], v[76:79], a[136:139]\n"
    "ds_read_b64_tr_b16 a[2:3], v234 offset:20480\n"
    "v_mfma_f32_16x16x32_bf16 a[136:139], a[28:31], v[92:95], a[136:139]\n"
    "ds_read_b64_tr_b16 a[4:5], v234 offset:24576\n"
    "v_mfma_f32_16x16x32_bf16 a[140:143], a[32:35], v[8:11], a[140:143]\n"
    "buffer_load_dwordx4 v241, s[64:67], s81 offen offset:1024 lds\n"
    "v_mfma_f32_16x16x32_bf16 a[140:143], a[36:39], v[24:27], a[140:143]\n"
    "ds_read_b64_tr_b16 a[6:7], v234 offset:28672\n"
    "v_mfma_f32_16x16x32_bf16 a[140:143], a[40:43], v[12:15], a[140:143]\n"
    "ds_read_b64_tr_b16 a[8:9], v238 offset:16384\n"
    "v_mfma_f32_16x16x32_bf16 a[140:143], a[44:47], v[28:31], a[140:143]\n"
    "ds_read_b64_tr_b16 a[10:11], v238 offset:20480\n"
    "v_mfma_f32_16x16x32_bf16 a[144:147], a[44:47], v[60:63], a[144:147]\n"
    "buffer_load_dwordx4 v240, s[64:67], s82 offen offset:2048 lds\n"
    "v_mfma_f32_16x16x32_bf16 a[144:147], a[40:43], v[44:47], a[144:147]\n"
    "ds_read_b64_tr_b16 a[12:13], v238 offset:24576\n"
    "v_mfma_f32_16x16x32_bf16 a[144:147], a[36:39], v[56:59], a[144:147]\n"
    "ds_read_b64_tr_b16 a[14:15], v238 offset:28672\n"
    "v_mfma_f32_16x16x32_bf16 a[144:147], a[32:35], v[40:43], a[144:147]\n"
    "v_mfma_f32_16x16x32_bf16 a[148:151], a[32:35], v[72:75], a[148:151]\n"
    "buffer_load_dwordx4 v[4:7], v243, s[60:63], s89 offen offset:0 sc1\n"
    "v_mfma_f32_16x16x32_bf16 a[148:151], a[36:39], v[88:91], a[148:151]\n"
    "v_mfma_f32_16x16x32_bf16 a[148:151], a[40:43], v[76:79], a[148:151]\n"
    "v_mfma_f32_16x16x32_bf16 a[148:151], a[44:47], v[92:95], a[148:151]\n"
    "v_mfma_f32_16x16x32_bf16 a[152:155], a[44:47], v[124:127], a[152:155]\n"
    "buffer_load_dwordx4 v241, s[64:67], s82 offen offset:3072 lds\n"
    "v_mfma_f32_16x16x32_bf16 a[152:155], a[40:43], v[108:111], a[152:155]\n"
    "ds_read_b64_tr_b16 a[16:17], v235 offset:16384\n"
    "s_waitcnt lgkmcnt(2)\n"
    "v_mfma_f32_16x16x32_bf16 a[152:155], a[36:39], v[120:123], a[152:155]\n"
    "ds_read_b64_tr_b16 a[18:19], v235 offset:20480\n"
    "s_add_u32 m0, s83, 0\n"
    "v_mfma_f32_16x16x32_bf16 a[152:155], a[32:35], v[104:107], a[152:155]\n"
    "ds_read_b64_tr_b16 a[20:21], v235 offset:24576\n"
    "s_xor_b32 s81, s93, 655360\n"
    "s_add_u32 s82, s81, 14336\n"
    "v_mfma_f32_16x16x32_bf16 a[156:159], a[48:51], v[8:11], a[156:159]\n"
    "buffer_load_dwordx4 v240, s[64:67], s81 offen offset:0 lds\n"
    "v_mfma_f32_16x16x32_bf16 a[156:159], a[52:55], v[24:27], a[156:159]\n"
    "ds_read_b64_tr_b16 a[22:23], v235 offset:28672\n"
    "v_mfma_f32_16x16x32_bf16 a[156:159], a[56:59], v[12:15], a[156:159]\n"
    "ds_read_b64_tr_b16 a[24:25], v239 offset:16384\n"
    "v_mfma_f32_16x16x32_bf16 a[156:159], a[60:63], v[28:31], a[156:159]\n"
    "ds_read_b64_tr_b16 a[26:27], v239 offset:20480\n"
    "v_mfma_f32_16x16x32_bf16 a[160:163], a[60:63], v[60:63], a[160:163]\n"
    "buffer_load_dwordx4 v241, s[64:67], s81 offen offset:1024 lds\n"
    "v_mfma_f32_16x16x32_bf16 a[160:163], a[56:59], v[44:47], a[160:163]\n"
    "ds_read_b64_tr_b16 a[28:29], v239 offset:24576\n"
    "v_mfma_f32_16x16x32_bf16 a[160:163], a[52:55], v[56:59], a[160:163]\n"
    "ds_read_b64_tr_b16 a[30:31], v239 offset:28672\n"
    "v_mfma_f32_16x16x32_bf16 a[160:163], a[48:51], v[40:43], a[160:163]\n"
    "v_mfma_f32_16x16x32_bf16 a[164:167], a[48:51], v[72:75], a[164:167]\n"
    "buffer_load_dwordx4 v[20:23], v243, s[60:63], s89 offen offset:1024 sc1\n"
    "v_mfma_f32_16x16x32_bf16 a[164:167], a[52:55], v[88:91], a[164:167]\n"
    "ds_read_b64_tr_b16 a[32:33], v234 offset:16896\n"
    "v_mfma_f32_16x16x32_bf16 a[164:167], a[56:59], v[76:79], a[164:167]\n"
    "ds_read_b64_tr_b16 a[34:35], v234 offset:20992\n"
    "v_mfma_f32_16x16x32_bf16 a[164:167], a[60:63], v[92:95], a[164:167]\n"
    "ds_read_b64_tr_b16 a[36:37], v234 offset:25088\n"
    "v_mfma_f32_16x16x32_bf16 a[168:171], a[0:3], v[8:11], a[168:171]\n"
    "buffer_load_dwordx4 v240, s[64:67], s82 offen offset:2048 lds\n"
    "v_mfma_f32_16x16x32_bf16 a[168:171], a[4:7], v[24:27], a[168:171]\n"
    "ds_read_b64_tr_b16 a[38:39], v234 offset:29184\n"
    "v_mfma_f32_16x16x32_bf16 a[168:171], a[8:11], v[12:15], a[168:171]\n"
    "ds_read_b64_tr_b16 a[40:41], v238 offset:16896\n"
    "s_waitcnt lgkmcnt(5)\n"
    "v_mfma_f32_16x16x32_bf16 a[168:171], a[12:15], v[28:31], a[168:171]\n"
    "ds_read_b64_tr_b16 a[42:43], v238 offset:20992\n"
    "v_mfma_f32_16x16x32_bf16 a[172:175], a[12:15], v[60:63], a[172:175]\n"
    "buffer_load_dwordx4 v241, s[64:67], s82 offen offset:3072 lds\n"
    "v_mfma_f32_16x16x32_bf16 a[172:175], a[8:11], v[44:47], a[172:175]\n"
    "ds_read_b64_tr_b16 a[44:45], v238 offset:25088\n"
    "v_mfma_f32_16x16x32_bf16 a[172:175], a[4:7], v[56:59], a[172:175]\n"
    "ds_read_b64_tr_b16 a[46:47], v238 offset:29184\n"
    "s_add_u32 m0, s83, 65536\n"
    "v_mfma_f32_16x16x32_bf16 a[172:175], a[0:3], v[40:43], a[172:175]\n"
    "ds_read_b64_tr_b16 a[48:49], v235 offset:16896\n"
    "s_xor_b32 s81, s93, 655488\n"
    "s_add_u32 s82, s81, 14336\n"
    "v_mfma_f32_16x16x32_bf16 a[64:67], a[0:3], v[72:75], a[64:67]\n"
    "buffer_load_dwordx4 v240, s[64:67], s81 offen offset:0 lds\n"
    "v_mfma_f32_16x16x32_bf16 a[64:67], a[4:7], v[88:91], a[64:67]\n"
    "ds_read_b64_tr_b16 a[50:51], v235 offset:20992\n"
    "v_mfma_f32_16x16x32_bf16 a[64:67], a[8:11], v[76:79], a[64:67]\n"
    "ds_read_b64_tr_b16 a[52:53], v235 offset:25088\n"
    "v_mfma_f32_16x16x32_bf16 a[64:67], a[12:15], v[92:95], a[64:67]\n"
    "ds_read_b64_tr_b16 a[54:55], v235 offset:29184\n"
    "v_mfma_f32_16x16x32_bf16 a[68:71], a[12:15], v[124:127], a[68:71]\n"
    "buffer_load_dwordx4 v241, s[64:67], s81 offen offset:1024 lds\n"
    "v_mfma_f32_16x16x32_bf16 a[68:71], a[8:11], v[108:111], a[68:71]\n"
    "ds_read_b64_tr_b16 a[56:57], v239 offset:16896\n"
    "v_mfma_f32_16x16x32_bf16 a[68:71], a[4:7], v[120:123], a[68:71]\n"
    "ds_read_b64_tr_b16 a[58:59], v239 offset:20992\n"
    "s_mov_b32 s96, 0x0\n"
    "s_mov_b32 s97, 0xfc\n"
    "s_waitcnt vmcnt(17)\n"
    "v_cmp_eq_u32 vcc, v231, v229\n"
    "s_nop 1\n"
    "s_and_b64 vcc, vcc, s[96:97]\n"
    "s_cmp_eq_u64 vcc, s[96:97]\n"
    "s_cbranch_scc0 L_slow2\n"
    "L_back2:\n"
    "v_mfma_f32_16x16x32_bf16 a[68:71], a[0:3], v[104:107], a[68:71]\n"
    "ds_read_b64_tr_b16 a[60:61], v239 offset:25088\n"
    "v_mfma_f32_16x16x32_bf16 a[72:75], a[16:19], v[8:11], a[72:75]\n"
    "buffer_load_dwordx4 v240, s[64:67], s82 offen offset:2048 lds\n"
    "v_mfma_f32_16x16x32_bf16 a[72:75], a[20:23], v[24:27], a[72:75]\n"
    "ds_read_b64_tr_b16 a[62:63], v239 offset:29184\n"
    "v_mfma_f32_16x16x32_bf16 a[72:75], a[24:27], v[12:15], a[72:75]\n"
    "s_xor_b32 s89, s77, s15\n"
    "v_mfma_f32_16x16x32_bf16 a[72:75], a[28:31], v[28:31], a[72:75]\n"
    "v_mfma_f32_16x16x32_bf16 a[76:79], a[28:31], v[60:63], a[76:79]\n"
    "buffer_load_dwordx4 v[32:35], v242, s[60:63], s89 offen offset:0 sc1\n"
    "v_mfma_f32_16x16x32_bf16 a[76:79], a[24:27], v[44:47], a[76:79]\n"
    "v_mfma_f32_16x16x32_bf16 a[76:79], a[20:23], v[56:59], a[76:79]\n"
    "v_mfma_f32_16x16x32_bf16 a[76:79], a[16:19], v[40:43], a[76:79]\n"
    "s_waitcnt lgkmcnt(0)\n"
    "s_barrier\n"
    "v_mfma_f32_16x16x32_bf16 a[80:83], a[16:19], v[72:75], a[80:83]\n"
    "buffer_load_dwordx4 v241, s[64:67], s82 offen offset:3072 lds\n"
    "v_mfma_f32_16x16x32_bf16 a[80:83], a[20:23], v[88:91], a[80:83]\n"
    "ds_read_b64_tr_b16 a[0:1], v232 offset:32768\n"
    "v_mfma_f32_16x16x32_bf16 a[80:83], a[24:27], v[76:79], a[80:83]\n"
    "ds_read_b64_tr_b16 a[2:3], v232 offset:36864\n"
    "v_mfma_f32_16x16x32_bf16 a[80:83], a[28:31], v[92:95], a[80:83]\n"
    "ds_read_b64_tr_b16 a[4:5], v232 offset:40960\n"
    "v_mfma_f32_16x16x32_bf16 a[84:87], a[32:35], v[8:11], a[84:87]\n"
    "buffer_load_dwordx4 v[48:51], v242, s[60:63], s89 offen offset:1024 sc1\n"
    "v_mfma_f32_16x16x32_bf16 a[84:87], a[36:39], v[24:27], a[84:87]\n"
    "ds_read_b64_tr_b16 a[6:7], v232 offset:45056\n"
    "v_mfma_f32_16x16x32_bf16 a[84:87], a[40:43], v[12:15], a[84:87]\n"
    "ds_read_b64_tr_b16 a[8:9], v236 offset:32768\n"
    "s_xor_b32 s88, s79, s16\n"
    "v_mfma_f32_16x16x32_bf16 a[84:87], a[44:47], v[28:31], a[84:87]\n"
    "ds_read_b64_tr_b16 a[10:11], v236 offset:36864\n"
    "v_mfma_f32_16x16x32_bf16 a[88:91], a[44:47], v[60:63], a[88:91]\n"
    "buffer_load_dwordx4 a[112:115], v242, s[60:63], s88 offen offset:0 sc1\n"
    "v_mfma_f32_16x16x32_bf16 a[88:91], a[40:43], v[44:47], a[88:91]\n"
    "ds_read_b64_tr_b16 a[12:13], v236 offset:40960\n"
    "v_mfma_f32_16x16x32_bf16 a[88:91], a[36:39], v[56:59], a[88:91]\n"
    "ds_read_b64_tr_b16 a[14:15], v236 offset:45056\n"
    "v_mfma_f32_16x16x32_bf16 a[88:91], a[32:35], v[40:43], a[88:91]\n"
    "v_mfma_f32_16x16x32_bf16 a[92:95], a[32:35], v[72:75], a[92:95]\n"
    "buffer_load_dwordx4 v[36:39], v243, s[60:63], s89 offen offset:0 sc1\n"
    "v_mfma_f32_16x16x32_bf16 a[92:95], a[36:39], v[88:91], a[92:95]\n"
    "v_mfma_f32_16x16x32_bf16 a[92:95], a[40:43], v[76:79], a[92:95]\n"
    "v_mfma_f32_16x16x32_bf16 a[92:95], a[44:47], v[92:95], a[92:95]\n"
    "v_mfma_f32_16x16x32_bf16 a[208:211], a[44:47], v[124:127], a[208:211]\n"
    "buffer_load_dwordx4 a[116:119], v242, s[60:63], s88 offen offset:1024 sc1\n"
    "v_mfma_f32_16x16x32_bf16 a[208:211], a[40:43], v[108:111], a[208:211]\n"
    "ds_read_b64_tr_b16 a[16:17], v233 offset:32768\n"
    "v_mfma_f32_16x16x32_bf16 a[208:211], a[36:39], v[120:123], a[208:211]\n"
    "ds_read_b64_tr_b16 a[18:19], v233 offset:36864\n"
    "v_mfma_f32_16x16x32_bf16 a[208:211], a[32:35], v[104:107], a[208:211]\n"
    "ds_read_b64_tr_b16 a[20:21], v233 offset:40960\n"
    "v_mfma_f32_16x16x32_bf16 a[212:215], a[48:51], v[8:11], a[212:215]\n"
    "buffer_load_dwordx4 v[52:55], v243, s[60:63], s89 offen offset:1024 sc1\n"
    "v_mfma_f32_16x16x32_bf16 a[212:215], a[52:55], v[24:27], a[212:215]\n"
    "ds_read_b64_tr_b16 a[22:23], v233 offset:45056\n"
    "v_mfma_f32_16x16x32_bf16 a[212:215], a[56:59], v[12:15], a[212:215]\n"
    "ds_read_b64_tr_b16 a[24:25], v237 offset:32768\n"
    "v_mfma_f32_16x16x32_bf16 a[212:215], a[60:63], v[28:31], a[212:215]\n"
    "ds_read_b64_tr_b16 a[26:27], v237 offset:36864\n"
    "v_mfma_f32_16x16x32_bf16 a[248:251], a[60:63], v[60:63], a[248:251]\n"
    "buffer_load_dwordx4 a[120:123], v243, s[60:63], s88 offen offset:0 sc1\n"
    "v_mfma_f32_16x16x32_bf16 a[248:251], a[56:59], v[44:47], a[248:251]\n"
    "ds_read_b64_tr_b16 a[28:29], v237 offset:40960\n"
    "v_mfma_f32_16x16x32_bf16 a[248:251], a[52:55], v[56:59], a[248:251]\n"
    "ds_read_b64_tr_b16 a[30:31], v237 offset:45056\n"
    "s_xor_b32 s89, s78, s15\n"
    "v_mfma_f32_16x16x32_bf16 a[248:251], a[48:51], v[40:43], a[248:251]\n"
    "v_mfma_f32_16x16x32_bf16 a[252:255], a[48:51], v[72:75], a[252:255]\n"
    "buffer_load_dwordx4 v[64:67], v242, s[60:63], s89 offen offset:0 sc1\n"
    "v_mfma_f32_16x16x32_bf16 a[252:255], a[52:55], v[88:91], a[252:255]\n"
    "ds_read_b64_tr_b16 a[32:33], v232 offset:33280\n"
    "v_mfma_f32_16x16x32_bf16 a[252:255], a[56:59], v[76:79], a[252:255]\n"
    "ds_read_b64_tr_b16 a[34:35], v232 offset:37376\n"
    "s_waitcnt lgkmcnt(5)\n"
    "v_mfma_f32_16x16x32_bf16 a[252:255], a[60:63], v[92:95], a[252:255]\n"
    "ds_read_b64_tr_b16 a[36:37], v232 offset:41472\n"
    "v_mfma_f32_16x16x32_bf16 a[176:179], a[0:3], v[128:131], a[176:179]\n"
    "buffer_load_dwordx4 a[124:127], v243, s[60:63], s88 offen offset:1024 sc1\n"
    "s_lshl_b32 s11, s40, 15\n"
    "v_mfma_f32_16x16x32_bf16 a[176:179], a[4:7], v[132:135], a[176:179]\n"
    "ds_read_b64_tr_b16 a[38:39], v232 offset:45568\n"
    "s_xor_b32 s11, s11, s8\n"
    "v_mfma_f32_16x16x32_bf16 a[176:179], a[8:11], v[136:139], a[176:179]\n"
    "ds_read_b64_tr_b16 a[40:41], v236 offset:33280\n"
    "s_xor_b32 s11, s11, s9\n"
    "v_mfma_f32_16x16x32_bf16 a[176:179], a[12:15], v[140:143], a[176:179]\n"
    "ds_read_b64_tr_b16 a[42:43], v236 offset:37376\n"
    "s_xor_b32 s11, s11, 262144\n"
    "v_mfma_f32_16x16x32_bf16 a[180:183], a[12:15], v[156:159], a[180:183]\n"
    "buffer_load_dwordx4 v[80:83], v242, s[60:63], s89 offen offset:1024 sc1\n"
    "v_mfma_f32_16x16x32_bf16 a[180:183], a[8:11], v[152:155], a[180:183]\n"
    "ds_read_b64_tr_b16 a[44:45], v236 offset:41472\n"
    "v_mfma_f32_16x16x32_bf16 a[180:183], a[4:7], v[148:151], a[180:183]\n"
    "ds_read_b64_tr_b16 a[46:47], v236 offset:45568\n"
    "s_xor_b32 s88, s76, s16\n"
    "v_mfma_f32_16x16x32_bf16 a[180:183], a[0:3], v[144:147], a[180:183]\n"
    "v_mfma_f32_16x16x32_bf16 a[184:187], a[0:3], v[160:163], a[184:187]\n"
    "buffer_load_dwordx4 a[216:219], v242, s[60:63], s88 offen offset:0 sc1\n"
    "v_mfma_f32_16x16x32_bf16 a[184:187], a[4:7], v[164:167], a[184:187]\n"
    "v_mfma_f32_16x16x32_bf16 a[184:187], a[8:11], v[168:171], a[184:187]\n"
    "v_mfma_f32_16x16x32_bf16 a[184:187], a[12:15], v[172:175], a[184:187]\n"
    "v_mfma_f32_16x16x32_bf16 a[188:191], a[12:15], v[188:191], a[188:191]\n"
    "buffer_load_dwordx4 v[68:71], v243, s[60:63], s89 offen offset:0 sc1\n"
    "v_mfma_f32_16x16x32_bf16 a[188:191], a[8:11], v[184:187], a[188:191]\n"
    "ds_read_b64_tr_b16 a[48:49], v233 offset:33280\n"
    "v_mfma_f32_16x16x32_bf16 a[188:191], a[4:7], v[180:183], a[188:191]\n"
    "ds_read_b64_tr_b16 a[50:51], v233 offset:37376\n"
    "v_mfma_f32_16x16x32_bf16 a[188:191], a[0:3], v[176:179], a[188:191]\n"
    "ds_read_b64_tr_b16 a[52:53], v233 offset:41472\n"
    "v_mfma_f32_16x16x32_bf16 a[128:131], a[16:19], v[128:131], a[128:131]\n"
    "buffer_load_dwordx4 a[220:223], v242, s[60:63], s88 offen offset:1024 sc1\n"
    "v_mfma_f32_16x16x32_bf16 a[128:131], a[20:23], v[132:135], a[128:131]\n"
    "ds_read_b64_tr_b16 a[54:55], v233 offset:45568\n"
    "s_waitcnt lgkmcnt(4)\n"
    "v_mfma_f32_16x16x32_bf16 a[128:131], a[24:27], v[136:139], a[128:131]\n"
    "ds_read_b64_tr_b16 a[56:57], v237 offset:33280\n"
    "v_mfma_f32_16x16x32_bf16 a[128:131], a[28:31], v[140:143], a[128:131]\n"
    "ds_read_b64_tr_b16 a[58:59], v237 offset:37376\n"
    "v_mfma_f32_16x16x32_bf16 a[132:135], a[28:31], v[156:159], a[132:135]\n"
    "buffer_load_dwordx4 v[84:87], v243, s[60:63], s89 offen offset:1024 sc1\n"
    "v_mfma_f32_16x16x32_bf16 a[132:135], a[24:27], v[152:155], a[132:135]\n"
    "ds_read_b64_tr_b16 a[60:61], v237 offset:41472\n"
    "v_mfma_f32_16x16x32_bf16 a[132:135], a[20:23], v[148:151], a[132:135]\n"
    "ds_read_b64_tr_b16 a[62:63], v237 offset:45568\n"
    "v_mfma_f32_16x16x32_bf16 a[132:135], a[16:19], v[144:147], a[132:135]\n"
    "v_mfma_f32_16x16x32_bf16 a[136:139], a[16:19], v[160:163], a[136:139]\n"
    "buffer_load_dwordx4 a[224:227], v243, s[60:63], s88 offen offset:0 sc1\n"
    "v_mfma_f32_16x16x32_bf16 a[136:139], a[20:23], v[164:167], a[136:139]\n"
    "ds_read_b64_tr_b16 a[0:1], v234 offset:32768\n"
    "v_mfma_f32_16x16x32_bf16 a[136:139], a[24:27], v[168:171], a[136:139]\n"
    "ds_read_b64_tr_b16 a[2:3], v234 offset:36864\n"
    "v_mfma_f32_16x16x32_bf16 a[136:139], a[28:31], v[172:175], a[136:139]\n"
    "ds_read_b64_tr_b16 a[4:5], v234 offset:40960\n"
    "v_mfma_f32_16x16x32_bf16 a[140:143], a[32:35], v[128:131], a[140:143]\n"
    "buffer_load_dwordx4 a[228:231], v243, s[60:63], s88 offen offset:1024 sc1\n"
    "v_mfma_f32_16x16x32_bf16 a[140:143], a[36:39], v[132:135], a[140:143]\n"
    "ds_read_b64_tr_b16 a[6:7], v234 offset:45056\n"
    "v_mfma_f32_16x16x32_bf16 a[140:143], a[40:43], v[136:139], a[140:143]\n"
    "ds_read_b64_tr_b16 a[8:9], v238 offset:32768\n"
    "s_add_u32 m0, s83, 16384\n"
    "v_mfma_f32_16x16x32_bf16 a[140:143], a[44:47], v[140:143], a[140:143]\n"
    "ds_read_b64_tr_b16 a[10:11], v238 offset:36864\n"
    "s_xor_b32 s81, s93, 917504\n"
    "s_add_u32 s82, s81, 14336\n"
    "v_mfma_f32_16x16x32_bf16 a[144:147], a[44:47], v[156:159], a[144:147]\n"
    "buffer_load_dwordx4 v240, s[64:67], s81 offen offset:0 lds\n"
    "v_mfma_f32_16x16x32_bf16 a[144:147], a[40:43], v[152:155], a[144:147]\n"
    "ds_read_b64_tr_b16 a[12:13], v238 offset:40960\n"
    "v_mfma_f32_16x16x32_bf16 a[144:147], a[36:39], v[148:151], a[144:147]\n"
    "ds_read_b64_tr_b16 a[14:15], v238 offset:45056\n"
    "v_mfma_f32_16x16x32_bf16 a[144:147], a[32:35], v[144:147], a[144:147]\n"
    "v_mfma_f32_16x16x32_bf16 a[148:151], a[32:35], v[160:163], a[148:151]\n"
    "buffer_load_dwordx4 v241, s[64:67], s81 offen offset:1024 lds\n"
    "v_mfma_f32_16x16x32_bf16 a[148:151], a[36:39], v[164:167], a[148:151]\n"
    "v_mfma_f32_16x16x32_bf16 a[148:151], a[40:43], v[168:171], a[148:151]\n"
    "v_mfma_f32_16x16x32_bf16 a[148:151], a[44:47], v[172:175], a[148:151]\n"
    "v_mfma_f32_16x16x32_bf16 a[152:155], a[44:47], v[188:191], a[152:155]\n"
    "buffer_load_dwordx4 v240, s[64:67], s82 offen offset:2048 lds\n"
    "v_mfma_f32_16x16x32_bf16 a[152:155], a[40:43], v[184:187], a[152:155]\n"
    "ds_read_b64_tr_b16 a[16:17], v235 offset:32768\n"
    "v_mfma_f32_16x16x32_bf16 a[152:155], a[36:39], v[180:183], a[152:155]\n"
    "ds_read_b64_tr_b16 a[18:19], v235 offset:36864\n"
    "v_mfma_f32_16x16x32_bf16 a[152:155], a[32:35], v[176:179], a[152:155]\n"
    "ds_read_b64_tr_b16 a[20:21], v235 offset:40960\n"
    "s_waitcnt lgkmcnt(3)\n"
    "v_mfma_f32_16x16x32_bf16 a[156:159], a[48:51], v[128:131], a[156:159]\n"
    "buffer_load_dwordx4 v241, s[64:67], s82 offen offset:3072 lds\n"
    "v_mfma_f32_16x16x32_bf16 a[156:159], a[52:55], v[132:135], a[156:159]\n"
    "ds_read_b64_tr_b16 a[22:23], v235 offset:45056\n"
    "v_mfma_f32_16x16x32_bf16 a[156:159], a[56:59], v[136:139], a[156:159]\n"
    "ds_read_b64_tr_b16 a[24:25], v239 offset:32768\n"
    "s_add_u32 m0, s83, 81920\n"
    "v_mfma_f32_16x16x32_bf16 a[156:159], a[60:63], v[140:143], a[156:159]\n"
    "ds_read_b64_tr_b16 a[26:27], v239 offset:36864\n"
    "s_xor_b32 s81, s93, 917632\n"
    "s_add_u32 s82, s81, 14336\n"
    "v_mfma_f32_16x16x32_bf16 a[160:163], a[60:63], v[156:159], a[160:163]\n"
    "buffer_load_dwordx4 v240, s[64:67], s81 offen offset:0 lds\n"
    "v_mfma_f32_16x16x32_bf16 a[160:163], a[56:59], v[152:155], a[160:163]\n"
    "ds_read_b64_tr_b16 a[28:29], v239 offset:40960\n"
    "v_mfma_f32_16x16x32_bf16 a[160:163], a[52:55], v[148:151], a[160:163]\n"
    "ds_read_b64_tr_b16 a[30:31], v239 offset:45056\n"
    "v_mfma_f32_16x16x32_bf16 a[160:163], a[48:51], v[144:147], a[160:163]\n"
    "s_waitcnt vmcnt(30)\n"
    "s_barrier\n"
    "v_mfma_f32_16x16x32_bf16 a[164:167], a[48:51], v[160:163], a[164:167]\n"
    "buffer_load_dwordx4 v241, s[64:67], s81 offen offset:1024 lds\n"
    "v_mfma_f32_16x16x32_bf16 a[164:167], a[52:55], v[164:167], a[164:167]\n"
    "ds_read_b64_tr_b16 a[32:33], v232 offset:49152\n"
    "v_mfma_f32_16x16x32_bf16 a[164:167], a[56:59], v[168:171], a[164:167]\n"
    "ds_read_b64_tr_b16 a[34:35], v232 offset:53248\n"
    "v_mfma_f32_16x16x32_bf16 a[164:167], a[60:63], v[172:175], a[164:167]\n"
    "ds_read_b64_tr_b16 a[36:37], v232 offset:57344\n"
    "v_mfma_f32_16x16x32_bf16 a[168:171], a[0:3], v[128:131], a[168:171]\n"
    "buffer_load_dwordx4 v240, s[64:67], s82 offen offset:2048 lds\n"
    "v_mfma_f32_16x16x32_bf16 a[168:171], a[4:7], v[132:135], a[168:171]\n"
    "ds_read_b64_tr_b16 a[38:39], v232 offset:61440\n"
    "v_mfma_f32_16x16x32_bf16 a[168:171], a[8:11], v[136:139], a[168:171]\n"
    "ds_read_b64_tr_b16 a[40:41], v236 offset:49152\n"
    "v_mfma_f32_16x16x32_bf16 a[168:171], a[12:15], v[140:143], a[168:171]\n"
    "ds_read_b64_tr_b16 a[42:43], v236 offset:53248\n"
    "v_mfma_f32_16x16x32_bf16 a[172:175], a[12:15], v[156:159], a[172:175]\n"
    "buffer_load_dwordx4 v241, s[64:67], s82 offen offset:3072 lds\n"
    "v_mfma_f32_16x16x32_bf16 a[172:175], a[8:11], v[152:155], a[172:175]\n"
    "ds_read_b64_tr_b16 a[44:45], v236 offset:57344\n"
    "v_mfma_f32_16x16x32_bf16 a[172:175], a[4:7], v[148:151], a[172:175]\n"
    "ds_read_b64_tr_b16 a[46:47], v236 offset:61440\n"
    "s_xor_b32 s88, s79, s17\n"
    "v_mfma_f32_16x16x32_bf16 a[172:175], a[0:3], v[144:147], a[172:175]\n"
    "v_mfma_f32_16x16x32_bf16 a[64:67], a[0:3], v[160:163], a[64:67]\n"
    "buffer_load_dwordx4 v[104:107], v242, s[60:63], s88 offen offset:0 sc1\n"
    "v_mfma_f32_16x16x32_bf16 a[64:67], a[4:7], v[164:167], a[64:67]\n"
    "v_mfma_f32_16x16x32_bf16 a[64:67], a[8:11], v[168:171], a[64:67]\n"
    "v_mfma_f32_16x16x32_bf16 a[64:67], a[12:15], v[172:175], a[64:67]\n"
    "v_mfma_f32_16x16x32_bf16 a[68:71], a[12:15], v[188:191], a[68:71]\n"
    "buffer_load_dwordx4 v[120:123], v242, s[60:63], s88 offen offset:1024 sc1\n"
    "v_mfma_f32_16x16x32_bf16 a[68:71], a[8:11], v[184:187], a[68:71]\n"
    "ds_read_b64_tr_b16 a[48:49], v233 offset:49152\n"
    "v_mfma_f32_16x16x32_bf16 a[68:71], a[4:7], v[180:183], a[68:71]\n"
    "ds_read_b64_tr_b16 a[50:51], v233 offset:53248\n"
    "v_mfma_f32_16x16x32_bf16 a[68:71], a[0:3], v[176:179], a[68:71]\n"
    "ds_read_b64_tr_b16 a[52:53], v233 offset:57344\n"
    "s_waitcnt lgkmcnt(3)\n"
    "v_mfma_f32_16x16x32_bf16 a[72:75], a[16:19], v[128:131], a[72:75]\n"
    "buffer_load_dwordx4 v[108:111], v243, s[60:63], s88 offen offset:0 sc1\n"
    "v_mfma_f32_16x16x32_bf16 a[72:75], a[20:23], v[132:135], a[72:75]\n"
    "ds_read_b64_tr_b16 a[54:55], v233 offset:61440\n"
    "v_mfma_f32_16x16x32_bf16 a[72:75], a[24:27], v[136:139], a[72:75]\n"
    "ds_read_b64_tr_b16 a[56:57], v237 offset:49152\n"
    "v_mfma_f32_16x16x32_bf16 a[72:75], a[28:31], v[140:143], a[72:75]\n"
    "ds_read_b64_tr_b16 a[58:59], v237 offset:53248\n"
    "v_mfma_f32_16x16x32_bf16 a[76:79], a[28:31], v[156:159], a[76:79]\n"
    "buffer_load_dwordx4 v[124:127], v243, s[60:63], s88 offen offset:1024 sc1\n"
    "v_mfma_f32_16x16x32_bf16 a[76:79], a[24:27], v[152:155], a[76:79]\n"
    "ds_read_b64_tr_b16 a[60:61], v237 offset:57344\n"
    "v_mfma_f32_16x16x32_bf16 a[76:79], a[20:23], v[148:151], a[76:79]\n"
    "ds_read_b64_tr_b16 a[62:63], v237 offset:61440\n"
    "s_xor_b32 s88, s76, s17\n"
    "v_mfma_f32_16x16x32_bf16 a[76:79], a[16:19], v[144:147], a[76:79]\n"
    "v_mfma_f32_16x16x32_bf16 a[80:83], a[16:19], v[160:163], a[80:83]\n"
    "buffer_load_dwordx4 v[8:11], v242, s[60:63], s88 offen offset:0 sc1\n"
    "v_mfma_f32_16x16x32_bf16 a[80:83], a[20:23], v[164:167], a[80:83]\n"
    "ds_read_b64_tr_b16 a[0:1], v232 offset:49664\n"
    "v_mfma_f32_16x16x32_bf16 a[80:83], a[24:27], v[168:171], a[80:83]\n"
    "ds_read_b64_tr_b16 a[2:3], v232 offset:53760\n"
    "v_mfma_f32_16x16x32_bf16 a[80:83], a[28:31], v[172:175], a[80:83]\n"
    "ds_read_b64_tr_b16 a[4:5], v232 offset:57856\n"
    "s_waitcnt vmcnt(15)\n"
    "v_mfma_f32_16x16x32_bf16 a[176:179], a[32:35], v[0:3], a[176:179]\n"
    "buffer_load_dwordx4 v[24:27], v242, s[60:63], s88 offen offset:1024 sc1\n"
    "v_bfe_u32 v255, v242, 0, 10\n"
    "v_mfma_f32_16x16x32_bf16 a[176:179], a[36:39], v[16:19], a[176:179]\n"
    "ds_read_b64_tr_b16 a[6:7], v232 offset:61952\n"
    "v_lshrrev_b32 v254, 1, v255\n"
    "v_mfma_f32_16x16x32_bf16 a[176:179], a[40:43], v[4:7], a[176:179]\n"
    "ds_read_b64_tr_b16 a[8:9], v236 offset:49664\n"
    "v_bfe_u32 v224, v242, 4, 4\n"
    "v_mfma_f32_16x16x32_bf16 a[176:179], a[44:47], v[20:23], a[176:179]\n"
    "ds_read_b64_tr_b16 a[10:11], v236 offset:53760\n"
    "v_bfe_u32 v225, v242, 8, 2\n"
    "L_h16:\n"
    "s_cbranch_execz L_ret2\n"
    "v_mfma_f32_16x16x32_bf16 a[180:183], a[44:47], v[52:55], a[180:183]\n"
    "buffer_load_dwordx4 v[12:15], v243, s[60:63], s88 offen offset:0 sc1\n"
    "v_lshlrev_b32 v225, 2, v225\n"
    "v_mfma_f32_16x16x32_bf16 a[180:183], a[40:43], v[36:39], a[180:183]\n"
    "ds_read_b64_tr_b16 a[12:13], v236 offset:57856\n"
    "v_sub_u32 v224, v224, v225\n"
    "v_mfma_f32_16x16x32_bf16 a[180:183], a[36:39], v[48:51], a[180:183]\n"
    "ds_read_b64_tr_b16 a[14:15], v236 offset:61952\n"
    "v_mov_b32 v248, 0\n"
    "v_mfma_f32_16x16x32_bf16 a[180:183], a[32:35], v[32:35], a[180:183]\n"
    "v_mov_b32 v249, 0\n"
    "v_mov_b32 v252, 0\n"
    "v_mfma_f32_16x16x32_bf16 a[184:187], a[32:35], v[64:67], a[184:187]\n"
    "buffer_load_dwordx4 v[28:31], v243, s[60:63], s88 offen offset:1024 sc1\n"
    "v_mov_b32 v253, 0x3f80\n"
    "v_mfma_f32_16x16x32_bf16 a[184:187], a[36:39], v[80:83], a[184:187]\n"
    "v_and_b32 v225, 1, v224\n"
    "v_lshlrev_b32 v225, 4, v225\n"
    "v_mfma_f32_16x16x32_bf16 a[184:187], a[40:43], v[68:71], a[184:187]\n"
    "s_xor_b32 s88, s77, s16\n"
    "v_lshlrev_b32 v225, v225, v253\n"
    "v_mfma_f32_16x16x32_bf16 a[184:187], a[44:47], v[84:87], a[184:187]\n"
    "v_mov_b32 v253, 0\n"
    "v_lshrrev_b32 v224, 1, v224\n"
    "v_mfma_f32_16x16x32_bf16 a[188:191], a[44:47], v[116:119], a[188:191]\n"
    "buffer_load_dwordx4 a[232:235], v242, s[60:63], s88 offen offset:0 sc1\n"
    "v_min_u32 v250, 1, v224\n"
    "v_mfma_f32_16x16x32_bf16 a[188:191], a[40:43], v[100:103], a[188:191]\n"
    "ds_read_b64_tr_b16 a[16:17], v233 offset:49664\n"
    "v_xor_b32 v224, 1, v224\n"
    "v_mfma_f32_16x16x32_bf16 a[188:191], a[36:39], v[112:115], a[188:191]\n"
    "ds_read_b64_tr_b16 a[18:19], v233 offset:53760\n"
    "v_min_u32 v251, 1, v224\n"
    "v_mfma_f32_16x16x32_bf16 a[188:191], a[32:35], v[96:99], a[188:191]\n"
    "ds_read_b64_tr_b16 a[20:21], v233 offset:57856\n"
    "v_add_u32 v250, -1, v250\n"
    "s_waitcnt lgkmcnt(3)\n"
    "v_mfma_f32_16x16x32_bf16 a[128:131], a[48:51], v[0:3], a[128:131]\n"
    "buffer_load_dwordx4 a[236:239], v242, s[60:63], s88 offen offset:1024 sc1\n"
    "v_add_u32 v251, -1, v251\n"
    "v_mfma_f32_16x16x32_bf16 a[128:131], a[52:55], v[16:19], a[128:131]\n"
    "ds_read_b64_tr_b16 a[22:23], v233 offset:61952\n"
    "v_and_b32 v250, v225, v250\n"
    "v_mfma_f32_16x16x32_bf16 a[128:131], a[56:59], v[4:7], a[128:131]\n"
    "ds_read_b64_tr_b16 a[24:25], v237 offset:49664\n"
    "v_and_b32 v251, v225, v251\n"
    "v_mfma_f32_16x16x32_bf16 a[128:131], a[60:63], v[20:23], a[128:131]\n"
    "ds_read_b64_tr_b16 a[26:27], v237 offset:53760\n"
    "v_mfma_f32_16x16x32_bf16 a[132:135], a[60:63], v[52:55], a[132:135]\n"
    "buffer_load_dwordx4 a[240:243], v243, s[60:63], s88 offen offset:0 sc1\n"
    "v_mfma_f32_16x16x32_bf16 a[132:135], a[56:59], v[36:39], a[132:135]\n"
    "ds_read_b64_tr_b16 a[28:29], v237 offset:57856\n"
    "v_mfma_f32_16x16x32_bf16 a[132:135], a[52:55], v[48:51], a[132:135]\n"
    "ds_read_b64_tr_b16 a[30:31], v237 offset:61952\n"
    "v_mfma_f32_16x16x32_bf16 a[132:135], a[48:51], v[32:35], a[132:135]\n"
    "v_mfma_f32_16x16x32_bf16 a[136:139], a[48:51], v[64:67], a[136:139]\n"
    "buffer_load_dwordx4 a[244:247], v243, s[60:63], s88 offen offset:1024 sc1\n"
    "v_mfma_f32_16x16x32_bf16 a[136:139], a[52:55], v[80:83], a[136:139]\n"
    "ds_read_b64_tr_b16 a[32:33], v234 offset:49152\n"
    "v_mfma_f32_16x16x32_bf16 a[136:139], a[56:59], v[68:71], a[136:139]\n"
    "ds_read_b64_tr_b16 a[34:35], v234 offset:53248\n"
    "s_xor_b32 s88, s78, s16\n"
    "v_mfma_f32_16x16x32_bf16 a[136:139], a[60:63], v[84:87], a[136:139]\n"
    "ds_read_b64_tr_b16 a[36:37], v234 offset:57344\n"
    "v_mfma_f32_16x16x32_bf16 a[140:143], a[0:3], v[0:3], a[140:143]\n"
    "buffer_load_dwordx4 a[96:99], v242, s[60:63], s88 offen offset:0 sc1\n"
    "v_mfma_f32_16x16x32_bf16 a[140:143], a[4:7], v[16:19], a[140:143]\n"
    "ds_read_b64_tr_b16 a[38:39], v234 offset:61440\n"
    "v_mfma_f32_16x16x32_bf16 a[140:143], a[8:11], v[4:7], a[140:143]\n"
    "ds_read_b64_tr_b16 a[40:41], v238 offset:49152\n"
    "v_mfma_f32_16x16x32_bf16 a[140:143], a[12:15], v[20:23], a[140:143]\n"
    "ds_read_b64_tr_b16 a[42:43], v238 offset:53248\n"
    "v_mfma_f32_16x16x32_bf16 a[144:147], a[12:15], v[52:55], a[144:147]\n"
    "buffer_load_dwordx4 a[100:103], v242, s[60:63], s88 offen offset:1024 sc1\n"
    "v_mfma_f32_16x16x32_bf16 a[144:147], a[8:11], v[36:39], a[144:147]\n"
    "ds_read_b64_tr_b16 a[44:45], v238 offset:57344\n"
    "v_mfma_f32_16x16x32_bf16 a[144:147], a[4:7], v[48:51], a[144:147]\n"
    "ds_read_b64_tr_b16 a[46:47], v238 offset:61440\n"
    "v_mfma_f32_16x16x32_bf16 a[144:147], a[0:3], v[32:35], a[144:147]\n"
    "v_mfma_f32_16x16x32_bf16 a[148:151], a[0:3], v[64:67], a[148:151]\n"
    "buffer_load_dwordx4 a[104:107], v243, s[60:63], s88 offen offset:0 sc1\n"
    "v_mfma_f32_16x16x32_bf16 a[148:151], a[4:7], v[80:83], a[148:151]\n"
    "v_mfma_f32_16x16x32_bf16 a[148:151], a[8:11], v[68:71], a[148:151]\n"
    "v_mfma_f32_16x16x32_bf16 a[148:151], a[12:15], v[84:87], a[148:151]\n"
    "v_mfma_f32_16x16x32_bf16 a[152:155], a[12:15], v[116:119], a[152:155]\n"
    "buffer_load_dwordx4 a[108:111], v243, s[60:63], s88 offen offset:1024 sc1\n"
    "v_mfma_f32_16x16x32_bf16 a[152:155], a[8:11], v[100:103], a[152:155]\n"
    "ds_read_b64_tr_b16 a[48:49], v235 offset:49152\n"
    "v_mfma_f32_16x16x32_bf16 a[152:155], a[4:7], v[112:115], a[152:155]\n"
    "ds_read_b64_tr_b16 a[50:51], v235 offset:53248\n"
    "s_xor_b32 s88, s77, s17\n"
    "v_mfma_f32_16x16x32_bf16 a[152:155], a[0:3], v[96:99], a[152:155]\n"
    "ds_read_b64_tr_b16 a[52:53], v235 offset:57344\n"
    "s_waitcnt lgkmcnt(3)\n"
    "v_mfma_f32_16x16x32_bf16 a[156:159], a[16:19], v[0:3], a[156:159]\n"
    "buffer_load_dwordx4 v[40:43], v242, s[60:63], s88 offen offset:0 sc1\n"
    "v_mfma_f32_16x16x32_bf16 a[156:159], a[20:23], v[16:19], a[156:159]\n"
    "ds_read_b64_tr_b16 a[54:55], v235 offset:61440\n"
    "v_mfma_f32_16x16x32_bf16 a[156:159], a[24:27], v[4:7], a[156:159]\n"
    "ds_read_b64_tr_b16 a[56:57], v239 offset:49152\n"
    "v_mfma_f32_16x16x32_bf16 a[156:159], a[28:31], v[20:23], a[156:159]\n"
    "ds_read_b64_tr_b16 a[58:59], v239 offset:53248\n"
    "v_mfma_f32_16x16x32_bf16 a[160:163], a[28:31], v[52:55], a[160:163]\n"
    "buffer_load_dwordx4 v[56:59], v242, s[60:63], s88 offen offset:1024 sc1\n"
    "v_mfma_f32_16x16x32_bf16 a[160:163], a[24:27], v[36:39], a[160:163]\n"
    "ds_read_b64_tr_b16 a[60:61], v239 offset:57344\n"
    "v_mfma_f32_16x16x32_bf16 a[160:163], a[20:23], v[48:51], a[160:163]\n"
    "ds_read_b64_tr_b16 a[62:63], v239 offset:61440\n"
    "v_mfma_f32_16x16x32_bf16 a[160:163], a[16:19], v[32:35], a[160:163]\n"
    "s_barrier\n"
    "v_mfma_f32_16x16x32_bf16 a[164:167], a[16:19], v[64:67], a[164:167]\n"
    "buffer_load_dwordx4 v[44:47], v243, s[60:63], s88 offen offset:0 sc1\n"
    "v_mfma_f32_16x16x32_bf16 a[164:167], a[20:23], v[80:83], a[164:167]\n"
    "ds_read_b64_tr_b16 a[0:1], v232 offset:0\n"
    "v_mfma_f32_16x16x32_bf16 a[164:167], a[24:27], v[68:71], a[164:167]\n"
    "ds_read_b64_tr_b16 a[2:3], v232 offset:4096\n"
    "v_mfma_f32_16x16x32_bf16 a[164:167], a[28:31], v[84:87], a[164:167]\n"
    "ds_read_b64_tr_b16 a[4:5], v232 offset:8192\n"
    "v_mfma_f32_16x16x32_bf16 a[168:171], a[32:35], v[0:3], a[168:171]\n"
    "buffer_load_dwordx4 v[60:63], v243, s[60:63], s88 offen offset:1024 sc1\n"
    "v_mfma_f32_16x16x32_bf16 a[168:171], a[36:39], v[16:19], a[168:171]\n"
    "ds_read_b64_tr_b16 a[6:7], v232 offset:12288\n"
    "v_mfma_f32_16x16x32_bf16 a[168:171], a[40:43], v[4:7], a[168:171]\n"
    "ds_read_b64_tr_b16 a[8:9], v236 offset:0\n"
    "s_xor_b32 s88, s78, s17\n"
    "v_mfma_f32_16x16x32_bf16 a[168:171], a[44:47], v[20:23], a[168:171]\n"
    "ds_read_b64_tr_b16 a[10:11], v236 offset:4096\n"
    "v_mfma_f32_16x16x32_bf16 a[172:175], a[44:47], v[52:55], a[172:175]\n"
    "buffer_load_dwordx4 v[72:75], v242, s[60:63], s88 offen offset:0 sc1\n"
    "v_mfma_f32_16x16x32_bf16 a[172:175], a[40:43], v[36:39], a[172:175]\n"
    "ds_read_b64_tr_b16 a[12:13], v236 offset:8192\n"
    "v_mfma_f32_16x16x32_bf16 a[172:175], a[36:39], v[48:51], a[172:175]\n"
    "ds_read_b64_tr_b16 a[14:15], v236 offset:12288\n"
    "v_mfma_f32_16x16x32_bf16 a[172:175], a[32:35], v[32:35], a[172:175]\n"
    "v_mfma_f32_16x16x32_bf16 a[64:67], a[32:35], v[64:67], a[64:67]\n"
    "buffer_load_dwordx4 v[88:91], v242, s[60:63], s88 offen offset:1024 sc1\n"
    "v_mfma_f32_16x16x32_bf16 a[64:67], a[36:39], v[80:83], a[64:67]\n"
    "v_mfma_f32_16x16x32_bf16 a[64:67], a[40:43], v[68:71], a[64:67]\n"
    "v_mfma_f32_16x16x32_bf16 a[64:67], a[44:47], v[84:87], a[64:67]\n"
    "v_mfma_f32_16x16x32_bf16 a[68:71], a[44:47], v[116:119], a[68:71]\n"
    "buffer_load_dwordx4 v[76:79], v243, s[60:63], s88 offen offset:0 sc1\n"
    "v_mfma_f32_16x16x32_bf16 a[68:71], a[40:43], v[100:103], a[68:71]\n"
    "ds_read_b64_tr_b16 a[16:17], v233 offset:0\n"
    "v_mfma_f32_16x16x32_bf16 a[68:71], a[36:39], v[112:115], a[68:71]\n"
    "ds_read_b64_tr_b16 a[18:19], v233 offset:4096\n"
    "v_mfma_f32_16x16x32_bf16 a[68:71], a[32:35], v[96:99], a[68:71]\n"
    "ds_read_b64_tr_b16 a[20:21], v233 offset:8192\n"
    "s_waitcnt lgkmcnt(3)\n"
    "v_mfma_f32_16x16x32_bf16 a[72:75], a[48:51], v[0:3], a[72:75]\n"
    "buffer_load_dwordx4 v[92:95], v243, s[60:63], s88 offen offset:1024 sc1\n"
    "v_mfma_f32_16x16x32_bf16 a[72:75], a[52:55], v[16:19], a[72:75]\n"
    "ds_read_b64_tr_b16 a[22:23], v233 offset:12288\n"
    "v_mfma_f32_16x16x32_bf16 a[72:75], a[56:59], v[4:7], a[72:75]\n"
    "ds_read_b64_tr_b16 a[24:25], v237 offset:0\n"
    "v_mfma_f32_16x16x32_bf16 a[72:75], a[60:63], v[20:23], a[72:75]\n"
    "ds_read_b64_tr_b16 a[26:27], v237 offset:4096\n"
    "v_mfma_f32_16x16x32_bf16 a[76:79], a[60:63], v[52:55], a[76:79]\n"
    "v_mfma_f32_16x16x32_bf16 a[76:79], a[56:59], v[36:39], a[76:79]\n"
    "ds_read_b64_tr_b16 a[28:29], v237 offset:8192\n"
    "v_mfma_f32_16x16x32_bf16 a[76:79], a[52:55], v[48:51], a[76:79]\n"
    "ds_read_b64_tr_b16 a[30:31], v237 offset:12288\n"
    "v_mfma_f32_16x16x32_bf16 a[76:79], a[48:51], v[32:35], a[76:79]\n"
    "s_waitcnt vmcnt(24)\n"
    "s_barrier\n"
    "v_mfma_f32_16x16x32_bf16 a[80:83], a[48:51], v[64:67], a[80:83]\n"
    "v_mfma_f32_16x16x32_bf16 a[80:83], a[52:55], v[80:83], a[80:83]\n"
    "ds_read_b64_tr_b16 a[32:33], v232 offset:16384\n"
    "v_mfma_f32_16x16x32_bf16 a[80:83], a[56:59], v[68:71], a[80:83]\n"
    "ds_read_b64_tr_b16 a[34:35], v232 offset:20480\n"
    "v_mfma_f32_16x16x32_bf16 a[80:83], a[60:63], v[84:87], a[80:83]\n"
    "ds_read_b64_tr_b16 a[36:37], v232 offset:24576\n"
    "s_waitcnt vmcnt(8)\n"
    "v_mfma_f32_16x16x32_bf16 a[176:179], a[0:3], a[216:219], a[176:179]\n"
    "v_mfma_f32_16x16x32_bf16 a[176:179], a[4:7], a[220:223], a[176:179]\n"
    "ds_read_b64_tr_b16 a[38:39], v232 offset:28672\n"
    "v_mfma_f32_16x16x32_bf16 a[176:179], a[8:11], a[224:227], a[176:179]\n"
    "ds_read_b64_tr_b16 a[40:41], v236 offset:16384\n"
    "v_mfma_f32_16x16x32_bf16 a[176:179], a[12:15], a[228:231], a[176:179]\n"
    "ds_read_b64_tr_b16 a[42:43], v236 offset:20480\n"
    "v_mfma_f32_16x16x32_bf16 a[180:183], a[12:15], a[244:247], a[180:183]\n"
    "v_mfma_f32_16x16x32_bf16 a[180:183], a[8:11], a[240:243], a[180:183]\n"
    "ds_read_b64_tr_b16 a[44:45], v236 offset:24576\n"
    "v_mfma_f32_16x16x32_bf16 a[180:183], a[4:7], a[236:239], a[180:183]\n"
    "ds_read_b64_tr_b16 a[46:47], v236 offset:28672\n"
    "v_mfma_f32_16x16x32_bf16 a[180:183], a[0:3], a[232:235], a[180:183]\n"
    "v_mfma_f32_16x16x32_bf16 a[184:187], a[0:3], a[96:99], a[184:187]\n"
    "v_mfma_f32_16x16x32_bf16 a[184:187], a[4:7], a[100:103], a[184:187]\n"
    "v_mfma_f32_16x16x32_bf16 a[184:187], a[8:11], a[104:107], a[184:187]\n"
    "v_mfma_f32_16x16x32_bf16 a[184:187], a[12:15], a[108:111], a[184:187]\n"
    "v_mfma_f32_16x16x32_bf16 a[188:191], a[12:15], a[124:127], a[188:191]\n"
    "v_mfma_f32_16x16x32_bf16 a[188:191], a[8:11], a[120:123], a[188:191]\n"
    "ds_read_b64_tr_b16 a[48:49], v233 offset:16384\n"
    "v_mfma_f32_16x16x32_bf16 a[188:191], a[4:7], a[116:119], a[188:191]\n"
    "ds_read_b64_tr_b16 a[50:51], v233 offset:20480\n"
    "v_mfma_f32_16x16x32_bf16 a[188:191], a[0:3], a[112:115], a[188:191]\n"
    "ds_read_b64_tr_b16 a[52:53], v233 offset:24576\n"
    "s_waitcnt lgkmcnt(3)\n"
    "v_mfma_f32_16x16x32_bf16 a[128:131], a[16:19], a[216:219], a[128:131]\n"
    "v_mfma_f32_16x16x32_bf16 a[128:131], a[20:23], a[220:223], a[128:131]\n"
    "ds_read_b64_tr_b16 a[54:55], v233 offset:28672\n"
    "v_mfma_f32_16x16x32_bf16 a[128:131], a[24:27], a[224:227], a[128:131]\n"
    "ds_read_b64_tr_b16 a[56:57], v237 offset:16384\n"
    "v_mfma_f32_16x16x32_bf16 a[128:131], a[28:31], a[228:231], a[128:131]\n"
    "ds_read_b64_tr_b16 a[58:59], v237 offset:20480\n"
    "v_mfma_f32_16x16x32_bf16 a[132:135], a[28:31], a[244:247], a[132:135]\n"
    "v_mfma_f32_16x16x32_bf16 a[132:135], a[24:27], a[240:243], a[132:135]\n"
    "ds_read_b64_tr_b16 a[60:61], v237 offset:24576\n"
    "v_mfma_f32_16x16x32_bf16 a[132:135], a[20:23], a[236:239], a[132:135]\n"
    "ds_read_b64_tr_b16 a[62:63], v237 offset:28672\n"
    "v_mfma_f32_16x16x32_bf16 a[132:135], a[16:19], a[232:235], a[132:135]\n"
    "v_mfma_f32_16x16x32_bf16 a[136:139], a[16:19], a[96:99], a[136:139]\n"
    "v_mfma_f32_16x16x32_bf16 a[136:139], a[20:23], a[100:103], a[136:139]\n"
    "ds_read_b64_tr_b16 a[0:1], v232 offset:512\n"
    "v_mfma_f32_16x16x32_bf16 a[136:139], a[24:27], a[104:107], a[136:139]\n"
    "ds_read_b64_tr_b16 a[2:3], v232 offset:4608\n"
    "v_mfma_f32_16x16x32_bf16 a[136:139], a[28:31], a[108:111], a[136:139]\n"
    "ds_read_b64_tr_b16 a[4:5], v232 offset:8704\n"
    "s_waitcnt vmcnt(0)\n"
    "v_mfma_f32_16x16x32_bf16 a[176:179], a[32:35], v[8:11], a[176:179]\n"
    "s_bfe_u32 s72, s10, 0x1000b\n"
    "s_lshl_b32 s72, s72, 13\n"
    "v_mfma_f32_16x16x32_bf16 a[176:179], a[36:39], v[24:27], a[176:179]\n"
    "ds_read_b64_tr_b16 a[6:7], v232 offset:12800\n"
    "s_xor_b32 s72, s72, s11\n"
    "v_mfma_f32_16x16x32_bf16 a[176:179], a[40:43], v[12:15], a[176:179]\n"
    "ds_read_b64_tr_b16 a[8:9], v236 offset:512\n"
    "s_xor_b32 s72, s72, 50352128\n"
    "v_mfma_f32_16x16x32_bf16 a[176:179], a[44:47], v[28:31], a[176:179]\n"
    "ds_read_b64_tr_b16 a[10:11], v236 offset:4608\n"
    "v_mfma_f32_16x16x32_bf16 a[180:183], a[44:47], v[60:63], a[180:183]\n"
    "v_mfma_f32_16x16x32_bf16 a[180:183], a[40:43], v[44:47], a[180:183]\n"
    "ds_read_b64_tr_b16 a[12:13], v236 offset:8704\n"
    "v_mfma_f32_16x16x32_bf16 a[180:183], a[36:39], v[56:59], a[180:183]\n"
    "ds_read_b64_tr_b16 a[14:15], v236 offset:12800\n"
    "v_mfma_f32_16x16x32_bf16 a[180:183], a[32:35], v[40:43], a[180:183]\n"
    "s_nop 0\n"
    "v_accvgpr_read_b32 v224, a176\n"
    "v_accvgpr_read_b32 v225, a177\n"
    "v_mfma_f32_16x16x32_bf16 a[184:187], a[32:35], v[72:75], a[184:187]\n"
    "v_accvgpr_read_b32 v226, a178\n"
    "v_accvgpr_read_b32 v227, a179\n"
    "v_mfma_f32_16x16x32_bf16 a[184:187], a[36:39], v[88:91], a[184:187]\n"
    "v_cvt_pk_bf16_f32 v192, v224, v225\n"
    "v_cvt_pk_bf16_f32 v193, v226, v227\n"
    "v_mfma_f32_16x16x32_bf16 a[184:187], a[40:43], v[76:79], a[184:187]\n"
    "s_waitcnt lgkmcnt(3)\n"
    "v_mfma_f32_16x16x32_bf16 a[184:187], a[44:47], v[92:95], a[184:187]\n"
    "v_accvgpr_read_b32 v224, a180\n"
    "v_accvgpr_read_b32 v225, a181\n"
    "v_mfma_f32_16x16x32_bf16 a[188:191], a[44:47], v[124:127], a[188:191]\n"
    "v_accvgpr_read_b32 v226, a182\n"
    "v_accvgpr_read_b32 v227, a183\n"
    "v_mfma_f32_16x16x32_bf16 a[188:191], a[40:43], v[108:111], a[188:191]\n"
    "ds_read_b64_tr_b16 a[16:17], v233 offset:512\n"
    "v_cvt_pk_bf16_f32 v196, v224, v225\n"
    "v_mfma_f32_16x16x32_bf16 a[188:191], a[36:39], v[120:123], a[188:191]\n"
    "ds_read_b64_tr_b16 a[18:19], v233 offset:4608\n"
    "v_cvt_pk_bf16_f32 v197, v226, v227\n"
    "v_mfma_f32_16x16x32_bf16 a[188:191], a[32:35], v[104:107], a[188:191]\n"
    "ds_read_b64_tr_b16 a[20:21], v233 offset:8704\n"
    "v_accvgpr_read_b32 v224, a184\n"
    "v_mfma_f32_16x16x32_bf16 a[128:131], a[48:51], v[8:11], a[128:131]\n"
    "v_accvgpr_read_b32 v225, a185\n"
    "v_accvgpr_read_b32 v226, a186\n"
    "v_mfma_f32_16x16x32_bf16 a[128:131], a[52:55], v[24:27], a[128:131]\n"
    "ds_read_b64_tr_b16 a[22:23], v233 offset:12800\n"
    "v_accvgpr_read_b32 v227, a187\n"
    "v_mfma_f32_16x16x32_bf16 a[128:131], a[56:59], v[12:15], a[128:131]\n"
    "ds_read_b64_tr_b16 a[24:25], v237 offset:512\n"
    "v_cvt_pk_bf16_f32 v200, v224, v225\n"
    "v_cvt_pk_bf16_f32 v201, v226, v227\n"
    "v_mfma_f32_16x16x32_bf16 a[128:131], a[60:63], v[28:31], a[128:131]\n"
    "ds_read_b64_tr_b16 a[26:27], v237 offset:4608\n"
    "v_accvgpr_read_b32 v224, a188\n"
    "v_mfma_f32_16x16x32_bf16 a[132:135], a[60:63], v[60:63], a[132:135]\n"
    "v_accvgpr_read_b32 v225, a189\n"
    "v_accvgpr_read_b32 v226, a190\n"
    "v_accvgpr_read_b32 v227, a191\n"
    "v_mfma_f32_16x16x32_bf16 a[132:135], a[56:59], v[44:47], a[132:135]\n"
    "ds_read_b64_tr_b16 a[28:29], v237 offset:8704\n"
    "v_cvt_pk_bf16_f32 v204, v224, v225\n"
    "v_cvt_pk_bf16_f32 v205, v226, v227\n"
    "v_mfma_f32_16x16x32_bf16 a[132:135], a[52:55], v[56:59], a[132:135]\n"
    "ds_read_b64_tr_b16 a[30:31], v237 offset:12800\n"
    "buffer_store_dwordx2 v[204:205], v254, s[48:51], s72 offen offset:3072 sc1\n"
    "v_mfma_f32_16x16x32_bf16 a[132:135], a[48:51], v[40:43], a[132:135]\n"
    "v_accvgpr_read_b32 v224, a128\n"
    "v_accvgpr_read_b32 v225, a129\n"
    "v_mfma_f32_16x16x32_bf16 a[136:139], a[48:51], v[72:75], a[136:139]\n"
    "v_accvgpr_read_b32 v226, a130\n"
    "v_accvgpr_read_b32 v227, a131\n"
    "v_cvt_pk_bf16_f32 v194, v224, v225\n"
    "v_mfma_f32_16x16x32_bf16 a[136:139], a[52:55], v[88:91], a[136:139]\n"
    "ds_read_b64_tr_b16 a[32:33], v232 offset:16896\n"
    "v_cvt_pk_bf16_f32 v195, v226, v227\n"
    "v_mfma_f32_16x16x32_bf16 a[136:139], a[56:59], v[76:79], a[136:139]\n"
    "ds_read_b64_tr_b16 a[34:35], v232 offset:20992\n"
    "buffer_store_dwordx4 v[192:195], v255, s[48:51], s72 offen offset:0 sc1\n"
    "v_mfma_f32_16x16x32_bf16 a[136:139], a[60:63], v[92:95], a[136:139]\n"
    "ds_read_b64_tr_b16 a[36:37], v232 offset:25088\n"
    "v_accvgpr_read_b32 v224, a132\n"
    "v_mfma_f32_16x16x32_bf16 a[140:143], a[0:3], a[216:219], a[140:143]\n"
    "v_accvgpr_read_b32 v225, a133\n"
    "v_accvgpr_read_b32 v226, a134\n"
    "v_accvgpr_read_b32 v227, a135\n"
    "v_mfma_f32_16x16x32_bf16 a[140:143], a[4:7], a[220:223], a[140:143]\n"
    "ds_read_b64_tr_b16 a[38:39], v232 offset:29184\n"
    "v_cvt_pk_bf16_f32 v198, v224, v225\n"
    "v_cvt_pk_bf16_f32 v199, v226, v227\n"
    "s_waitcnt lgkmcnt(4)\n"
    "v_mfma_f32_16x16x32_bf16 a[140:143], a[8:11], a[224:227], a[140:143]\n"
    "ds_read_b64_tr_b16 a[40:41], v236 offset:16896\n"
    "buffer_store_dwordx4 v[196:199], v255, s[48:51], s72 offen offset:1024 sc1\n"
    "v_mfma_f32_16x16x32_bf16 a[140:143], a[12:15], a[228:231], a[140:143]\n"
    "ds_read_b64_tr_b16 a[42:43], v236 offset:20992\n"
    "v_accvgpr_read_b32 v224, a136\n"
    "v_mfma_f32_16x16x32_bf16 a[144:147], a[12:15], a[244:247], a[144:147]\n"
    "v_accvgpr_read_b32 v225, a137\n"
    "v_accvgpr_read_b32 v226, a138\n"
    "v_accvgpr_read_b32 v227, a139\n"
    "v_mfma_f32_16x16x32_bf16 a[144:147], a[8:11], a[240:243], a[144:147]\n"
    "ds_read_b64_tr_b16 a[44:45], v236 offset:25088\n"
    "v_cvt_pk_bf16_f32 v202, v224, v225\n"
    "v_cvt_pk_bf16_f32 v203, v226, v227\n"
    "v_mfma_f32_16x16x32_bf16 a[144:147], a[4:7], a[236:239], a[144:147]\n"
    "ds_read_b64_tr_b16 a[46:47], v236 offset:29184\n"
    "buffer_store_dwordx4 v[200:203], v255, s[48:51], s72 offen offset:2048 sc1\n"
    "v_mfma_f32_16x16x32_bf16 a[144:147], a[0:3], a[232:235], a[144:147]\n"
    "v_mfma_f32_16x16x32_bf16 a[148:151], a[0:3], a[96:99], a[148:151]\n"
    "v_mfma_f32_16x16x32_bf16 a[148:151], a[4:7], a[100:103], a[148:151]\n"
    "v_mfma_f32_16x16x32_bf16 a[148:151], a[8:11], a[104:107], a[148:151]\n"
    "v_mfma_f32_16x16x32_bf16 a[148:151], a[12:15], a[108:111], a[148:151]\n"
    "v_mfma_f32_16x16x32_bf16 a[152:155], a[12:15], a[124:127], a[152:155]\n"
    "v_mfma_f32_16x16x32_bf16 a[152:155], a[8:11], a[120:123], a[152:155]\n"
    "ds_read_b64_tr_b16 a[48:49], v233 offset:16896\n"
    "v_mfma_f32_16x16x32_bf16 a[152:155], a[4:7], a[116:119], a[152:155]\n"
    "ds_read_b64_tr_b16 a[50:51], v233 offset:20992\n"
    "v_mfma_f32_16x16x32_bf16 a[152:155], a[0:3], a[112:115], a[152:155]\n"
    "ds_read_b64_tr_b16 a[52:53], v233 offset:25088\n"
    "v_mfma_f32_16x16x32_bf16 a[156:159], a[16:19], a[216:219], a[156:159]\n"
    "v_mfma_f32_16x16x32_bf16 a[156:159], a[20:23], a[220:223], a[156:159]\n"
    "ds_read_b64_tr_b16 a[54:55], v233 offset:29184\n"
    "v_mfma_f32_16x16x32_bf16 a[156:159], a[24:27], a[224:227], a[156:159]\n"
    "ds_read_b64_tr_b16 a[56:57], v237 offset:16896\n"
    "v_mfma_f32_16x16x32_bf16 a[156:159], a[28:31], a[228:231], a[156:159]\n"
    "ds_read_b64_tr_b16 a[58:59], v237 offset:20992\n"
    "v_mfma_f32_16x16x32_bf16 a[160:163], a[28:31], a[244:247], a[160:163]\n"
    "v_mfma_f32_16x16x32_bf16 a[160:163], a[24:27], a[240:243], a[160:163]\n"
    "ds_read_b64_tr_b16 a[60:61], v237 offset:25088\n"
    "v_mfma_f32_16x16x32_bf16 a[160:163], a[20:23], a[236:239], a[160:163]\n"
    "ds_read_b64_tr_b16 a[62:63], v237 offset:29184\n"
    "v_mfma_f32_16x16x32_bf16 a[160:163], a[16:19], a[232:235], a[160:163]\n"
    "v_mfma_f32_16x16x32_bf16 a[164:167], a[16:19], a[96:99], a[164:167]\n"
    "v_mfma_f32_16x16x32_bf16 a[164:167], a[20:23], a[100:103], a[164:167]\n"
    "ds_read_b64_tr_b16 a[0:1], v234 offset:0\n"
    "v_mfma_f32_16x16x32_bf16 a[164:167], a[24:27], a[104:107], a[164:167]\n"
    "ds_read_b64_tr_b16 a[2:3], v234 offset:4096\n"
    "v_mfma_f32_16x16x32_bf16 a[164:167], a[28:31], a[108:111], a[164:167]\n"
    "ds_read_b64_tr_b16 a[4:5], v234 offset:8192\n"
    "s_waitcnt lgkmcnt(5)\n"
    "v_mfma_f32_16x16x32_bf16 a[140:143], a[32:35], v[8:11], a[140:143]\n"
    "s_bfe_u32 s73, s10, 0x1000a\n"
    "s_lshl_b32 s73, s73, 13\n"
    "v_mfma_f32_16x16x32_bf16 a[140:143], a[36:39], v[24:27], a[140:143]\n"
    "ds_read_b64_tr_b16 a[6:7], v234 offset:12288\n"
    "s_xor_b32 s73, s73, s11\n"
    "v_mfma_f32_16x16x32_bf16 a[140:143], a[40:43], v[12:15], a[140:143]\n"
    "ds_read_b64_tr_b16 a[8:9], v238 offset:0\n"
    "s_xor_b32 s73, s73, 33570816\n"
    "v_mfma_f32_16x16x32_bf16 a[140:143], a[44:47], v[28:31], a[140:143]\n"
    "ds_read_b64_tr_b16 a[10:11], v238 offset:4096\n"
    "v_mfma_f32_16x16x32_bf16 a[144:147], a[44:47], v[60:63], a[144:147]\n"
    "v_mfma_f32_16x16x32_bf16 a[144:147], a[40:43], v[44:47], a[144:147]\n"
    "ds_read_b64_tr_b16 a[12:13], v238 offset:8192\n"
    "v_mfma_f32_16x16x32_bf16 a[144:147], a[36:39], v[56:59], a[144:147]\n"
    "ds_read_b64_tr_b16 a[14:15], v238 offset:12288\n"
    "v_mfma_f32_16x16x32_bf16 a[144:147], a[32:35], v[40:43], a[144:147]\n"
    "s_nop 0\n"
    "v_accvgpr_read_b32 v224, a140\n"
    "v_accvgpr_read_b32 v225, a141\n"
    "v_mfma_f32_16x16x32_bf16 a[148:151], a[32:35], v[72:75], a[148:151]\n"
    "v_accvgpr_read_b32 v226, a142\n"
    "v_accvgpr_read_b32 v227, a143\n"
    "v_mfma_f32_16x16x32_bf16 a[148:151], a[36:39], v[88:91], a[148:151]\n"
    "v_cvt_pk_bf16_f32 v208, v224, v225\n"
    "v_cvt_pk_bf16_f32 v209, v226, v227\n"
    "v_mfma_f32_16x16x32_bf16 a[148:151], a[40:43], v[76:79], a[148:151]\n"
    "v_mfma_f32_16x16x32_bf16 a[148:151], a[44:47], v[92:95], a[148:151]\n"
    "v_accvgpr_read_b32 v224, a144\n"
    "v_accvgpr_read_b32 v225, a145\n"
    "v_mfma_f32_16x16x32_bf16 a[152:155], a[44:47], v[124:127], a[152:155]\n"
    "v_accvgpr_read_b32 v226, a146\n"
    "v_accvgpr_read_b32 v227, a147\n"
    "v_mfma_f32_16x16x32_bf16 a[152:155], a[40:43], v[108:111], a[152:155]\n"
    "ds_read_b64_tr_b16 a[16:17], v235 offset:0\n"
    "v_cvt_pk_bf16_f32 v212, v224, v225\n"
    "v_mfma_f32_16x16x32_bf16 a[152:155], a[36:39], v[120:123], a[152:155]\n"
    "ds_read_b64_tr_b16 a[18:19], v235 offset:4096\n"
    "v_cvt_pk_bf16_f32 v213, v226, v227\n"
    "v_mfma_f32_16x16x32_bf16 a[152:155], a[32:35], v[104:107], a[152:155]\n"
    "ds_read_b64_tr_b16 a[20:21], v235 offset:8192\n"
    "v_accvgpr_read_b32 v224, a148\n"
    "v_mfma_f32_16x16x32_bf16 a[156:159], a[48:51], v[8:11], a[156:159]\n"
    "v_accvgpr_read_b32 v225, a149\n"
    "v_accvgpr_read_b32 v226, a150\n"
    "v_mfma_f32_16x16x32_bf16 a[156:159], a[52:55], v[24:27], a[156:159]\n"
    "ds_read_b64_tr_b16 a[22:23], v235 offset:12288\n"
    "v_accvgpr_read_b32 v227, a151\n"
    "v_mfma_f32_16x16x32_bf16 a[156:159], a[56:59], v[12:15], a[156:159]\n"
    "ds_read_b64_tr_b16 a[24:25], v239 offset:0\n"
    "v_cvt_pk_bf16_f32 v216, v224, v225\n"
    "v_cvt_pk_bf16_f32 v217, v226, v227\n"
    "s_waitcnt lgkmcnt(5)\n"
    "v_mfma_f32_16x16x32_bf16 a[156:159], a[60:63], v[28:31], a[156:159]\n"
    "ds_read_b64_tr_b16 a[26:27], v239 offset:4096\n"
    "v_accvgpr_read_b32 v224, a152\n"
    "v_mfma_f32_16x16x32_bf16 a[160:163], a[60:63], v[60:63], a[160:163]\n"
    "v_accvgpr_read_b32 v225, a153\n"
    "v_accvgpr_read_b32 v226, a154\n"
    "v_accvgpr_read_b32 v227, a155\n"
    "v_mfma_f32_16x16x32_bf16 a[160:163], a[56:59], v[44:47], a[160:163]\n"
    "ds_read_b64_tr_b16 a[28:29], v239 offset:8192\n"
    "v_cvt_pk_bf16_f32 v220, v224, v225\n"
    "v_cvt_pk_bf16_f32 v221, v226, v227\n"
    "v_mfma_f32_16x16x32_bf16 a[160:163], a[52:55], v[56:59], a[160:163]\n"
    "ds_read_b64_tr_b16 a[30:31], v239 offset:12288\n"
    "buffer_store_dwordx2 v[220:221], v254, s[48:51], s73 offen offset:3072 sc1\n"
    "v_mfma_f32_16x16x32_bf16 a[160:163], a[48:51], v[40:43], a[160:163]\n"
    "v_accvgpr_read_b32 v224, a156\n"
    "v_accvgpr_read_b32 v225, a157\n"
    "v_mfma_f32_16x16x32_bf16 a[164:167], a[48:51], v[72:75], a[164:167]\n"
    "v_accvgpr_read_b32 v226, a158\n"
    "v_accvgpr_read_b32 v227, a159\n"
    "v_cvt_pk_bf16_f32 v210, v224, v225\n"
    "v_mfma_f32_16x16x32_bf16 a[164:167], a[52:55], v[88:91], a[164:167]\n"
    "ds_read_b64_tr_b16 a[32:33], v234 offset:16384\n"
    "v_cvt_pk_bf16_f32 v211, v226, v227\n"
    "v_mfma_f32_16x16x32_bf16 a[164:167], a[56:59], v[76:79], a[164:167]\n"
    "ds_read_b64_tr_b16 a[34:35], v234 offset:20480\n"
    "buffer_store_dwordx4 v[208:211], v255, s[48:51], s73 offen offset:0 sc1\n"
    "v_mfma_f32_16x16x32_bf16 a[164:167], a[60:63], v[92:95], a[164:167]\n"
    "ds_read_b64_tr_b16 a[36:37], v234 offset:24576\n"
    "v_accvgpr_read_b32 v224, a160\n"
    "v_mfma_f32_16x16x32_bf16 a[168:171], a[0:3], a[216:219], a[168:171]\n"
    "v_accvgpr_read_b32 v225, a161\n"
    "v_accvgpr_read_b32 v226, a162\n"
    "v_accvgpr_read_b32 v227, a163\n"
    "v_mfma_f32_16x16x32_bf16 a[168:171], a[4:7], a[220:223], a[168:171]\n"
    "ds_read_b64_tr_b16 a[38:39], v234 offset:28672\n"
    "v_cvt_pk_bf16_f32 v214, v224, v225\n"
    "v_cvt_pk_bf16_f32 v215, v226, v227\n"
    "v_mfma_f32_16x16x32_bf16 a[168:171], a[8:11], a[224:227], a[168:171]\n"
    "ds_read_b64_tr_b16 a[40:41], v238 offset:16384\n"
    "buffer_store_dwordx4 v[212:215], v255, s[48:51], s73 offen offset:1024 sc1\n"
    "v_mfma_f32_16x16x32_bf16 a[168:171], a[12:15], a[228:231], a[168:171]\n"
    "ds_read_b64_tr_b16 a[42:43], v238 offset:20480\n"
    "v_accvgpr_read_b32 v224, a164\n"
    "v_mfma_f32_16x16x32_bf16 a[172:175], a[12:15], a[244:247], a[172:175]\n"
    "v_accvgpr_read_b32 v225, a165\n"
    "v_accvgpr_read_b32 v226, a166\n"
    "v_accvgpr_read_b32 v227, a167\n"
    "v_mfma_f32_16x16x32_bf16 a[172:175], a[8:11], a[240:243], a[172:175]\n"
    "ds_read_b64_tr_b16 a[44:45], v238 offset:24576\n"
    "v_cvt_pk_bf16_f32 v218, v224, v225\n"
    "v_cvt_pk_bf16_f32 v219, v226, v227\n"
    "v_mfma_f32_16x16x32_bf16 a[172:175], a[4:7], a[236:239], a[172:175]\n"
    "ds_read_b64_tr_b16 a[46:47], v238 offset:28672\n"
    "buffer_store_dwordx4 v[216:219], v255, s[48:51], s73 offen offset:2048 sc1\n"
    "v_mfma_f32_16x16x32_bf16 a[172:175], a[0:3], a[232:235], a[172:175]\n"
    "v_mfma_f32_16x16x32_bf16 a[64:67], a[0:3], a[96:99], a[64:67]\n"
    "v_mfma_f32_16x16x32_bf16 a[64:67], a[4:7], a[100:103], a[64:67]\n"
    "v_mfma_f32_16x16x32_bf16 a[64:67], a[8:11], a[104:107], a[64:67]\n"
    "v_mfma_f32_16x16x32_bf16 a[64:67], a[12:15], a[108:111], a[64:67]\n"
    "v_mfma_f32_16x16x32_bf16 a[68:71], a[12:15], a[124:127], a[68:71]\n"
    "v_mfma_f32_16x16x32_bf16 a[68:71], a[8:11], a[120:123], a[68:71]\n"
    "ds_read_b64_tr_b16 a[48:49], v235 offset:16384\n"
    "v_mfma_f32_16x16x32_bf16 a[68:71], a[4:7], a[116:119], a[68:71]\n"
    "ds_read_b64_tr_b16 a[50:51], v235 offset:20480\n"
    "v_mfma_f32_16x16x32_bf16 a[68:71], a[0:3], a[112:115], a[68:71]\n"
    "ds_read_b64_tr_b16 a[52:53], v235 offset:24576\n"
    "s_waitcnt lgkmcnt(3)\n"
    "v_mfma_f32_16x16x32_bf16 a[72:75], a[16:19], a[216:219], a[72:75]\n"
    "v_mfma_f32_16x16x32_bf16 a[72:75], a[20:23], a[220:223], a[72:75]\n"
    "ds_read_b64_tr_b16 a[54:55], v235 offset:28672\n"
    "v_mfma_f32_16x16x32_bf16 a[72:75], a[24:27], a[224:227], a[72:75]\n"
    "ds_read_b64_tr_b16 a[56:57], v239 offset:16384\n"
    "v_mfma_f32_16x16x32_bf16 a[72:75], a[28:31], a[228:231], a[72:75]\n"
    "ds_read_b64_tr_b16 a[58:59], v239 offset:20480\n"
    "v_mfma_f32_16x16x32_bf16 a[76:79], a[28:31], a[244:247], a[76:79]\n"
    "v_mfma_f32_16x16x32_bf16 a[76:79], a[24:27], a[240:243], a[76:79]\n"
    "ds_read_b64_tr_b16 a[60:61], v239 offset:24576\n"
    "v_mfma_f32_16x16x32_bf16 a[76:79], a[20:23], a[236:239], a[76:79]\n"
    "ds_read_b64_tr_b16 a[62:63], v239 offset:28672\n"
    "v_mfma_f32_16x16x32_bf16 a[76:79], a[16:19], a[232:235], a[76:79]\n"
    "v_mfma_f32_16x16x32_bf16 a[80:83], a[16:19], a[96:99], a[80:83]\n"
    "v_mfma_f32_16x16x32_bf16 a[80:83], a[20:23], a[100:103], a[80:83]\n"
    "ds_read_b64_tr_b16 a[0:1], v234 offset:33280\n"
    "v_mfma_f32_16x16x32_bf16 a[80:83], a[24:27], a[104:107], a[80:83]\n"
    "ds_read_b64_tr_b16 a[2:3], v234 offset:37376\n"
    "v_mfma_f32_16x16x32_bf16 a[80:83], a[28:31], a[108:111], a[80:83]\n"
    "ds_read_b64_tr_b16 a[4:5], v234 offset:41472\n"
    "v_mfma_f32_16x16x32_bf16 a[168:171], a[32:35], v[8:11], a[168:171]\n"
    "s_bfe_u32 s74, s10, 0x10009\n"
    "s_lshl_b32 s74, s74, 13\n"
    "v_mfma_f32_16x16x32_bf16 a[168:171], a[36:39], v[24:27], a[168:171]\n"
    "ds_read_b64_tr_b16 a[6:7], v234 offset:45568\n"
    "s_xor_b32 s74, s74, s11\n"
    "v_mfma_f32_16x16x32_bf16 a[168:171], a[40:43], v[12:15], a[168:171]\n"
    "ds_read_b64_tr_b16 a[8:9], v238 offset:33280\n"
    "s_xor_b32 s74, s74, 16781312\n"
    "v_mfma_f32_16x16x32_bf16 a[168:171], a[44:47], v[28:31], a[168:171]\n"
    "ds_read_b64_tr_b16 a[10:11], v238 offset:37376\n"
    "v_mfma_f32_16x16x32_bf16 a[172:175], a[44:47], v[60:63], a[172:175]\n"
    "v_mfma_f32_16x16x32_bf16 a[172:175], a[40:43], v[44:47], a[172:175]\n"
    "ds_read_b64_tr_b16 a[12:13], v238 offset:41472\n"
    "v_mfma_f32_16x16x32_bf16 a[172:175], a[36:39], v[56:59], a[172:175]\n"
    "ds_read_b64_tr_b16 a[14:15], v238 offset:45568\n"
    "v_mfma_f32_16x16x32_bf16 a[172:175], a[32:35], v[40:43], a[172:175]\n"
    "s_nop 0\n"
    "v_accvgpr_read_b32 v224, a168\n"
    "v_accvgpr_read_b32 v225, a169\n"
    "v_mfma_f32_16x16x32_bf16 a[64:67], a[32:35], v[72:75], a[64:67]\n"
    "v_accvgpr_read_b32 v226, a170\n"
    "v_accvgpr_read_b32 v227, a171\n"
    "v_mfma_f32_16x16x32_bf16 a[64:67], a[36:39], v[88:91], a[64:67]\n"
    "v_cvt_pk_bf16_f32 v192, v224, v225\n"
    "v_cvt_pk_bf16_f32 v193, v226, v227\n"
    "v_mfma_f32_16x16x32_bf16 a[64:67], a[40:43], v[76:79], a[64:67]\n"
    "s_waitcnt lgkmcnt(3)\n"
    "v_mfma_f32_16x16x32_bf16 a[64:67], a[44:47], v[92:95], a[64:67]\n"
    "v_accvgpr_read_b32 v224, a172\n"
    "v_accvgpr_read_b32 v225, a173\n"
    "v_mfma_f32_16x16x32_bf16 a[68:71], a[44:47], v[124:127], a[68:71]\n"
    "v_accvgpr_read_b32 v226, a174\n"
    "L_h17:\n"
    "s_cbranch_execz L_ret2\n"
    "v_accvgpr_read_b32 v227, a175\n"
    "v_mfma_f32_16x16x32_bf16 a[68:71], a[40:43], v[108:111], a[68:71]\n"
    "ds_read_b64_tr_b16 a[16:17], v235 offset:33280\n"
    "v_cvt_pk_bf16_f32 v196, v224, v225\n"
    "v_mfma_f32_16x16x32_bf16 a[68:71], a[36:39], v[120:123], a[68:71]\n"
    "ds_read_b64_tr_b16 a[18:19], v235 offset:37376\n"
    "v_cvt_pk_bf16_f32 v197, v226, v227\n"
    "v_mfma_f32_16x16x32_bf16 a[68:71], a[32:35], v[104:107], a[68:71]\n"
    "ds_read_b64_tr_b16 a[20:21], v235 offset:41472\n"
    "v_accvgpr_read_b32 v224, a64\n"
    "v_mfma_f32_16x16x32_bf16 a[72:75], a[48:51], v[8:11], a[72:75]\n"
    "v_accvgpr_read_b32 v225, a65\n"
    "v_accvgpr_read_b32 v226, a66\n"
    "v_mfma_f32_16x16x32_bf16 a[72:75], a[52:55], v[24:27], a[72:75]\n"
    "ds_read_b64_tr_b16 a[22:23], v235 offset:45568\n"
    "v_accvgpr_read_b32 v227, a67\n"
    "v_mfma_f32_16x16x32_bf16 a[72:75], a[56:59], v[12:15], a[72:75]\n"
    "ds_read_b64_tr_b16 a[24:25], v239 offset:33280\n"
    "v_cvt_pk_bf16_f32 v200, v224, v225\n"
    "v_cvt_pk_bf16_f32 v201, v226, v227\n"
    "s_waitcnt vmcnt(0)\n"
    "s_mov_b32 s96, 0x0\n"
    "s_mov_b32 s97, 0xc000000\n"
    "s_mov_b64 exec, s[96:97]\n"
    "buffer_store_dword v230, v228, s[68:71], 0 offen offset:0 sc1\n"
    "s_mov_b64 exec, -1\n"
    "v_mfma_f32_16x16x32_bf16 a[72:75], a[60:63], v[28:31], a[72:75]\n"
    "ds_read_b64_tr_b16 a[26:27], v239 offset:37376\n"
    "v_accvgpr_read_b32 v224, a68\n"
    "v_mfma_f32_16x16x32_bf16 a[76:79], a[60:63], v[60:63], a[76:79]\n"
    "v_accvgpr_read_b32 v225, a69\n"
    "v_accvgpr_read_b32 v226, a70\n"
    "v_accvgpr_read_b32 v227, a71\n"
    "v_mfma_f32_16x16x32_bf16 a[76:79], a[56:59], v[44:47], a[76:79]\n"
    "ds_read_b64_tr_b16 a[28:29], v239 offset:41472\n"
    "v_cvt_pk_bf16_f32 v204, v224, v225\n"
    "v_cvt_pk_bf16_f32 v205, v226, v227\n"
    "v_mfma_f32_16x16x32_bf16 a[76:79], a[52:55], v[56:59], a[76:79]\n"
    "ds_read_b64_tr_b16 a[30:31], v239 offset:45568\n"
    "buffer_store_dwordx2 v[204:205], v254, s[48:51], s74 offen offset:3072 sc1\n"
    "v_mfma_f32_16x16x32_bf16 a[76:79], a[48:51], v[40:43], a[76:79]\n"
    "v_accvgpr_read_b32 v224, a72\n"
    "v_accvgpr_read_b32 v225, a73\n"
    "v_mfma_f32_16x16x32_bf16 a[80:83], a[48:51], v[72:75], a[80:83]\n"
    "v_accvgpr_read_b32 v226, a74\n"
    "v_accvgpr_read_b32 v227, a75\n"
    "v_cvt_pk_bf16_f32 v194, v224, v225\n"
    "v_mfma_f32_16x16x32_bf16 a[80:83], a[52:55], v[88:91], a[80:83]\n"
    "ds_read_b64_tr_b16 a[32:33], v234 offset:49664\n"
    "v_cvt_pk_bf16_f32 v195, v226, v227\n"
    "v_mfma_f32_16x16x32_bf16 a[80:83], a[56:59], v[76:79], a[80:83]\n"
    "ds_read_b64_tr_b16 a[34:35], v234 offset:53760\n"
    "buffer_store_dwordx4 v[192:195], v255, s[48:51], s74 offen offset:0 sc1\n"
    "v_mfma_f32_16x16x32_bf16 a[80:83], a[60:63], v[92:95], a[80:83]\n"
    "ds_read_b64_tr_b16 a[36:37], v234 offset:57856\n"
    "v_accvgpr_read_b32 v224, a76\n"
    "v_mfma_f32_16x16x32_bf16 a[84:87], a[0:3], v[128:131], a[84:87]\n"
    "v_accvgpr_read_b32 v225, a77\n"
    "v_accvgpr_read_b32 v226, a78\n"
    "v_accvgpr_read_b32 v227, a79\n"
    "v_mfma_f32_16x16x32_bf16 a[84:87], a[4:7], v[132:135], a[84:87]\n"
    "ds_read_b64_tr_b16 a[38:39], v234 offset:61952\n"
    "v_cvt_pk_bf16_f32 v198, v224, v225\n"
    "v_cvt_pk_bf16_f32 v199, v226, v227\n"
    "s_waitcnt lgkmcnt(4)\n"
    "v_mfma_f32_16x16x32_bf16 a[84:87], a[8:11], v[136:139], a[84:87]\n"
    "ds_read_b64_tr_b16 a[40:41], v238 offset:49664\n"
    "buffer_store_dwordx4 v[196:199], v255, s[48:51], s74 offen offset:1024 sc1\n"
    "v_mfma_f32_16x16x32_bf16 a[84:87], a[12:15], v[140:143], a[84:87]\n"
    "ds_read_b64_tr_b16 a[42:43], v238 offset:53760\n"
    "v_accvgpr_read_b32 v224, a80\n"
    "v_mfma_f32_16x16x32_bf16 a[88:91], a[12:15], v[156:159], a[88:91]\n"
    "v_accvgpr_read_b32 v225, a81\n"
    "v_accvgpr_read_b32 v226, a82\n"
    "v_accvgpr_read_b32 v227, a83\n"
    "v_mfma_f32_16x16x32_bf16 a[88:91], a[8:11], v[152:155], a[88:91]\n"
    "ds_read_b64_tr_b16 a[44:45], v238 offset:57856\n"
    "v_cvt_pk_bf16_f32 v202, v224, v225\n"
    "v_cvt_pk_bf16_f32 v203, v226, v227\n"
    "v_mfma_f32_16x16x32_bf16 a[88:91], a[4:7], v[148:151], a[88:91]\n"
    "ds_read_b64_tr_b16 a[46:47], v238 offset:61952\n"
    "buffer_store_dwordx4 v[200:203], v255, s[48:51], s74 offen offset:2048 sc1\n"
    "v_mfma_f32_16x16x32_bf16 a[88:91], a[0:3], v[144:147], a[88:91]\n"
    "v_bfe_u32 v226, v242, 4, 4\n"
    "v_mul_u32_u24 v226, 80, v226\n"
    "v_mfma_f32_16x16x32_bf16 a[92:95], a[0:3], v[160:163], a[92:95]\n"
    "v_bfe_u32 v227, v242, 8, 2\n"
    "v_lshl_add_u32 v226, v227, 3, v226\n"
    "v_mfma_f32_16x16x32_bf16 a[92:95], a[4:7], v[164:167], a[92:95]\n"
    "s_add_u32 s81, s42, 131072\n"
    "s_lshl_b32 s82, s41, 5\n"
    "v_mfma_f32_16x16x32_bf16 a[92:95], a[8:11], v[168:171], a[92:95]\n"
    "s_add_u32 s82, s82, s81\n"
    "v_add_u32 v240, s82, v226\n"
    "v_mfma_f32_16x16x32_bf16 a[92:95], a[12:15], v[172:175], a[92:95]\n"
    "s_xor_b32 s82, s82, 32\n"
    "v_add_u32 v241, s82, v226\n"
    "v_mfma_f32_16x16x32_bf16 a[208:211], a[12:15], v[188:191], a[208:211]\n"
    "v_bfe_u32 v226, v242, 6, 4\n"
    "v_bfe_u32 v227, v242, 4, 2\n"
    "v_mfma_f32_16x16x32_bf16 a[208:211], a[8:11], v[184:187], a[208:211]\n"
    "ds_read_b64_tr_b16 a[48:49], v235 offset:49664\n"
    "v_mul_u32_u24 v244, 80, v226\n"
    "v_mfma_f32_16x16x32_bf16 a[208:211], a[4:7], v[180:183], a[208:211]\n"
    "ds_read_b64_tr_b16 a[50:51], v235 offset:53760\n"
    "v_lshl_add_u32 v244, v227, 4, v244\n"
    "v_mfma_f32_16x16x32_bf16 a[208:211], a[0:3], v[176:179], a[208:211]\n"
    "ds_read_b64_tr_b16 a[52:53], v235 offset:57856\n"
    "v_add_u32 v244, s81, v244\n"
    "buffer_load_dword v247, v246, s[68:71], s92 offen offset:0 sc1\n"
    "v_mfma_f32_16x16x32_bf16 a[212:215], a[16:19], v[128:131], a[212:215]\n"
    "v_lshlrev_b32 v245, 11, v226\n"
    "v_lshl_add_u32 v245, v227, 4, v245\n"
    "v_mfma_f32_16x16x32_bf16 a[212:215], a[20:23], v[132:135], a[212:215]\n"
    "ds_read_b64_tr_b16 a[54:55], v235 offset:61952\n"
    "v_lshrrev_b32 v227, 1, v227\n"
    "v_mfma_f32_16x16x32_bf16 a[212:215], a[24:27], v[136:139], a[212:215]\n"
    "ds_read_b64_tr_b16 a[56:57], v239 offset:49664\n"
    "v_xor_b32 v227, s41, v227\n"
    "v_mfma_f32_16x16x32_bf16 a[212:215], a[28:31], v[140:143], a[212:215]\n"
    "ds_read_b64_tr_b16 a[58:59], v239 offset:53760\n"
    "v_lshl_add_u32 v243, v227, 24, v245\n"
    "v_mfma_f32_16x16x32_bf16 a[248:251], a[28:31], v[156:159], a[248:251]\n"
    "s_lshr_b32 s79, s40, 1\n"
    "s_mul_i32 s79, s79, 229376\n"
    "v_mfma_f32_16x16x32_bf16 a[248:251], a[24:27], v[152:155], a[248:251]\n"
    "ds_read_b64_tr_b16 a[60:61], v239 offset:57856\n"
    "s_lshl_b32 s76, s41, 17\n"
    "v_mfma_f32_16x16x32_bf16 a[248:251], a[20:23], v[148:151], a[248:251]\n"
    "ds_read_b64_tr_b16 a[62:63], v239 offset:61952\n"
    "s_add_u32 s76, s76, s79\n"
    "v_mfma_f32_16x16x32_bf16 a[248:251], a[16:19], v[144:147], a[248:251]\n"
    "s_add_u32 s79, s79, 98304\n"
    "s_add_u32 s77, s76, 32768\n"
    "v_mfma_f32_16x16x32_bf16 a[252:255], a[16:19], v[160:163], a[252:255]\n"
    "s_add_u32 s78, s76, 65536\n"
    "s_mov_b64 s[86:87], 0\n"
    "v_mfma_f32_16x16x32_bf16 a[252:255], a[20:23], v[164:167], a[252:255]\n"
    "ds_read_b64_tr_b16 a[0:1], v234 offset:512\n"
    "v_mov_b32 v224, 0x207\n"
    "v_mfma_f32_16x16x32_bf16 a[252:255], a[24:27], v[168:171], a[252:255]\n"
    "ds_read_b64_tr_b16 a[2:3], v234 offset:4608\n"
    "v_mfma_f32_16x16x32_bf16 a[252:255], a[28:31], v[172:175], a[252:255]\n"
    "ds_read_b64_tr_b16 a[4:5], v234 offset:8704\n"
    "s_waitcnt lgkmcnt(5)\n"
    "v_mfma_f32_16x16x32_bf16 a[84:87], a[32:35], v[0:3], a[84:87]\n"
    "v_mfma_f32_16x16x32_bf16 a[84:87], a[36:39], v[16:19], a[84:87]\n"
    "ds_read_b64_tr_b16 a[6:7], v234 offset:12800\n"
    "v_mfma_f32_16x16x32_bf16 a[84:87], a[40:43], v[4:7], a[84:87]\n"
    "ds_read_b64_tr_b16 a[8:9], v238 offset:512\n"
    "v_mfma_f32_16x16x32_bf16 a[84:87], a[44:47], v[20:23], a[84:87]\n"
    "ds_read_b64_tr_b16 a[10:11], v238 offset:4608\n"
    "v_mfma_f32_16x16x32_bf16 a[88:91], a[44:47], v[52:55], a[88:91]\n"
    "v_mfma_f32_16x16x32_bf16 a[88:91], a[40:43], v[36:39], a[88:91]\n"
    "ds_read_b64_tr_b16 a[12:13], v238 offset:8704\n"
    "v_mfma_f32_16x16x32_bf16 a[88:91], a[36:39], v[48:51], a[88:91]\n"
    "ds_read_b64_tr_b16 a[14:15], v238 offset:12800\n"
    "v_mfma_f32_16x16x32_bf16 a[88:91], a[32:35], v[32:35], a[88:91]\n"
    "v_mfma_f32_16x16x32_bf16 a[92:95], a[32:35], v[64:67], a[92:95]\n"
    "v_mfma_f32_16x16x32_bf16 a[92:95], a[36:39], v[80:83], a[92:95]\n"
    "v_mfma_f32_16x16x32_bf16 a[92:95], a[40:43], v[68:71], a[92:95]\n"
    "v_mfma_f32_16x16x32_bf16 a[92:95], a[44:47], v[84:87], a[92:95]\n"
    "v_mfma_f32_16x16x32_bf16 a[208:211], a[44:47], v[116:119], a[208:211]\n"
    "v_mfma_f32_16x16x32_bf16 a[208:211], a[40:43], v[100:103], a[208:211]\n"
    "ds_read_b64_tr_b16 a[16:17], v235 offset:512\n"
    "v_mfma_f32_16x16x32_bf16 a[208:211], a[36:39], v[112:115], a[208:211]\n"
    "ds_read_b64_tr_b16 a[18:19], v235 offset:4608\n"
    "v_mfma_f32_16x16x32_bf16 a[208:211], a[32:35], v[96:99], a[208:211]\n"
    "ds_read_b64_tr_b16 a[20:21], v235 offset:8704\n"
    "v_mfma_f32_16x16x32_bf16 a[212:215], a[48:51], v[0:3], a[212:215]\n"
    "v_mfma_f32_16x16x32_bf16 a[212:215], a[52:55], v[16:19], a[212:215]\n"
    "ds_read_b64_tr_b16 a[22:23], v235 offset:12800\n"
    "v_mfma_f32_16x16x32_bf16 a[212:215], a[56:59], v[4:7], a[212:215]\n"
    "ds_read_b64_tr_b16 a[24:25], v239 offset:512\n"
    "s_waitcnt vmcnt(1)\n"
    "s_mov_b32 s96, 0x0\n"
    "s_mov_b32 s97, 0x2000000\n"
    "s_mov_b64 exec, s[96:97]\n"
    "buffer_store_dword v230, v228, s[68:71], 0 offen offset:0 sc1\n"
    "s_mov_b64 exec, -1\n"
    "s_waitcnt lgkmcnt(5)\n"
    "v_mfma_f32_16x16x32_bf16 a[212:215], a[60:63], v[20:23], a[212:215]\n"
    "ds_read_b64_tr_b16 a[26:27], v239 offset:4608\n"
    "v_mfma_f32_16x16x32_bf16 a[248:251], a[60:63], v[52:55], a[248:251]\n"
    "v_mfma_f32_16x16x32_bf16 a[248:251], a[56:59], v[36:39], a[248:251]\n"
    "ds_read_b64_tr_b16 a[28:29], v239 offset:8704\n"
    "v_mfma_f32_16x16x32_bf16 a[248:251], a[52:55], v[48:51], a[248:251]\n"
    "ds_read_b64_tr_b16 a[30:31], v239 offset:12800\n"
    "s_mov_b32 s96, 0x0\n"
    "s_mov_b32 s97, 0xc000000\n"
    "s_waitcnt vmcnt(1)\n"
    "v_cmp_eq_u32 vcc, v247, v229\n"
    "s_nop 1\n"
    "s_and_b64 vcc, vcc, s[96:97]\n"
    "s_cmp_eq_u64 vcc, s[96:97]\n"
    "s_cbranch_scc0 L_slow8\n"
    "L_back8:\n"
    "s_xor_b32 s91, s11, 20480\n"
    "v_accvgpr_write_b32 a206, 0\n"
    "v_accvgpr_write_b32 a207, 0\n"
    "buffer_load_dwordx4 a[192:195], v255, s[48:51], s91 offen offset:0 sc1\n"
    "buffer_load_dwordx4 a[196:199], v255, s[48:51], s91 offen offset:1024 sc1\n"
    "buffer_load_dwordx4 a[200:203], v255, s[48:51], s91 offen offset:2048 sc1\n"
    "buffer_load_dwordx2 a[204:205], v254, s[48:51], s91 offen offset:3072 sc1\n"
    "s_xor_b32 s90, s11, 16384\n"
    "v_mov_b32 v222, 0\n"
    "v_mov_b32 v223, 0\n"
    "buffer_load_dwordx4 v[208:211], v255, s[48:51], s90 offen offset:0 sc1\n"
    "buffer_load_dwordx4 v[212:215], v255, s[48:51], s90 offen offset:1024 sc1\n"
    "buffer_load_dwordx4 v[216:219], v255, s[48:51], s90 offen offset:2048 sc1\n"
    "buffer_load_dwordx2 v[220:221], v254, s[48:51], s90 offen offset:3072 sc1\n"
    "v_mfma_f32_16x16x32_bf16 a[248:251], a[48:51], v[32:35], a[248:251]\n"
    "v_mfma_f32_16x16x32_bf16 a[252:255], a[48:51], v[64:67], a[252:255]\n"
    "v_mfma_f32_16x16x32_bf16 a[252:255], a[52:55], v[80:83], a[252:255]\n"
    "ds_read_b64_tr_b16 a[32:33], v234 offset:16896\n"
    "v_mfma_f32_16x16x32_bf16 a[252:255], a[56:59], v[68:71], a[252:255]\n"
    "ds_read_b64_tr_b16 a[34:35], v234 offset:20992\n"
    "v_mfma_f32_16x16x32_bf16 a[252:255], a[60:63], v[84:87], a[252:255]\n"
    "ds_read_b64_tr_b16 a[36:37], v234 offset:25088\n"
    "v_mfma_f32_16x16x32_bf16 a[84:87], a[0:3], a[216:219], a[84:87]\n"
    "v_mfma_f32_16x16x32_bf16 a[84:87], a[4:7], a[220:223], a[84:87]\n"
    "ds_read_b64_tr_b16 a[38:39], v234 offset:29184\n"
    "v_mfma_f32_16x16x32_bf16 a[84:87], a[8:11], a[224:227], a[84:87]\n"
    "ds_read_b64_tr_b16 a[40:41], v238 offset:16896\n"
    "v_mfma_f32_16x16x32_bf16 a[84:87], a[12:15], a[228:231], a[84:87]\n"
    "ds_read_b64_tr_b16 a[42:43], v238 offset:20992\n"
    "v_mfma_f32_16x16x32_bf16 a[88:91], a[12:15], a[244:247], a[88:91]\n"
    "v_mfma_f32_16x16x32_bf16 a[88:91], a[8:11], a[240:243], a[88:91]\n"
    "ds_read_b64_tr_b16 a[44:45], v238 offset:25088\n"
    "buffer_load_dword v231, v246, s[68:71], s92 offen offset:0 sc1\n"
    "v_mfma_f32_16x16x32_bf16 a[88:91], a[4:7], a[236:239], a[88:91]\n"
    "ds_read_b64_tr_b16 a[46:47], v238 offset:29184\n"
    "v_mfma_f32_16x16x32_bf16 a[88:91], a[0:3], a[232:235], a[88:91]\n"
    "v_mfma_f32_16x16x32_bf16 a[92:95], a[0:3], a[96:99], a[92:95]\n"
    "v_mfma_f32_16x16x32_bf16 a[92:95], a[4:7], a[100:103], a[92:95]\n"
    "v_mfma_f32_16x16x32_bf16 a[92:95], a[8:11], a[104:107], a[92:95]\n"
    "v_mfma_f32_16x16x32_bf16 a[92:95], a[12:15], a[108:111], a[92:95]\n"
    "v_mfma_f32_16x16x32_bf16 a[208:211], a[12:15], a[124:127], a[208:211]\n"
    "v_mfma_f32_16x16x32_bf16 a[208:211], a[8:11], a[120:123], a[208:211]\n"
    "ds_read_b64_tr_b16 a[48:49], v235 offset:16896\n"
    "v_mfma_f32_16x16x32_bf16 a[208:211], a[4:7], a[116:119], a[208:211]\n"
    "ds_read_b64_tr_b16 a[50:51], v235 offset:20992\n"
    "v_mfma_f32_16x16x32_bf16 a[208:211], a[0:3], a[112:115], a[208:211]\n"
    "ds_read_b64_tr_b16 a[52:53], v235 offset:25088\n"
    "s_waitcnt lgkmcnt(3)\n"
    "v_mfma_f32_16x16x32_bf16 a[212:215], a[16:19], a[216:219], a[212:215]\n"
    "v_mfma_f32_16x16x32_bf16 a[212:215], a[20:23], a[220:223], a[212:215]\n"
    "ds_read_b64_tr_b16 a[54:55], v235 offset:29184\n"
    "v_mfma_f32_16x16x32_bf16 a[212:215], a[24:27], a[224:227], a[212:215]\n"
    "ds_read_b64_tr_b16 a[56:57], v239 offset:16896\n"
    "v_mfma_f32_16x16x32_bf16 a[212:215], a[28:31], a[228:231], a[212:215]\n"
    "ds_read_b64_tr_b16 a[58:59], v239 offset:20992\n"
    "v_mfma_f32_16x16x32_bf16 a[248:251], a[28:31], a[244:247], a[248:251]\n"
    "v_mfma_f32_16x16x32_bf16 a[248:251], a[24:27], a[240:243], a[248:251]\n"
    "ds_read_b64_tr_b16 a[60:61], v239 offset:25088\n"
    "v_mfma_f32_16x16x32_bf16 a[248:251], a[20:23], a[236:239], a[248:251]\n"
    "ds_read_b64_tr_b16 a[62:63], v239 offset:29184\n"
    "v_mfma_f32_16x16x32_bf16 a[248:251], a[16:19], a[232:235], a[248:251]\n"
    "v_mfma_f32_16x16x32_bf16 a[252:255], a[16:19], a[96:99], a[252:255]\n"
    "v_mfma_f32_16x16x32_bf16 a[252:255], a[20:23], a[100:103], a[252:255]\n"
    "v_mfma_f32_16x16x32_bf16 a[252:255], a[24:27], a[104:107], a[252:255]\n"
    "v_mfma_f32_16x16x32_bf16 a[252:255], a[28:31], a[108:111], a[252:255]\n"
    "v_mfma_f32_16x16x32_bf16 a[84:87], a[32:35], v[8:11], a[84:87]\n"
    "v_mfma_f32_16x16x32_bf16 a[84:87], a[36:39], v[24:27], a[84:87]\n"
    "v_mfma_f32_16x16x32_bf16 a[84:87], a[40:43], v[12:15], a[84:87]\n"
    "v_mfma_f32_16x16x32_bf16 a[84:87], a[44:47], v[28:31], a[84:87]\n"
    "v_mfma_f32_16x16x32_bf16 a[88:91], a[44:47], v[60:63], a[88:91]\n"
    "v_mfma_f32_16x16x32_bf16 a[88:91], a[40:43], v[44:47], a[88:91]\n"
    "v_mfma_f32_16x16x32_bf16 a[88:91], a[36:39], v[56:59], a[88:91]\n"
    "v_mfma_f32_16x16x32_bf16 a[88:91], a[32:35], v[40:43], a[88:91]\n"
    "s_mov_b32 s96, 0x0\n"
    "s_mov_b32 s97, 0x2000000\n"
    "s_waitcnt vmcnt(0)\n"
    "v_cmp_eq_u32 vcc, v231, v229\n"
    "s_nop 1\n"
    "s_and_b64 vcc, vcc, s[96:97]\n"
    "s_cmp_eq_u64 vcc, s[96:97]\n"
    "s_cbranch_scc0 L_slow5\n"
    "L_back5:\n"
    "s_xor_b32 s89, s11, 4096\n"
    "v_mov_b32 v142, 0\n"
    "v_mov_b32 v143, 0\n"
    "buffer_load_dwordx4 v[128:131], v255, s[48:51], s89 offen offset:0 sc1\n"
    "buffer_load_dwordx4 v[132:135], v255, s[48:51], s89 offen offset:1024 sc1\n"
    "buffer_load_dwordx4 v[136:139], v255, s[48:51], s89 offen offset:2048 sc1\n"
    "buffer_load_dwordx2 v[140:141], v254, s[48:51], s89 offen offset:3072 sc1\n"
    "v_mfma_f32_16x16x32_bf16 a[92:95], a[32:35], v[72:75], a[92:95]\n"
    "v_mfma_f32_16x16x32_bf16 a[92:95], a[36:39], v[88:91], a[92:95]\n"
    "v_mfma_f32_16x16x32_bf16 a[92:95], a[40:43], v[76:79], a[92:95]\n"
    "v_mfma_f32_16x16x32_bf16 a[92:95], a[44:47], v[92:95], a[92:95]\n"
    "v_mfma_f32_16x16x32_bf16 a[208:211], a[44:47], v[124:127], a[208:211]\n"
    "v_mfma_f32_16x16x32_bf16 a[208:211], a[40:43], v[108:111], a[208:211]\n"
    "v_mfma_f32_16x16x32_bf16 a[208:211], a[36:39], v[120:123], a[208:211]\n"
    "v_mfma_f32_16x16x32_bf16 a[208:211], a[32:35], v[104:107], a[208:211]\n"
    "s_waitcnt lgkmcnt(0)\n"
    "v_mfma_f32_16x16x32_bf16 a[212:215], a[48:51], v[8:11], a[212:215]\n"
    "v_mfma_f32_16x16x32_bf16 a[212:215], a[52:55], v[24:27], a[212:215]\n"
    "v_mfma_f32_16x16x32_bf16 a[212:215], a[56:59], v[12:15], a[212:215]\n"
    "v_mfma_f32_16x16x32_bf16 a[212:215], a[60:63], v[28:31], a[212:215]\n"
    "v_mfma_f32_16x16x32_bf16 a[248:251], a[60:63], v[60:63], a[248:251]\n"
    "v_mfma_f32_16x16x32_bf16 a[248:251], a[56:59], v[44:47], a[248:251]\n"
    "v_mfma_f32_16x16x32_bf16 a[248:251], a[52:55], v[56:59], a[248:251]\n"
    "v_mfma_f32_16x16x32_bf16 a[248:251], a[48:51], v[40:43], a[248:251]\n"
    "v_mfma_f32_16x16x32_bf16 a[252:255], a[48:51], v[72:75], a[252:255]\n"
    "v_mfma_f32_16x16x32_bf16 a[252:255], a[52:55], v[88:91], a[252:255]\n"
    "v_mfma_f32_16x16x32_bf16 a[252:255], a[56:59], v[76:79], a[252:255]\n"
    "v_mfma_f32_16x16x32_bf16 a[252:255], a[60:63], v[92:95], a[252:255]\n"
    "v_mfma_f32_16x16x32_bf16 a[84:87], v[250:253], a[192:195], a[84:87]\n"
    "v_mfma_f32_16x16x32_bf16 a[212:215], v[248:251], a[192:195], a[212:215]\n"
    "v_mfma_f32_16x16x32_bf16 a[88:91], v[250:253], a[196:199], a[88:91]\n"
    "v_mfma_f32_16x16x32_bf16 a[248:251], v[248:251], a[196:199], a[248:251]\n"
    "v_mfma_f32_16x16x32_bf16 a[92:95], v[250:253], a[200:203], a[92:95]\n"
    "v_mfma_f32_16x16x32_bf16 a[252:255], v[248:251], a[200:203], a[252:255]\n"
    "v_mfma_f32_16x16x32_bf16 a[208:211], v[250:253], a[204:207], a[208:211]\n"
    "v_mfma_f32_16x16x32_bf16 a[84:87], v[250:253], v[208:211], a[84:87]\n"
    "v_mfma_f32_16x16x32_bf16 a[212:215], v[248:251], v[208:211], a[212:215]\n"
    "v_mfma_f32_16x16x32_bf16 a[88:91], v[250:253], v[212:215], a[88:91]\n"
    "v_mfma_f32_16x16x32_bf16 a[248:251], v[248:251], v[212:215], a[248:251]\n"
    "v_mfma_f32_16x16x32_bf16 a[92:95], v[250:253], v[216:219], a[92:95]\n"
    "v_mfma_f32_16x16x32_bf16 a[252:255], v[248:251], v[216:219], a[252:255]\n"
    "v_mfma_f32_16x16x32_bf16 a[208:211], v[250:253], v[220:223], a[208:211]\n"
    "s_waitcnt vmcnt(3)\n"
    "v_mfma_f32_16x16x32_bf16 a[84:87], v[250:253], v[128:131], a[84:87]\n"
    "v_mfma_f32_16x16x32_bf16 a[212:215], v[248:251], v[128:131], a[212:215]\n"
    "s_waitcnt vmcnt(2)\n"
    "v_mfma_f32_16x16x32_bf16 a[88:91], v[250:253], v[132:135], a[88:91]\n"
    "v_mfma_f32_16x16x32_bf16 a[248:251], v[248:251], v[132:135], a[248:251]\n"
    "s_waitcnt vmcnt(1)\n"
    "v_mfma_f32_16x16x32_bf16 a[92:95], v[250:253], v[136:139], a[92:95]\n"
    "v_mfma_f32_16x16x32_bf16 a[252:255], v[248:251], v[136:139], a[252:255]\n"
    "s_waitcnt vmcnt(0)\n"
    "v_mfma_f32_16x16x32_bf16 a[208:211], v[250:253], v[140:143], a[208:211]\n"
    "v_accvgpr_read_b32 v0, a84\n"
    "v_accvgpr_read_b32 v1, a85\n"
    "v_accvgpr_read_b32 v2, a86\n"
    "v_accvgpr_read_b32 v3, a87\n"
    "v_cvt_pk_bf16_f32 v4, v0, v1\n"
    "v_cvt_pk_bf16_f32 v5, v2, v3\n"
    "ds_write_b64 v240, v[4:5] offset:0\n"
    "v_accvgpr_read_b32 v8, a212\n"
    "v_accvgpr_read_b32 v9, a213\n"
    "v_accvgpr_read_b32 v10, a214\n"
    "v_accvgpr_read_b32 v11, a215\n"
    "v_cvt_pk_bf16_f32 v12, v8, v9\n"
    "v_cvt_pk_bf16_f32 v13, v10, v11\n"
    "ds_write_b64 v241, v[12:13] offset:0\n"
    "v_accvgpr_read_b32 v16, a88\n"
    "v_accvgpr_read_b32 v17, a89\n"
    "v_accvgpr_read_b32 v18, a90\n"
    "v_accvgpr_read_b32 v19, a91\n"
    "v_cvt_pk_bf16_f32 v20, v16, v17\n"
    "v_cvt_pk_bf16_f32 v21, v18, v19\n"
    "ds_write_b64 v240, v[20:21] offset:1280\n"
    "v_accvgpr_read_b32 v24, a248\n"
    "v_accvgpr_read_b32 v25, a249\n"
    "v_accvgpr_read_b32 v26, a250\n"
    "v_accvgpr_read_b32 v27, a251\n"
    "v_cvt_pk_bf16_f32 v28, v24, v25\n"
    "v_cvt_pk_bf16_f32 v29, v26, v27\n"
    "ds_write_b64 v241, v[28:29] offset:1280\n"
    "v_accvgpr_read_b32 v32, a92\n"
    "v_accvgpr_read_b32 v33, a93\n"
    "v_accvgpr_read_b32 v34, a94\n"
    "v_accvgpr_read_b32 v35, a95\n"
    "v_cvt_pk_bf16_f32 v36, v32, v33\n"
    "v_cvt_pk_bf16_f32 v37, v34, v35\n"
    "ds_write_b64 v240, v[36:37] offset:2560\n"
    "v_accvgpr_read_b32 v40, a252\n"
    "v_accvgpr_read_b32 v41, a253\n"
    "v_accvgpr_read_b32 v42, a254\n"
    "v_accvgpr_read_b32 v43, a255\n"
    "v_cvt_pk_bf16_f32 v44, v40, v41\n"
    "v_cvt_pk_bf16_f32 v45, v42, v43\n"
    "ds_write_b64 v241, v[44:45] offset:2560\n"
    "v_accvgpr_read_b32 v48, a208\n"
    "v_accvgpr_read_b32 v49, a209\n"
    "v_accvgpr_read_b32 v50, a210\n"
    "v_accvgpr_read_b32 v51, a211\n"
    "v_cvt_pk_bf16_f32 v52, v48, v49\n"
    "v_cvt_pk_bf16_f32 v53, v50, v51\n"
    "ds_write_b64 v240, v[52:53] offset:3840\n"
    "s_waitcnt lgkmcnt(0)\n"
    "ds_read_b128 v[64:67], v244 offset:0\n"
    "ds_read_b128 v[68:71], v244 offset:1280\n"
    "ds_read_b128 v[72:75], v244 offset:2560\n"
    "ds_read_b128 v[76:79], v244 offset:3840\n"
    "v_cmp_class_f32 vcc, v0, v224\n"
    "s_nop 0\n"
    "s_or_b64 s[86:87], s[86:87], vcc\n"
    "v_cmp_class_f32 vcc, v16, v224\n"
    "s_nop 0\n"
    "s_or_b64 s[86:87], s[86:87], vcc\n"
    "v_cmp_class_f32 vcc, v32, v224\n"
    "s_nop 0\n"
    "s_or_b64 s[86:87], s[86:87], vcc\n"
    "v_cmp_class_f32 vcc, v48, v224\n"
    "s_nop 0\n"
    "s_or_b64 s[86:87], s[86:87], vcc\n"
    "s_waitcnt lgkmcnt(3)\n"
    "buffer_store_dwordx4 v[64:67], v245, s[44:47], s76 offen offset:0\n"
    "s_waitcnt lgkmcnt(2)\n"
    "buffer_store_dwordx4 v[68:71], v245, s[44:47], s77 offen offset:0\n"
    "s_waitcnt lgkmcnt(1)\n"
    "buffer_store_dwordx4 v[72:75], v245, s[44:47], s78 offen offset:0\n"
    "s_waitcnt lgkmcnt(0)\n"
    "buffer_store_dwordx4 v[76:79], v243, s[44:47], s79 offen offset:0\n"
    "s_or_b32 s86, s86, s24\n"
    "s_or_b32 s86, s86, s87\n"
    "s_cselect_b32 s86, 0x10000, 0\n"
    "s_or_b32 %0, s86, s95\n"
    "L_end:\n"
    : "=s"(bad) : "s"(ka), "s"(id), "s"(w) : "v0", "v1", "v2", "v3", "v4", "v5", "v6", "v7", "v8", "v9", "v10", "v11", "v12", "v13", "v14", "v15", "v16", "v17", "v18", "v19", "v20", "v21", "v22", "v23", "v24", "v25", "v26", "v27", "v28", "v29", "v30", "v31", "v32", "v33", "v34", "v35", "v36", "v37", "v38", "v39", "v40", "v41", "v42", "v43", "v44", "v45", "v46", "v47", "v48", "v49", "v50", "v51", "v52", "v53", "v54", "v55", "v56", "v57", "v58", "v59", "v60", "v61", "v62", "v63", "v64", "v65", "v66", "v67", "v68", "v69", "v70", "v71", "v72", "v73", "v74", "v75", "v76", "v77", "v78", "v79", "v80", "v81", "v82", "v83", "v84", "v85", "v86", "v87", "v88", "v89", "v90", "v91", "v92", "v93", "v94", "v95", "v96", "v97", "v98", "v99", "v100", "v101", "v102", "v103", "v104", "v105", "v106", "v107", "v108", "v109", "v110", "v111", "v112", "v113", "v114", "v115", "v116", "v117", "v118", "v119", "v120", "v121", "v122", "v123", "v124", "v125", "v126", "v127", "v128", "v129", "v130", "v131", "v132", "v133", "v134", "v135", "v136", "v137", "v138", "v139", "v140", "v141", "v142", "v143", "v144", "v145", "v146", "v147", "v148", "v149", "v150", "v151", "v152", "v153", "v154", "v155", "v156", "v157", "v158", "v159", "v160", "v161", "v162", "v163", "v164", "v165", "v166", "v167", "v168", "v169", "v170", "v171", "v172", "v173", "v174", "v175", "v176", "v177", "v178", "v179", "v180", "v181", "v182", "v183", "v184", "v185", "v186", "v187", "v188", "v189", "v190", "v191", "v192", "v193", "v194", "v195", "v196", "v197", "v198", "v199", "v200", "v201", "v202", "v203", "v204", "v205", "v206", "v207", "v208", "v209", "v210", "v211", "v212", "v213", "v214", "v215", "v216", "v217", "v218", "v219", "v220", "v221", "v222", "v223", "v224", "v225", "v226", "v227", "v228", "v229", "v230", "v231", "v232", "v233", "v234", "v235", "v236", "v237", "v238", "v239", "v240", "v241", "v242", "v243", "v244", "v245", "v246", "v247", "v248", "v249", "v250", "v251", "v252", "v253", "v254", "v255", "a0", "a1", "a2", "a3", "a4", "a5", "a6", "a7", "a8", "a9", "a10", "a11", "a12", "a13", "a14", "a15", "a16", "a17", "a18", "a19", "a20", "a21", "a22", "a23", "a24", "a25", "a26", "a27", "a28", "a29", "a30", "a31", "a32", "a33", "a34", "a35", "a36", "a37", "a38", "a39", "a40", "a41", "a42", "a43", "a44", "a45", "a46", "a47", "a48", "a49", "a50", "a51", "a52", "a53", "a54", "a55", "a56", "a57", "a58", "a59", "a60", "a61", "a62", "a63", "a64", "a65", "a66", "a67", "a68", "a69", "a70", "a71", "a72", "a73", "a74", "a75", "a76", "a77", "a78", "a79", "a80", "a81", "a82", "a83", "a84", "a85", "a86", "a87", "a88", "a89", "a90", "a91", "a92", "a93", "a94", "a95", "a96", "a97", "a98", "a99", "a100", "a101", "a102", "a103", "a104", "a105", "a106", "a107", "a108", "a109", "a110", "a111", "a112", "a113", "a114", "a115", "a116", "a117", "a118", "a119", "a120", "a121", "a122", "a123", "a124", "a125", "a126", "a127", "a128", "a129", "a130", "a131", "a132", "a133", "a134", "a135", "a136", "a137", "a138", "a139", "a140", "a141", "a142", "a143", "a144", "a145", "a146", "a147", "a148", "a149", "a150", "a151", "a152", "a153", "a154", "a155", "a156", "a157", "a158", "a159", "a160", "a161", "a162", "a163", "a164", "a165", "a166", "a167", "a168", "a169", "a170", "a171", "a172", "a173", "a174", "a175", "a176", "a177", "a178", "a179", "a180", "a181", "a182", "a183", "a184", "a185", "a186", "a187", "a188", "a189", "a190", "a191", "a192", "a193", "a194", "a195", "a196", "a197", "a198", "a199", "a200", "a201", "a202", "a203", "a204", "a205", "a206", "a207", "a208", "a209", "a210", "a211", "a212", "a213", "a214", "a215", "a216", "a217", "a218", "a219", "a220", "a221", "a222", "a223", "a224", "a225", "a226", "a227", "a228", "a229", "a230", "a231", "a232", "a233", "a234", "a235", "a236", "a237", "a238", "a239", "a240", "a241", "a242", "a243", "a244", "a245", "a246", "a247", "a248", "a249", "a250", "a251", "a252", "a253", "a254", "a255", "s4", "s5", "s6", "s7", "s8", "s9", "s10", "s11", "s12", "s13", "s14", "s15", "s16", "s17", "s18", "s19", "s20", "s21", "s22", "s23", "s24", "s25", "s40", "s41", "s42", "s43", "s44", "s45", "s46", "s47", "s48", "s49", "s50", "s51", "s52", "s53", "s54", "s55", "s56", "s57", "s58", "s59", "s60", "s61", "s62", "s63", "s64", "s65", "s66", "s67", "s68", "s69", "s70", "s71", "s72", "s73", "s74", "s75", "s76", "s77", "s78", "s79", "s80", "s81", "s82", "s83", "s84", "s85", "s86", "s87", "s88", "s89", "s90", "s91", "s92", "s93", "s94", "s95", "s96", "s97", "s98", "s99", "vcc", "scc", "memory");

  if (bad >> 16) {
    asm volatile("s_waitcnt vmcnt(0)" ::: "memory");
    const int id2 = (bad & 0xffff) >> 2, w2 = bad & 3, blk2 = (id2 >> 2) & 1, hq2 = id2 & 3, b2 = id2 >> 5, j2 = (id2 >> 3) & 3;
    slow_rows((const u16*)ka->br[blk2].x + (size_t)b2 * 114688, (const u16*)ka->br[blk2].wq, (const u16*)ka->br[blk2].k + (size_t)b2 * 2097152, (const u16*)ka->br[blk2].v + (size_t)b2 * 2097152,
              (const u16*)ka->br[blk2].wo, (u16*)ka->br[blk2].out + (size_t)b2 * 229376 + blk2 * 512 + j2 * 128 + hq2 * 32, w2, j2 * 128 + hq2 * 32, smem + 32768 + w2 * 2048);
  }
}
extern "C" void kernel_launch(void* const* d_in, const int* in_sizes, int n_in, void* d_out, int out_size, void* d_ws, size_t ws_size, hipStream_t stream) {
  Args a;
  for (int i = 0; i < 2; ++i) {
    a.br[i].x = (const char*)d_in[5 * i]; a.br[i].k = (const char*)d_in[5 * i + 1]; a.br[i].v = (const char*)d_in[5 * i + 2]; a.br[i].wq = (const char*)d_in[5 * i + 3]; a.br[i].wo = (const char*)d_in[5 * i + 4];
    a.br[i].out = (char*)d_out; a.br[i].ws = (char*)d_ws; a.br[i].ws2 = (char*)d_ws;
  }
  (void)hipFuncSetAttribute((const void*)k_main, hipFuncAttributeMaxDynamicSharedMemorySize, 163840);
  k_main<<<256, 256, 163840, stream>>>(a);
}
```
